```python
import torch

def _softmax(x, axis=-1):
    return torch.softmax(x, dim=axis)

def _gelu(x):
    return torch.nn.functional.gelu(x, approximate='tanh')

def _silu(x):
    return torch.nn.functional.silu(x)

def _sigmoid(x):
    return torch.sigmoid(x)

def _layernorm(x, axis=-1, eps=1e-05):
    m = x.mean(dim=axis, keepdim=True)
    v = x.var(dim=axis, keepdim=True, unbiased=False)
    return (x - m) / torch.sqrt(v + eps)

def _scatter_add(tgt, idx, src, axis=0):
    return tgt.clone().index_add_(axis, idx, src)

def _segment_sum(src, seg, n):
    out = torch.zeros((n,) + src.shape[1:], dtype=src.dtype)
    return out.index_add_(0, seg, src)

def _pad(x, pw):
    flat = tuple((v for pair in reversed(pw) for v in pair))
    return torch.nn.functional.pad(x, flat)
kernel_name = 'adaLN_cond__sliding_block_cached__moe_arch_mod__decode'

def setup_inputs():

    _w = lambda a: (lambda t: t.to(torch.bfloat16) if t.is_floating_point() else t)(torch.as_tensor(a))

    def _A_setup():
        B, Sq, D = (8, 1, 576)
        Hq, Hkv, Sk, Dh = (32, 16, 8192, 128)
        assert Hq % Hkv == 0, (Hq, Hkv)
        return {'x': _w((lambda *s: torch.randn(*s, dtype=torch.bfloat16) * 0.1)(B, Sq, D)), 'k_cache': _w((lambda *s: torch.randn(*s, dtype=torch.bfloat16) * 0.8)(B, Hkv, Sk, Dh)), 'v_cache': _w((lambda *s: torch.randn(*s, dtype=torch.bfloat16) * 0.8)(B, Hkv, Sk, Dh)), 'wq': _w((lambda *s: torch.randn(*s, dtype=torch.bfloat16) * 0.8)(D, Hq * Dh) * D ** (-0.5)), 'wo': _w((lambda *s: torch.randn(*s, dtype=torch.bfloat16) * 0.8)(Hq * Dh, D) * (Hq * Dh) ** (-0.5))}
    _A_in = _A_setup()

    def _B_setup():
        B, S, D, F, C = (8, 1, 576, 4608, 1)
        return {'x': _w((lambda *s: torch.randn(*s, dtype=torch.bfloat16) * 0.1)(B, S, D)), 'w_r': _w((lambda *s: torch.randn(*s, dtype=torch.bfloat16) * 0.2)(D)), 'w_gate': _w((lambda *s: torch.randn(*s, dtype=torch.bfloat16) * 0.1)(D, F)), 'w_up': _w((lambda *s: torch.randn(*s, dtype=torch.bfloat16) * 0.1)(D, F)), 'w_down': _w((lambda *s: torch.randn(*s, dtype=torch.bfloat16) * 0.1)(F, D))}
    _B_in = _B_setup()
    return {'A_x': _A_in['x'], 'A_k_cache': _A_in['k_cache'], 'A_v_cache': _A_in['v_cache'], 'A_wq': _A_in['wq'], 'A_wo': _A_in['wo'], 'B_x': _B_in['x'], 'B_w_r': _B_in['w_r'], 'B_w_gate': _B_in['w_gate'], 'B_w_up': _B_in['w_up'], 'B_w_down': _B_in['w_down'], 'c': _w((lambda *s: torch.randn(*s, dtype=torch.bfloat16) * 0.4)(8, 576) * 8.0), 'w_mod': _w((lambda *s: torch.randn(*s, dtype=torch.bfloat16) * 0.4)(576, 6 * 576) * 576 ** (-0.5) * 8.0)}

def reference(A_x, A_k_cache, A_v_cache, A_wq, A_wo, B_x, B_w_r, B_w_gate, B_w_up, B_w_down, c, w_mod):

    def _A_ref(x, k_cache, v_cache, wq, wo):
        B, Sq, D = x.shape
        Hq, Dh = (32, 128)
        Hkv, Sk = (k_cache.shape[1], k_cache.shape[2])
        W = min(4096, max(256, Sk // 4))
        rep = Hq // Hkv
        q = (lambda x, *d: x.permute(*d))((x @ wq).reshape(B, Sq, Hq, Dh), 0, 2, 1, 3)
        k = (lambda x, n, axis: torch.repeat_interleave(x, n, dim=axis))(k_cache[:, :, Sk - W:, :], rep, axis=1)
        v = (lambda x, n, axis: torch.repeat_interleave(x, n, dim=axis))(v_cache[:, :, Sk - W:, :], rep, axis=1)
        scores = torch.einsum('bhqd,bhkd->bhqk', q, k) * Dh ** (-0.5)
        p = _softmax(scores, axis=-1)
        o = torch.einsum('bhqk,bhkd->bhqd', p, v)
        return (lambda x, *d: x.permute(*d))(o, 0, 2, 1, 3).reshape(B, Sq, Hq * Dh) @ wo

    def _B_ref(x, w_r, w_gate, w_up, w_down):
        B, S, D = x.shape
        C = 1
        r = x @ w_r
        idx = torch.argsort(-r, axis=-1)[:, :C]
        idx_e = (lambda x, n, axis: torch.repeat_interleave(x, n, dim=axis))(idx.reshape(B, C, 1), D, axis=2)
        x_sel = (lambda x, i, axis: torch.take_along_dim(x, i, dim=axis))(x, idx_e, axis=1)
        g = _silu(x_sel @ w_gate)
        u = x_sel @ w_up
        y = g * u @ w_down
        r_sel = _sigmoid((lambda x, i, axis: torch.take_along_dim(x, i, dim=axis))(r, idx, axis=1))
        y = y * r_sel.reshape(B, C, 1)
        b_off = (torch.arange(B) * S).reshape(B, 1)
        flat_idx = (idx + b_off).reshape(B * C)
        zero = (x * 0.0).reshape(B * S, D)
        upd = _scatter_add(zero, flat_idx, y.reshape(B * C, D), axis=0)
        return x + upd.reshape(B, S, D)
    x = A_x
    mod = (_silu(c) @ w_mod).reshape(c.shape[0], 6, 1, -1)
    sa, ca, ga, sb, cb, gb = (mod[:, 0].clone(), mod[:, 1].clone(), mod[:, 2].clone(), mod[:, 3].clone(), mod[:, 4].clone(), mod[:, 5].clone())
    h = _layernorm(x, axis=-1) * (1 + ca) + sa
    x = x + ga * _A_ref(h, A_k_cache, A_v_cache, A_wq, A_wo)
    h = _layernorm(x, axis=-1) * (1 + cb) + sb
    x = x + gb * _B_ref(h, B_w_r, B_w_gate, B_w_up, B_w_down)
    return x
```

```cpp
#include <hip/hip_runtime.h>
#include <hip/hip_bf16.h>
#include <cstdint>

typedef unsigned u32;
typedef unsigned short u16;
typedef u32 u32x4 __attribute__((ext_vector_type(4)));
typedef u32 u32x2 __attribute__((ext_vector_type(2)));
typedef float f32x4 __attribute__((ext_vector_type(4)));
typedef float f32x2 __attribute__((ext_vector_type(2)));
typedef short s16x4 __attribute__((ext_vector_type(4)));
typedef __bf16 bf16x8 __attribute__((ext_vector_type(8)));
typedef __bf16 bf16x2 __attribute__((ext_vector_type(2)));
typedef __attribute__((address_space(3))) unsigned char lds_t;

constexpr int D = 576, SK = 8192, WIN = 2048, FF = 4608, NQ = 4096, NMOD = 6 * D;
constexpr int NWG = 256, NT = 512, NG = 72;
constexpr int SPIN_CAP = 1 << 15;

constexpr int W_O = 0;
constexpr int W_XP = W_O + 262144;
constexpr int W_Y = W_XP + 147456;
constexpr int W_MS = W_Y + NG * NG * 256;
constexpr int W_FL = W_MS;
constexpr int W_H = W_FL + 2048;
constexpr int W_M = W_H + 36864;
constexpr int W_Q = W_M + 73728;
constexpr int W_ST = W_Q + 131072;
constexpr int W_H2 = W_ST + 32768;
constexpr int W_X1 = W_H2 + 18432;
constexpr int W_END = W_X1 + 8 * 152 * 16;
constexpr int NPZ = 174, PZ = (W_MS / 16 + NPZ - 1) / NPZ;
static_assert(PZ <= 2 * NT, "poison split");

constexpr int L_W = 0;
constexpr int L_IMG = 73728;
constexpr int L_TILE = L_IMG + 9216;
constexpr int L_OI = 65536;
constexpr int L_B = 147456;
constexpr int L_C = 155648;
constexpr int LDS_BYTES = 163840;

#define STAMP(i) do {} while (0)

#define SCHED() __builtin_amdgcn_sched_barrier(0)
#define BAR() asm volatile("s_barrier" ::: "memory")
#define TS(i) do {} while (0)
#define TSOUT() do {} while (0)
#define TSA(i) do {} while (0)
#define TSB(i) do {} while (0)
#define TSC(i) do {} while (0)
#define WAITV_(n) do { asm volatile("s_waitcnt vmcnt(" #n ")" ::: "memory"); SCHED(); } while (0)
#define WAITV(n) WAITV_(n)
#define DK 5
#define DV DK
#define PR 117
#define PR2 126
#define PR3 120

__device__ __forceinline__ float lo16(u32 u) { return __uint_as_float(u << 16); }
__device__ __forceinline__ float hi16(u32 u) { return __uint_as_float(u & 0xffff0000u); }
__device__ __forceinline__ float b2f(u16 u) { return __uint_as_float((u32)u << 16); }
__device__ __forceinline__ u32 cvt2(float a, float b) { return __builtin_bit_cast(u32, __builtin_convertvector(f32x2{a, b}, bf16x2)); }
__device__ __forceinline__ float rbf(float v) { return __uint_as_float(cvt2(0.f, v)); }
__device__ __forceinline__ u32 sanf(float f) { u32 v = __float_as_uint(f); return v < 0xfffffffeu ? v : 0xfffffffeu; }
__device__ __forceinline__ f32x2 up2(u32 u) { return f32x2{lo16(u), hi16(u)}; }
__device__ __forceinline__ f32x2 rbf2(f32x2 v) { return f32x2{rbf(v.x), rbf(v.y)}; }
__device__ __forceinline__ void lds_barrier() { asm volatile("s_waitcnt lgkmcnt(0)\n\ts_barrier" ::: "memory"); }
__device__ __forceinline__ f32x4 mfma(u32x4 a, u32x4 b, f32x4 c) {
  return __builtin_amdgcn_mfma_f32_16x16x32_bf16(__builtin_bit_cast(bf16x8, a), __builtin_bit_cast(bf16x8, b), c, 0, 0, 0);
}
__device__ __forceinline__ u32 plo(u32 a, u32 b) { return __builtin_amdgcn_perm(b, a, 0x05040100u); }
__device__ __forceinline__ u32 phi(u32 a, u32 b) { return __builtin_amdgcn_perm(b, a, 0x07060302u); }

extern "C" __device__ __attribute__((const)) unsigned long long llvm_dispatch_id() __asm("llvm.amdgcn.dispatch.id");
typedef __amdgpu_buffer_rsrc_t rsrc_t;
__device__ __forceinline__ u32x4 ld128(rsrc_t rs, int off) { return __builtin_amdgcn_raw_buffer_load_b128(rs, off, 0, 16); }
__device__ __forceinline__ void st128(rsrc_t rs, int off, u32x4 v) { __builtin_amdgcn_raw_buffer_store_b128(v, rs, off, 0, 16); }
__device__ __forceinline__ void st64(rsrc_t rs, int off, u32x2 v) { __builtin_amdgcn_raw_buffer_store_b64(v, rs, off, 0, 16); }
__device__ __forceinline__ void dma(const void* gbase, u32 voff, unsigned lds_dst) {
  unsigned keep;
  asm volatile("s_mov_b32 %0, m0\n\ts_mov_b32 m0, %3\n\ts_nop 2\n\tglobal_load_lds_dwordx4 %1, %2 nt\n\ts_mov_b32 m0, %0"
               : "=&s"(keep) : "v"(voff), "s"(gbase), "s"(lds_dst) : "memory");
}
__device__ __forceinline__ void dma_keep(const void* gbase, u32 voff, unsigned lds_dst) {
  unsigned keep;
  asm volatile("s_mov_b32 %0, m0\n\ts_mov_b32 m0, %3\n\ts_nop 2\n\tglobal_load_lds_dword %1, %2\n\ts_mov_b32 m0, %0"
               : "=&s"(keep) : "v"(voff), "s"(gbase), "s"(lds_dst) : "memory");
}
__device__ __forceinline__ void dma_sc(const void* gbase, u32 voff, unsigned lds_dst) {
  unsigned keep;
  asm volatile("s_mov_b32 %0, m0\n\ts_mov_b32 m0, %3\n\ts_nop 2\n\tglobal_load_lds_dwordx4 %1, %2 sc1\n\ts_mov_b32 m0, %0"
               : "=&s"(keep) : "v"(voff), "s"(gbase), "s"(lds_dst) : "memory");
}
__device__ __forceinline__ void ald128(u32x4& d, const void* gbase, u32 voff) { unsigned long long t; asm volatile("s_mov_b64 %1, %3\n\tglobal_load_dwordx4 %0, %2, %1" : "=v"(d), "=&s"(t) : "v"(voff), "s"(gbase) : "memory"); }
__device__ __forceinline__ void ald64(u32x2& d, const void* gbase, u32 voff) { unsigned long long t; asm volatile("s_mov_b64 %1, %3\n\tglobal_load_dwordx2 %0, %2, %1" : "=v"(d), "=&s"(t) : "v"(voff), "s"(gbase) : "memory"); }
__device__ __forceinline__ void ald64nt(u32x2& d, const void* gbase, u32 voff) { unsigned long long t; asm volatile("s_mov_b64 %1, %3\n\tglobal_load_dwordx2 %0, %2, %1 nt" : "=v"(d), "=&s"(t) : "v"(voff), "s"(gbase) : "memory"); }
__device__ __forceinline__ void ald128sc(u32x4& d, const void* gbase, u32 voff) { unsigned long long t; asm volatile("s_mov_b64 %1, %3\n\tglobal_load_dwordx4 %0, %2, %1 sc1" : "=v"(d), "=&s"(t) : "v"(voff), "s"(gbase) : "memory"); }
__device__ __forceinline__ u32x2 trd(const unsigned char* lds, int off) {
  return __builtin_bit_cast(u32x2, __builtin_amdgcn_ds_read_tr16_b64_v4i16((__attribute__((address_space(3))) s16x4*)((lds_t*)lds + off)));
}
#define nap(n) do { __builtin_amdgcn_s_sleep(n); asm volatile("" ::: "memory"); } while (0)
__device__ __forceinline__ float frcp(float v) { return __builtin_amdgcn_rcpf(v); }
__device__ __forceinline__ float fdiv(float a, float d, float r) { const float q = a * r; return fmaf(fmaf(-q, d, a), r, q); }
__device__ __forceinline__ float silu(float v) { const float d = 1.f + __expf(-v); const float r = frcp(d); return d < 3.0e38f ? fdiv(v, d, r) : v * r; }
template <int CTRL> __device__ __forceinline__ float dppf(float x) {
  return __builtin_bit_cast(float, __builtin_amdgcn_mov_dpp(__builtin_bit_cast(int, x), CTRL, 0xf, 0xf, true));
}
__device__ __forceinline__ u32 mxu(u32 a, u32 b) { return a > b ? a : b; }
__device__ __forceinline__ u32 mx4(u32x4 v) { return mxu(mxu(v[0], v[1]), mxu(v[2], v[3])); }
__device__ __forceinline__ float rl(float v, int l) { return __builtin_bit_cast(float, __builtin_amdgcn_readlane(__builtin_bit_cast(int, v), l)); }
__device__ __forceinline__ float wsum(float v) {
  v += dppf<0xB1>(v); v += dppf<0x4E>(v); v += dppf<0x141>(v); v += dppf<0x140>(v);
  return (rl(v, 0) + rl(v, 16)) + (rl(v, 32) + rl(v, 48));
}
__device__ __forceinline__ float up32(float v) { const u32 u = __float_as_uint(v); return __uint_as_float(__builtin_amdgcn_permlane32_swap(u, u, false, false)[1]); }
__device__ __forceinline__ float xsum32(float v) { const u32 u = __float_as_uint(v); const auto r = __builtin_amdgcn_permlane32_swap(u, u, false, false); return __uint_as_float(r[0]) + __uint_as_float(r[1]); }
__device__ __forceinline__ float xsum16(float v) { const u32 u = __float_as_uint(v); const auto r = __builtin_amdgcn_permlane16_swap(u, u, false, false); return __uint_as_float(r[0]) + __uint_as_float(r[1]); }
__device__ __forceinline__ f32x4 half_sum(f32x4 d) {
  f32x4 r;
#pragma unroll
  for (int j = 0; j < 4; ++j) r[j] = d[j] + dppf<0x108>(up32(d[j]));
  return r;
}
__device__ __forceinline__ f32x2 half_sum2(f32x4 d) {
  f32x2 r;
#pragma unroll
  for (int j = 0; j < 2; ++j) {
    const auto s = __builtin_amdgcn_permlane32_swap(__float_as_uint(d[j]), __float_as_uint(d[j + 2]), false, false);
    r[j] = __uint_as_float(s[0]) + dppf<0x108>(__uint_as_float(s[1]));
  }
  return r;
}
__device__ __forceinline__ void unpack8(u32x4 u, float* f) {
#pragma unroll
  for (int j = 0; j < 4; ++j) { f[2 * j] = lo16(u[j]); f[2 * j + 1] = hi16(u[j]); }
}
__device__ __forceinline__ void ln_row(const float (&v)[16], bool has2, float& mean, float& den) {
  float s = 0.f;
#pragma unroll
  for (int i = 0; i < 8; ++i) s += v[i];
  if (has2) {
#pragma unroll
    for (int i = 8; i < 16; ++i) s += v[i];
  }
  s = wsum(s);
  const float mu = s * (1.f / D);
  mean = rbf(mu);
  float q = 0.f;
#pragma unroll
  for (int i = 0; i < 8; ++i) { const float t = v[i] - mu; q = fmaf(t, t, q); }
  if (has2) {
#pragma unroll
    for (int i = 8; i < 16; ++i) { const float t = v[i] - mu; q = fmaf(t, t, q); }
  }
  q = wsum(q);
  const float var = rbf(q * (1.f / D));
  den = rbf(sqrtf(rbf(var + 1e-5f)));
}
template <int J0 = 0, int J1 = 9>
__device__ __forceinline__ void slab_dma(const u16* base, int stride_b, unsigned lds_dst, int wave, int lane) {
  const u32 v0 = (lane >> 3) * stride_b + (((lane & 7) ^ ((wave & 1) * 4 + (lane >> 4))) << 4), v1 = v0 ^ 32u;
  const u32 v4 = wave >= 4 ? v1 : v0;
  const char* gb = (const char*)base + (size_t)wave * 8 * stride_b;
#pragma unroll
  for (int j = J0; j < J1; ++j) dma(gb + (size_t)j * 64 * stride_b, j < 4 ? v0 : j == 4 ? v4 : v1, lds_dst + (j * 8 + wave) * 1024);
}
__device__ __forceinline__ void slab_frags(const unsigned char* lds, int base, int wave, int lane, u32x4 (&wf)[9]) {
  const int g = lane >> 4, q = (lane & 15) >> 2, p = lane & 3, hf = p >> 1;
  const int a0 = base + (hf * 288 + 8 * g + q) * 128 + ((wave ^ ((g & 1) * 4 + (q >> 1)) ^ (hf * 2)) << 4) + (p & 1) * 8, a1 = (a0 ^ 32) + 512;
#pragma unroll
  for (int s = 0; s < 9; ++s) {
    const u32x2 lo = trd(lds, a0 + s * 4096), hi = trd(lds, a1 + s * 4096);
    wf[s] = u32x4{lo[0], lo[1], hi[0], hi[1]};
  }
}
__device__ __forceinline__ void img_frags(const unsigned char* lds, int img, int lane, u32x4 (&af)[9]) {
#pragma unroll
  for (int s = 0; s < 9; ++s) af[s] = *(const u32x4*)(lds + img + (s * 64 + lane) * 16);
}
__device__ __forceinline__ f32x4 gemv9(const u32x4 (&wf)[9], const u32x4 (&af)[9]) {
  f32x4 acc[3];
  SCHED();
#pragma unroll
  for (int s = 0; s < 9; ++s) acc[s % 3] = mfma(wf[s], af[s], s < 3 ? f32x4{0.f, 0.f, 0.f, 0.f} : acc[s % 3]);
  return (acc[0] + acc[1]) + acc[2];
}
__device__ __forceinline__ int img_off(int b, int k8) {
  const int hf = k8 >= 36 ? 1 : 0, kk = k8 - hf * 36;
  return ((kk >> 2) * 64 + (kk & 3) * 16 + hf * 8 + b) * 16;
}
__device__ __forceinline__ u32 tag_x(u32x4 v, u32 e0, u32 e1) { return (v[2] ^ e0) | (v[3] ^ e1); }
__device__ __forceinline__ bool tag_ok(u32x4 v, u32 e0, u32 e1) { return tag_x(v, e0, e1) == 0u; }

constexpr int CODE_BYTES = 29696;

__global__ void __launch_bounds__(NT, 2) fused_kernel(const u16* __restrict__ x, const u16* __restrict__ kc, const u16* __restrict__ vc,
                                                      const u16* __restrict__ wq, const u16* __restrict__ wo, const u16* __restrict__ w_r,
                                                      const u16* __restrict__ wg, const u16* __restrict__ wu, const u16* __restrict__ wd,
                                                      const u16* __restrict__ c, const u16* __restrict__ w_mod, unsigned char* ws, u16* out) {
  __shared__ __attribute__((aligned(16))) unsigned char lds[LDS_BYTES];
  const int w = blockIdx.x, xcd = w & 7, idx = w >> 3;
  const int tid = threadIdx.x, lane = tid & 63, wave = __builtin_amdgcn_readfirstlane(tid >> 6);
  const int n = lane & 15, grp = lane >> 4;
  const rsrc_t rs = __builtin_amdgcn_make_buffer_rsrc(ws, 0, W_END, 0x00020000);
  const unsigned lds_base = (unsigned)(uintptr_t)(lds_t*)lds;
  const unsigned long long tA_ = __builtin_amdgcn_s_memrealtime();
  u32 lat_ = 0;
  if (blockIdx.x == 0 && wave == 0) {
    const char* pp_ = (const char*)kc + ((size_t)(7 * 16 + 15) * SK + (SK - WIN)) * 256;
    u32x4 d_; ald128(d_, pp_, 0); asm volatile("s_waitcnt vmcnt(0)" : "+v"(d_) :: "memory");
    for (int i_ = 1; i_ <= 8; ++i_) {
      const unsigned long long a_ = __builtin_amdgcn_s_memtime(); asm volatile("s_waitcnt lgkmcnt(0)" ::: "memory");
      ald128(d_, pp_ + i_ * 4352, 0); asm volatile("s_waitcnt vmcnt(0)" : "+v"(d_) :: "memory");
      const unsigned long long b_ = __builtin_amdgcn_s_memtime(); asm volatile("s_waitcnt lgkmcnt(0)" ::: "memory");
      lat_ += (u32)(b_ - a_);
    }
    lat_ = lat_ * 10 / 8; if (lat_ > 30000u) lat_ = 30000u;
  }
  while (__builtin_amdgcn_s_memrealtime() - tA_ < 1200ull) __builtin_amdgcn_s_sleep(1);
  const unsigned long long epoch = (llvm_dispatch_id() + 1ull) * 0x9E3779B97F4A7C15ull;
  const u32 e0 = (u32)epoch, e1 = (u32)(epoch >> 32);
  unsigned long long pc0; asm volatile("s_getpc_b64 %0" : "=s"(pc0));
  asm volatile("" : "+s"(vc), "+s"(w_r), "+s"(wu), "+s"(wd), "+s"(out), "+s"(x), "+s"(w_mod), "+s"(wg), "+s"(wq), "+s"(wo));

  const int mw = idx < 6 ? idx * 8 + xcd : -1;
  const int md = mw < 18 ? mw : -1;
  const int ml = mw >= 18 ? mw : mw >= 0 && mw < 6 ? 48 + mw : -1;
  const int lnb = mw >= 6 && mw < 14 ? mw - 6 : -1;
  const int qs = idx >= 6 && idx < 14 ? xcd * 8 + idx - 6 : -1;
  const int oj = idx >= 14 && idx < 23 ? idx - 14 : -1, op = xcd;
  const int gs = idx >= 23 ? xcd * 9 + idx - 23 : -1;

  const int s_att = w & 1, kvh = (w >> 1) & 15, b_att = w >> 5;
  {
    const size_t pairbase = ((size_t)(b_att * 16 + kvh) * SK + (SK - WIN)) * 256;
    const char* kp = (const char*)kc + pairbase;
    const char* vp = (const char*)vc + pairbase;
    const int wpart = (wave & 3) * 4096 + s_att * 16384 + (wave >> 2) * 262144;
    const int ko = wpart + (n >> 1) * 256 + (n & 1) * 64 + grp * 16;
    const int vo = wpart + n * 16 + grp * 512;
#define KLD(u) do { unsigned long long t_; asm volatile("s_mov_b64 %1, %3\n\tglobal_load_dwordx4 %0, %2, %1 offset:%4 nt" : "=v"(kb[u]), "=&s"(t_) : "v"(ko), "s"(kp + ((u) >> 2) * 32768), "i"((((u) >> 1) & 1) * 2048 + ((u) & 1) * 128) : "memory"); } while (0)
#define VLD(u) do { unsigned long long t_; asm volatile("s_mov_b64 %1, %3\n\tglobal_load_dwordx4 %0, %2, %1 offset:%4 nt" : "=v"(vb[u]), "=&s"(t_) : "v"(vo), "s"(vp + ((u) >> 2) * 32768), "i"((((u) >> 1) & 1) * 2048 + ((u) & 1) * 256) : "memory"); } while (0)
#define TIE8(a, o) asm volatile("" : "+v"(a[o]), "+v"(a[(o) + 1]), "+v"(a[(o) + 2]), "+v"(a[(o) + 3]), "+v"(a[(o) + 4]), "+v"(a[(o) + 5]), "+v"(a[(o) + 6]), "+v"(a[(o) + 7]))
    const int pr_ = gs >= 0 ? PR2 : mw >= 18 ? PR3 : PR;
    const u32 tp0 = (u32)__builtin_amdgcn_s_memrealtime() + (gs >= 0 || mw >= 18 ? 0 : 9 * PR / 4);
#define PACE(i) do { _Pragma("nounroll") while ((int)((u32)__builtin_amdgcn_s_memrealtime() - tp0) < (i) * pr_ / 4) __builtin_amdgcn_s_sleep(2); SCHED(); } while (0)
#define REP4(M, u, W) do { M(u); W; M((u) + 1); W; M((u) + 2); W; M((u) + 3); W; } while (0)
    static_assert(DK <= 7 && DV <= 7, "the hand-placed waits assume it");
    u32x4 kb[32], vb[32];
    const int pz = mw >= 18 ? mw - 18 : idx >= 14 ? w - 82 : -1;
#define POISON() do { if (pz >= 0) { _Pragma("unroll") for (int i = 0; i < 2; ++i) { const int g_ = pz * PZ + i * NT + tid; \
      if (i * NT + tid < PZ && g_ < W_MS / 16) st128(rs, g_ * 16, u32x4{0xffffffffu, 0xffffffffu, 0xffffffffu, 0xffffffffu}); } } SCHED(); } while (0)
#define PFLAG() do { if (pz >= 0 && tid == 448) st64(rs, W_FL + pz * 8, u32x2{e0, e1}); SCHED(); } while (0)
#define SILU_IMAGE(c0, c1) do { float f[8]; unpack8(c0, f); u32x4 o; \
        _Pragma("unroll") for (int j = 0; j < 4; ++j) o[j] = cvt2(silu(f[2 * j]), silu(f[2 * j + 1])); \
        const int b_ = tid / 72, ch_ = tid - b_ * 72; \
        *(u32x4*)(lds + L_IMG + img_off(b_, ch_)) = o; \
        if (wave == 0) { unpack8(c1, f); \
          _Pragma("unroll") for (int j = 0; j < 4; ++j) o[j] = cvt2(silu(f[2 * j]), silu(f[2 * j + 1])); \
          *(u32x4*)(lds + L_IMG + img_off(7, 8 + lane)) = o; } } while (0)
    if (md >= 0) {
      u32x4 c0, c1, x0, x1; u32x2 xc;
      __builtin_amdgcn_s_waitcnt(0x0f70);
      TSA(0);
      int ho = W_H + img_off(n & 7, (md >= 9 ? md - 9 : md) * 8 + wave) * 2 + (grp & 1) * 16;
      ald128(c0, c, tid * 16);
      ald128(c1, c, (512 + lane) * 16);
      ald128(x0, x, wave * 1152 + lane * 16);
      ald128(x1, x, wave * 1152 + 1024 + (lane & 7) * 16);
      ald64(xc, x, (n & 7) * 1152 + ((md >= 9 ? md - 9 : 0) * 64 + wave * 8 + (grp & 1) * 4) * 2);
      BAR();
      float* st = (float*)(lds + L_TILE + 1024);
      if (wave < 4) {
        slab_dma(w_mod + md * 64, NMOD * 2, lds_base + L_W, wave, lane);
        asm volatile("s_waitcnt vmcnt(9)" ::: "memory");
      } else asm volatile("s_waitcnt vmcnt(0)" ::: "memory");
      asm volatile("s_nop 0 ; both branches above cover V" : "+v"(c0), "+v"(c1), "+v"(x0), "+v"(x1), "+v"(xc) :: "memory");
      TSA(1);
      {
        SILU_IMAGE(c0, c1);
        float v[16]; unpack8(x0, v); unpack8(x1, v + 8);
        float mean, den; ln_row(v, lane < 8, mean, den);
        if (lane == 0) { st[wave] = mean; st[8 + wave] = den; }
      }
      TSA(2);
      if (wave >= 4) slab_dma(w_mod + md * 64, NMOD * 2, lds_base + L_W, wave, lane);
      TSA(3);
      lds_barrier();
      REP4(KLD, 0, );
      u32x4 wf[9], af[9];
      img_frags(lds, L_IMG, lane, af);
      const float mean = st[n & 7], rd = frcp(st[8 + (n & 7)]);
      float ln[4] = {rbf(rbf(lo16(xc[0]) - mean) * rd), rbf(rbf(hi16(xc[0]) - mean) * rd), rbf(rbf(lo16(xc[1]) - mean) * rd), rbf(rbf(hi16(xc[1]) - mean) * rd)};
#pragma unroll
      for (int s = 0; s < 9; ++s) asm volatile("" : "+v"(af[s]));
      asm volatile("" : "+v"(ln[0]), "+v"(ln[1]), "+v"(ln[2]), "+v"(ln[3]), "+v"(ho));
      WAITV(4); BAR(); SCHED();
      TSA(5);
      slab_frags(lds, L_W, wave, lane, wf);
      const f32x4 s = half_sum(gemv9(wf, af));
      STAMP(14); TSA(6);
      f32x4 r = s;
      if (md >= 9) {
#pragma unroll
        for (int j = 0; j < 4; ++j) r[j] = ln[j] * rbf(1.f + rbf(s[j]));
      }
      if (n < 8 && grp < 2) st128(rs, ho + (md >= 9 ? 18432 : 0), u32x4{cvt2(r[0], r[1]), cvt2(r[2], r[3]), e0, e1});
      SCHED();
      STAMP(15); TSA(7);
      BAR();
      KLD(4); KLD(5); WAITV(DK);
      KLD(6); WAITV(DK); KLD(7); WAITV(DK); KLD(8); WAITV(DK); KLD(9); WAITV(DK); KLD(10); WAITV(DK);
      KLD(11); WAITV(DK); REP4(KLD, 12, WAITV(DK)); REP4(KLD, 16, WAITV(DK));
      REP4(KLD, 20, WAITV(DK)); KLD(24); WAITV(DK); KLD(25);
    } else {
      constexpr int L_LAND = 98304;
      const unsigned land = lds_base + L_LAND + wave * 6144;
      const unsigned char* lp = lds + L_LAND + wave * 6144 + lane * 16;
      const int sl_ = (3 - (int)((uintptr_t)wq >> 7)) & 7;
      const bool slowq = qs >= 0 && (qs & 7) == sl_;
      if (gs >= 0 && wave == 0) dma_keep((const char*)wq + (size_t)(idx - 23) * 64 * 8192 + xcd * 1024 + sl_ * 128, lane * 8192, land);
      POISON();
      if (oj >= 0 && idx < 17 && wave * 8192 < CODE_BYTES)
        dma_keep((const void*)pc0, min(wave * 8192 + lane * 128, CODE_BYTES - 128), lds_base + 107520 + wave * 256);
      const u32 v0 = (lane >> 3) * 1152 + (((lane & 7) ^ ((lane >> 4) & 1)) << 4);
      const char* gb = (const char*)(wo + (size_t)(op * 512 + wave * 64) * D + oj * 64);
      if (ml >= 0) { dma_sc(c, tid * 16, land); dma_sc(c, (512 + lane) * 16, land + 1024); }
#define WK(j) do { if (oj >= 0) { if ((j) < 8) dma(gb + (j) * 8 * 1152, v0 ^ (((j) & 1) * 32 + ((j) >> 2) * 64), lds_base + L_W + (wave * 8 + (j)) * 1024); } \
                   else if (slowq) { if ((j) == 8) slab_dma(wq + qs * 64, NQ * 2, lds_base + L_W, wave, lane); } \
                   else if (qs >= 0) slab_dma<j, (j) + 1>(wq + qs * 64, NQ * 2, lds_base + L_W, wave, lane); \
                   KLD(j); WAITV(DK); } while (0)
      WK(0); WK(1); WK(2); WK(3);
      WK(4); WK(5);
      WK(6); WK(7);
      if (ml >= 0) { const u32x4 c0 = *(const u32x4*)lp, c1 = *(const u32x4*)(lp + 1024); SILU_IMAGE(c0, c1); }
      BAR();
      PFLAG();
      WK(8); KLD(9); WAITV(DK); KLD(10); WAITV(DK); KLD(11); WAITV(DK);
      PACE(12); REP4(KLD, 12, WAITV(DK));
      const int hg0 = (wave * 144 + lane) * 16, hg2 = (wave * 144 + 128 + n) * 16;
#define HREQ() do { dma_sc(ws + W_H, hg0, land); dma_sc(ws + W_H + 18432, hg0, land + 1024); dma_sc(ws + W_H + 1024, hg0, land + 2048); \
                    dma_sc(ws + W_H + 18432 + 1024, hg0, land + 3072); dma_sc(ws + W_H, hg2, land + 4096); dma_sc(ws + W_H + 18432, hg2, land + 5120); } while (0)
#define HGET() do { _Pragma("unroll") for (int i = 0; i < 6; ++i) hq[i] = *(const u32x4*)(lp + i * 1024); } while (0)
#define HQOK() __all((tag_x(hq[0], e0, e1) | tag_x(hq[1], e0, e1) | tag_x(hq[2], e0, e1) | tag_x(hq[3], e0, e1) | tag_x(hq[4], e0, e1) | tag_x(hq[5], e0, e1)) == 0u)
#define HOK() __all((tag_x(hp[0], e0, e1) | tag_x(hp[1], e0, e1) | tag_x(hp[2], e0, e1)) == 0u)
      TSA(4);
      if (qs >= 0) HREQ();
      PACE(16); REP4(KLD, 16, WAITV(DK));
      if (qs >= 0) WAITV(4);
      TSA(5);
      PACE(20); KLD(20); KLD(21);
      if (qs >= 0) {
        u32x4 hq[6]; HGET();
        _Pragma("nounroll") for (int sp = 0; sp < SPIN_CAP && !HQOK(); ++sp) { nap(16); HREQ(); WAITV(0); HGET(); }
        STAMP(14); TSA(6);
#define HSUM(a, b) cvt2(lo16(a) + lo16(b), hi16(a) + hi16(b))
        *(u32x2*)(lds + L_IMG + hg0 / 2) = u32x2{HSUM(hq[0][0], hq[1][0]), HSUM(hq[0][1], hq[1][1])};
        *(u32x2*)(lds + L_IMG + 512 + hg0 / 2) = u32x2{HSUM(hq[2][0], hq[3][0]), HSUM(hq[2][1], hq[3][1])};
        if (lane < 16) *(u32x2*)(lds + L_IMG + hg2 / 2) = u32x2{HSUM(hq[4][0], hq[5][0]), HSUM(hq[4][1], hq[5][1])};
      } else WAITV(DK);
      KLD(22); KLD(23);
      if (qs >= 0) {
        u32x4 wf[9], af[9];
        lds_barrier();
        slab_frags(lds, L_W, wave, lane, wf);
        img_frags(lds, L_IMG, lane, af);
        const f32x4 s = half_sum(gemv9(wf, af));
        if (n < 8 && grp < 2) *(u32x2*)(lds + L_TILE + n * 128 + wave * 16 + grp * 8) = u32x2{cvt2(s[0], s[1]), cvt2(s[2], s[3])};
      } else WAITV(DK);
      PACE(24); KLD(24); KLD(25);
      if (qs >= 0) {
        lds_barrier();
        if (wave < 2) {
          const u32x2 t2 = *(const u32x2*)(lds + L_TILE + tid * 8);
          st128(rs, W_Q + ((tid >> 4) * 1024 + qs * 16 + n) * 16, u32x4{t2[0], t2[1], e0, e1});
        }
        STAMP(15); TSA(7);
      }
      SCHED();
    }
    STAMP(3); TSB(0);
    STAMP(2);
    const unsigned char* qg = ws + W_Q + (b_att * 1024 + kvh * 64) * 16;
    KLD(26); WAITV(DK); KLD(27); WAITV(DK); PACE(28); KLD(28); WAITV(DK); KLD(29); WAITV(DK); PACE(30); KLD(30); WAITV(DK); KLD(31); WAITV(DK);
    TSB(1);
    PACE(32); VLD(0); WAITV(DK); VLD(1); WAITV(DK); PACE(34); VLD(2); WAITV(DK); VLD(3); WAITV(DK);
    if (wave == 1) dma_sc(qg, lane * 16, lds_base + L_B + 1024);
    PACE(36); VLD(4); WAITV(DK); VLD(5); WAITV(DK); PACE(38); VLD(6); WAITV(DK); VLD(7); WAITV(DK);
    if (wave == 1) dma_sc(qg, lane * 16, lds_base + L_B + 1024);
    PACE(40); VLD(8); WAITV(DV); VLD(9); WAITV(DV); PACE(42); VLD(10); WAITV(DV); VLD(11); WAITV(DV);
    TIE8(kb, 0); TIE8(kb, 8); TIE8(kb, 16); TIE8(kb, 24);
    PACE(44); VLD(12); VLD(13);
    u32x4 qf[2];
    {
      const u32x4* ql = (const u32x4*)(lds + L_B + 1024 + (((n >> 1) & 1) * 32 + (n & 1) * 8 + grp * 2) * 16);
      u32x4 g[4];
      _Pragma("nounroll") for (int sp = 0; sp < SPIN_CAP; ++sp) {
        g[0] = ql[0]; g[1] = ql[1]; g[2] = ql[16]; g[3] = ql[17];
        if (__all((tag_x(g[0], e0, e1) | tag_x(g[1], e0, e1) | tag_x(g[2], e0, e1) | tag_x(g[3], e0, e1)) == 0u)) break;
        if (wave == 1) { dma_sc(qg, lane * 16, lds_base + L_B + 1024); WAITV(0); } else nap(4);
      }
      asm volatile("" ::: "memory");
      g[0] = ql[0]; g[1] = ql[1]; g[2] = ql[16]; g[3] = ql[17];
      qf[0] = n < 4 ? u32x4{g[0][0], g[0][1], g[1][0], g[1][1]} : u32x4{0, 0, 0, 0};
      qf[1] = n < 4 ? u32x4{g[2][0], g[2][1], g[3][0], g[3][1]} : u32x4{0, 0, 0, 0};
    }
    STAMP(4); TSB(2);
    constexpr float SCALE = 0.088388347648318440f;
    f32x2 sc[16];
    float mx = -INFINITY;
#pragma unroll
    for (int T = 0; T < 16; ++T) {
      f32x4 acc = mfma(kb[T * 2], qf[0], f32x4{0.f, 0.f, 0.f, 0.f});
      acc = mfma(kb[T * 2 + 1], qf[1], acc);
      sc[T] = f32x2{acc[0] + dppf<0xB1>(acc[1]), acc[2] + dppf<0xB1>(acc[3])};
      if (T == 7 || T == 15) { SCHED(); if (T == 7) VLD(14); else VLD(15); SCHED(); }
    }
#pragma unroll
    for (int T = 0; T < 16; ++T)
#pragma unroll
      for (int j = 0; j < 2; ++j) { const float v = rbf(rbf(sc[T][j]) * SCALE); sc[T][j] = v; mx = fmaxf(mx, v); }
    SCHED(); VLD(16); SCHED();
    mx = fmaxf(mx, __shfl_xor(mx, 16));
    mx = fmaxf(mx, __shfl_xor(mx, 32));
    float ls = 0.f;
#pragma unroll
    for (int T = 0; T < 16; ++T)
#pragma unroll
      for (int j = 0; j < 2; ++j) { sc[T][j] = __expf(sc[T][j] - mx); ls += sc[T][j]; }
    SCHED(); VLD(17); SCHED();
    ls += __shfl_xor(ls, 16);
    ls += __shfl_xor(ls, 32);
    float* stat = (float*)(lds + L_B);
    if (lane == 0 || lane == 2) *(f32x2*)(stat + (wave * 2 + (lane >> 1)) * 2) = f32x2{mx, ls};
    SCHED(); VLD(18); VLD(19); SCHED();
    TSB(3);
    lds_barrier();
    TSB(4);
    float M = -INFINITY, L = 0.f;
    {
      f32x2 t[8];
#pragma unroll
      for (int q = 0; q < 8; ++q) { t[q] = *(const f32x2*)(stat + (q * 2 + ((n >> 1) & 1)) * 2); M = fmaxf(M, t[q].x); }
#pragma unroll
      for (int q = 0; q < 8; ++q) L = fmaf(__expf(t[q].x - M), t[q].y, L);
    }
    if (tid < 8) st128(rs, W_ST + (w * 8 + lane) * 16, u32x4{__float_as_uint(M), __float_as_uint(L), e0, e1});
    SCHED();
#define VTR(G) do { u32x4 f_[8]; _Pragma("unroll") for (int t = 0; t < 8; ++t) _Pragma("unroll") for (int j = 0; j < 4; ++j) \
      f_[t][j] = (t & 1) ? phi(vb[(G) * 8 + 2 * j][t >> 1], vb[(G) * 8 + 2 * j + 1][t >> 1]) : plo(vb[(G) * 8 + 2 * j][t >> 1], vb[(G) * 8 + 2 * j + 1][t >> 1]); \
      _Pragma("unroll") for (int t = 0; t < 8; ++t) { vb[(G) * 8 + t] = f_[t]; asm volatile("" : "+v"(vb[(G) * 8 + t])); } SCHED(); } while (0)
    WAITV(12);
    TIE8(vb, 0);
    VTR(0);
    PACE(52); VLD(20); WAITV(5);
    TIE8(vb, 8);
    VTR(1);
    VLD(21); WAITV(DV);
    const int po = W_ST + ((w ^ 1) * 8 + (n & 2)) * 16;
    int l1 = lane; asm volatile("" : "+v"(l1));
    const int fl = 1024 + min(l1, NPZ / 2 - 65) * 16;
    if (wave == 1) { dma_sc(ws + W_FL, l1 * 16, lds_base + L_B + 2048); dma_sc(ws + W_FL, fl, lds_base + L_B + 3072); }
    PACE(54);
    VLD(22); WAITV(DV); VLD(23); WAITV(DV);
    u32x4 ps;
    ald128sc(ps, ws, po);
    PACE(56);
    VLD(24); WAITV(DV); VLD(25); WAITV(DV);
    PACE(58);
    VLD(26); WAITV(DV); VLD(27); WAITV(DV);
    VLD(28); VLD(29); WAITV(6);
    asm volatile("" : "+v"(ps));
    TIE8(vb, 16);
    VLD(30); VLD(31);
    VTR(2);
    if (!__all(tag_ok(ps, e0, e1))) {
      int sp = 0;
      _Pragma("nounroll") do { nap(1); ps = ld128(rs, po); } while (!__all(tag_ok(ps, e0, e1)) && ++sp < SPIN_CAP);
    }
#define FOK(a, b) __all(((a[0] ^ e0) | (a[1] ^ e1) | tag_x(a, e0, e1) | (b[0] ^ e0) | (b[1] ^ e1) | tag_x(b, e0, e1)) == 0u)
    if (wave == 1 && !FOK((*(const u32x4*)(lds + L_B + 2048 + l1 * 16)), (*(const u32x4*)(lds + L_B + 3072 + l1 * 16)))) {
      _Pragma("nounroll") for (int sp = 0; sp < SPIN_CAP; ++sp) {
        nap(1);
        const u32x4 a = ld128(rs, W_FL + l1 * 16), b = ld128(rs, W_FL + fl);
        if (FOK(a, b)) break;
      }
    }
    float fac;
    {
      const float M2 = __uint_as_float(ps[0]), L2 = __uint_as_float(ps[1]);
      const float Mg = fmaxf(M, M2);
      const float Lg = __expf(M - Mg) * L + __expf(M2 - Mg) * L2;
      fac = __expf(mx - Mg) / Lg;
    }
    STAMP(1); TSB(5);
    u32x4 pf[4];
#pragma unroll
    for (int G = 0; G < 4; ++G) {
#pragma unroll
      for (int j = 0; j < 4; ++j) pf[G][j] = cvt2(sc[4 * G + j][0] * fac, sc[4 * G + j][1] * fac);
    }
    f32x4 oa[8];
#pragma unroll
    for (int t = 0; t < 8; ++t) oa[t] = f32x4{0.f, 0.f, 0.f, 0.f};
    SCHED();
#pragma unroll
    for (int G = 0; G < 2; ++G)
#pragma unroll
      for (int t = 0; t < 8; ++t) oa[t] = mfma(pf[G], vb[G * 8 + t], oa[t]);
    SCHED();
#pragma unroll
    for (int t = 0; t < 8; ++t) oa[t] = mfma(pf[2], vb[16 + t], oa[t]);
    SCHED();
    int t2_ = threadIdx.x; asm volatile("" : "+v"(t2_));
    const int tid = t2_, lane = tid & 63, n = lane & 15, grp = lane >> 4;
    int oo = W_O + ((kvh * 8 + b_att) * 2 + s_att) * 1024 + ((lane >> 4) * 16 + (lane & 1) * 8 + ((lane >> 1) & 7)) * 16;
    int mw_ = L_C + wave * 1024 + n * 32, mr_ = L_C + lane * 16;
    asm volatile("" : "+v"(oo), "+v"(mw_), "+v"(mr_), "+v"(pf[3]));
    const bool late = gs >= 0 || mw >= 18;
    if (late) {
      BAR();
      slab_dma<0, 4>(gs >= 0 ? wg + gs * 64 : w_mod + ml * 64, gs >= 0 ? FF * 2 : NMOD * 2, lds_base + L_W + (gs >= 0 ? 73728 : 0), wave, lane);
      WAITV(6);
    } else WAITV(2);
    u32x4 vf[8];
    asm volatile("s_nop 0 ; both branches above cover V up to the last two" : "+v"(vb[24]), "+v"(vb[25]), "+v"(vb[26]), "+v"(vb[27]), "+v"(vb[28]), "+v"(vb[29]));
#pragma unroll
    for (int t = 0; t < 8; ++t)
#pragma unroll
      for (int j = 0; j < 3; ++j) vf[t][j] = (t & 1) ? phi(vb[24 + 2 * j][t >> 1], vb[25 + 2 * j][t >> 1]) : plo(vb[24 + 2 * j][t >> 1], vb[25 + 2 * j][t >> 1]);
#pragma unroll
    for (int t = 0; t < 8; ++t) asm volatile("" : "+v"(vf[t][0]), "+v"(vf[t][1]), "+v"(vf[t][2]));
    if (late) WAITV(4); else WAITV(0);
    asm volatile("s_nop 0 ; both branches above cover V" : "+v"(vb[30]), "+v"(vb[31]));
#pragma unroll
    for (int t = 0; t < 8; ++t) {
      vf[t][3] = (t & 1) ? phi(vb[30][t >> 1], vb[31][t >> 1]) : plo(vb[30][t >> 1], vb[31][t >> 1]);
      oa[t] = mfma(pf[3], vf[t], oa[t]);
    }
    STAMP(5); TSB(6);
    if (grp == 0) {
#pragma unroll
      for (int g = 0; g < 2; ++g) {
        *(f32x4*)(lds + mw_ + g * 512) = f32x4{oa[0][2 * g], oa[1][2 * g], oa[2][2 * g], oa[3][2 * g]};
        *(f32x4*)(lds + mw_ + g * 512 + 16) = f32x4{oa[4][2 * g], oa[5][2 * g], oa[6][2 * g], oa[7][2 * g]};
      }
    }
    lds_barrier();
    if (wave == 0) {
      f32x4 m[8];
#pragma unroll
      for (int q = 0; q < 8; ++q) m[q] = *(const f32x4*)(lds + mr_ + q * 1024);
      SCHED();
      const f32x4 o = ((m[0] + m[1]) + (m[2] + m[3])) + ((m[4] + m[5]) + (m[6] + m[7]));
      st128(rs, oo, u32x4{sanf(o[0]), sanf(o[1]), sanf(o[2]), sanf(o[3])});
    }
    STAMP(6); TSB(7); SCHED();
  if (gs >= 0) {
    constexpr int L_H2 = L_W + 73728;
    constexpr int L_ACT = L_C + 4608;
    const int b = wave;
    BAR(); SCHED();
    TSC(0);
    u32x4 bg[9], bu[9], wdf[2][5];
    {
      u32x2 rd[2][8];
      const char* db = (const char*)(wd + (size_t)gs * 64 * D + wave * 64);
      const u32 dv = grp * 9216 + n * 8;
#pragma unroll
      for (int ks = 0; ks < 2; ++ks)
#pragma unroll
        for (int i = 0; i < 8; ++i) ald64nt(rd[ks][i], db + (ks * 32 + i) * 1152, dv);
      {
        const int r = lane >> 3;
        dma((const char*)(wd + (size_t)(gs * 64 + wave * 8) * D + 512), r * 1152 + (((lane & 7) ^ (r & 2) ^ ((wave & 1) * 4)) << 4), lds_base + L_B + wave * 1024);
      }
      BAR();
      slab_dma<4, 9>(wg + gs * 64, FF * 2, lds_base + L_W + 73728, wave, lane);
      BAR();
      slab_dma(wu + gs * 64, FF * 2, lds_base + L_W, wave, lane);
      asm volatile("s_waitcnt vmcnt(15)" : "+v"(rd[0][0]), "+v"(rd[0][1]), "+v"(rd[0][2]), "+v"(rd[0][3]), "+v"(rd[0][4]), "+v"(rd[0][5]), "+v"(rd[0][6]), "+v"(rd[0][7]),
                   "+v"(rd[1][0]), "+v"(rd[1][1]), "+v"(rd[1][2]), "+v"(rd[1][3]), "+v"(rd[1][4]), "+v"(rd[1][5]), "+v"(rd[1][6]), "+v"(rd[1][7]) :: "memory");
#pragma unroll
      for (int ks = 0; ks < 2; ++ks)
#pragma unroll
        for (int j = 0; j < 4; ++j) {
          wdf[ks][0][j] = plo(rd[ks][2 * j][0], rd[ks][2 * j + 1][0]); wdf[ks][1][j] = phi(rd[ks][2 * j][0], rd[ks][2 * j + 1][0]);
          wdf[ks][2][j] = plo(rd[ks][2 * j][1], rd[ks][2 * j + 1][1]); wdf[ks][3][j] = phi(rd[ks][2 * j][1], rd[ks][2 * j + 1][1]);
        }
#pragma unroll
      for (int t = 0; t < 4; ++t) asm volatile("" : "+v"(wdf[0][t]), "+v"(wdf[1][t]));
      asm volatile("s_waitcnt vmcnt(9)\n\ts_barrier" ::: "memory");
      slab_frags(lds, L_W + 73728, wave, lane, bg);
      const int q = n >> 2, p = lane & 3;
      const int a0 = L_B + (8 * grp + q) * 128 + ((((wave & 3) * 2 + (p >> 1)) ^ (q & 2) ^ ((grp & 1) * 4)) << 4) + (p & 1) * 8;
#pragma unroll
      for (int ks = 0; ks < 2; ++ks) {
        const u32x2 lo = trd(lds, a0 + ks * 4096), hi = trd(lds, a0 + ks * 4096 + 512);
        wdf[ks][4] = u32x4{lo[0], lo[1], hi[0], hi[1]};
      }
    }
    TSC(1);
    int ia[3], ra[4], yo[3], fo[7];
#pragma unroll
    for (int i = 0; i < 3; ++i) {
      const int k = i < 2 ? i * 256 + lane * 4 : 512 + n * 4, hf = k >= 288 ? 1 : 0, kk = k - hf * 288, s = kk >> 5, g = (kk >> 3) & 3;
      ia[i] = L_H2 + (s * 64 + g * 16 + ((hf * 8 + b) ^ ((s & 3) * 4 + g))) * 16 + (kk & 7) * 2;
    }
#pragma unroll
    for (int i = 0; i < 4; ++i) ra[i] = L_H2 + (grp * 16 + (n ^ (i * 4 + grp))) * 16;
    int tw = L_ACT + n * 144 + wave * 16 + (grp & 1) * 8 + (grp >> 1) * 4, tr_ = L_ACT + (n & 7) * 144 + grp * 16;
    yo[0] = W_Y + (((wave * 8 + grp * 2) * NG + gs) * 16 + n) * 16; yo[1] = yo[0] + NG * 256;
    yo[2] = W_Y + ((((64 + wave * 2 + (grp >> 1)) * NG + gs) * 2 + (grp & 1)) * 8 + n) * 16;
    {
      const int b_ = lane >> 3, cc = lane & 7, col = gs * 8 + cc, hf = col >= 288 ? 1 : 0, kk = col - hf * 288, s_ = kk >> 5, g = (kk >> 3) & 3;
      fo[0] = W_M + ((3 * 8 + b_) * 144 + gs * 2 + (cc >> 2)) * 16;
      fo[1] = W_X1 + (b_ * 152 + gs * 2 + (cc >> 2)) * 16; fo[2] = W_X1 + (b_ * 152 + 144) * 16;
      fo[3] = W_Y + (gs * 1152 + tid) * 16;
      fo[4] = L_H2 + (s_ * 64 + g * 16 + ((hf * 8 + b_) ^ ((s_ & 3) * 4 + g))) * 16 + cc * 2;
      fo[5] = L_C + (((cc >> 2) * 8 + b_) * 4 + (cc & 3)) * 4;
      fo[6] = (b_ * D + col) * 2;
    }
    asm volatile("" : "+v"(ia[0]), "+v"(ia[1]), "+v"(ia[2]), "+v"(ra[0]), "+v"(ra[1]), "+v"(ra[2]), "+v"(ra[3]), "+v"(tw), "+v"(tr_), "+v"(yo[0]), "+v"(yo[1]), "+v"(yo[2]),
                 "+v"(fo[0]), "+v"(fo[1]), "+v"(fo[2]), "+v"(fo[3]), "+v"(fo[4]), "+v"(fo[5]), "+v"(fo[6]));
    asm volatile("s_waitcnt vmcnt(0) lgkmcnt(0)\n\ts_barrier" ::: "memory");
    {
      u32x4 hp[3];
      const int h0 = W_H2 + (b * 144 + lane) * 16, h2o = W_H2 + (b * 144 + 128 + n) * 16;
      hp[0] = ld128(rs, h0); hp[1] = ld128(rs, h0 + 1024); hp[2] = ld128(rs, h2o);
      slab_frags(lds, L_W, wave, lane, bu);
      STAMP(13);
#pragma unroll
      for (int s = 0; s < 9; ++s) asm volatile("" : "+v"(bg[s]), "+v"(bu[s]));
      asm volatile("" : "+v"(wdf[0][4]), "+v"(wdf[1][4]));
      TSC(3);
      if (!HOK()) {
        int sp = 0;
        _Pragma("nounroll") do { nap(2); hp[0] = ld128(rs, h0); hp[1] = ld128(rs, h0 + 1024); hp[2] = ld128(rs, h2o); } while (!HOK() && ++sp < SPIN_CAP);
      }
      STAMP(9); TSC(4);
      *(u32x2*)(lds + ia[0]) = u32x2{hp[0][0], hp[0][1]};
      *(u32x2*)(lds + ia[1]) = u32x2{hp[1][0], hp[1][1]};
      if (lane < 16) *(u32x2*)(lds + ia[2]) = u32x2{hp[2][0], hp[2][1]};
      TSC(5);
      lds_barrier();
      u32x4 af[9];
#pragma unroll
      for (int s = 0; s < 9; ++s) af[s] = *(const u32x4*)(lds + ra[s & 3] + s * 1024);
      SCHED();
      f32x4 ag[2], au[2];
#pragma unroll
      for (int s = 0; s < 9; ++s) {
        ag[s & 1] = mfma(bg[s], af[s], s < 2 ? f32x4{0.f, 0.f, 0.f, 0.f} : ag[s & 1]);
        au[s & 1] = mfma(bu[s], af[s], s < 2 ? f32x4{0.f, 0.f, 0.f, 0.f} : au[s & 1]);
      }
      const f32x2 hg = half_sum2(ag[0] + ag[1]), hu = half_sum2(au[0] + au[1]);
      const u32 act = cvt2(rbf(silu(rbf(hg.x))) * rbf(hu.x), rbf(silu(rbf(hg.y))) * rbf(hu.y));
      if (n < 8) *(u32*)(lds + tw) = act;
    }
    lds_barrier();
    STAMP(10); TSC(6);
    {
      u32x4 af[2];
#pragma unroll
      for (int ks = 0; ks < 2; ++ks) af[ks] = *(const u32x4*)(lds + tr_ + ks * 64);
      f32x4 d[5];
#pragma unroll
      for (int t = 0; t < 5; ++t) d[t] = mfma(wdf[1][t], af[1], mfma(wdf[0][t], af[0], f32x4{0.f, 0.f, 0.f, 0.f}));
      if (n < 8) {
#pragma unroll
        for (int j = 0; j < 4; ++j)
          st128(rs, yo[j >> 1] + (j & 1) * 128, u32x4{sanf(d[0][j]), sanf(d[1][j]), sanf(d[2][j]), sanf(d[3][j])});
        if (wave < 4)
          st128(rs, yo[2], u32x4{sanf(d[4][0]), sanf(d[4][1]), sanf(d[4][2]), sanf(d[4][3])});
      }
    }
    {
      float gbv = 0.f, x1v = 0.f, h2 = 0.f, rsel = 0.f;
      if (wave == 7) {
        u32x4 gq, xq, rq;
        _Pragma("nounroll") for (int sp = 0; sp < SPIN_CAP; ++sp) {
          gq = ld128(rs, fo[0]); xq = ld128(rs, fo[1]); rq = ld128(rs, fo[2]);
          if (__all((tag_x(gq, e0, e1) | tag_x(xq, e0, e1) | tag_x(rq, e0, e1)) == 0u)) break;
          nap(1);
        }
        const u32 gw = (lane & 2) ? gq[1] : gq[0], xw = (lane & 2) ? xq[1] : xq[0];
        gbv = (lane & 1) ? hi16(gw) : lo16(gw); x1v = (lane & 1) ? hi16(xw) : lo16(xw);
        h2 = b2f(*(const u16*)(lds + fo[4])); rsel = __uint_as_float(rq[0]);
        asm volatile("" : "+v"(gbv), "+v"(x1v), "+v"(h2), "+v"(rsel));
      }
      WAITV(0);
      u32x4 y[3] = {};
      _Pragma("nounroll") for (int sp = 0; sp < SPIN_CAP; ++sp) {
        y[0] = ld128(rs, fo[3]); y[1] = ld128(rs, fo[3] + 8192);
        if (wave < 2) y[2] = ld128(rs, fo[3] + 16384);
        if (__all(mxu(mxu(mx4(y[0]), mx4(y[1])), mx4(y[2])) != 0xffffffffu)) break;
        nap(1);
      }
      STAMP(11); TSC(7);
      f32x4 s;
#pragma unroll
      for (int k = 0; k < 4; ++k) s[k] = xsum32(xsum16((__uint_as_float(y[0][k]) + __uint_as_float(y[1][k])) + __uint_as_float(y[2][k])));
      if (lane < 16) *(f32x4*)(lds + L_C + (wave * 16 + lane) * 16) = s;
      lds_barrier();
      if (wave == 7) {
        float t[8];
#pragma unroll
        for (int ww = 0; ww < 8; ++ww) t[ww] = *(const float*)(lds + fo[5] + ww * 256);
        SCHED();
        const float y2 = rbf(rbf(((t[0] + t[1]) + (t[2] + t[3])) + ((t[4] + t[5]) + (t[6] + t[7]))) * rsel);
        const float res = x1v + rbf(gbv * rbf(h2 + y2));
        __builtin_amdgcn_raw_buffer_store_b16((u16)(cvt2(res, 0.f) & 0xffffu), __builtin_amdgcn_make_buffer_rsrc(out, 0, 8 * D * 2, 0x00020000), fo[6], 0, 0);
      }
      STAMP(12);
    }
  }
  {
  int t3_ = threadIdx.x; asm volatile("" : "+v"(t3_));
  const int tid = t3_, lane = tid & 63, n = lane & 15, grp = lane >> 4;
  if (qs >= 0) {
    const u16* mat = (wave & 1) ? wg : wu;
    const int g0 = ((3 - (int)((uintptr_t)mat >> 7) - xcd * 9) & 7) + (wave >= 4 ? 8 : 0);
    const int r = (idx - 6) * 72 + ((wave >> 1) & 1) * 36 + min(lane, 35);
    if (g0 < 9) dma_keep(mat, r * 9216 + (xcd * 9 + g0) * 128, lds_base + L_W + wave * 256);
  }
  if (ml >= 0) {
    BAR();
    if (mw < 18) slab_dma<0, 4>(w_mod + ml * 64, NMOD * 2, lds_base + L_W, wave, lane);
    slab_dma<4, 9>(w_mod + ml * 64, NMOD * 2, lds_base + L_W, wave, lane);
    u32x4 wf[9], af[9];
    img_frags(lds, L_IMG, lane, af);
    asm volatile("s_waitcnt vmcnt(0)\n\ts_barrier" ::: "memory");
    slab_frags(lds, L_W, wave, lane, wf);
    const f32x4 s = half_sum(gemv9(wf, af));
    if (n < 8 && grp < 2) *(u32x2*)(lds + L_TILE + n * 128 + wave * 16 + grp * 8) = u32x2{cvt2(s[0], s[1]), cvt2(s[2], s[3])};
    lds_barrier();
    if (wave < 2) {
      const u32x2 t2 = *(const u32x2*)(lds + L_TILE + tid * 8);
      const int ch = (ml - 18) / 9, sl = ml - 18 - ch * 9;
      st128(rs, W_M + ((ch * 8 + (tid >> 4)) * 144 + sl * 16 + n) * 16, u32x4{t2[0], t2[1], e0, e1});
    }
  }

  if (lnb >= 0 && wave < 3) {
    const int b = lnb, g = tid < 144 ? tid : 143;
    const bool on = tid < 144;
    float* ex = (float*)(lds + L_TILE);
    const u32x2 xa = __builtin_amdgcn_raw_buffer_load_b64(__builtin_amdgcn_make_buffer_rsrc((void*)x, 0, 8 * D * 2, 0x00020000), (b * D + g * 4) * 2, 0, 0);
    const u32x2 wra = __builtin_amdgcn_raw_buffer_load_b64(__builtin_amdgcn_make_buffer_rsrc((void*)w_r, 0, D * 2, 0x00020000), g * 8, 0, 0);
    u32x4 mm[3];
    _Pragma("nounroll") for (int sp = 0; sp < SPIN_CAP; ++sp) {
#pragma unroll
      for (int k = 0; k < 3; ++k) mm[k] = ld128(rs, W_M + ((k * 8 + b) * 144 + g) * 16);
      if (__all((tag_x(mm[0], e0, e1) | tag_x(mm[1], e0, e1) | tag_x(mm[2], e0, e1)) == 0u)) break;
      nap(4);
    }
    f32x2 xv[2], ga[2], sb[2], cb1[2], wr[2];
#pragma unroll
    for (int i = 0; i < 2; ++i) {
      xv[i] = up2(xa[i]); wr[i] = up2(wra[i]); ga[i] = up2(mm[0][i]); sb[i] = up2(mm[1][i]); cb1[i] = rbf2(f32x2{1.f, 1.f} + up2(mm[2][i]));
      asm volatile("" : "+v"(xv[i]), "+v"(ga[i]), "+v"(sb[i]), "+v"(cb1[i]), "+v"(wr[i]));
    }
    int ho = W_H2 + (b * 144 + g) * 16, xo = W_X1 + (b * 152 + g) * 16;
    asm volatile("" : "+v"(ho), "+v"(xo));
    TSC(0);
    u32x4 xp[8];
    _Pragma("nounroll") for (int sp = 0; sp < SPIN_CAP; ++sp) {
#pragma unroll
      for (int p = 0; p < 8; ++p) xp[p] = ld128(rs, W_XP + (p * 8 + b) * 2304 + g * 16);
      if (__all(mxu(mxu(mxu(mx4(xp[0]), mx4(xp[1])), mxu(mx4(xp[2]), mx4(xp[3]))), mxu(mxu(mx4(xp[4]), mx4(xp[5])), mxu(mx4(xp[6]), mx4(xp[7])))) != 0xffffffffu)) break;
      nap(1);
    }
    TSC(1);
    f32x2 v[2];
#pragma unroll
    for (int i = 0; i < 2; ++i) {
      f32x2 pp[8];
#pragma unroll
      for (int p = 0; p < 8; ++p) pp[p] = f32x2{__uint_as_float(xp[p][2 * i]), __uint_as_float(xp[p][2 * i + 1])};
      v[i] = rbf2(xv[i] + rbf2(ga[i] * rbf2(((pp[0] + pp[1]) + (pp[2] + pp[3])) + ((pp[4] + pp[5]) + (pp[6] + pp[7])))));
    }
    const f32x2 s2 = v[0] + v[1];
    const float mw = wsum(on ? s2.x + s2.y : 0.f) * (wave < 2 ? 1.f / 256 : 1.f / 64);
    const f32x2 t0 = v[0] - f32x2{mw, mw}, t1 = v[1] - f32x2{mw, mw}, q2 = t0 * t0 + t1 * t1;
    const float m2 = wsum(on ? q2.x + q2.y : 0.f);
    if (lane == 0) *(f32x2*)(ex + wave * 2) = f32x2{mw, m2};
    lds_barrier();
    const f32x4 e01 = *(const f32x4*)ex; const f32x2 e2 = *(const f32x2*)(ex + 4);
    const float mu = (e01[0] + e01[2]) * (256.f / D) + e2.x * (64.f / D);
    const float d0 = e01[0] - mu, d1 = e01[2] - mu, d2 = e2.x - mu;
    const float var = rbf(((e01[1] + e01[3]) + e2.y + 256.f * (d0 * d0 + d1 * d1) + 64.f * d2 * d2) * (1.f / D));
    const float mean = rbf(mu), rden = frcp(rbf(sqrtf(rbf(var + 1e-5f))));
    u32 hw[2];
#pragma unroll
    for (int i = 0; i < 2; ++i) {
      const f32x2 t = rbf2(rbf2(rbf2(v[i] - f32x2{mean, mean}) * f32x2{rden, rden}) * cb1[i]) + sb[i];
      hw[i] = cvt2(t.x, t.y);
    }
    if (on) {
      st128(rs, ho, u32x4{hw[0], hw[1], e0, e1});
      st128(rs, xo, u32x4{cvt2(v[0].x, v[0].y), cvt2(v[1].x, v[1].y), e0, e1});
    }
    TSC(2);
    const f32x2 r2 = up2(hw[0]) * wr[0] + up2(hw[1]) * wr[1];
    const float rw = wsum(on ? r2.x + r2.y : 0.f);
    if (lane == 0) ex[8 + wave] = rw;
    lds_barrier();
    if (tid < 8) {
      const float r = rbf((ex[8] + ex[9]) + ex[10]);
      st128(rs, W_X1 + (b * 152 + 144 + tid) * 16, u32x4{__float_as_uint(rbf(1.f / (1.f + __expf(-r)))), 0u, e0, e1});
    }
  }

  if (oj >= 0) {
    u32x4 wf[8];
    {
      const int q = n >> 2, p = lane & 3, hf = p >> 1;
      const int a0 = L_W + (wave * 64 + hf * 32 + 8 * grp + q) * 128 + (((q >> 1) ^ ((grp & 1) * 2) ^ (hf * 4)) << 4) + (p & 1) * 8;
#pragma unroll
      for (int cg = 0; cg < 8; ++cg) {
        const u32x2 lo = trd(lds, a0 ^ (cg * 16)), hi = trd(lds, (a0 ^ (cg * 16)) + 512);
        wf[cg] = u32x4{lo[0], lo[1], hi[0], hi[1]};
      }
    }
    int rw = L_OI + (n & 7) * 4128 + (wave * 2 + (n >> 3)) * 256 + (grp & 1) * 16, rr = L_OI + wave * 4128 + grp * 1024 + n * 16;
    int xo = W_XP + (op * 8 + wave) * 2304 + oj * 256 + lane * 16;
#pragma unroll
    for (int cg = 0; cg < 8; ++cg) asm volatile("" : "+v"(wf[cg]));
    asm volatile("" : "+v"(rw), "+v"(rr), "+v"(xo));
    TSC(0);
    const int ob = W_O + ((op * 2 + (wave >> 2)) * 8 + (n & 7)) * 2048 + (wave & 3) * 256 + ((n >> 3) * 4 + grp) * 16;
    u32x4 a[4];
    _Pragma("nounroll") for (int sp = 0; sp < SPIN_CAP; ++sp) {
#pragma unroll
      for (int p = 0; p < 4; ++p) a[p] = ld128(rs, ob + (p >> 1) * 1024 + (p & 1) * 128);
      if (__all(mxu(mxu(mx4(a[0]), mx4(a[1])), mxu(mx4(a[2]), mx4(a[3]))) != 0xffffffffu)) break;
      nap(4);
    }
    STAMP(7); TSC(1);
#define OS(p, j) (__uint_as_float(a[p][j]) + __uint_as_float(a[(p) + 2][j]))
    const u32x4 of = {cvt2(OS(0, 0), OS(0, 1)), cvt2(OS(0, 2), OS(0, 3)), cvt2(OS(1, 0), OS(1, 1)), cvt2(OS(1, 2), OS(1, 3))};
    f32x4 acc[8];
#pragma unroll
    for (int cg = 0; cg < 8; ++cg) acc[cg] = mfma(wf[cg], of, f32x4{0.f, 0.f, 0.f, 0.f});
    if ((n >> 3) == (grp >> 1)) {
#pragma unroll
      for (int cg = 0; cg < 8; ++cg) *(f32x4*)(lds + rw + cg * 32) = acc[cg];
    }
    TSC(2);
    lds_barrier();
    TSC(3);
    f32x4 s = (*(const f32x4*)(lds + rr) + *(const f32x4*)(lds + rr + 256)) + (*(const f32x4*)(lds + rr + 512) + *(const f32x4*)(lds + rr + 768));
#pragma unroll
    for (int k = 0; k < 4; ++k) s[k] = xsum32(xsum16(s[k]));
    if (lane < 16) st128(rs, xo, u32x4{sanf(s[0]), sanf(s[1]), sanf(s[2]), sanf(s[3])});
    TSC(4);
  }
  if (gs < 0) { STAMP(8); STAMP(13); TSC(6); }
  TSOUT();
  if (blockIdx.x == 0 && wave == 0) while (__builtin_amdgcn_s_memrealtime() - tA_ < 6000ull + lat_) __builtin_amdgcn_s_sleep(1);
  }
  }
}

extern "C" void kernel_launch(void* const* d_in, const int* in_sizes, int n_in, void* d_out, int out_size, void* d_ws, size_t ws_size,
                              hipStream_t stream) {
  fused_kernel<<<NWG, NT, 0, stream>>>((const u16*)d_in[0], (const u16*)d_in[1], (const u16*)d_in[2], (const u16*)d_in[3], (const u16*)d_in[4],
                                       (const u16*)d_in[6], (const u16*)d_in[7], (const u16*)d_in[8], (const u16*)d_in[9], (const u16*)d_in[10],
                                       (const u16*)d_in[11], (unsigned char*)d_ws, (u16*)d_out);
}
```

```cpp
#include <hip/hip_runtime.h>
#include <hip/hip_bf16.h>
#include <cstdint>

typedef unsigned u32;
typedef unsigned short u16;
typedef u32 u32x4 __attribute__((ext_vector_type(4)));
typedef u32 u32x2 __attribute__((ext_vector_type(2)));
typedef float f32x4 __attribute__((ext_vector_type(4)));
typedef float f32x2 __attribute__((ext_vector_type(2)));
typedef short s16x4 __attribute__((ext_vector_type(4)));
typedef __bf16 bf16x8 __attribute__((ext_vector_type(8)));
typedef __bf16 bf16x2 __attribute__((ext_vector_type(2)));
typedef __attribute__((address_space(3))) unsigned char lds_t;

constexpr int D = 576, SK = 8192, WIN = 2048, FF = 4608, NQ = 4096, NMOD = 6 * D;
constexpr int NWG = 256, NT = 512, NG = 72;
constexpr int SPIN_CAP = 1 << 15;

constexpr int W_O = 0;
constexpr int W_XP = W_O + 262144;
constexpr int W_Y = W_XP + 147456;
constexpr int W_MS = W_Y + NG * NG * 256;
constexpr int W_FL = W_MS;
constexpr int W_H = W_FL + 2048;
constexpr int W_M = W_H + 36864;
constexpr int W_Q = W_M + 73728;
constexpr int W_ST = W_Q + 131072;
constexpr int W_H2 = W_ST + 32768;
constexpr int W_X1 = W_H2 + 18432;
constexpr int W_END = W_X1 + 8 * 152 * 16;
constexpr int NPZ = 174, PZ = (W_MS / 16 + NPZ - 1) / NPZ;
static_assert(PZ <= 2 * NT, "poison split");

constexpr int L_W = 0;
constexpr int L_IMG = 73728;
constexpr int L_TILE = L_IMG + 9216;
constexpr int L_OI = 65536;
constexpr int L_B = 147456;
constexpr int L_C = 155648;
constexpr int LDS_BYTES = 163840;

#define SCHED() __builtin_amdgcn_sched_barrier(0)
#define BAR() asm volatile("s_barrier" ::: "memory")
#define TL(i) do {} while (0)
#define TLOUT() do {} while (0)
#define WAITV_(n) do { asm volatile("s_waitcnt vmcnt(" #n ")" ::: "memory"); SCHED(); } while (0)
#define WAITV(n) WAITV_(n)
#define DK 5
#define DV DK
#define PR 117
#define PR2 126
#define PR3 120

__device__ __forceinline__ float lo16(u32 u) { return __uint_as_float(u << 16); }
__device__ __forceinline__ float hi16(u32 u) { return __uint_as_float(u & 0xffff0000u); }
__device__ __forceinline__ float b2f(u16 u) { return __uint_as_float((u32)u << 16); }
__device__ __forceinline__ u32 cvt2(float a, float b) { return __builtin_bit_cast(u32, __builtin_convertvector(f32x2{a, b}, bf16x2)); }
__device__ __forceinline__ float rbf(float v) { return __uint_as_float(cvt2(0.f, v)); }
__device__ __forceinline__ u32 sanf(float f) { u32 v = __float_as_uint(f); return v < 0xfffffffeu ? v : 0xfffffffeu; }
__device__ __forceinline__ f32x2 up2(u32 u) { return f32x2{lo16(u), hi16(u)}; }
__device__ __forceinline__ f32x2 rbf2(f32x2 v) { return f32x2{rbf(v.x), rbf(v.y)}; }
__device__ __forceinline__ void lds_barrier() { asm volatile("s_waitcnt lgkmcnt(0)\n\ts_barrier" ::: "memory"); }
__device__ __forceinline__ f32x4 mfma(u32x4 a, u32x4 b, f32x4 c) {
  return __builtin_amdgcn_mfma_f32_16x16x32_bf16(__builtin_bit_cast(bf16x8, a), __builtin_bit_cast(bf16x8, b), c, 0, 0, 0);
}
__device__ __forceinline__ u32 plo(u32 a, u32 b) { return __builtin_amdgcn_perm(b, a, 0x05040100u); }
__device__ __forceinline__ u32 phi(u32 a, u32 b) { return __builtin_amdgcn_perm(b, a, 0x07060302u); }

extern "C" __device__ __attribute__((const)) unsigned long long llvm_dispatch_id() __asm("llvm.amdgcn.dispatch.id");
typedef __amdgpu_buffer_rsrc_t rsrc_t;
__device__ __forceinline__ u32x4 ld128(rsrc_t rs, int off) { return __builtin_amdgcn_raw_buffer_load_b128(rs, off, 0, 16); }
__device__ __forceinline__ void st128(rsrc_t rs, int off, u32x4 v) { __builtin_amdgcn_raw_buffer_store_b128(v, rs, off, 0, 16); }
__device__ __forceinline__ void st64(rsrc_t rs, int off, u32x2 v) { __builtin_amdgcn_raw_buffer_store_b64(v, rs, off, 0, 16); }
__device__ __forceinline__ void dma(const void* gbase, u32 voff, unsigned lds_dst) {
  unsigned keep;
  asm volatile("s_mov_b32 %0, m0\n\ts_mov_b32 m0, %3\n\ts_nop 2\n\tglobal_load_lds_dwordx4 %1, %2 nt\n\ts_mov_b32 m0, %0"
               : "=&s"(keep) : "v"(voff), "s"(gbase), "s"(lds_dst) : "memory");
}
__device__ __forceinline__ void dma_keep(const void* gbase, u32 voff, unsigned lds_dst) {
  unsigned keep;
  asm volatile("s_mov_b32 %0, m0\n\ts_mov_b32 m0, %3\n\ts_nop 2\n\tglobal_load_lds_dword %1, %2\n\ts_mov_b32 m0, %0"
               : "=&s"(keep) : "v"(voff), "s"(gbase), "s"(lds_dst) : "memory");
}
__device__ __forceinline__ void dma_sc(const void* gbase, u32 voff, unsigned lds_dst) {
  unsigned keep;
  asm volatile("s_mov_b32 %0, m0\n\ts_mov_b32 m0, %3\n\ts_nop 2\n\tglobal_load_lds_dwordx4 %1, %2 sc1\n\ts_mov_b32 m0, %0"
               : "=&s"(keep) : "v"(voff), "s"(gbase), "s"(lds_dst) : "memory");
}
__device__ __forceinline__ void ald128(u32x4& d, const void* gbase, u32 voff) { unsigned long long t; asm volatile("s_mov_b64 %1, %3\n\tglobal_load_dwordx4 %0, %2, %1" : "=v"(d), "=&s"(t) : "v"(voff), "s"(gbase) : "memory"); }
__device__ __forceinline__ void ald64(u32x2& d, const void* gbase, u32 voff) { unsigned long long t; asm volatile("s_mov_b64 %1, %3\n\tglobal_load_dwordx2 %0, %2, %1" : "=v"(d), "=&s"(t) : "v"(voff), "s"(gbase) : "memory"); }
__device__ __forceinline__ void ald64nt(u32x2& d, const void* gbase, u32 voff) { unsigned long long t; asm volatile("s_mov_b64 %1, %3\n\tglobal_load_dwordx2 %0, %2, %1 nt" : "=v"(d), "=&s"(t) : "v"(voff), "s"(gbase) : "memory"); }
__device__ __forceinline__ void ald128sc(u32x4& d, const void* gbase, u32 voff) { unsigned long long t; asm volatile("s_mov_b64 %1, %3\n\tglobal_load_dwordx4 %0, %2, %1 sc1" : "=v"(d), "=&s"(t) : "v"(voff), "s"(gbase) : "memory"); }
__device__ __forceinline__ u32x2 trd(const unsigned char* lds, int off) {
  return __builtin_bit_cast(u32x2, __builtin_amdgcn_ds_read_tr16_b64_v4i16((__attribute__((address_space(3))) s16x4*)((lds_t*)lds + off)));
}
#define nap(n) do { __builtin_amdgcn_s_sleep(n); asm volatile("" ::: "memory"); } while (0)
__device__ __forceinline__ float frcp(float v) { return __builtin_amdgcn_rcpf(v); }
__device__ __forceinline__ float fdiv(float a, float d, float r) { const float q = a * r; return fmaf(fmaf(-q, d, a), r, q); }
__device__ __forceinline__ float silu(float v) { const float d = 1.f + __expf(-v); const float r = frcp(d); return d < 3.0e38f ? fdiv(v, d, r) : v * r; }
template <int CTRL> __device__ __forceinline__ float dppf(float x) {
  return __builtin_bit_cast(float, __builtin_amdgcn_mov_dpp(__builtin_bit_cast(int, x), CTRL, 0xf, 0xf, true));
}
__device__ __forceinline__ u32 mxu(u32 a, u32 b) { return a > b ? a : b; }
__device__ __forceinline__ u32 mx4(u32x4 v) { return mxu(mxu(v[0], v[1]), mxu(v[2], v[3])); }
__device__ __forceinline__ float rl(float v, int l) { return __builtin_bit_cast(float, __builtin_amdgcn_readlane(__builtin_bit_cast(int, v), l)); }
__device__ __forceinline__ float wsum(float v) {
  v += dppf<0xB1>(v); v += dppf<0x4E>(v); v += dppf<0x141>(v); v += dppf<0x140>(v);
  return (rl(v, 0) + rl(v, 16)) + (rl(v, 32) + rl(v, 48));
}
__device__ __forceinline__ float up32(float v) { const u32 u = __float_as_uint(v); return __uint_as_float(__builtin_amdgcn_permlane32_swap(u, u, false, false)[1]); }
__device__ __forceinline__ float xsum32(float v) { const u32 u = __float_as_uint(v); const auto r = __builtin_amdgcn_permlane32_swap(u, u, false, false); return __uint_as_float(r[0]) + __uint_as_float(r[1]); }
__device__ __forceinline__ float xsum16(float v) { const u32 u = __float_as_uint(v); const auto r = __builtin_amdgcn_permlane16_swap(u, u, false, false); return __uint_as_float(r[0]) + __uint_as_float(r[1]); }
__device__ __forceinline__ f32x4 half_sum(f32x4 d) {
  f32x4 r;
#pragma unroll
  for (int j = 0; j < 4; ++j) r[j] = d[j] + dppf<0x108>(up32(d[j]));
  return r;
}
__device__ __forceinline__ f32x2 half_sum2(f32x4 d) {
  f32x2 r;
#pragma unroll
  for (int j = 0; j < 2; ++j) {
    const auto s = __builtin_amdgcn_permlane32_swap(__float_as_uint(d[j]), __float_as_uint(d[j + 2]), false, false);
    r[j] = __uint_as_float(s[0]) + dppf<0x108>(__uint_as_float(s[1]));
  }
  return r;
}
__device__ __forceinline__ void unpack8(u32x4 u, float* f) {
#pragma unroll
  for (int j = 0; j < 4; ++j) { f[2 * j] = lo16(u[j]); f[2 * j + 1] = hi16(u[j]); }
}
__device__ __forceinline__ void ln_row(const float (&v)[16], bool has2, float& mean, float& den) {
  float s = 0.f;
#pragma unroll
  for (int i = 0; i < 8; ++i) s += v[i];
  if (has2) {
#pragma unroll
    for (int i = 8; i < 16; ++i) s += v[i];
  }
  s = wsum(s);
  const float mu = s * (1.f / D);
  mean = rbf(mu);
  float q = 0.f;
#pragma unroll
  for (int i = 0; i < 8; ++i) { const float t = v[i] - mu; q = fmaf(t, t, q); }
  if (has2) {
#pragma unroll
    for (int i = 8; i < 16; ++i) { const float t = v[i] - mu; q = fmaf(t, t, q); }
  }
  q = wsum(q);
  const float var = rbf(q * (1.f / D));
  den = rbf(sqrtf(rbf(var + 1e-5f)));
}
template <int J0 = 0, int J1 = 9>
__device__ __forceinline__ void slab_dma(const u16* base, int stride_b, unsigned lds_dst, int wave, int lane) {
  const u32 v0 = (lane >> 3) * stride_b + (((lane & 7) ^ ((wave & 1) * 4 + (lane >> 4))) << 4), v1 = v0 ^ 32u;
  const u32 v4 = wave >= 4 ? v1 : v0;
  const char* gb = (const char*)base + (size_t)wave * 8 * stride_b;
#pragma unroll
  for (int j = J0; j < J1; ++j) dma(gb + (size_t)j * 64 * stride_b, j < 4 ? v0 : j == 4 ? v4 : v1, lds_dst + (j * 8 + wave) * 1024);
}
__device__ __forceinline__ void slab_frags(const unsigned char* lds, int base, int wave, int lane, u32x4 (&wf)[9]) {
  const int g = lane >> 4, q = (lane & 15) >> 2, p = lane & 3, hf = p >> 1;
  const int a0 = base + (hf * 288 + 8 * g + q) * 128 + ((wave ^ ((g & 1) * 4 + (q >> 1)) ^ (hf * 2)) << 4) + (p & 1) * 8, a1 = (a0 ^ 32) + 512;
#pragma unroll
  for (int s = 0; s < 9; ++s) {
    const u32x2 lo = trd(lds, a0 + s * 4096), hi = trd(lds, a1 + s * 4096);
    wf[s] = u32x4{lo[0], lo[1], hi[0], hi[1]};
  }
}
__device__ __forceinline__ void img_frags(const unsigned char* lds, int img, int lane, u32x4 (&af)[9]) {
#pragma unroll
  for (int s = 0; s < 9; ++s) af[s] = *(const u32x4*)(lds + img + (s * 64 + lane) * 16);
}
__device__ __forceinline__ f32x4 gemv9(const u32x4 (&wf)[9], const u32x4 (&af)[9]) {
  f32x4 acc[3];
  SCHED();
#pragma unroll
  for (int s = 0; s < 9; ++s) acc[s % 3] = mfma(wf[s], af[s], s < 3 ? f32x4{0.f, 0.f, 0.f, 0.f} : acc[s % 3]);
  return (acc[0] + acc[1]) + acc[2];
}
__device__ __forceinline__ int img_off(int b, int k8) {
  const int hf = k8 >= 36 ? 1 : 0, kk = k8 - hf * 36;
  return ((kk >> 2) * 64 + (kk & 3) * 16 + hf * 8 + b) * 16;
}
__device__ __forceinline__ u32 tag_x(u32x4 v, u32 e0, u32 e1) { return (v[2] ^ e0) | (v[3] ^ e1); }
__device__ __forceinline__ bool tag_ok(u32x4 v, u32 e0, u32 e1) { return tag_x(v, e0, e1) == 0u; }

constexpr int CODE_BYTES = 30080;

__global__ void __launch_bounds__(NT, 2) fused_kernel(const u16* __restrict__ x, const u16* __restrict__ kc, const u16* __restrict__ vc,
                                                      const u16* __restrict__ wq, const u16* __restrict__ wo, const u16* __restrict__ w_r,
                                                      const u16* __restrict__ wg, const u16* __restrict__ wu, const u16* __restrict__ wd,
                                                      const u16* __restrict__ c, const u16* __restrict__ w_mod, unsigned char* ws, u16* out) {
  __shared__ __attribute__((aligned(16))) unsigned char lds[LDS_BYTES];
  const int w = blockIdx.x, xcd = w & 7, idx = w >> 3;
  const int tid = threadIdx.x, lane = tid & 63, wave = __builtin_amdgcn_readfirstlane(tid >> 6);
  const int n = lane & 15, grp = lane >> 4;
  const rsrc_t rs = __builtin_amdgcn_make_buffer_rsrc(ws, 0, W_END, 0x00020000);
  const unsigned lds_base = (unsigned)(uintptr_t)(lds_t*)lds;
  const unsigned long long epoch = (llvm_dispatch_id() + 1ull) * 0x9E3779B97F4A7C15ull;
  const u32 e0 = (u32)epoch, e1 = (u32)(epoch >> 32);
  unsigned long long pc0; asm volatile("s_getpc_b64 %0" : "=s"(pc0));
  asm volatile("" : "+s"(vc), "+s"(w_r), "+s"(wu), "+s"(wd), "+s"(out), "+s"(x), "+s"(w_mod), "+s"(wg), "+s"(wq), "+s"(wo));

  const int mw = idx < 6 ? idx * 8 + xcd : -1;
  const int md = mw < 18 ? mw : -1;
  const int ml = mw >= 18 ? mw : mw >= 0 && mw < 6 ? 48 + mw : -1;
  const int lnb = mw >= 6 && mw < 14 ? mw - 6 : -1;
  const int qs = idx >= 6 && idx < 14 ? xcd * 8 + idx - 6 : -1;
  const int oj = idx >= 14 && idx < 23 ? idx - 14 : -1, op = xcd;
  const int gs = idx >= 23 ? xcd * 9 + idx - 23 : -1;

  const int s_att = w & 1, kvh = (w >> 1) & 15, b_att = w >> 5;
  {
    const size_t pairbase = ((size_t)(b_att * 16 + kvh) * SK + (SK - WIN)) * 256;
    const char* kp = (const char*)kc + pairbase;
    const char* vp = (const char*)vc + pairbase;
    const int wpart = (wave & 3) * 4096 + s_att * 16384 + (wave >> 2) * 262144;
    const int ko = wpart + (n >> 1) * 256 + (n & 1) * 64 + grp * 16;
    const int vo = wpart + n * 16 + grp * 512;
#define KLD(u) do { unsigned long long t_; asm volatile("s_mov_b64 %1, %3\n\tglobal_load_dwordx4 %0, %2, %1 offset:%4 nt" : "=v"(kb[u]), "=&s"(t_) : "v"(ko), "s"(kp + ((u) >> 2) * 32768), "i"((((u) >> 1) & 1) * 2048 + ((u) & 1) * 128) : "memory"); } while (0)
#define VLD(u) do { unsigned long long t_; asm volatile("s_mov_b64 %1, %3\n\tglobal_load_dwordx4 %0, %2, %1 offset:%4 nt" : "=v"(vb[u]), "=&s"(t_) : "v"(vo), "s"(vp + ((u) >> 2) * 32768), "i"((((u) >> 1) & 1) * 2048 + ((u) & 1) * 256) : "memory"); } while (0)
#define TIE8(a, o) asm volatile("" : "+v"(a[o]), "+v"(a[(o) + 1]), "+v"(a[(o) + 2]), "+v"(a[(o) + 3]), "+v"(a[(o) + 4]), "+v"(a[(o) + 5]), "+v"(a[(o) + 6]), "+v"(a[(o) + 7]))
    const int pr_ = gs >= 0 ? PR2 : mw >= 18 ? PR3 : PR;
    const u32 tp0 = (u32)__builtin_amdgcn_s_memrealtime() + (gs >= 0 || mw >= 18 ? 0 : 9 * PR / 4);
#define PACE(i) do { _Pragma("nounroll") while ((int)((u32)__builtin_amdgcn_s_memrealtime() - tp0) < (i) * pr_ / 4) __builtin_amdgcn_s_sleep(2); SCHED(); } while (0)
#define REP4(M, u, W) do { M(u); W; M((u) + 1); W; M((u) + 2); W; M((u) + 3); W; } while (0)
    static_assert(DK <= 7 && DV <= 7, "the hand-placed waits assume it");
    u32x4 kb[32], vb[32];
    const int pz = mw >= 18 ? mw - 18 : idx >= 14 ? w - 82 : -1;
#define POISON() do { if (pz >= 0) { _Pragma("unroll") for (int i = 0; i < 2; ++i) { const int g_ = pz * PZ + i * NT + tid; \
      if (i * NT + tid < PZ && g_ < W_MS / 16) st128(rs, g_ * 16, u32x4{0xffffffffu, 0xffffffffu, 0xffffffffu, 0xffffffffu}); } } SCHED(); } while (0)
#define PFLAG() do { if (pz >= 0 && tid == 448) st64(rs, W_FL + pz * 8, u32x2{e0, e1}); SCHED(); } while (0)
#define SILU_IMAGE(c0, c1) do { float f[8]; unpack8(c0, f); u32x4 o; \
        _Pragma("unroll") for (int j = 0; j < 4; ++j) o[j] = cvt2(silu(f[2 * j]), silu(f[2 * j + 1])); \
        const int b_ = tid / 72, ch_ = tid - b_ * 72; \
        *(u32x4*)(lds + L_IMG + img_off(b_, ch_)) = o; \
        if (wave == 0) { unpack8(c1, f); \
          _Pragma("unroll") for (int j = 0; j < 4; ++j) o[j] = cvt2(silu(f[2 * j]), silu(f[2 * j + 1])); \
          *(u32x4*)(lds + L_IMG + img_off(7, 8 + lane)) = o; } } while (0)
    if (md >= 0) {
      u32x4 c0, c1, x0, x1; u32x2 xc;
      __builtin_amdgcn_s_waitcnt(0x0f70);
      int ho = W_H + img_off(n & 7, (md >= 9 ? md - 9 : md) * 8 + wave) * 2 + (grp & 1) * 16;
      ald128(c0, c, tid * 16);
      ald128(c1, c, (512 + lane) * 16);
      ald128(x0, x, wave * 1152 + lane * 16);
      ald128(x1, x, wave * 1152 + 1024 + (lane & 7) * 16);
      ald64(xc, x, (n & 7) * 1152 + ((md >= 9 ? md - 9 : 0) * 64 + wave * 8 + (grp & 1) * 4) * 2);
      BAR();
      float* st = (float*)(lds + L_TILE + 1024);
      if (wave < 4) {
        slab_dma(w_mod + md * 64, NMOD * 2, lds_base + L_W, wave, lane);
        asm volatile("s_waitcnt vmcnt(9)" ::: "memory");
      } else asm volatile("s_waitcnt vmcnt(0)" ::: "memory");
      asm volatile("s_nop 0 ; both branches above cover V" : "+v"(c0), "+v"(c1), "+v"(x0), "+v"(x1), "+v"(xc) :: "memory");
      {
        SILU_IMAGE(c0, c1);
        float v[16]; unpack8(x0, v); unpack8(x1, v + 8);
        float mean, den; ln_row(v, lane < 8, mean, den);
        if (lane == 0) { st[wave] = mean; st[8 + wave] = den; }
      }
      if (wave >= 4) slab_dma(w_mod + md * 64, NMOD * 2, lds_base + L_W, wave, lane);
      lds_barrier();
      REP4(KLD, 0, );
      u32x4 wf[9], af[9];
      img_frags(lds, L_IMG, lane, af);
      const float mean = st[n & 7], rd = frcp(st[8 + (n & 7)]);
      float ln[4] = {rbf(rbf(lo16(xc[0]) - mean) * rd), rbf(rbf(hi16(xc[0]) - mean) * rd), rbf(rbf(lo16(xc[1]) - mean) * rd), rbf(rbf(hi16(xc[1]) - mean) * rd)};
#pragma unroll
      for (int s = 0; s < 9; ++s) asm volatile("" : "+v"(af[s]));
      asm volatile("" : "+v"(ln[0]), "+v"(ln[1]), "+v"(ln[2]), "+v"(ln[3]), "+v"(ho));
      WAITV(4); BAR(); SCHED();
      slab_frags(lds, L_W, wave, lane, wf);
      const f32x4 s = half_sum(gemv9(wf, af));
      f32x4 r = s;
      if (md >= 9) {
#pragma unroll
        for (int j = 0; j < 4; ++j) r[j] = ln[j] * rbf(1.f + rbf(s[j]));
      }
      if (n < 8 && grp < 2) st128(rs, ho + (md >= 9 ? 18432 : 0), u32x4{cvt2(r[0], r[1]), cvt2(r[2], r[3]), e0, e1});
      SCHED();
      TL(1);
      BAR();
      KLD(4); KLD(5); WAITV(DK);
      KLD(6); WAITV(DK); KLD(7); WAITV(DK); KLD(8); WAITV(DK); KLD(9); WAITV(DK); KLD(10); WAITV(DK);
      KLD(11); WAITV(DK); REP4(KLD, 12, WAITV(DK)); REP4(KLD, 16, WAITV(DK));
      REP4(KLD, 20, WAITV(DK)); KLD(24); WAITV(DK); KLD(25);
    } else {
      constexpr int L_LAND = 98304;
      const unsigned land = lds_base + L_LAND + wave * 6144;
      const unsigned char* lp = lds + L_LAND + wave * 6144 + lane * 16;
      const int sl_ = (3 - (int)((uintptr_t)wq >> 7)) & 7;
      const bool slowq = qs >= 0 && (qs & 7) == sl_;
      if (gs >= 0 && wave == 0) dma_keep((const char*)wq + (size_t)(idx - 23) * 64 * 8192 + xcd * 1024 + sl_ * 128, lane * 8192, land);
      if (gs >= 0 && (wave == 1 || (wave == 2 && xcd < 2))) dma_keep((const char*)w_mod + (size_t)((idx - 23) * 64 + 32) * (NMOD * 2) + xcd * 128 + (wave - 1) * 2048, (lane & 31) * (NMOD * 2) + (wave == 1 ? (lane >> 5) * 1024 : 0), land);
      POISON();
      if (oj >= 0 && idx < 17 && wave * 8192 < CODE_BYTES)
        dma_keep((const void*)pc0, min(wave * 8192 + lane * 128, CODE_BYTES - 128), lds_base + 107520 + wave * 256);
      const u32 v0 = (lane >> 3) * 1152 + (((lane & 7) ^ ((lane >> 4) & 1)) << 4);
      const char* gb = (const char*)(wo + (size_t)(op * 512 + wave * 64) * D + oj * 64);
      if (ml >= 0) { dma_sc(c, tid * 16, land); dma_sc(c, (512 + lane) * 16, land + 1024); }
#define WK(j) do { if (oj >= 0) { if ((j) < 8) dma(gb + (j) * 8 * 1152, v0 ^ (((j) & 1) * 32 + ((j) >> 2) * 64), lds_base + L_W + (wave * 8 + (j)) * 1024); } \
                   else if (slowq) { if ((j) == 8) slab_dma(wq + qs * 64, NQ * 2, lds_base + L_W, wave, lane); } \
                   else if (qs >= 0 && (j) < 9) slab_dma<(j) < 9 ? (j) : 0, (j) < 9 ? (j) + 1 : 0>(wq + qs * 64, NQ * 2, lds_base + L_W, wave, lane); \
                   KLD(j); WAITV(DK); } while (0)
      WK(0); WK(1); WK(2); WK(3);
      WK(4); WK(5);
      WK(6); WK(7);
      TL(24);
      if (ml >= 0) { const u32x4 c0 = *(const u32x4*)lp, c1 = *(const u32x4*)(lp + 1024); SILU_IMAGE(c0, c1); }
      BAR();
      PFLAG();
      WK(8); WK(9); WK(10); WK(11);
      TL(25);
      PACE(12); REP4(KLD, 12, WAITV(DK));
      TL(26);
      const int hg0 = (wave * 144 + lane) * 16, hg2 = (wave * 144 + 128 + n) * 16;
#define HREQ() do { dma_sc(ws + W_H, hg0, land); dma_sc(ws + W_H + 18432, hg0, land + 1024); dma_sc(ws + W_H + 1024, hg0, land + 2048); \
                    dma_sc(ws + W_H + 18432 + 1024, hg0, land + 3072); dma_sc(ws + W_H, hg2, land + 4096); dma_sc(ws + W_H + 18432, hg2, land + 5120); } while (0)
#define HGET() do { _Pragma("unroll") for (int i = 0; i < 6; ++i) hq[i] = *(const u32x4*)(lp + i * 1024); } while (0)
#define HQOK() __all((tag_x(hq[0], e0, e1) | tag_x(hq[1], e0, e1) | tag_x(hq[2], e0, e1) | tag_x(hq[3], e0, e1) | tag_x(hq[4], e0, e1) | tag_x(hq[5], e0, e1)) == 0u)
#define HOK() __all((tag_x(hp[0], e0, e1) | tag_x(hp[1], e0, e1) | tag_x(hp[2], e0, e1)) == 0u)
      if (qs >= 0) HREQ();
      PACE(16); REP4(KLD, 16, WAITV(DK));
      if (qs >= 0) WAITV(4);
      TL(27);
      PACE(20); KLD(20); KLD(21);
      if (qs >= 0) {
        u32x4 hq[6]; HGET();
        _Pragma("nounroll") for (int sp = 0; sp < SPIN_CAP && !HQOK(); ++sp) { nap(16); HREQ(); WAITV(0); HGET(); }
        TL(2);
#define HSUM(a, b) cvt2(lo16(a) + lo16(b), hi16(a) + hi16(b))
        *(u32x2*)(lds + L_IMG + hg0 / 2) = u32x2{HSUM(hq[0][0], hq[1][0]), HSUM(hq[0][1], hq[1][1])};
        *(u32x2*)(lds + L_IMG + 512 + hg0 / 2) = u32x2{HSUM(hq[2][0], hq[3][0]), HSUM(hq[2][1], hq[3][1])};
        if (lane < 16) *(u32x2*)(lds + L_IMG + hg2 / 2) = u32x2{HSUM(hq[4][0], hq[5][0]), HSUM(hq[4][1], hq[5][1])};
      } else WAITV(DK);
      KLD(22); KLD(23);
      if (qs >= 0) {
        u32x4 wf[9], af[9];
        lds_barrier();
        slab_frags(lds, L_W, wave, lane, wf);
        img_frags(lds, L_IMG, lane, af);
        const f32x4 s = half_sum(gemv9(wf, af));
        if (n < 8 && grp < 2) *(u32x2*)(lds + L_TILE + n * 128 + wave * 16 + grp * 8) = u32x2{cvt2(s[0], s[1]), cvt2(s[2], s[3])};
      } else WAITV(DK);
      PACE(24); KLD(24); KLD(25);
      if (qs >= 0) {
        lds_barrier();
        if (wave < 2) {
          const u32x2 t2 = *(const u32x2*)(lds + L_TILE + tid * 8);
          st128(rs, W_Q + ((tid >> 4) * 1024 + qs * 16 + n) * 16, u32x4{t2[0], t2[1], e0, e1});
        }
        TL(3);
      }
      SCHED();
    }
    const unsigned char* qg = ws + W_Q + (b_att * 1024 + kvh * 64) * 16;
    KLD(26); WAITV(DK); KLD(27); WAITV(DK); PACE(28); KLD(28); WAITV(DK); KLD(29); WAITV(DK);
    PACE(30); KLD(30); WAITV(DK); KLD(31); WAITV(DK);
    TL(28);
    PACE(32); VLD(0); WAITV(DK); VLD(1); WAITV(DK); PACE(34); VLD(2); WAITV(DK); VLD(3); WAITV(DK);
    if (wave == 1) dma_sc(qg, lane * 16, lds_base + L_B + 1024);
    PACE(36); VLD(4); WAITV(DK); VLD(5); WAITV(DK); PACE(38); VLD(6); WAITV(DK); VLD(7); WAITV(DK);
    if (wave == 1) dma_sc(qg, lane * 16, lds_base + L_B + 1024);
    TL(29);
    PACE(40); VLD(8); WAITV(DV); VLD(9); WAITV(DV); PACE(42); VLD(10); WAITV(DV); VLD(11); WAITV(DV);
    TIE8(kb, 0); TIE8(kb, 8); TIE8(kb, 16); TIE8(kb, 24);
    PACE(44); VLD(12); VLD(13);
    u32x4 qf[2];
    {
      const u32x4* ql = (const u32x4*)(lds + L_B + 1024 + (((n >> 1) & 1) * 32 + (n & 1) * 8 + grp * 2) * 16);
      u32x4 g[4];
      _Pragma("nounroll") for (int sp = 0; sp < SPIN_CAP; ++sp) {
        g[0] = ql[0]; g[1] = ql[1]; g[2] = ql[16]; g[3] = ql[17];
        if (__all((tag_x(g[0], e0, e1) | tag_x(g[1], e0, e1) | tag_x(g[2], e0, e1) | tag_x(g[3], e0, e1)) == 0u)) break;
        if (wave == 1) { dma_sc(qg, lane * 16, lds_base + L_B + 1024); WAITV(0); } else nap(4);
      }
      asm volatile("" ::: "memory");
      g[0] = ql[0]; g[1] = ql[1]; g[2] = ql[16]; g[3] = ql[17];
      qf[0] = n < 4 ? u32x4{g[0][0], g[0][1], g[1][0], g[1][1]} : u32x4{0, 0, 0, 0};
      qf[1] = n < 4 ? u32x4{g[2][0], g[2][1], g[3][0], g[3][1]} : u32x4{0, 0, 0, 0};
    }
    TL(4);
    constexpr float SCALE = 0.088388347648318440f;
    f32x2 sc[16];
    float mx = -INFINITY;
#pragma unroll
    for (int T = 0; T < 16; ++T) {
      f32x4 acc = mfma(kb[T * 2], qf[0], f32x4{0.f, 0.f, 0.f, 0.f});
      acc = mfma(kb[T * 2 + 1], qf[1], acc);
      sc[T] = f32x2{acc[0] + dppf<0xB1>(acc[1]), acc[2] + dppf<0xB1>(acc[3])};
      if (T == 7 || T == 15) { SCHED(); if (T == 7) VLD(14); else VLD(15); SCHED(); }
    }
#pragma unroll
    for (int T = 0; T < 16; ++T)
#pragma unroll
      for (int j = 0; j < 2; ++j) { const float v = rbf(rbf(sc[T][j]) * SCALE); sc[T][j] = v; mx = fmaxf(mx, v); }
    SCHED(); VLD(16); SCHED();
    mx = fmaxf(mx, __shfl_xor(mx, 16));
    mx = fmaxf(mx, __shfl_xor(mx, 32));
    float ls = 0.f;
#pragma unroll
    for (int T = 0; T < 16; ++T)
#pragma unroll
      for (int j = 0; j < 2; ++j) { sc[T][j] = __expf(sc[T][j] - mx); ls += sc[T][j]; }
    SCHED(); VLD(17); SCHED();
    ls += __shfl_xor(ls, 16);
    ls += __shfl_xor(ls, 32);
    float* stat = (float*)(lds + L_B);
    if (lane == 0 || lane == 2) *(f32x2*)(stat + (wave * 2 + (lane >> 1)) * 2) = f32x2{mx, ls};
    SCHED(); VLD(18); VLD(19); SCHED();
    lds_barrier();
    float M = -INFINITY, L = 0.f;
    {
      f32x2 t[8];
#pragma unroll
      for (int q = 0; q < 8; ++q) { t[q] = *(const f32x2*)(stat + (q * 2 + ((n >> 1) & 1)) * 2); M = fmaxf(M, t[q].x); }
#pragma unroll
      for (int q = 0; q < 8; ++q) L = fmaf(__expf(t[q].x - M), t[q].y, L);
    }
    if (tid < 8) st128(rs, W_ST + (w * 8 + lane) * 16, u32x4{__float_as_uint(M), __float_as_uint(L), e0, e1});
    SCHED();
#define VTR(G) do { u32x4 f_[8]; _Pragma("unroll") for (int t = 0; t < 8; ++t) _Pragma("unroll") for (int j = 0; j < 4; ++j) \
      f_[t][j] = (t & 1) ? phi(vb[(G) * 8 + 2 * j][t >> 1], vb[(G) * 8 + 2 * j + 1][t >> 1]) : plo(vb[(G) * 8 + 2 * j][t >> 1], vb[(G) * 8 + 2 * j + 1][t >> 1]); \
      _Pragma("unroll") for (int t = 0; t < 8; ++t) { vb[(G) * 8 + t] = f_[t]; asm volatile("" : "+v"(vb[(G) * 8 + t])); } SCHED(); } while (0)
    WAITV(12);
    TIE8(vb, 0);
    VTR(0);
    TL(30);
    PACE(52); VLD(20); WAITV(5);
    TIE8(vb, 8);
    VTR(1);
    VLD(21); WAITV(DV);
    const int po = W_ST + ((w ^ 1) * 8 + (n & 2)) * 16;
    int l1 = lane; asm volatile("" : "+v"(l1));
    const int fl = 1024 + min(l1, NPZ / 2 - 65) * 16;
    if (wave == 1) { dma_sc(ws + W_FL, l1 * 16, lds_base + L_B + 2048); dma_sc(ws + W_FL, fl, lds_base + L_B + 3072); }
    PACE(54);
    VLD(22); WAITV(DV); VLD(23); WAITV(DV);
    u32x4 ps;
    ald128sc(ps, ws, po);
    PACE(56);
    VLD(24); WAITV(DV); VLD(25); WAITV(DV);
    TL(31);
    PACE(58);
    VLD(26); WAITV(DV); VLD(27); WAITV(DV);
    u32x4 ps2;
    VLD(28); VLD(29); WAITV(6);
    asm volatile("" : "+v"(ps));
    TIE8(vb, 16);
    ald128sc(ps2, ws, po);
    VLD(30); VLD(31);
    VTR(2);
    if (!__all(tag_ok(ps, e0, e1))) {
      WAITV(2);
      asm volatile("" : "+v"(ps2));
      ps = ps2;
      if (!__all(tag_ok(ps, e0, e1))) {
        int sp = 0;
        _Pragma("nounroll") do { nap(1); ps = ld128(rs, po); } while (!__all(tag_ok(ps, e0, e1)) && ++sp < SPIN_CAP);
      }
    }
#define FOK(a, b) __all(((a[0] ^ e0) | (a[1] ^ e1) | tag_x(a, e0, e1) | (b[0] ^ e0) | (b[1] ^ e1) | tag_x(b, e0, e1)) == 0u)
    if (wave == 1 && !FOK((*(const u32x4*)(lds + L_B + 2048 + l1 * 16)), (*(const u32x4*)(lds + L_B + 3072 + l1 * 16)))) {
      _Pragma("nounroll") for (int sp = 0; sp < SPIN_CAP; ++sp) {
        nap(1);
        const u32x4 a = ld128(rs, W_FL + l1 * 16), b = ld128(rs, W_FL + fl);
        if (FOK(a, b)) break;
      }
    }
    float fac;
    {
      const float M2 = __uint_as_float(ps[0]), L2 = __uint_as_float(ps[1]);
      const float Mg = fmaxf(M, M2);
      const float Lg = __expf(M - Mg) * L + __expf(M2 - Mg) * L2;
      fac = __expf(mx - Mg) / Lg;
    }
    TL(5);
    u32x4 pf[4];
#pragma unroll
    for (int G = 0; G < 4; ++G) {
#pragma unroll
      for (int j = 0; j < 4; ++j) pf[G][j] = cvt2(sc[4 * G + j][0] * fac, sc[4 * G + j][1] * fac);
    }
    f32x4 oa[8];
#pragma unroll
    for (int t = 0; t < 8; ++t) oa[t] = f32x4{0.f, 0.f, 0.f, 0.f};
    SCHED();
#pragma unroll
    for (int G = 0; G < 2; ++G)
#pragma unroll
      for (int t = 0; t < 8; ++t) oa[t] = mfma(pf[G], vb[G * 8 + t], oa[t]);
    SCHED();
#pragma unroll
    for (int t = 0; t < 8; ++t) oa[t] = mfma(pf[2], vb[16 + t], oa[t]);
    SCHED();
    int t2_ = threadIdx.x; asm volatile("" : "+v"(t2_));
    const int tid = t2_, lane = tid & 63, n = lane & 15, grp = lane >> 4;
    int oo = W_O + ((kvh * 8 + b_att) * 2 + s_att) * 1024 + ((lane >> 4) * 16 + (lane & 1) * 8 + ((lane >> 1) & 7)) * 16;
    int mw_ = L_C + wave * 1024 + n * 32, mr_ = L_C + lane * 16;
    asm volatile("" : "+v"(oo), "+v"(mw_), "+v"(mr_), "+v"(pf[3]));
    constexpr int L_H2 = L_W + 73728;
    constexpr int L_ACT = L_C + 4608;
    int ia[3], ra[4], yo[3], fo[7], tw, tr_;
    const bool late = gs >= 0 || mw >= 18;
    if (late) {
      BAR();
      slab_dma<0, 4>(gs >= 0 ? wg + gs * 64 : w_mod + ml * 64, gs >= 0 ? FF * 2 : NMOD * 2, lds_base + L_W + (gs >= 0 ? 73728 : 0), wave, lane);
      {
    const int b = wave;
#pragma unroll
    for (int i = 0; i < 3; ++i) {
      const int k = i < 2 ? i * 256 + lane * 4 : 512 + n * 4, hf = k >= 288 ? 1 : 0, kk = k - hf * 288, s = kk >> 5, g = (kk >> 3) & 3;
      ia[i] = L_H2 + (s * 64 + g * 16 + ((hf * 8 + b) ^ ((s & 3) * 4 + g))) * 16 + (kk & 7) * 2;
    }
#pragma unroll
    for (int i = 0; i < 4; ++i) ra[i] = L_H2 + (grp * 16 + (n ^ (i * 4 + grp))) * 16;
    tw = L_ACT + n * 144 + wave * 16 + (grp & 1) * 8 + (grp >> 1) * 4; tr_ = L_ACT + (n & 7) * 144 + grp * 16;
    yo[0] = W_Y + (((wave * 8 + grp * 2) * NG + gs) * 16 + n) * 16; yo[1] = yo[0] + NG * 256;
    yo[2] = W_Y + ((((64 + wave * 2 + (grp >> 1)) * NG + gs) * 2 + (grp & 1)) * 8 + n) * 16;
    {
      const int b_ = lane >> 3, cc = lane & 7, col = gs * 8 + cc, hf = col >= 288 ? 1 : 0, kk = col - hf * 288, s_ = kk >> 5, g = (kk >> 3) & 3;
      fo[0] = W_M + ((3 * 8 + b_) * 144 + gs * 2 + (cc >> 2)) * 16;
      fo[1] = W_X1 + (b_ * 152 + gs * 2 + (cc >> 2)) * 16; fo[2] = W_X1 + (b_ * 152 + 144) * 16;
      fo[3] = W_Y + (gs * 1152 + tid) * 16;
      fo[4] = L_H2 + (s_ * 64 + g * 16 + ((hf * 8 + b_) ^ ((s_ & 3) * 4 + g))) * 16 + cc * 2;
      fo[5] = L_C + (((cc >> 2) * 8 + b_) * 4 + (cc & 3)) * 4;
      fo[6] = (b_ * D + col) * 2;
    }
    asm volatile("" : "+v"(ia[0]), "+v"(ia[1]), "+v"(ia[2]), "+v"(ra[0]), "+v"(ra[1]), "+v"(ra[2]), "+v"(ra[3]), "+v"(tw), "+v"(tr_), "+v"(yo[0]), "+v"(yo[1]), "+v"(yo[2]),
                 "+v"(fo[0]), "+v"(fo[1]), "+v"(fo[2]), "+v"(fo[3]), "+v"(fo[4]), "+v"(fo[5]), "+v"(fo[6]));
      }
      WAITV(6);
    } else WAITV(2);
    u32x4 vf[8];
    asm volatile("s_nop 0 ; both branches above cover V up to the last two" : "+v"(vb[24]), "+v"(vb[25]), "+v"(vb[26]), "+v"(vb[27]), "+v"(vb[28]), "+v"(vb[29]));
    asm volatile("" :: "v"(ps2));
#pragma unroll
    for (int t = 0; t < 8; ++t)
#pragma unroll
      for (int j = 0; j < 3; ++j) vf[t][j] = (t & 1) ? phi(vb[24 + 2 * j][t >> 1], vb[25 + 2 * j][t >> 1]) : plo(vb[24 + 2 * j][t >> 1], vb[25 + 2 * j][t >> 1]);
#pragma unroll
    for (int t = 0; t < 8; ++t) asm volatile("" : "+v"(vf[t][0]), "+v"(vf[t][1]), "+v"(vf[t][2]));
    if (late) WAITV(4); else WAITV(0);
    asm volatile("s_nop 0 ; both branches above cover V" : "+v"(vb[30]), "+v"(vb[31]));
#pragma unroll
    for (int t = 0; t < 8; ++t) {
      vf[t][3] = (t & 1) ? phi(vb[30][t >> 1], vb[31][t >> 1]) : plo(vb[30][t >> 1], vb[31][t >> 1]);
      oa[t] = mfma(pf[3], vf[t], oa[t]);
    }
    TL(6);
    if (grp == 0) {
#pragma unroll
      for (int g = 0; g < 2; ++g) {
        *(f32x4*)(lds + mw_ + g * 512) = f32x4{oa[0][2 * g], oa[1][2 * g], oa[2][2 * g], oa[3][2 * g]};
        *(f32x4*)(lds + mw_ + g * 512 + 16) = f32x4{oa[4][2 * g], oa[5][2 * g], oa[6][2 * g], oa[7][2 * g]};
      }
    }
    lds_barrier();
    if (wave == 0) {
      f32x4 m[8];
#pragma unroll
      for (int q = 0; q < 8; ++q) m[q] = *(const f32x4*)(lds + mr_ + q * 1024);
      SCHED();
      const f32x4 o = ((m[0] + m[1]) + (m[2] + m[3])) + ((m[4] + m[5]) + (m[6] + m[7]));
      st128(rs, oo, u32x4{sanf(o[0]), sanf(o[1]), sanf(o[2]), sanf(o[3])});
    }
    TL(7); SCHED();
  if (gs >= 0) {
    const int b = wave;
    BAR(); SCHED();
    u32x4 bg[9], bu[9], wdf[2][5];
    {
      u32x2 rd[2][8];
      slab_dma<4, 9>(wg + gs * 64, FF * 2, lds_base + L_W + 73728, wave, lane);
      TL(23);
      BAR();
      const char* db = (const char*)(wd + (size_t)gs * 64 * D + wave * 64);
      const u32 dv = grp * 9216 + n * 8;
#pragma unroll
      for (int ks = 0; ks < 2; ++ks)
#pragma unroll
        for (int i = 0; i < 8; ++i) ald64nt(rd[ks][i], db + (ks * 32 + i) * 1152, dv);
      {
        const int r = lane >> 3;
        dma((const char*)(wd + (size_t)(gs * 64 + wave * 8) * D + 512), r * 1152 + (((lane & 7) ^ (r & 2) ^ ((wave & 1) * 4)) << 4), lds_base + L_B + wave * 1024);
      }
      TL(19);
      asm volatile("s_waitcnt vmcnt(17)\n\ts_barrier" ::: "memory");
      TL(22);
      slab_frags(lds, L_W + 73728, wave, lane, bg);
      SCHED();
      slab_dma(wu + gs * 64, FF * 2, lds_base + L_W, wave, lane);
      TL(20);
      asm volatile("s_waitcnt vmcnt(10)" : "+v"(rd[0][0]), "+v"(rd[0][1]), "+v"(rd[0][2]), "+v"(rd[0][3]), "+v"(rd[0][4]), "+v"(rd[0][5]), "+v"(rd[0][6]), "+v"(rd[0][7]),
                   "+v"(rd[1][0]), "+v"(rd[1][1]), "+v"(rd[1][2]), "+v"(rd[1][3]), "+v"(rd[1][4]), "+v"(rd[1][5]), "+v"(rd[1][6]), "+v"(rd[1][7]) :: "memory");
#pragma unroll
      for (int ks = 0; ks < 2; ++ks)
#pragma unroll
        for (int j = 0; j < 4; ++j) {
          wdf[ks][0][j] = plo(rd[ks][2 * j][0], rd[ks][2 * j + 1][0]); wdf[ks][1][j] = phi(rd[ks][2 * j][0], rd[ks][2 * j + 1][0]);
          wdf[ks][2][j] = plo(rd[ks][2 * j][1], rd[ks][2 * j + 1][1]); wdf[ks][3][j] = phi(rd[ks][2 * j][1], rd[ks][2 * j + 1][1]);
        }
#pragma unroll
      for (int t = 0; t < 4; ++t) asm volatile("" : "+v"(wdf[0][t]), "+v"(wdf[1][t]));
      TL(21);
    }
    asm volatile("s_waitcnt vmcnt(0) lgkmcnt(0)\n\ts_barrier" ::: "memory");
    {
      u32x4 hp[3];
      const int h0 = W_H2 + (b * 144 + lane) * 16, h2o = W_H2 + (b * 144 + 128 + n) * 16;
      hp[0] = ld128(rs, h0); hp[1] = ld128(rs, h0 + 1024); hp[2] = ld128(rs, h2o);
      slab_frags(lds, L_W, wave, lane, bu);
      const int q = n >> 2, p = lane & 3;
      const int a0 = L_B + (8 * grp + q) * 128 + ((((wave & 3) * 2 + (p >> 1)) ^ (q & 2) ^ ((grp & 1) * 4)) << 4) + (p & 1) * 8;
#pragma unroll
      for (int ks = 0; ks < 2; ++ks) {
        const u32x2 lo = trd(lds, a0 + ks * 4096), hi = trd(lds, a0 + ks * 4096 + 512);
        wdf[ks][4] = u32x4{lo[0], lo[1], hi[0], hi[1]};
      }
      TL(12);
#pragma unroll
      for (int s = 0; s < 9; ++s) asm volatile("" : "+v"(bg[s]), "+v"(bu[s]));
      asm volatile("" : "+v"(wdf[0][4]), "+v"(wdf[1][4]));
      if (!HOK()) {
        int sp = 0;
        _Pragma("nounroll") do { nap(2); hp[0] = ld128(rs, h0); hp[1] = ld128(rs, h0 + 1024); hp[2] = ld128(rs, h2o); } while (!HOK() && ++sp < SPIN_CAP);
      }
      TL(13);
      *(u32x2*)(lds + ia[0]) = u32x2{hp[0][0], hp[0][1]};
      *(u32x2*)(lds + ia[1]) = u32x2{hp[1][0], hp[1][1]};
      if (lane < 16) *(u32x2*)(lds + ia[2]) = u32x2{hp[2][0], hp[2][1]};
      lds_barrier();
      u32x4 af[9];
#pragma unroll
      for (int s = 0; s < 9; ++s) af[s] = *(const u32x4*)(lds + ra[s & 3] + s * 1024);
      SCHED();
      f32x4 ag[2], au[2];
#pragma unroll
      for (int s = 0; s < 9; ++s) {
        ag[s & 1] = mfma(bg[s], af[s], s < 2 ? f32x4{0.f, 0.f, 0.f, 0.f} : ag[s & 1]);
        au[s & 1] = mfma(bu[s], af[s], s < 2 ? f32x4{0.f, 0.f, 0.f, 0.f} : au[s & 1]);
      }
      const f32x2 hg = half_sum2(ag[0] + ag[1]), hu = half_sum2(au[0] + au[1]);
      const u32 act = cvt2(rbf(silu(rbf(hg.x))) * rbf(hu.x), rbf(silu(rbf(hg.y))) * rbf(hu.y));
      if (n < 8) *(u32*)(lds + tw) = act;
    }
    lds_barrier();
    TL(14);
    {
      u32x4 af[2];
#pragma unroll
      for (int ks = 0; ks < 2; ++ks) af[ks] = *(const u32x4*)(lds + tr_ + ks * 64);
      f32x4 d[5];
#pragma unroll
      for (int t = 0; t < 5; ++t) d[t] = mfma(wdf[1][t], af[1], mfma(wdf[0][t], af[0], f32x4{0.f, 0.f, 0.f, 0.f}));
      if (n < 8) {
#pragma unroll
        for (int j = 0; j < 4; ++j)
          st128(rs, yo[j >> 1] + (j & 1) * 128, u32x4{sanf(d[0][j]), sanf(d[1][j]), sanf(d[2][j]), sanf(d[3][j])});
        if (wave < 4)
          st128(rs, yo[2], u32x4{sanf(d[4][0]), sanf(d[4][1]), sanf(d[4][2]), sanf(d[4][3])});
      }
    }
    TL(15);
    {
      float gbv = 0.f, x1v = 0.f, h2 = 0.f, rsel = 0.f;
      if (wave == 7) {
        u32x4 gq, xq, rq;
        _Pragma("nounroll") for (int sp = 0; sp < SPIN_CAP; ++sp) {
          gq = ld128(rs, fo[0]); xq = ld128(rs, fo[1]); rq = ld128(rs, fo[2]);
          if (__all((tag_x(gq, e0, e1) | tag_x(xq, e0, e1) | tag_x(rq, e0, e1)) == 0u)) break;
          nap(1);
        }
        const u32 gw = (lane & 2) ? gq[1] : gq[0], xw = (lane & 2) ? xq[1] : xq[0];
        gbv = (lane & 1) ? hi16(gw) : lo16(gw); x1v = (lane & 1) ? hi16(xw) : lo16(xw);
        h2 = b2f(*(const u16*)(lds + fo[4])); rsel = __uint_as_float(rq[0]);
        asm volatile("" : "+v"(gbv), "+v"(x1v), "+v"(h2), "+v"(rsel));
      }
      WAITV(0);
      u32x4 y[3] = {};
      _Pragma("nounroll") for (int sp = 0; sp < SPIN_CAP; ++sp) {
        y[0] = ld128(rs, fo[3]); y[1] = ld128(rs, fo[3] + 8192);
        if (wave < 2) y[2] = ld128(rs, fo[3] + 16384);
        if (__all(mxu(mxu(mx4(y[0]), mx4(y[1])), mx4(y[2])) != 0xffffffffu)) break;
        nap(1);
      }
      TL(16);
      f32x4 s;
#pragma unroll
      for (int k = 0; k < 4; ++k) s[k] = xsum32(xsum16((__uint_as_float(y[0][k]) + __uint_as_float(y[1][k])) + __uint_as_float(y[2][k])));
      if (lane < 16) *(f32x4*)(lds + L_C + (wave * 16 + lane) * 16) = s;
      lds_barrier();
      if (wave == 7) {
        float t[8];
#pragma unroll
        for (int ww = 0; ww < 8; ++ww) t[ww] = *(const float*)(lds + fo[5] + ww * 256);
        SCHED();
        const float y2 = rbf(rbf(((t[0] + t[1]) + (t[2] + t[3])) + ((t[4] + t[5]) + (t[6] + t[7]))) * rsel);
        const float res = x1v + rbf(gbv * rbf(h2 + y2));
        __builtin_amdgcn_raw_buffer_store_b16((u16)(cvt2(res, 0.f) & 0xffffu), __builtin_amdgcn_make_buffer_rsrc(out, 0, 8 * D * 2, 0x00020000), fo[6], 0, 0);
      }
      TL(17);
    }
  }
  {
  int t3_ = threadIdx.x; asm volatile("" : "+v"(t3_));
  const int tid = t3_, lane = tid & 63, n = lane & 15, grp = lane >> 4;
  if (qs >= 0) {
    const u16* mat = (wave & 1) ? wg : wu;
    const int g0 = ((3 - (int)((uintptr_t)mat >> 7) - xcd * 9) & 7) + (wave >= 4 ? 8 : 0);
    const int r = (idx - 6) * 72 + ((wave >> 1) & 1) * 36 + min(lane, 35);
    if (g0 < 9) dma_keep(mat, r * 9216 + (xcd * 9 + g0) * 128, lds_base + L_W + wave * 256);
  }
  if (ml >= 0) {
    BAR();
    if (mw < 18) slab_dma<0, 4>(w_mod + ml * 64, NMOD * 2, lds_base + L_W, wave, lane);
    slab_dma<4, 9>(w_mod + ml * 64, NMOD * 2, lds_base + L_W, wave, lane);
    u32x4 wf[9], af[9];
    img_frags(lds, L_IMG, lane, af);
    asm volatile("s_waitcnt vmcnt(0)\n\ts_barrier" ::: "memory");
    slab_frags(lds, L_W, wave, lane, wf);
    const f32x4 s = half_sum(gemv9(wf, af));
    if (n < 8 && grp < 2) *(u32x2*)(lds + L_TILE + n * 128 + wave * 16 + grp * 8) = u32x2{cvt2(s[0], s[1]), cvt2(s[2], s[3])};
    lds_barrier();
    if (wave < 2) {
      const u32x2 t2 = *(const u32x2*)(lds + L_TILE + tid * 8);
      const int ch = (ml - 18) / 9, sl = ml - 18 - ch * 9;
      st128(rs, W_M + ((ch * 8 + (tid >> 4)) * 144 + sl * 16 + n) * 16, u32x4{t2[0], t2[1], e0, e1});
    }
    TL(18);
  }

  if (lnb >= 0 && wave < 3) {
    const int b = lnb, g = tid < 144 ? tid : 143;
    const bool on = tid < 144;
    float* ex = (float*)(lds + L_TILE);
    const u32x2 xa = __builtin_amdgcn_raw_buffer_load_b64(__builtin_amdgcn_make_buffer_rsrc((void*)x, 0, 8 * D * 2, 0x00020000), (b * D + g * 4) * 2, 0, 0);
    const u32x2 wra = __builtin_amdgcn_raw_buffer_load_b64(__builtin_amdgcn_make_buffer_rsrc((void*)w_r, 0, D * 2, 0x00020000), g * 8, 0, 0);
    u32x4 mm[3];
    _Pragma("nounroll") for (int sp = 0; sp < SPIN_CAP; ++sp) {
#pragma unroll
      for (int k = 0; k < 3; ++k) mm[k] = ld128(rs, W_M + ((k * 8 + b) * 144 + g) * 16);
      if (__all((tag_x(mm[0], e0, e1) | tag_x(mm[1], e0, e1) | tag_x(mm[2], e0, e1)) == 0u)) break;
      nap(4);
    }
    f32x2 xv[2], ga[2], sb[2], cb1[2], wr[2];
#pragma unroll
    for (int i = 0; i < 2; ++i) {
      xv[i] = up2(xa[i]); wr[i] = up2(wra[i]); ga[i] = up2(mm[0][i]); sb[i] = up2(mm[1][i]); cb1[i] = rbf2(f32x2{1.f, 1.f} + up2(mm[2][i]));
      asm volatile("" : "+v"(xv[i]), "+v"(ga[i]), "+v"(sb[i]), "+v"(cb1[i]), "+v"(wr[i]));
    }
    int ho = W_H2 + (b * 144 + g) * 16, xo = W_X1 + (b * 152 + g) * 16;
    asm volatile("" : "+v"(ho), "+v"(xo));
    u32x4 xp[8];
    _Pragma("nounroll") for (int sp = 0; sp < SPIN_CAP; ++sp) {
#pragma unroll
      for (int p = 0; p < 8; ++p) xp[p] = ld128(rs, W_XP + (p * 8 + b) * 2304 + g * 16);
      if (__all(mxu(mxu(mxu(mx4(xp[0]), mx4(xp[1])), mxu(mx4(xp[2]), mx4(xp[3]))), mxu(mxu(mx4(xp[4]), mx4(xp[5])), mxu(mx4(xp[6]), mx4(xp[7])))) != 0xffffffffu)) break;
      nap(1);
    }
    TL(10);
    f32x2 v[2];
#pragma unroll
    for (int i = 0; i < 2; ++i) {
      f32x2 pp[8];
#pragma unroll
      for (int p = 0; p < 8; ++p) pp[p] = f32x2{__uint_as_float(xp[p][2 * i]), __uint_as_float(xp[p][2 * i + 1])};
      v[i] = rbf2(xv[i] + rbf2(ga[i] * rbf2(((pp[0] + pp[1]) + (pp[2] + pp[3])) + ((pp[4] + pp[5]) + (pp[6] + pp[7])))));
    }
    const f32x2 s2 = v[0] + v[1];
    const float mw = wsum(on ? s2.x + s2.y : 0.f) * (wave < 2 ? 1.f / 256 : 1.f / 64);
    const f32x2 t0 = v[0] - f32x2{mw, mw}, t1 = v[1] - f32x2{mw, mw}, q2 = t0 * t0 + t1 * t1;
    const float m2 = wsum(on ? q2.x + q2.y : 0.f);
    if (lane == 0) *(f32x2*)(ex + wave * 2) = f32x2{mw, m2};
    lds_barrier();
    const f32x4 e01 = *(const f32x4*)ex; const f32x2 e2 = *(const f32x2*)(ex + 4);
    const float mu = (e01[0] + e01[2]) * (256.f / D) + e2.x * (64.f / D);
    const float d0 = e01[0] - mu, d1 = e01[2] - mu, d2 = e2.x - mu;
    const float var = rbf(((e01[1] + e01[3]) + e2.y + 256.f * (d0 * d0 + d1 * d1) + 64.f * d2 * d2) * (1.f / D));
    const float mean = rbf(mu), rden = frcp(rbf(sqrtf(rbf(var + 1e-5f))));
    u32 hw[2];
#pragma unroll
    for (int i = 0; i < 2; ++i) {
      const f32x2 t = rbf2(rbf2(rbf2(v[i] - f32x2{mean, mean}) * f32x2{rden, rden}) * cb1[i]) + sb[i];
      hw[i] = cvt2(t.x, t.y);
    }
    if (on) {
      st128(rs, ho, u32x4{hw[0], hw[1], e0, e1});
      st128(rs, xo, u32x4{cvt2(v[0].x, v[0].y), cvt2(v[1].x, v[1].y), e0, e1});
    }
    TL(11);
    const f32x2 r2 = up2(hw[0]) * wr[0] + up2(hw[1]) * wr[1];
    const float rw = wsum(on ? r2.x + r2.y : 0.f);
    if (lane == 0) ex[8 + wave] = rw;
    lds_barrier();
    if (tid < 8) {
      const float r = rbf((ex[8] + ex[9]) + ex[10]);
      st128(rs, W_X1 + (b * 152 + 144 + tid) * 16, u32x4{__float_as_uint(rbf(1.f / (1.f + __expf(-r)))), 0u, e0, e1});
    }
  }

  if (oj >= 0) {
    const int ob = W_O + ((op * 2 + (wave >> 2)) * 8 + (lane >> 3)) * 2048 + (wave & 3) * 256 + (lane & 7) * 16;
    u32x4 a[4];
#pragma unroll
    for (int p = 0; p < 4; ++p) a[p] = ld128(rs, ob + (p >> 1) * 1024 + (p & 1) * 128);
    SCHED();
    u32x4 wf[8];
    {
      const int q = n >> 2, p = lane & 3, hf = p >> 1;
      const int a0 = L_W + (wave * 64 + hf * 32 + 8 * grp + q) * 128 + (((q >> 1) ^ ((grp & 1) * 2) ^ (hf * 4)) << 4) + (p & 1) * 8;
#pragma unroll
      for (int cg = 0; cg < 8; ++cg) {
        const u32x2 lo = trd(lds, a0 ^ (cg * 16)), hi = trd(lds, (a0 ^ (cg * 16)) + 512);
        wf[cg] = u32x4{lo[0], lo[1], hi[0], hi[1]};
      }
    }
    int rw = L_OI + (n & 7) * 4128 + (wave * 2 + (n >> 3)) * 256 + (grp & 1) * 16, rr = L_OI + wave * 4128 + grp * 1024 + n * 16;
    int xo = W_XP + (op * 8 + wave) * 2304 + oj * 256 + lane * 16, sl = ((n & 7) * 8 + (n >> 3) * 4 + grp) * 4;
#pragma unroll
    for (int cg = 0; cg < 8; ++cg) asm volatile("" : "+v"(wf[cg]));
    asm volatile("" : "+v"(rw), "+v"(rr), "+v"(xo), "+v"(sl));
    _Pragma("nounroll") for (int sp = 0; sp < SPIN_CAP; ++sp) {
      const bool m0 = mxu(mx4(a[0]), mx4(a[1])) == 0xffffffffu, m1 = mxu(mx4(a[2]), mx4(a[3])) == 0xffffffffu;
      if (!__any(m0 || m1)) break;
      nap(2);
      if (m0) { a[0] = ld128(rs, ob); a[1] = ld128(rs, ob + 128); }
      if (m1) { a[2] = ld128(rs, ob + 1024); a[3] = ld128(rs, ob + 1152); }
    }
    TL(8);
#define OS(p, j) (__uint_as_float(a[p][j]) + __uint_as_float(a[(p) + 2][j]))
#define OP(v) (u32)__builtin_amdgcn_ds_bpermute(sl, (int)(v))
    const u32x4 of = {OP(cvt2(OS(0, 0), OS(0, 1))), OP(cvt2(OS(0, 2), OS(0, 3))), OP(cvt2(OS(1, 0), OS(1, 1))), OP(cvt2(OS(1, 2), OS(1, 3)))};
    f32x4 acc[8];
#pragma unroll
    for (int cg = 0; cg < 8; ++cg) acc[cg] = mfma(wf[cg], of, f32x4{0.f, 0.f, 0.f, 0.f});
    if ((n >> 3) == (grp >> 1)) {
#pragma unroll
      for (int cg = 0; cg < 8; ++cg) *(f32x4*)(lds + rw + cg * 32) = acc[cg];
    }
    lds_barrier();
    f32x4 s = (*(const f32x4*)(lds + rr) + *(const f32x4*)(lds + rr + 256)) + (*(const f32x4*)(lds + rr + 512) + *(const f32x4*)(lds + rr + 768));
#pragma unroll
    for (int k = 0; k < 4; ++k) s[k] = xsum32(xsum16(s[k]));
    if (lane < 16) st128(rs, xo, u32x4{sanf(s[0]), sanf(s[1]), sanf(s[2]), sanf(s[3])});
    TL(9);
  }
  TLOUT();
  }
  }
}

extern "C" void kernel_launch(void* const* d_in, const int* in_sizes, int n_in, void* d_out, int out_size, void* d_ws, size_t ws_size,
                              hipStream_t stream) {
  fused_kernel<<<NWG, NT, 0, stream>>>((const u16*)d_in[0], (const u16*)d_in[1], (const u16*)d_in[2], (const u16*)d_in[3], (const u16*)d_in[4],
                                       (const u16*)d_in[6], (const u16*)d_in[7], (const u16*)d_in[8], (const u16*)d_in[9], (const u16*)d_in[10],
                                       (const u16*)d_in[11], (unsigned char*)d_ws, (u16*)d_out);
}
```

```cpp
#include <hip/hip_runtime.h>
#include <hip/hip_bf16.h>
#include <cstdint>

typedef unsigned u32;
typedef unsigned short u16;
typedef u32 u32x4 __attribute__((ext_vector_type(4)));
typedef u32 u32x2 __attribute__((ext_vector_type(2)));
typedef float f32x4 __attribute__((ext_vector_type(4)));
typedef float f32x2 __attribute__((ext_vector_type(2)));
typedef short s16x4 __attribute__((ext_vector_type(4)));
typedef __bf16 bf16x8 __attribute__((ext_vector_type(8)));
typedef __bf16 bf16x2 __attribute__((ext_vector_type(2)));
typedef __attribute__((address_space(3))) unsigned char lds_t;

constexpr int D = 576, SK = 8192, WIN = 2048, FF = 4608, NQ = 4096, NMOD = 6 * D;
constexpr int NWG = 256, NT = 512, NG = 72;
constexpr int SPIN_CAP = 1 << 15;

constexpr int W_O = 0;
constexpr int W_XP = W_O + 262144;
constexpr int W_Y = W_XP + 147456;
constexpr int W_MS = W_Y + NG * NG * 256;
constexpr int W_FL = W_MS;
constexpr int W_H = W_FL + 2048;
constexpr int W_M = W_H + 36864;
constexpr int W_Q = W_M + 73728;
constexpr int W_ST = W_Q + 131072;
constexpr int W_H2 = W_ST + 32768;
constexpr int W_X1 = W_H2 + 18432;
constexpr int W_END = W_X1 + 8 * 152 * 16;
constexpr int NPZ = 174, PZ = (W_MS / 16 + NPZ - 1) / NPZ;
static_assert(PZ <= 2 * NT, "poison split");

constexpr int L_W = 0;
constexpr int L_IMG = 73728;
constexpr int L_TILE = L_IMG + 9216;
constexpr int L_OI = 65536;
constexpr int L_B = 147456;
constexpr int L_C = 155648;
constexpr int LDS_BYTES = 163840;

#define STAMP(i) do {} while (0)

#define SCHED() __builtin_amdgcn_sched_barrier(0)
#define BAR() asm volatile("s_barrier" ::: "memory")
#define TS(i) do {} while (0)
#define TSOUT() do {} while (0)
#define TSA(i) do {} while (0)
#define TSB(i) do {} while (0)
#define TSC(i) do {} while (0)
#define WAITV_(n) do { asm volatile("s_waitcnt vmcnt(" #n ")" ::: "memory"); SCHED(); } while (0)
#define WAITV(n) WAITV_(n)
#define DK 5
#define DV DK
#define PR 117
#define PR2 126
#define PR3 120

__device__ __forceinline__ float lo16(u32 u) { return __uint_as_float(u << 16); }
__device__ __forceinline__ float hi16(u32 u) { return __uint_as_float(u & 0xffff0000u); }
__device__ __forceinline__ float b2f(u16 u) { return __uint_as_float((u32)u << 16); }
__device__ __forceinline__ u32 cvt2(float a, float b) { return __builtin_bit_cast(u32, __builtin_convertvector(f32x2{a, b}, bf16x2)); }
__device__ __forceinline__ float rbf(float v) { return __uint_as_float(cvt2(0.f, v)); }
__device__ __forceinline__ u32 sanf(float f) { u32 v = __float_as_uint(f); return v < 0xfffffffeu ? v : 0xfffffffeu; }
__device__ __forceinline__ f32x2 up2(u32 u) { return f32x2{lo16(u), hi16(u)}; }
__device__ __forceinline__ f32x2 rbf2(f32x2 v) { return f32x2{rbf(v.x), rbf(v.y)}; }
__device__ __forceinline__ void lds_barrier() { asm volatile("s_waitcnt lgkmcnt(0)\n\ts_barrier" ::: "memory"); }
__device__ __forceinline__ f32x4 mfma(u32x4 a, u32x4 b, f32x4 c) {
  return __builtin_amdgcn_mfma_f32_16x16x32_bf16(__builtin_bit_cast(bf16x8, a), __builtin_bit_cast(bf16x8, b), c, 0, 0, 0);
}
__device__ __forceinline__ u32 plo(u32 a, u32 b) { return __builtin_amdgcn_perm(b, a, 0x05040100u); }
__device__ __forceinline__ u32 phi(u32 a, u32 b) { return __builtin_amdgcn_perm(b, a, 0x07060302u); }

extern "C" __device__ __attribute__((const)) unsigned long long llvm_dispatch_id() __asm("llvm.amdgcn.dispatch.id");
typedef __amdgpu_buffer_rsrc_t rsrc_t;
__device__ __forceinline__ u32x4 ld128(rsrc_t rs, int off) { return __builtin_amdgcn_raw_buffer_load_b128(rs, off, 0, 16); }
__device__ __forceinline__ void st128(rsrc_t rs, int off, u32x4 v) { __builtin_amdgcn_raw_buffer_store_b128(v, rs, off, 0, 16); }
__device__ __forceinline__ void st64(rsrc_t rs, int off, u32x2 v) { __builtin_amdgcn_raw_buffer_store_b64(v, rs, off, 0, 16); }
__device__ __forceinline__ void dma(const void* gbase, u32 voff, unsigned lds_dst) {
  unsigned keep;
  asm volatile("s_mov_b32 %0, m0\n\ts_mov_b32 m0, %3\n\ts_nop 2\n\tglobal_load_lds_dwordx4 %1, %2 nt\n\ts_mov_b32 m0, %0"
               : "=&s"(keep) : "v"(voff), "s"(gbase), "s"(lds_dst) : "memory");
}
__device__ __forceinline__ void dma_keep(const void* gbase, u32 voff, unsigned lds_dst) {
  unsigned keep;
  asm volatile("s_mov_b32 %0, m0\n\ts_mov_b32 m0, %3\n\ts_nop 2\n\tglobal_load_lds_dword %1, %2\n\ts_mov_b32 m0, %0"
               : "=&s"(keep) : "v"(voff), "s"(gbase), "s"(lds_dst) : "memory");
}
__device__ __forceinline__ void dma_sc(const void* gbase, u32 voff, unsigned lds_dst) {
  unsigned keep;
  asm volatile("s_mov_b32 %0, m0\n\ts_mov_b32 m0, %3\n\ts_nop 2\n\tglobal_load_lds_dwordx4 %1, %2 sc1\n\ts_mov_b32 m0, %0"
               : "=&s"(keep) : "v"(voff), "s"(gbase), "s"(lds_dst) : "memory");
}
__device__ __forceinline__ void ald128(u32x4& d, const void* gbase, u32 voff) { unsigned long long t; asm volatile("s_mov_b64 %1, %3\n\tglobal_load_dwordx4 %0, %2, %1" : "=v"(d), "=&s"(t) : "v"(voff), "s"(gbase) : "memory"); }
__device__ __forceinline__ void ald64(u32x2& d, const void* gbase, u32 voff) { unsigned long long t; asm volatile("s_mov_b64 %1, %3\n\tglobal_load_dwordx2 %0, %2, %1" : "=v"(d), "=&s"(t) : "v"(voff), "s"(gbase) : "memory"); }
__device__ __forceinline__ void ald64nt(u32x2& d, const void* gbase, u32 voff) { unsigned long long t; asm volatile("s_mov_b64 %1, %3\n\tglobal_load_dwordx2 %0, %2, %1 nt" : "=v"(d), "=&s"(t) : "v"(voff), "s"(gbase) : "memory"); }
__device__ __forceinline__ void ald128sc(u32x4& d, const void* gbase, u32 voff) { unsigned long long t; asm volatile("s_mov_b64 %1, %3\n\tglobal_load_dwordx4 %0, %2, %1 sc1" : "=v"(d), "=&s"(t) : "v"(voff), "s"(gbase) : "memory"); }
__device__ __forceinline__ u32x2 trd(const unsigned char* lds, int off) {
  return __builtin_bit_cast(u32x2, __builtin_amdgcn_ds_read_tr16_b64_v4i16((__attribute__((address_space(3))) s16x4*)((lds_t*)lds + off)));
}
#define nap(n) do { __builtin_amdgcn_s_sleep(n); asm volatile("" ::: "memory"); } while (0)
__device__ __forceinline__ float frcp(float v) { return __builtin_amdgcn_rcpf(v); }
__device__ __forceinline__ float fdiv(float a, float d, float r) { const float q = a * r; return fmaf(fmaf(-q, d, a), r, q); }
__device__ __forceinline__ float silu(float v) { const float d = 1.f + __expf(-v); const float r = frcp(d); return d < 3.0e38f ? fdiv(v, d, r) : v * r; }
template <int CTRL> __device__ __forceinline__ float dppf(float x) {
  return __builtin_bit_cast(float, __builtin_amdgcn_mov_dpp(__builtin_bit_cast(int, x), CTRL, 0xf, 0xf, true));
}
__device__ __forceinline__ u32 mxu(u32 a, u32 b) { return a > b ? a : b; }
__device__ __forceinline__ u32 mx4(u32x4 v) { return mxu(mxu(v[0], v[1]), mxu(v[2], v[3])); }
__device__ __forceinline__ float rl(float v, int l) { return __builtin_bit_cast(float, __builtin_amdgcn_readlane(__builtin_bit_cast(int, v), l)); }
__device__ __forceinline__ float wsum(float v) {
  v += dppf<0xB1>(v); v += dppf<0x4E>(v); v += dppf<0x141>(v); v += dppf<0x140>(v);
  return (rl(v, 0) + rl(v, 16)) + (rl(v, 32) + rl(v, 48));
}
__device__ __forceinline__ float up32(float v) { const u32 u = __float_as_uint(v); return __uint_as_float(__builtin_amdgcn_permlane32_swap(u, u, false, false)[1]); }
__device__ __forceinline__ float xsum32(float v) { const u32 u = __float_as_uint(v); const auto r = __builtin_amdgcn_permlane32_swap(u, u, false, false); return __uint_as_float(r[0]) + __uint_as_float(r[1]); }
__device__ __forceinline__ float xsum16(float v) { const u32 u = __float_as_uint(v); const auto r = __builtin_amdgcn_permlane16_swap(u, u, false, false); return __uint_as_float(r[0]) + __uint_as_float(r[1]); }
__device__ __forceinline__ f32x4 half_sum(f32x4 d) {
  f32x4 r;
#pragma unroll
  for (int j = 0; j < 4; ++j) r[j] = d[j] + dppf<0x108>(up32(d[j]));
  return r;
}
__device__ __forceinline__ f32x2 half_sum2(f32x4 d) {
  f32x2 r;
#pragma unroll
  for (int j = 0; j < 2; ++j) {
    const auto s = __builtin_amdgcn_permlane32_swap(__float_as_uint(d[j]), __float_as_uint(d[j + 2]), false, false);
    r[j] = __uint_as_float(s[0]) + dppf<0x108>(__uint_as_float(s[1]));
  }
  return r;
}
__device__ __forceinline__ void unpack8(u32x4 u, float* f) {
#pragma unroll
  for (int j = 0; j < 4; ++j) { f[2 * j] = lo16(u[j]); f[2 * j + 1] = hi16(u[j]); }
}
__device__ __forceinline__ void ln_row(const float (&v)[16], bool has2, float& mean, float& den) {
  float s = 0.f;
#pragma unroll
  for (int i = 0; i < 8; ++i) s += v[i];
  if (has2) {
#pragma unroll
    for (int i = 8; i < 16; ++i) s += v[i];
  }
  s = wsum(s);
  const float mu = s * (1.f / D);
  mean = rbf(mu);
  float q = 0.f;
#pragma unroll
  for (int i = 0; i < 8; ++i) { const float t = v[i] - mu; q = fmaf(t, t, q); }
  if (has2) {
#pragma unroll
    for (int i = 8; i < 16; ++i) { const float t = v[i] - mu; q = fmaf(t, t, q); }
  }
  q = wsum(q);
  const float var = rbf(q * (1.f / D));
  den = rbf(sqrtf(rbf(var + 1e-5f)));
}
template <int J0 = 0, int J1 = 9>
__device__ __forceinline__ void slab_dma(const u16* base, int stride_b, unsigned lds_dst, int wave, int lane) {
  const u32 v0 = (lane >> 3) * stride_b + (((lane & 7) ^ ((wave & 1) * 4 + (lane >> 4))) << 4), v1 = v0 ^ 32u;
  const u32 v4 = wave >= 4 ? v1 : v0;
  const char* gb = (const char*)base + (size_t)wave * 8 * stride_b;
#pragma unroll
  for (int j = J0; j < J1; ++j) dma(gb + (size_t)j * 64 * stride_b, j < 4 ? v0 : j == 4 ? v4 : v1, lds_dst + (j * 8 + wave) * 1024);
}
__device__ __forceinline__ void slab_frags(const unsigned char* lds, int base, int wave, int lane, u32x4 (&wf)[9]) {
  const int g = lane >> 4, q = (lane & 15) >> 2, p = lane & 3, hf = p >> 1;
  const int a0 = base + (hf * 288 + 8 * g + q) * 128 + ((wave ^ ((g & 1) * 4 + (q >> 1)) ^ (hf * 2)) << 4) + (p & 1) * 8, a1 = (a0 ^ 32) + 512;
#pragma unroll
  for (int s = 0; s < 9; ++s) {
    const u32x2 lo = trd(lds, a0 + s * 4096), hi = trd(lds, a1 + s * 4096);
    wf[s] = u32x4{lo[0], lo[1], hi[0], hi[1]};
  }
}
__device__ __forceinline__ void img_frags(const unsigned char* lds, int img, int lane, u32x4 (&af)[9]) {
#pragma unroll
  for (int s = 0; s < 9; ++s) af[s] = *(const u32x4*)(lds + img + (s * 64 + lane) * 16);
}
__device__ __forceinline__ f32x4 gemv9(const u32x4 (&wf)[9], const u32x4 (&af)[9]) {
  f32x4 acc[3];
  SCHED();
#pragma unroll
  for (int s = 0; s < 9; ++s) acc[s % 3] = mfma(wf[s], af[s], s < 3 ? f32x4{0.f, 0.f, 0.f, 0.f} : acc[s % 3]);
  return (acc[0] + acc[1]) + acc[2];
}
__device__ __forceinline__ int img_off(int b, int k8) {
  const int hf = k8 >= 36 ? 1 : 0, kk = k8 - hf * 36;
  return ((kk >> 2) * 64 + (kk & 3) * 16 + hf * 8 + b) * 16;
}
__device__ __forceinline__ u32 tag_x(u32x4 v, u32 e0, u32 e1) { return (v[2] ^ e0) | (v[3] ^ e1); }
__device__ __forceinline__ bool tag_ok(u32x4 v, u32 e0, u32 e1) { return tag_x(v, e0, e1) == 0u; }

constexpr int CODE_BYTES = 29696;

__global__ void __launch_bounds__(NT, 2) fused_kernel(const u16* __restrict__ x, const u16* __restrict__ kc, const u16* __restrict__ vc,
                                                      const u16* __restrict__ wq, const u16* __restrict__ wo, const u16* __restrict__ w_r,
                                                      const u16* __restrict__ wg, const u16* __restrict__ wu, const u16* __restrict__ wd,
                                                      const u16* __restrict__ c, const u16* __restrict__ w_mod, unsigned char* ws, u16* out) {
  __shared__ __attribute__((aligned(16))) unsigned char lds[LDS_BYTES];
  const int w = blockIdx.x, xcd = w & 7, idx = w >> 3;
  const int tid = threadIdx.x, lane = tid & 63, wave = __builtin_amdgcn_readfirstlane(tid >> 6);
  const int n = lane & 15, grp = lane >> 4;
  const rsrc_t rs = __builtin_amdgcn_make_buffer_rsrc(ws, 0, W_END, 0x00020000);
  const unsigned lds_base = (unsigned)(uintptr_t)(lds_t*)lds;
  const unsigned long long tA_ = __builtin_amdgcn_s_memrealtime();
  u32 lat_ = 0;
  if (blockIdx.x == 0 && wave == 0) {
    const char* pp_ = (const char*)vc + ((size_t)(7 * 16 + 15) * SK + (SK - WIN)) * 256;
    u32x4 d_; ald128(d_, pp_, 0); asm volatile("s_waitcnt vmcnt(0)" : "+v"(d_) :: "memory");
    _Pragma("nounroll") for (int i_ = 1; i_ <= 9; ++i_) {
      const unsigned long long a_ = __builtin_amdgcn_s_memtime(); asm volatile("s_waitcnt lgkmcnt(0)" ::: "memory");
      ald128(d_, pp_ + i_ * 4352, 0); asm volatile("s_waitcnt vmcnt(0)" : "+v"(d_) :: "memory");
      const unsigned long long b_ = __builtin_amdgcn_s_memtime(); asm volatile("s_waitcnt lgkmcnt(0)" ::: "memory");
      if (i_ > 1) lat_ += (u32)(b_ - a_);
    }
    lat_ = lat_ * 10 / 8; if (lat_ > 30000u) lat_ = 30000u;
  }
  while (__builtin_amdgcn_s_memrealtime() - tA_ < 1200ull) __builtin_amdgcn_s_sleep(1);
  const unsigned long long epoch = (llvm_dispatch_id() + 1ull) * 0x9E3779B97F4A7C15ull;
  const u32 e0 = (u32)epoch, e1 = (u32)(epoch >> 32);
  unsigned long long pc0; asm volatile("s_getpc_b64 %0" : "=s"(pc0));
  asm volatile("" : "+s"(vc), "+s"(w_r), "+s"(wu), "+s"(wd), "+s"(out), "+s"(x), "+s"(w_mod), "+s"(wg), "+s"(wq), "+s"(wo));

  const int mw = idx < 6 ? idx * 8 + xcd : -1;
  const int md = mw < 18 ? mw : -1;
  const int ml = mw >= 18 ? mw : mw >= 0 && mw < 6 ? 48 + mw : -1;
  const int lnb = mw >= 6 && mw < 14 ? mw - 6 : -1;
  const int qs = idx >= 6 && idx < 14 ? xcd * 8 + idx - 6 : -1;
  const int oj = idx >= 14 && idx < 23 ? idx - 14 : -1, op = xcd;
  const int gs = idx >= 23 ? xcd * 9 + idx - 23 : -1;

  const int s_att = w & 1, kvh = (w >> 1) & 15, b_att = w >> 5;
  {
    const size_t pairbase = ((size_t)(b_att * 16 + kvh) * SK + (SK - WIN)) * 256;
    const char* kp = (const char*)kc + pairbase;
    const char* vp = (const char*)vc + pairbase;
    const int wpart = (wave & 3) * 4096 + s_att * 16384 + (wave >> 2) * 262144;
    const int ko = wpart + (n >> 1) * 256 + (n & 1) * 64 + grp * 16;
    const int vo = wpart + n * 16 + grp * 512;
#define KLD(u) do { unsigned long long t_; asm volatile("s_mov_b64 %1, %3\n\tglobal_load_dwordx4 %0, %2, %1 offset:%4 nt" : "=v"(kb[u]), "=&s"(t_) : "v"(ko), "s"(kp + ((u) >> 2) * 32768), "i"((((u) >> 1) & 1) * 2048 + ((u) & 1) * 128) : "memory"); } while (0)
#define VLD(u) do { unsigned long long t_; asm volatile("s_mov_b64 %1, %3\n\tglobal_load_dwordx4 %0, %2, %1 offset:%4 nt" : "=v"(vb[u]), "=&s"(t_) : "v"(vo), "s"(vp + ((u) >> 2) * 32768), "i"((((u) >> 1) & 1) * 2048 + ((u) & 1) * 256) : "memory"); } while (0)
#define TIE8(a, o) asm volatile("" : "+v"(a[o]), "+v"(a[(o) + 1]), "+v"(a[(o) + 2]), "+v"(a[(o) + 3]), "+v"(a[(o) + 4]), "+v"(a[(o) + 5]), "+v"(a[(o) + 6]), "+v"(a[(o) + 7]))
    const int pr_ = gs >= 0 ? PR2 : mw >= 18 ? PR3 : PR;
    const u32 tp0 = (u32)__builtin_amdgcn_s_memrealtime() + (gs >= 0 || mw >= 18 ? 0 : 9 * PR / 4);
#define PACE(i) do { _Pragma("nounroll") while ((int)((u32)__builtin_amdgcn_s_memrealtime() - tp0) < (i) * pr_ / 4) __builtin_amdgcn_s_sleep(2); SCHED(); } while (0)
#define REP4(M, u, W) do { M(u); W; M((u) + 1); W; M((u) + 2); W; M((u) + 3); W; } while (0)
    static_assert(DK <= 7 && DV <= 7, "the hand-placed waits assume it");
    u32x4 kb[32], vb[32];
    const int pz = mw >= 18 ? mw - 18 : idx >= 14 ? w - 82 : -1;
#define POISON() do { if (pz >= 0) { _Pragma("unroll") for (int i = 0; i < 2; ++i) { const int g_ = pz * PZ + i * NT + tid; \
      if (i * NT + tid < PZ && g_ < W_MS / 16) st128(rs, g_ * 16, u32x4{0xffffffffu, 0xffffffffu, 0xffffffffu, 0xffffffffu}); } } SCHED(); } while (0)
#define PFLAG() do { if (pz >= 0 && tid == 448) st64(rs, W_FL + pz * 8, u32x2{e0, e1}); SCHED(); } while (0)
#define SILU_IMAGE(c0, c1) do { float f[8]; unpack8(c0, f); u32x4 o; \
        _Pragma("unroll") for (int j = 0; j < 4; ++j) o[j] = cvt2(silu(f[2 * j]), silu(f[2 * j + 1])); \
        const int b_ = tid / 72, ch_ = tid - b_ * 72; \
        *(u32x4*)(lds + L_IMG + img_off(b_, ch_)) = o; \
        if (wave == 0) { unpack8(c1, f); \
          _Pragma("unroll") for (int j = 0; j < 4; ++j) o[j] = cvt2(silu(f[2 * j]), silu(f[2 * j + 1])); \
          *(u32x4*)(lds + L_IMG + img_off(7, 8 + lane)) = o; } } while (0)
    if (md >= 0) {
      u32x4 c0, c1, x0, x1; u32x2 xc;
      __builtin_amdgcn_s_waitcnt(0x0f70);
      TSA(0);
      int ho = W_H + img_off(n & 7, (md >= 9 ? md - 9 : md) * 8 + wave) * 2 + (grp & 1) * 16;
      ald128(c0, c, tid * 16);
      ald128(c1, c, (512 + lane) * 16);
      ald128(x0, x, wave * 1152 + lane * 16);
      ald128(x1, x, wave * 1152 + 1024 + (lane & 7) * 16);
      ald64(xc, x, (n & 7) * 1152 + ((md >= 9 ? md - 9 : 0) * 64 + wave * 8 + (grp & 1) * 4) * 2);
      BAR();
      float* st = (float*)(lds + L_TILE + 1024);
      if (wave < 4) {
        slab_dma(w_mod + md * 64, NMOD * 2, lds_base + L_W, wave, lane);
        asm volatile("s_waitcnt vmcnt(9)" ::: "memory");
      } else asm volatile("s_waitcnt vmcnt(0)" ::: "memory");
      asm volatile("s_nop 0 ; both branches above cover V" : "+v"(c0), "+v"(c1), "+v"(x0), "+v"(x1), "+v"(xc) :: "memory");
      TSA(1);
      {
        SILU_IMAGE(c0, c1);
        float v[16]; unpack8(x0, v); unpack8(x1, v + 8);
        float mean, den; ln_row(v, lane < 8, mean, den);
        if (lane == 0) { st[wave] = mean; st[8 + wave] = den; }
      }
      TSA(2);
      if (wave >= 4) slab_dma(w_mod + md * 64, NMOD * 2, lds_base + L_W, wave, lane);
      TSA(3);
      lds_barrier();
      REP4(KLD, 0, );
      u32x4 wf[9], af[9];
      img_frags(lds, L_IMG, lane, af);
      const float mean = st[n & 7], rd = frcp(st[8 + (n & 7)]);
      float ln[4] = {rbf(rbf(lo16(xc[0]) - mean) * rd), rbf(rbf(hi16(xc[0]) - mean) * rd), rbf(rbf(lo16(xc[1]) - mean) * rd), rbf(rbf(hi16(xc[1]) - mean) * rd)};
#pragma unroll
      for (int s = 0; s < 9; ++s) asm volatile("" : "+v"(af[s]));
      asm volatile("" : "+v"(ln[0]), "+v"(ln[1]), "+v"(ln[2]), "+v"(ln[3]), "+v"(ho));
      WAITV(4); BAR(); SCHED();
      TSA(5);
      slab_frags(lds, L_W, wave, lane, wf);
      const f32x4 s = half_sum(gemv9(wf, af));
      STAMP(14); TSA(6);
      f32x4 r = s;
      if (md >= 9) {
#pragma unroll
        for (int j = 0; j < 4; ++j) r[j] = ln[j] * rbf(1.f + rbf(s[j]));
      }
      if (n < 8 && grp < 2) st128(rs, ho + (md >= 9 ? 18432 : 0), u32x4{cvt2(r[0], r[1]), cvt2(r[2], r[3]), e0, e1});
      SCHED();
      STAMP(15); TSA(7);
      BAR();
      KLD(4); KLD(5); WAITV(DK);
      KLD(6); WAITV(DK); KLD(7); WAITV(DK); KLD(8); WAITV(DK); KLD(9); WAITV(DK); KLD(10); WAITV(DK);
      KLD(11); WAITV(DK); REP4(KLD, 12, WAITV(DK)); REP4(KLD, 16, WAITV(DK));
      REP4(KLD, 20, WAITV(DK)); KLD(24); WAITV(DK); KLD(25);
    } else {
      constexpr int L_LAND = 98304;
      const unsigned land = lds_base + L_LAND + wave * 6144;
      const unsigned char* lp = lds + L_LAND + wave * 6144 + lane * 16;
      const int sl_ = (3 - (int)((uintptr_t)wq >> 7)) & 7;
      const bool slowq = qs >= 0 && (qs & 7) == sl_;
      if (gs >= 0 && wave == 0) dma_keep((const char*)wq + (size_t)(idx - 23) * 64 * 8192 + xcd * 1024 + sl_ * 128, lane * 8192, land);
      POISON();
      if (oj >= 0 && idx < 17 && wave * 8192 < CODE_BYTES)
        dma_keep((const void*)pc0, min(wave * 8192 + lane * 128, CODE_BYTES - 128), lds_base + 107520 + wave * 256);
      const u32 v0 = (lane >> 3) * 1152 + (((lane & 7) ^ ((lane >> 4) & 1)) << 4);
      const char* gb = (const char*)(wo + (size_t)(op * 512 + wave * 64) * D + oj * 64);
      if (ml >= 0) { dma_sc(c, tid * 16, land); dma_sc(c, (512 + lane) * 16, land + 1024); }
#define WK(j) do { if (oj >= 0) { if ((j) < 8) dma(gb + (j) * 8 * 1152, v0 ^ (((j) & 1) * 32 + ((j) >> 2) * 64), lds_base + L_W + (wave * 8 + (j)) * 1024); } \
                   else if (slowq) { if ((j) == 8) slab_dma(wq + qs * 64, NQ * 2, lds_base + L_W, wave, lane); } \
                   else if (qs >= 0) slab_dma<j, (j) + 1>(wq + qs * 64, NQ * 2, lds_base + L_W, wave, lane); \
                   KLD(j); WAITV(DK); } while (0)
      WK(0); WK(1); WK(2); WK(3);
      WK(4); WK(5);
      WK(6); WK(7);
      if (ml >= 0) { const u32x4 c0 = *(const u32x4*)lp, c1 = *(const u32x4*)(lp + 1024); SILU_IMAGE(c0, c1); }
      BAR();
      PFLAG();
      WK(8); KLD(9); WAITV(DK); KLD(10); WAITV(DK); KLD(11); WAITV(DK);
      PACE(12); REP4(KLD, 12, WAITV(DK));
      const int hg0 = (wave * 144 + lane) * 16, hg2 = (wave * 144 + 128 + n) * 16;
#define HREQ() do { dma_sc(ws + W_H, hg0, land); dma_sc(ws + W_H + 18432, hg0, land + 1024); dma_sc(ws + W_H + 1024, hg0, land + 2048); \
                    dma_sc(ws + W_H + 18432 + 1024, hg0, land + 3072); dma_sc(ws + W_H, hg2, land + 4096); dma_sc(ws + W_H + 18432, hg2, land + 5120); } while (0)
#define HGET() do { _Pragma("unroll") for (int i = 0; i < 6; ++i) hq[i] = *(const u32x4*)(lp + i * 1024); } while (0)
#define HQOK() __all((tag_x(hq[0], e0, e1) | tag_x(hq[1], e0, e1) | tag_x(hq[2], e0, e1) | tag_x(hq[3], e0, e1) | tag_x(hq[4], e0, e1) | tag_x(hq[5], e0, e1)) == 0u)
#define HOK() __all((tag_x(hp[0], e0, e1) | tag_x(hp[1], e0, e1) | tag_x(hp[2], e0, e1)) == 0u)
      TSA(4);
      if (qs >= 0) HREQ();
      PACE(16); REP4(KLD, 16, WAITV(DK));
      if (qs >= 0) WAITV(4);
      TSA(5);
      PACE(20); KLD(20); KLD(21);
      if (qs >= 0) {
        u32x4 hq[6]; HGET();
        _Pragma("nounroll") for (int sp = 0; sp < SPIN_CAP && !HQOK(); ++sp) { nap(16); HREQ(); WAITV(0); HGET(); }
        STAMP(14); TSA(6);
#define HSUM(a, b) cvt2(lo16(a) + lo16(b), hi16(a) + hi16(b))
        *(u32x2*)(lds + L_IMG + hg0 / 2) = u32x2{HSUM(hq[0][0], hq[1][0]), HSUM(hq[0][1], hq[1][1])};
        *(u32x2*)(lds + L_IMG + 512 + hg0 / 2) = u32x2{HSUM(hq[2][0], hq[3][0]), HSUM(hq[2][1], hq[3][1])};
        if (lane < 16) *(u32x2*)(lds + L_IMG + hg2 / 2) = u32x2{HSUM(hq[4][0], hq[5][0]), HSUM(hq[4][1], hq[5][1])};
      } else WAITV(DK);
      KLD(22); KLD(23);
      if (qs >= 0) {
        u32x4 wf[9], af[9];
        lds_barrier();
        slab_frags(lds, L_W, wave, lane, wf);
        img_frags(lds, L_IMG, lane, af);
        const f32x4 s = half_sum(gemv9(wf, af));
        if (n < 8 && grp < 2) *(u32x2*)(lds + L_TILE + n * 128 + wave * 16 + grp * 8) = u32x2{cvt2(s[0], s[1]), cvt2(s[2], s[3])};
      } else WAITV(DK);
      PACE(24); KLD(24); KLD(25);
      if (qs >= 0) {
        lds_barrier();
        if (wave < 2) {
          const u32x2 t2 = *(const u32x2*)(lds + L_TILE + tid * 8);
          st128(rs, W_Q + ((tid >> 4) * 1024 + qs * 16 + n) * 16, u32x4{t2[0], t2[1], e0, e1});
        }
        STAMP(15); TSA(7);
      }
      SCHED();
    }
    STAMP(3); TSB(0);
    STAMP(2);
    const unsigned char* qg = ws + W_Q + (b_att * 1024 + kvh * 64) * 16;
    KLD(26); WAITV(DK); KLD(27); WAITV(DK); PACE(28); KLD(28); WAITV(DK); KLD(29); WAITV(DK); PACE(30); KLD(30); WAITV(DK); KLD(31); WAITV(DK);
    TSB(1);
    PACE(32); VLD(0); WAITV(DK); VLD(1); WAITV(DK); PACE(34); VLD(2); WAITV(DK); VLD(3); WAITV(DK);
    if (wave == 1) dma_sc(qg, lane * 16, lds_base + L_B + 1024);
    PACE(36); VLD(4); WAITV(DK); VLD(5); WAITV(DK); PACE(38); VLD(6); WAITV(DK); VLD(7); WAITV(DK);
    if (wave == 1) dma_sc(qg, lane * 16, lds_base + L_B + 1024);
    PACE(40); VLD(8); WAITV(DV); VLD(9); WAITV(DV); PACE(42); VLD(10); WAITV(DV); VLD(11); WAITV(DV);
    TIE8(kb, 0); TIE8(kb, 8); TIE8(kb, 16); TIE8(kb, 24);
    PACE(44); VLD(12); VLD(13);
    u32x4 qf[2];
    {
      const u32x4* ql = (const u32x4*)(lds + L_B + 1024 + (((n >> 1) & 1) * 32 + (n & 1) * 8 + grp * 2) * 16);
      u32x4 g[4];
      _Pragma("nounroll") for (int sp = 0; sp < SPIN_CAP; ++sp) {
        g[0] = ql[0]; g[1] = ql[1]; g[2] = ql[16]; g[3] = ql[17];
        if (__all((tag_x(g[0], e0, e1) | tag_x(g[1], e0, e1) | tag_x(g[2], e0, e1) | tag_x(g[3], e0, e1)) == 0u)) break;
        if (wave == 1) { dma_sc(qg, lane * 16, lds_base + L_B + 1024); WAITV(0); } else nap(4);
      }
      asm volatile("" ::: "memory");
      g[0] = ql[0]; g[1] = ql[1]; g[2] = ql[16]; g[3] = ql[17];
      qf[0] = n < 4 ? u32x4{g[0][0], g[0][1], g[1][0], g[1][1]} : u32x4{0, 0, 0, 0};
      qf[1] = n < 4 ? u32x4{g[2][0], g[2][1], g[3][0], g[3][1]} : u32x4{0, 0, 0, 0};
    }
    STAMP(4); TSB(2);
    constexpr float SCALE = 0.088388347648318440f;
    f32x2 sc[16];
    float mx = -INFINITY;
#pragma unroll
    for (int T = 0; T < 16; ++T) {
      f32x4 acc = mfma(kb[T * 2], qf[0], f32x4{0.f, 0.f, 0.f, 0.f});
      acc = mfma(kb[T * 2 + 1], qf[1], acc);
      sc[T] = f32x2{acc[0] + dppf<0xB1>(acc[1]), acc[2] + dppf<0xB1>(acc[3])};
      if (T == 7 || T == 15) { SCHED(); if (T == 7) VLD(14); else VLD(15); SCHED(); }
    }
#pragma unroll
    for (int T = 0; T < 16; ++T)
#pragma unroll
      for (int j = 0; j < 2; ++j) { const float v = rbf(rbf(sc[T][j]) * SCALE); sc[T][j] = v; mx = fmaxf(mx, v); }
    SCHED(); VLD(16); SCHED();
    mx = fmaxf(mx, __shfl_xor(mx, 16));
    mx = fmaxf(mx, __shfl_xor(mx, 32));
    float ls = 0.f;
#pragma unroll
    for (int T = 0; T < 16; ++T)
#pragma unroll
      for (int j = 0; j < 2; ++j) { sc[T][j] = __expf(sc[T][j] - mx); ls += sc[T][j]; }
    SCHED(); VLD(17); SCHED();
    ls += __shfl_xor(ls, 16);
    ls += __shfl_xor(ls, 32);
    float* stat = (float*)(lds + L_B);
    if (lane == 0 || lane == 2) *(f32x2*)(stat + (wave * 2 + (lane >> 1)) * 2) = f32x2{mx, ls};
    SCHED(); VLD(18); VLD(19); SCHED();
    TSB(3);
    lds_barrier();
    TSB(4);
    float M = -INFINITY, L = 0.f;
    {
      f32x2 t[8];
#pragma unroll
      for (int q = 0; q < 8; ++q) { t[q] = *(const f32x2*)(stat + (q * 2 + ((n >> 1) & 1)) * 2); M = fmaxf(M, t[q].x); }
#pragma unroll
      for (int q = 0; q < 8; ++q) L = fmaf(__expf(t[q].x - M), t[q].y, L);
    }
    if (tid < 8) st128(rs, W_ST + (w * 8 + lane) * 16, u32x4{__float_as_uint(M), __float_as_uint(L), e0, e1});
    SCHED();
#define VTR(G) do { u32x4 f_[8]; _Pragma("unroll") for (int t = 0; t < 8; ++t) _Pragma("unroll") for (int j = 0; j < 4; ++j) \
      f_[t][j] = (t & 1) ? phi(vb[(G) * 8 + 2 * j][t >> 1], vb[(G) * 8 + 2 * j + 1][t >> 1]) : plo(vb[(G) * 8 + 2 * j][t >> 1], vb[(G) * 8 + 2 * j + 1][t >> 1]); \
      _Pragma("unroll") for (int t = 0; t < 8; ++t) { vb[(G) * 8 + t] = f_[t]; asm volatile("" : "+v"(vb[(G) * 8 + t])); } SCHED(); } while (0)
    WAITV(12);
    TIE8(vb, 0);
    VTR(0);
    PACE(52); VLD(20); WAITV(5);
    TIE8(vb, 8);
    VTR(1);
    VLD(21); WAITV(DV);
    const int po = W_ST + ((w ^ 1) * 8 + (n & 2)) * 16;
    int l1 = lane; asm volatile("" : "+v"(l1));
    const int fl = 1024 + min(l1, NPZ / 2 - 65) * 16;
    if (wave == 1) { dma_sc(ws + W_FL, l1 * 16, lds_base + L_B + 2048); dma_sc(ws + W_FL, fl, lds_base + L_B + 3072); }
    PACE(54);
    VLD(22); WAITV(DV); VLD(23); WAITV(DV);
    u32x4 ps;
    ald128sc(ps, ws, po);
    PACE(56);
    VLD(24); WAITV(DV); VLD(25); WAITV(DV);
    PACE(58);
    VLD(26); WAITV(DV); VLD(27); WAITV(DV);
    VLD(28); VLD(29); WAITV(6);
    asm volatile("" : "+v"(ps));
    TIE8(vb, 16);
    VLD(30); VLD(31);
    VTR(2);
    if (!__all(tag_ok(ps, e0, e1))) {
      int sp = 0;
      _Pragma("nounroll") do { nap(1); ps = ld128(rs, po); } while (!__all(tag_ok(ps, e0, e1)) && ++sp < SPIN_CAP);
    }
#define FOK(a, b) __all(((a[0] ^ e0) | (a[1] ^ e1) | tag_x(a, e0, e1) | (b[0] ^ e0) | (b[1] ^ e1) | tag_x(b, e0, e1)) == 0u)
    if (wave == 1 && !FOK((*(const u32x4*)(lds + L_B + 2048 + l1 * 16)), (*(const u32x4*)(lds + L_B + 3072 + l1 * 16)))) {
      _Pragma("nounroll") for (int sp = 0; sp < SPIN_CAP; ++sp) {
        nap(1);
        const u32x4 a = ld128(rs, W_FL + l1 * 16), b = ld128(rs, W_FL + fl);
        if (FOK(a, b)) break;
      }
    }
    float fac;
    {
      const float M2 = __uint_as_float(ps[0]), L2 = __uint_as_float(ps[1]);
      const float Mg = fmaxf(M, M2);
      const float Lg = __expf(M - Mg) * L + __expf(M2 - Mg) * L2;
      fac = __expf(mx - Mg) / Lg;
    }
    STAMP(1); TSB(5);
    u32x4 pf[4];
#pragma unroll
    for (int G = 0; G < 4; ++G) {
#pragma unroll
      for (int j = 0; j < 4; ++j) pf[G][j] = cvt2(sc[4 * G + j][0] * fac, sc[4 * G + j][1] * fac);
    }
    f32x4 oa[8];
#pragma unroll
    for (int t = 0; t < 8; ++t) oa[t] = f32x4{0.f, 0.f, 0.f, 0.f};
    SCHED();
#pragma unroll
    for (int G = 0; G < 2; ++G)
#pragma unroll
      for (int t = 0; t < 8; ++t) oa[t] = mfma(pf[G], vb[G * 8 + t], oa[t]);
    SCHED();
#pragma unroll
    for (int t = 0; t < 8; ++t) oa[t] = mfma(pf[2], vb[16 + t], oa[t]);
    SCHED();
    int t2_ = threadIdx.x; asm volatile("" : "+v"(t2_));
    const int tid = t2_, lane = tid & 63, n = lane & 15, grp = lane >> 4;
    int oo = W_O + ((kvh * 8 + b_att) * 2 + s_att) * 1024 + ((lane >> 4) * 16 + (lane & 1) * 8 + ((lane >> 1) & 7)) * 16;
    int mw_ = L_C + wave * 1024 + n * 32, mr_ = L_C + lane * 16;
    asm volatile("" : "+v"(oo), "+v"(mw_), "+v"(mr_), "+v"(pf[3]));
    const bool late = gs >= 0 || mw >= 18;
    if (late) {
      BAR();
      slab_dma<0, 4>(gs >= 0 ? wg + gs * 64 : w_mod + ml * 64, gs >= 0 ? FF * 2 : NMOD * 2, lds_base + L_W + (gs >= 0 ? 73728 : 0), wave, lane);
      WAITV(6);
    } else WAITV(2);
    u32x4 vf[8];
    asm volatile("s_nop 0 ; both branches above cover V up to the last two" : "+v"(vb[24]), "+v"(vb[25]), "+v"(vb[26]), "+v"(vb[27]), "+v"(vb[28]), "+v"(vb[29]));
#pragma unroll
    for (int t = 0; t < 8; ++t)
#pragma unroll
      for (int j = 0; j < 3; ++j) vf[t][j] = (t & 1) ? phi(vb[24 + 2 * j][t >> 1], vb[25 + 2 * j][t >> 1]) : plo(vb[24 + 2 * j][t >> 1], vb[25 + 2 * j][t >> 1]);
#pragma unroll
    for (int t = 0; t < 8; ++t) asm volatile("" : "+v"(vf[t][0]), "+v"(vf[t][1]), "+v"(vf[t][2]));
    if (late) WAITV(4); else WAITV(0);
    asm volatile("s_nop 0 ; both branches above cover V" : "+v"(vb[30]), "+v"(vb[31]));
#pragma unroll
    for (int t = 0; t < 8; ++t) {
      vf[t][3] = (t & 1) ? phi(vb[30][t >> 1], vb[31][t >> 1]) : plo(vb[30][t >> 1], vb[31][t >> 1]);
      oa[t] = mfma(pf[3], vf[t], oa[t]);
    }
    STAMP(5); TSB(6);
    if (grp == 0) {
#pragma unroll
      for (int g = 0; g < 2; ++g) {
        *(f32x4*)(lds + mw_ + g * 512) = f32x4{oa[0][2 * g], oa[1][2 * g], oa[2][2 * g], oa[3][2 * g]};
        *(f32x4*)(lds + mw_ + g * 512 + 16) = f32x4{oa[4][2 * g], oa[5][2 * g], oa[6][2 * g], oa[7][2 * g]};
      }
    }
    lds_barrier();
    if (wave == 0) {
      f32x4 m[8];
#pragma unroll
      for (int q = 0; q < 8; ++q) m[q] = *(const f32x4*)(lds + mr_ + q * 1024);
      SCHED();
      const f32x4 o = ((m[0] + m[1]) + (m[2] + m[3])) + ((m[4] + m[5]) + (m[6] + m[7]));
      st128(rs, oo, u32x4{sanf(o[0]), sanf(o[1]), sanf(o[2]), sanf(o[3])});
    }
    STAMP(6); TSB(7); SCHED();
  if (gs >= 0) {
    constexpr int L_H2 = L_W + 73728;
    constexpr int L_ACT = L_C + 4608;
    const int b = wave;
    BAR(); SCHED();
    TSC(0);
    u32x4 bg[9], bu[9], wdf[2][5];
    {
      u32x2 rd[2][8];
      const char* db = (const char*)(wd + (size_t)gs * 64 * D + wave * 64);
      const u32 dv = grp * 9216 + n * 8;
#pragma unroll
      for (int ks = 0; ks < 2; ++ks)
#pragma unroll
        for (int i = 0; i < 8; ++i) ald64nt(rd[ks][i], db + (ks * 32 + i) * 1152, dv);
      {
        const int r = lane >> 3;
        dma((const char*)(wd + (size_t)(gs * 64 + wave * 8) * D + 512), r * 1152 + (((lane & 7) ^ (r & 2) ^ ((wave & 1) * 4)) << 4), lds_base + L_B + wave * 1024);
      }
      BAR();
      slab_dma<4, 9>(wg + gs * 64, FF * 2, lds_base + L_W + 73728, wave, lane);
      BAR();
      slab_dma(wu + gs * 64, FF * 2, lds_base + L_W, wave, lane);
      asm volatile("s_waitcnt vmcnt(15)" : "+v"(rd[0][0]), "+v"(rd[0][1]), "+v"(rd[0][2]), "+v"(rd[0][3]), "+v"(rd[0][4]), "+v"(rd[0][5]), "+v"(rd[0][6]), "+v"(rd[0][7]),
                   "+v"(rd[1][0]), "+v"(rd[1][1]), "+v"(rd[1][2]), "+v"(rd[1][3]), "+v"(rd[1][4]), "+v"(rd[1][5]), "+v"(rd[1][6]), "+v"(rd[1][7]) :: "memory");
#pragma unroll
      for (int ks = 0; ks < 2; ++ks)
#pragma unroll
        for (int j = 0; j < 4; ++j) {
          wdf[ks][0][j] = plo(rd[ks][2 * j][0], rd[ks][2 * j + 1][0]); wdf[ks][1][j] = phi(rd[ks][2 * j][0], rd[ks][2 * j + 1][0]);
          wdf[ks][2][j] = plo(rd[ks][2 * j][1], rd[ks][2 * j + 1][1]); wdf[ks][3][j] = phi(rd[ks][2 * j][1], rd[ks][2 * j + 1][1]);
        }
#pragma unroll
      for (int t = 0; t < 4; ++t) asm volatile("" : "+v"(wdf[0][t]), "+v"(wdf[1][t]));
      asm volatile("s_waitcnt vmcnt(9)\n\ts_barrier" ::: "memory");
      slab_frags(lds, L_W + 73728, wave, lane, bg);
      const int q = n >> 2, p = lane & 3;
      const int a0 = L_B + (8 * grp + q) * 128 + ((((wave & 3) * 2 + (p >> 1)) ^ (q & 2) ^ ((grp & 1) * 4)) << 4) + (p & 1) * 8;
#pragma unroll
      for (int ks = 0; ks < 2; ++ks) {
        const u32x2 lo = trd(lds, a0 + ks * 4096), hi = trd(lds, a0 + ks * 4096 + 512);
        wdf[ks][4] = u32x4{lo[0], lo[1], hi[0], hi[1]};
      }
    }
    TSC(1);
    int ia[3], ra[4], yo[3], fo[7];
#pragma unroll
    for (int i = 0; i < 3; ++i) {
      const int k = i < 2 ? i * 256 + lane * 4 : 512 + n * 4, hf = k >= 288 ? 1 : 0, kk = k - hf * 288, s = kk >> 5, g = (kk >> 3) & 3;
      ia[i] = L_H2 + (s * 64 + g * 16 + ((hf * 8 + b) ^ ((s & 3) * 4 + g))) * 16 + (kk & 7) * 2;
    }
#pragma unroll
    for (int i = 0; i < 4; ++i) ra[i] = L_H2 + (grp * 16 + (n ^ (i * 4 + grp))) * 16;
    int tw = L_ACT + n * 144 + wave * 16 + (grp & 1) * 8 + (grp >> 1) * 4, tr_ = L_ACT + (n & 7) * 144 + grp * 16;
    yo[0] = W_Y + (((wave * 8 + grp * 2) * NG + gs) * 16 + n) * 16; yo[1] = yo[0] + NG * 256;
    yo[2] = W_Y + ((((64 + wave * 2 + (grp >> 1)) * NG + gs) * 2 + (grp & 1)) * 8 + n) * 16;
    {
      const int b_ = lane >> 3, cc = lane & 7, col = gs * 8 + cc, hf = col >= 288 ? 1 : 0, kk = col - hf * 288, s_ = kk >> 5, g = (kk >> 3) & 3;
      fo[0] = W_M + ((3 * 8 + b_) * 144 + gs * 2 + (cc >> 2)) * 16;
      fo[1] = W_X1 + (b_ * 152 + gs * 2 + (cc >> 2)) * 16; fo[2] = W_X1 + (b_ * 152 + 144) * 16;
      fo[3] = W_Y + (gs * 1152 + tid) * 16;
      fo[4] = L_H2 + (s_ * 64 + g * 16 + ((hf * 8 + b_) ^ ((s_ & 3) * 4 + g))) * 16 + cc * 2;
      fo[5] = L_C + (((cc >> 2) * 8 + b_) * 4 + (cc & 3)) * 4;
      fo[6] = (b_ * D + col) * 2;
    }
    asm volatile("" : "+v"(ia[0]), "+v"(ia[1]), "+v"(ia[2]), "+v"(ra[0]), "+v"(ra[1]), "+v"(ra[2]), "+v"(ra[3]), "+v"(tw), "+v"(tr_), "+v"(yo[0]), "+v"(yo[1]), "+v"(yo[2]),
                 "+v"(fo[0]), "+v"(fo[1]), "+v"(fo[2]), "+v"(fo[3]), "+v"(fo[4]), "+v"(fo[5]), "+v"(fo[6]));
    asm volatile("s_waitcnt vmcnt(0) lgkmcnt(0)\n\ts_barrier" ::: "memory");
    {
      u32x4 hp[3];
      const int h0 = W_H2 + (b * 144 + lane) * 16, h2o = W_H2 + (b * 144 + 128 + n) * 16;
      hp[0] = ld128(rs, h0); hp[1] = ld128(rs, h0 + 1024); hp[2] = ld128(rs, h2o);
      slab_frags(lds, L_W, wave, lane, bu);
      STAMP(13);
#pragma unroll
      for (int s = 0; s < 9; ++s) asm volatile("" : "+v"(bg[s]), "+v"(bu[s]));
      asm volatile("" : "+v"(wdf[0][4]), "+v"(wdf[1][4]));
      TSC(3);
      if (!HOK()) {
        int sp = 0;
        _Pragma("nounroll") do { nap(2); hp[0] = ld128(rs, h0); hp[1] = ld128(rs, h0 + 1024); hp[2] = ld128(rs, h2o); } while (!HOK() && ++sp < SPIN_CAP);
      }
      STAMP(9); TSC(4);
      *(u32x2*)(lds + ia[0]) = u32x2{hp[0][0], hp[0][1]};
      *(u32x2*)(lds + ia[1]) = u32x2{hp[1][0], hp[1][1]};
      if (lane < 16) *(u32x2*)(lds + ia[2]) = u32x2{hp[2][0], hp[2][1]};
      TSC(5);
      lds_barrier();
      u32x4 af[9];
#pragma unroll
      for (int s = 0; s < 9; ++s) af[s] = *(const u32x4*)(lds + ra[s & 3] + s * 1024);
      SCHED();
      f32x4 ag[2], au[2];
#pragma unroll
      for (int s = 0; s < 9; ++s) {
        ag[s & 1] = mfma(bg[s], af[s], s < 2 ? f32x4{0.f, 0.f, 0.f, 0.f} : ag[s & 1]);
        au[s & 1] = mfma(bu[s], af[s], s < 2 ? f32x4{0.f, 0.f, 0.f, 0.f} : au[s & 1]);
      }
      const f32x2 hg = half_sum2(ag[0] + ag[1]), hu = half_sum2(au[0] + au[1]);
      const u32 act = cvt2(rbf(silu(rbf(hg.x))) * rbf(hu.x), rbf(silu(rbf(hg.y))) * rbf(hu.y));
      if (n < 8) *(u32*)(lds + tw) = act;
    }
    lds_barrier();
    STAMP(10); TSC(6);
    {
      u32x4 af[2];
#pragma unroll
      for (int ks = 0; ks < 2; ++ks) af[ks] = *(const u32x4*)(lds + tr_ + ks * 64);
      f32x4 d[5];
#pragma unroll
      for (int t = 0; t < 5; ++t) d[t] = mfma(wdf[1][t], af[1], mfma(wdf[0][t], af[0], f32x4{0.f, 0.f, 0.f, 0.f}));
      if (n < 8) {
#pragma unroll
        for (int j = 0; j < 4; ++j)
          st128(rs, yo[j >> 1] + (j & 1) * 128, u32x4{sanf(d[0][j]), sanf(d[1][j]), sanf(d[2][j]), sanf(d[3][j])});
        if (wave < 4)
          st128(rs, yo[2], u32x4{sanf(d[4][0]), sanf(d[4][1]), sanf(d[4][2]), sanf(d[4][3])});
      }
    }
    {
      float gbv = 0.f, x1v = 0.f, h2 = 0.f, rsel = 0.f;
      if (wave == 7) {
        u32x4 gq, xq, rq;
        _Pragma("nounroll") for (int sp = 0; sp < SPIN_CAP; ++sp) {
          gq = ld128(rs, fo[0]); xq = ld128(rs, fo[1]); rq = ld128(rs, fo[2]);
          if (__all((tag_x(gq, e0, e1) | tag_x(xq, e0, e1) | tag_x(rq, e0, e1)) == 0u)) break;
          nap(1);
        }
        const u32 gw = (lane & 2) ? gq[1] : gq[0], xw = (lane & 2) ? xq[1] : xq[0];
        gbv = (lane & 1) ? hi16(gw) : lo16(gw); x1v = (lane & 1) ? hi16(xw) : lo16(xw);
        h2 = b2f(*(const u16*)(lds + fo[4])); rsel = __uint_as_float(rq[0]);
        asm volatile("" : "+v"(gbv), "+v"(x1v), "+v"(h2), "+v"(rsel));
      }
      WAITV(0);
      u32x4 y[3] = {};
      _Pragma("nounroll") for (int sp = 0; sp < SPIN_CAP; ++sp) {
        y[0] = ld128(rs, fo[3]); y[1] = ld128(rs, fo[3] + 8192);
        if (wave < 2) y[2] = ld128(rs, fo[3] + 16384);
        if (__all(mxu(mxu(mx4(y[0]), mx4(y[1])), mx4(y[2])) != 0xffffffffu)) break;
        nap(1);
      }
      STAMP(11); TSC(7);
      f32x4 s;
#pragma unroll
      for (int k = 0; k < 4; ++k) s[k] = xsum32(xsum16((__uint_as_float(y[0][k]) + __uint_as_float(y[1][k])) + __uint_as_float(y[2][k])));
      if (lane < 16) *(f32x4*)(lds + L_C + (wave * 16 + lane) * 16) = s;
      lds_barrier();
      if (wave == 7) {
        float t[8];
#pragma unroll
        for (int ww = 0; ww < 8; ++ww) t[ww] = *(const float*)(lds + fo[5] + ww * 256);
        SCHED();
        const float y2 = rbf(rbf(((t[0] + t[1]) + (t[2] + t[3])) + ((t[4] + t[5]) + (t[6] + t[7]))) * rsel);
        const float res = x1v + rbf(gbv * rbf(h2 + y2));
        __builtin_amdgcn_raw_buffer_store_b16((u16)(cvt2(res, 0.f) & 0xffffu), __builtin_amdgcn_make_buffer_rsrc(out, 0, 8 * D * 2, 0x00020000), fo[6], 0, 0);
      }
      STAMP(12);
    }
  }
  {
  int t3_ = threadIdx.x; asm volatile("" : "+v"(t3_));
  const int tid = t3_, lane = tid & 63, n = lane & 15, grp = lane >> 4;
  if (qs >= 0) {
    const u16* mat = (wave & 1) ? wg : wu;
    const int g0 = ((3 - (int)((uintptr_t)mat >> 7) - xcd * 9) & 7) + (wave >= 4 ? 8 : 0);
    const int r = (idx - 6) * 72 + ((wave >> 1) & 1) * 36 + min(lane, 35);
    if (g0 < 9) dma_keep(mat, r * 9216 + (xcd * 9 + g0) * 128, lds_base + L_W + wave * 256);
  }
  if (ml >= 0) {
    BAR();
    if (mw < 18) slab_dma<0, 4>(w_mod + ml * 64, NMOD * 2, lds_base + L_W, wave, lane);
    slab_dma<4, 9>(w_mod + ml * 64, NMOD * 2, lds_base + L_W, wave, lane);
    u32x4 wf[9], af[9];
    img_frags(lds, L_IMG, lane, af);
    asm volatile("s_waitcnt vmcnt(0)\n\ts_barrier" ::: "memory");
    slab_frags(lds, L_W, wave, lane, wf);
    const f32x4 s = half_sum(gemv9(wf, af));
    if (n < 8 && grp < 2) *(u32x2*)(lds + L_TILE + n * 128 + wave * 16 + grp * 8) = u32x2{cvt2(s[0], s[1]), cvt2(s[2], s[3])};
    lds_barrier();
    if (wave < 2) {
      const u32x2 t2 = *(const u32x2*)(lds + L_TILE + tid * 8);
      const int ch = (ml - 18) / 9, sl = ml - 18 - ch * 9;
      st128(rs, W_M + ((ch * 8 + (tid >> 4)) * 144 + sl * 16 + n) * 16, u32x4{t2[0], t2[1], e0, e1});
    }
  }

  if (lnb >= 0 && wave < 3) {
    const int b = lnb, g = tid < 144 ? tid : 143;
    const bool on = tid < 144;
    float* ex = (float*)(lds + L_TILE);
    const u32x2 xa = __builtin_amdgcn_raw_buffer_load_b64(__builtin_amdgcn_make_buffer_rsrc((void*)x, 0, 8 * D * 2, 0x00020000), (b * D + g * 4) * 2, 0, 0);
    const u32x2 wra = __builtin_amdgcn_raw_buffer_load_b64(__builtin_amdgcn_make_buffer_rsrc((void*)w_r, 0, D * 2, 0x00020000), g * 8, 0, 0);
    u32x4 mm[3];
    _Pragma("nounroll") for (int sp = 0; sp < SPIN_CAP; ++sp) {
#pragma unroll
      for (int k = 0; k < 3; ++k) mm[k] = ld128(rs, W_M + ((k * 8 + b) * 144 + g) * 16);
      if (__all((tag_x(mm[0], e0, e1) | tag_x(mm[1], e0, e1) | tag_x(mm[2], e0, e1)) == 0u)) break;
      nap(4);
    }
    f32x2 xv[2], ga[2], sb[2], cb1[2], wr[2];
#pragma unroll
    for (int i = 0; i < 2; ++i) {
      xv[i] = up2(xa[i]); wr[i] = up2(wra[i]); ga[i] = up2(mm[0][i]); sb[i] = up2(mm[1][i]); cb1[i] = rbf2(f32x2{1.f, 1.f} + up2(mm[2][i]));
      asm volatile("" : "+v"(xv[i]), "+v"(ga[i]), "+v"(sb[i]), "+v"(cb1[i]), "+v"(wr[i]));
    }
    int ho = W_H2 + (b * 144 + g) * 16, xo = W_X1 + (b * 152 + g) * 16;
    asm volatile("" : "+v"(ho), "+v"(xo));
    TSC(0);
    u32x4 xp[8];
    _Pragma("nounroll") for (int sp = 0; sp < SPIN_CAP; ++sp) {
#pragma unroll
      for (int p = 0; p < 8; ++p) xp[p] = ld128(rs, W_XP + (p * 8 + b) * 2304 + g * 16);
      if (__all(mxu(mxu(mxu(mx4(xp[0]), mx4(xp[1])), mxu(mx4(xp[2]), mx4(xp[3]))), mxu(mxu(mx4(xp[4]), mx4(xp[5])), mxu(mx4(xp[6]), mx4(xp[7])))) != 0xffffffffu)) break;
      nap(1);
    }
    TSC(1);
    f32x2 v[2];
#pragma unroll
    for (int i = 0; i < 2; ++i) {
      f32x2 pp[8];
#pragma unroll
      for (int p = 0; p < 8; ++p) pp[p] = f32x2{__uint_as_float(xp[p][2 * i]), __uint_as_float(xp[p][2 * i + 1])};
      v[i] = rbf2(xv[i] + rbf2(ga[i] * rbf2(((pp[0] + pp[1]) + (pp[2] + pp[3])) + ((pp[4] + pp[5]) + (pp[6] + pp[7])))));
    }
    const f32x2 s2 = v[0] + v[1];
    const float mw = wsum(on ? s2.x + s2.y : 0.f) * (wave < 2 ? 1.f / 256 : 1.f / 64);
    const f32x2 t0 = v[0] - f32x2{mw, mw}, t1 = v[1] - f32x2{mw, mw}, q2 = t0 * t0 + t1 * t1;
    const float m2 = wsum(on ? q2.x + q2.y : 0.f);
    if (lane == 0) *(f32x2*)(ex + wave * 2) = f32x2{mw, m2};
    lds_barrier();
    const f32x4 e01 = *(const f32x4*)ex; const f32x2 e2 = *(const f32x2*)(ex + 4);
    const float mu = (e01[0] + e01[2]) * (256.f / D) + e2.x * (64.f / D);
    const float d0 = e01[0] - mu, d1 = e01[2] - mu, d2 = e2.x - mu;
    const float var = rbf(((e01[1] + e01[3]) + e2.y + 256.f * (d0 * d0 + d1 * d1) + 64.f * d2 * d2) * (1.f / D));
    const float mean = rbf(mu), rden = frcp(rbf(sqrtf(rbf(var + 1e-5f))));
    u32 hw[2];
#pragma unroll
    for (int i = 0; i < 2; ++i) {
      const f32x2 t = rbf2(rbf2(rbf2(v[i] - f32x2{mean, mean}) * f32x2{rden, rden}) * cb1[i]) + sb[i];
      hw[i] = cvt2(t.x, t.y);
    }
    if (on) {
      st128(rs, ho, u32x4{hw[0], hw[1], e0, e1});
      st128(rs, xo, u32x4{cvt2(v[0].x, v[0].y), cvt2(v[1].x, v[1].y), e0, e1});
    }
    TSC(2);
    const f32x2 r2 = up2(hw[0]) * wr[0] + up2(hw[1]) * wr[1];
    const float rw = wsum(on ? r2.x + r2.y : 0.f);
    if (lane == 0) ex[8 + wave] = rw;
    lds_barrier();
    if (tid < 8) {
      const float r = rbf((ex[8] + ex[9]) + ex[10]);
      st128(rs, W_X1 + (b * 152 + 144 + tid) * 16, u32x4{__float_as_uint(rbf(1.f / (1.f + __expf(-r)))), 0u, e0, e1});
    }
  }

  if (oj >= 0) {
    u32x4 wf[8];
    {
      const int q = n >> 2, p = lane & 3, hf = p >> 1;
      const int a0 = L_W + (wave * 64 + hf * 32 + 8 * grp + q) * 128 + (((q >> 1) ^ ((grp & 1) * 2) ^ (hf * 4)) << 4) + (p & 1) * 8;
#pragma unroll
      for (int cg = 0; cg < 8; ++cg) {
        const u32x2 lo = trd(lds, a0 ^ (cg * 16)), hi = trd(lds, (a0 ^ (cg * 16)) + 512);
        wf[cg] = u32x4{lo[0], lo[1], hi[0], hi[1]};
      }
    }
    int rw = L_OI + (n & 7) * 4128 + (wave * 2 + (n >> 3)) * 256 + (grp & 1) * 16, rr = L_OI + wave * 4128 + grp * 1024 + n * 16;
    int xo = W_XP + (op * 8 + wave) * 2304 + oj * 256 + lane * 16;
#pragma unroll
    for (int cg = 0; cg < 8; ++cg) asm volatile("" : "+v"(wf[cg]));
    asm volatile("" : "+v"(rw), "+v"(rr), "+v"(xo));
    TSC(0);
    const int ob = W_O + ((op * 2 + (wave >> 2)) * 8 + (n & 7)) * 2048 + (wave & 3) * 256 + ((n >> 3) * 4 + grp) * 16;
    u32x4 a[4];
    _Pragma("nounroll") for (int sp = 0; sp < SPIN_CAP; ++sp) {
#pragma unroll
      for (int p = 0; p < 4; ++p) a[p] = ld128(rs, ob + (p >> 1) * 1024 + (p & 1) * 128);
      if (__all(mxu(mxu(mx4(a[0]), mx4(a[1])), mxu(mx4(a[2]), mx4(a[3]))) != 0xffffffffu)) break;
      nap(4);
    }
    STAMP(7); TSC(1);
#define OS(p, j) (__uint_as_float(a[p][j]) + __uint_as_float(a[(p) + 2][j]))
    const u32x4 of = {cvt2(OS(0, 0), OS(0, 1)), cvt2(OS(0, 2), OS(0, 3)), cvt2(OS(1, 0), OS(1, 1)), cvt2(OS(1, 2), OS(1, 3))};
    f32x4 acc[8];
#pragma unroll
    for (int cg = 0; cg < 8; ++cg) acc[cg] = mfma(wf[cg], of, f32x4{0.f, 0.f, 0.f, 0.f});
    if ((n >> 3) == (grp >> 1)) {
#pragma unroll
      for (int cg = 0; cg < 8; ++cg) *(f32x4*)(lds + rw + cg * 32) = acc[cg];
    }
    TSC(2);
    lds_barrier();
    TSC(3);
    f32x4 s = (*(const f32x4*)(lds + rr) + *(const f32x4*)(lds + rr + 256)) + (*(const f32x4*)(lds + rr + 512) + *(const f32x4*)(lds + rr + 768));
#pragma unroll
    for (int k = 0; k < 4; ++k) s[k] = xsum32(xsum16(s[k]));
    if (lane < 16) st128(rs, xo, u32x4{sanf(s[0]), sanf(s[1]), sanf(s[2]), sanf(s[3])});
    TSC(4);
  }
  if (gs < 0) { STAMP(8); STAMP(13); TSC(6); }
  TSOUT();
  if (blockIdx.x == 0 && wave == 0) while (__builtin_amdgcn_s_memrealtime() - tA_ < 6000ull + lat_) __builtin_amdgcn_s_sleep(1);
  }
  }
}

extern "C" void kernel_launch(void* const* d_in, const int* in_sizes, int n_in, void* d_out, int out_size, void* d_ws, size_t ws_size,
                              hipStream_t stream) {
  fused_kernel<<<NWG, NT, 0, stream>>>((const u16*)d_in[0], (const u16*)d_in[1], (const u16*)d_in[2], (const u16*)d_in[3], (const u16*)d_in[4],
                                       (const u16*)d_in[6], (const u16*)d_in[7], (const u16*)d_in[8], (const u16*)d_in[9], (const u16*)d_in[10],
                                       (const u16*)d_in[11], (unsigned char*)d_ws, (u16*)d_out);
}
```

```cpp
#include <hip/hip_runtime.h>
#include <hip/hip_bf16.h>
#include <cstdint>

typedef unsigned u32;
typedef unsigned short u16;
typedef u32 u32x4 __attribute__((ext_vector_type(4)));
typedef u32 u32x2 __attribute__((ext_vector_type(2)));
typedef float f32x4 __attribute__((ext_vector_type(4)));
typedef float f32x2 __attribute__((ext_vector_type(2)));
typedef short s16x4 __attribute__((ext_vector_type(4)));
typedef __bf16 bf16x8 __attribute__((ext_vector_type(8)));
typedef __bf16 bf16x2 __attribute__((ext_vector_type(2)));
typedef __attribute__((address_space(3))) unsigned char lds_t;

constexpr int D = 576, SK = 8192, WIN = 2048, FF = 4608, NQ = 4096, NMOD = 6 * D;
constexpr int NWG = 256, NT = 512, NG = 72;
constexpr int SPIN_CAP = 1 << 15;

constexpr int W_O = 0;
constexpr int W_XP = W_O + 262144;
constexpr int W_Y = W_XP + 147456;
constexpr int W_MS = W_Y + NG * NG * 256;
constexpr int W_FL = W_MS;
constexpr int W_H = W_FL + 2048;
constexpr int W_M = W_H + 36864;
constexpr int W_Q = W_M + 73728;
constexpr int W_ST = W_Q + 131072;
constexpr int W_H2 = W_ST + 32768;
constexpr int W_X1 = W_H2 + 18432;
constexpr int W_END = W_X1 + 8 * 152 * 16;
constexpr int NPZ = 174, PZ = (W_MS / 16 + NPZ - 1) / NPZ;
static_assert(PZ <= 2 * NT, "poison split");

constexpr int L_W = 0;
constexpr int L_IMG = 73728;
constexpr int L_TILE = L_IMG + 9216;
constexpr int L_OI = 65536;
constexpr int L_B = 147456;
constexpr int L_C = 155648;
constexpr int LDS_BYTES = 163840;

#define SCHED() __builtin_amdgcn_sched_barrier(0)
#define BAR() asm volatile("s_barrier" ::: "memory")
#define TL(i) do { if ((i) == 7) { SCHED(); amp_ = (u32)__builtin_amdgcn_s_memrealtime(); SCHED(); } } while (0)
#define TLOUT() do { if (amp_ != 0u) { const int lim_ = 4000 + 10 * max(0, min((int)(amp_ - amp0_) - 2000, 2000)); _Pragma("nounroll") while ((int)((u32)__builtin_amdgcn_s_memrealtime() - amp0_) < lim_) __builtin_amdgcn_s_sleep(4); } } while (0)
#define WAITV_(n) do { asm volatile("s_waitcnt vmcnt(" #n ")" ::: "memory"); SCHED(); } while (0)
#define WAITV(n) WAITV_(n)
#define DK 5
#define DV DK
#define PR 115
#define PR2 124
#define PR3 118

__device__ __forceinline__ float lo16(u32 u) { return __uint_as_float(u << 16); }
__device__ __forceinline__ float hi16(u32 u) { return __uint_as_float(u & 0xffff0000u); }
__device__ __forceinline__ float b2f(u16 u) { return __uint_as_float((u32)u << 16); }
__device__ __forceinline__ u32 cvt2(float a, float b) { return __builtin_bit_cast(u32, __builtin_convertvector(f32x2{a, b}, bf16x2)); }
__device__ __forceinline__ float rbf(float v) { return __uint_as_float(cvt2(0.f, v)); }
__device__ __forceinline__ u32 sanf(float f) { u32 v = __float_as_uint(f); return v < 0xfffffffeu ? v : 0xfffffffeu; }
__device__ __forceinline__ f32x2 up2(u32 u) { return f32x2{lo16(u), hi16(u)}; }
__device__ __forceinline__ f32x2 rbf2(f32x2 v) { return f32x2{rbf(v.x), rbf(v.y)}; }
__device__ __forceinline__ void lds_barrier() { asm volatile("s_waitcnt lgkmcnt(0)\n\ts_barrier" ::: "memory"); }
__device__ __forceinline__ f32x4 mfma(u32x4 a, u32x4 b, f32x4 c) {
  return __builtin_amdgcn_mfma_f32_16x16x32_bf16(__builtin_bit_cast(bf16x8, a), __builtin_bit_cast(bf16x8, b), c, 0, 0, 0);
}
__device__ __forceinline__ u32 plo(u32 a, u32 b) { return __builtin_amdgcn_perm(b, a, 0x05040100u); }
__device__ __forceinline__ u32 phi(u32 a, u32 b) { return __builtin_amdgcn_perm(b, a, 0x07060302u); }

extern "C" __device__ __attribute__((const)) unsigned long long llvm_dispatch_id() __asm("llvm.amdgcn.dispatch.id");
typedef __amdgpu_buffer_rsrc_t rsrc_t;
__device__ __forceinline__ u32x4 ld128(rsrc_t rs, int off) { return __builtin_amdgcn_raw_buffer_load_b128(rs, off, 0, 16); }
__device__ __forceinline__ void st128(rsrc_t rs, int off, u32x4 v) { __builtin_amdgcn_raw_buffer_store_b128(v, rs, off, 0, 16); }
__device__ __forceinline__ void st64(rsrc_t rs, int off, u32x2 v) { __builtin_amdgcn_raw_buffer_store_b64(v, rs, off, 0, 16); }
__device__ __forceinline__ void dma(const void* gbase, u32 voff, unsigned lds_dst) {
  unsigned keep;
  asm volatile("s_mov_b32 %0, m0\n\ts_mov_b32 m0, %3\n\ts_nop 2\n\tglobal_load_lds_dwordx4 %1, %2 nt\n\ts_mov_b32 m0, %0"
               : "=&s"(keep) : "v"(voff), "s"(gbase), "s"(lds_dst) : "memory");
}
__device__ __forceinline__ void dma_keep(const void* gbase, u32 voff, unsigned lds_dst) {
  unsigned keep;
  asm volatile("s_mov_b32 %0, m0\n\ts_mov_b32 m0, %3\n\ts_nop 2\n\tglobal_load_lds_dword %1, %2\n\ts_mov_b32 m0, %0"
               : "=&s"(keep) : "v"(voff), "s"(gbase), "s"(lds_dst) : "memory");
}
__device__ __forceinline__ void dma_sc(const void* gbase, u32 voff, unsigned lds_dst) {
  unsigned keep;
  asm volatile("s_mov_b32 %0, m0\n\ts_mov_b32 m0, %3\n\ts_nop 2\n\tglobal_load_lds_dwordx4 %1, %2 sc1\n\ts_mov_b32 m0, %0"
               : "=&s"(keep) : "v"(voff), "s"(gbase), "s"(lds_dst) : "memory");
}
__device__ __forceinline__ void ald128(u32x4& d, const void* gbase, u32 voff) { unsigned long long t; asm volatile("s_mov_b64 %1, %3\n\tglobal_load_dwordx4 %0, %2, %1" : "=v"(d), "=&s"(t) : "v"(voff), "s"(gbase) : "memory"); }
__device__ __forceinline__ void ald64(u32x2& d, const void* gbase, u32 voff) { unsigned long long t; asm volatile("s_mov_b64 %1, %3\n\tglobal_load_dwordx2 %0, %2, %1" : "=v"(d), "=&s"(t) : "v"(voff), "s"(gbase) : "memory"); }
__device__ __forceinline__ void ald64nt(u32x2& d, const void* gbase, u32 voff) { unsigned long long t; asm volatile("s_mov_b64 %1, %3\n\tglobal_load_dwordx2 %0, %2, %1 nt" : "=v"(d), "=&s"(t) : "v"(voff), "s"(gbase) : "memory"); }
__device__ __forceinline__ void ald128sc(u32x4& d, const void* gbase, u32 voff) { unsigned long long t; asm volatile("s_mov_b64 %1, %3\n\tglobal_load_dwordx4 %0, %2, %1 sc1" : "=v"(d), "=&s"(t) : "v"(voff), "s"(gbase) : "memory"); }
__device__ __forceinline__ u32x2 trd(const unsigned char* lds, int off) {
  return __builtin_bit_cast(u32x2, __builtin_amdgcn_ds_read_tr16_b64_v4i16((__attribute__((address_space(3))) s16x4*)((lds_t*)lds + off)));
}
#define nap(n) do { __builtin_amdgcn_s_sleep(n); asm volatile("" ::: "memory"); } while (0)
__device__ __forceinline__ float frcp(float v) { return __builtin_amdgcn_rcpf(v); }
__device__ __forceinline__ float fdiv(float a, float d, float r) { const float q = a * r; return fmaf(fmaf(-q, d, a), r, q); }
__device__ __forceinline__ float silu(float v) { const float d = 1.f + __expf(-v); const float r = frcp(d); return d < 3.0e38f ? fdiv(v, d, r) : v * r; }
template <int CTRL> __device__ __forceinline__ float dppf(float x) {
  return __builtin_bit_cast(float, __builtin_amdgcn_mov_dpp(__builtin_bit_cast(int, x), CTRL, 0xf, 0xf, true));
}
__device__ __forceinline__ u32 mxu(u32 a, u32 b) { return a > b ? a : b; }
__device__ __forceinline__ u32 mx4(u32x4 v) { return mxu(mxu(v[0], v[1]), mxu(v[2], v[3])); }
__device__ __forceinline__ float rl(float v, int l) { return __builtin_bit_cast(float, __builtin_amdgcn_readlane(__builtin_bit_cast(int, v), l)); }
__device__ __forceinline__ float wsum(float v) {
  v += dppf<0xB1>(v); v += dppf<0x4E>(v); v += dppf<0x141>(v); v += dppf<0x140>(v);
  return (rl(v, 0) + rl(v, 16)) + (rl(v, 32) + rl(v, 48));
}
__device__ __forceinline__ float up32(float v) { const u32 u = __float_as_uint(v); return __uint_as_float(__builtin_amdgcn_permlane32_swap(u, u, false, false)[1]); }
__device__ __forceinline__ float xsum32(float v) { const u32 u = __float_as_uint(v); const auto r = __builtin_amdgcn_permlane32_swap(u, u, false, false); return __uint_as_float(r[0]) + __uint_as_float(r[1]); }
__device__ __forceinline__ float xsum16(float v) { const u32 u = __float_as_uint(v); const auto r = __builtin_amdgcn_permlane16_swap(u, u, false, false); return __uint_as_float(r[0]) + __uint_as_float(r[1]); }
__device__ __forceinline__ f32x4 half_sum(f32x4 d) {
  f32x4 r;
#pragma unroll
  for (int j = 0; j < 4; ++j) r[j] = d[j] + dppf<0x108>(up32(d[j]));
  return r;
}
__device__ __forceinline__ f32x2 half_sum2(f32x4 d) {
  f32x2 r;
#pragma unroll
  for (int j = 0; j < 2; ++j) {
    const auto s = __builtin_amdgcn_permlane32_swap(__float_as_uint(d[j]), __float_as_uint(d[j + 2]), false, false);
    r[j] = __uint_as_float(s[0]) + dppf<0x108>(__uint_as_float(s[1]));
  }
  return r;
}
__device__ __forceinline__ void unpack8(u32x4 u, float* f) {
#pragma unroll
  for (int j = 0; j < 4; ++j) { f[2 * j] = lo16(u[j]); f[2 * j + 1] = hi16(u[j]); }
}
__device__ __forceinline__ void ln_row(const float (&v)[16], bool has2, float& mean, float& den) {
  float s = 0.f;
#pragma unroll
  for (int i = 0; i < 8; ++i) s += v[i];
  if (has2) {
#pragma unroll
    for (int i = 8; i < 16; ++i) s += v[i];
  }
  s = wsum(s);
  const float mu = s * (1.f / D);
  mean = rbf(mu);
  float q = 0.f;
#pragma unroll
  for (int i = 0; i < 8; ++i) { const float t = v[i] - mu; q = fmaf(t, t, q); }
  if (has2) {
#pragma unroll
    for (int i = 8; i < 16; ++i) { const float t = v[i] - mu; q = fmaf(t, t, q); }
  }
  q = wsum(q);
  const float var = rbf(q * (1.f / D));
  den = rbf(sqrtf(rbf(var + 1e-5f)));
}
template <int J0 = 0, int J1 = 9>
__device__ __forceinline__ void slab_dma(const u16* base, int stride_b, unsigned lds_dst, int wave, int lane) {
  const u32 v0 = (lane >> 3) * stride_b + (((lane & 7) ^ ((wave & 1) * 4 + (lane >> 4))) << 4), v1 = v0 ^ 32u;
  const u32 v4 = wave >= 4 ? v1 : v0;
  const char* gb = (const char*)base + (size_t)wave * 8 * stride_b;
#pragma unroll
  for (int j = J0; j < J1; ++j) dma(gb + (size_t)j * 64 * stride_b, j < 4 ? v0 : j == 4 ? v4 : v1, lds_dst + (j * 8 + wave) * 1024);
}
__device__ __forceinline__ void slab_frags(const unsigned char* lds, int base, int wave, int lane, u32x4 (&wf)[9]) {
  const int g = lane >> 4, q = (lane & 15) >> 2, p = lane & 3, hf = p >> 1;
  const int a0 = base + (hf * 288 + 8 * g + q) * 128 + ((wave ^ ((g & 1) * 4 + (q >> 1)) ^ (hf * 2)) << 4) + (p & 1) * 8, a1 = (a0 ^ 32) + 512;
#pragma unroll
  for (int s = 0; s < 9; ++s) {
    const u32x2 lo = trd(lds, a0 + s * 4096), hi = trd(lds, a1 + s * 4096);
    wf[s] = u32x4{lo[0], lo[1], hi[0], hi[1]};
  }
}
__device__ __forceinline__ void img_frags(const unsigned char* lds, int img, int lane, u32x4 (&af)[9]) {
#pragma unroll
  for (int s = 0; s < 9; ++s) af[s] = *(const u32x4*)(lds + img + (s * 64 + lane) * 16);
}
__device__ __forceinline__ f32x4 gemv9(const u32x4 (&wf)[9], const u32x4 (&af)[9]) {
  f32x4 acc[3];
  SCHED();
#pragma unroll
  for (int s = 0; s < 9; ++s) acc[s % 3] = mfma(wf[s], af[s], s < 3 ? f32x4{0.f, 0.f, 0.f, 0.f} : acc[s % 3]);
  return (acc[0] + acc[1]) + acc[2];
}
__device__ __forceinline__ int img_off(int b, int k8) {
  const int hf = k8 >= 36 ? 1 : 0, kk = k8 - hf * 36;
  return ((kk >> 2) * 64 + (kk & 3) * 16 + hf * 8 + b) * 16;
}
__device__ __forceinline__ u32 tag_x(u32x4 v, u32 e0, u32 e1) { return (v[2] ^ e0) | (v[3] ^ e1); }
__device__ __forceinline__ bool tag_ok(u32x4 v, u32 e0, u32 e1) { return tag_x(v, e0, e1) == 0u; }

constexpr int CODE_BYTES = 30080;

__global__ void __launch_bounds__(NT, 2) fused_kernel(const u16* __restrict__ x, const u16* __restrict__ kc, const u16* __restrict__ vc,
                                                      const u16* __restrict__ wq, const u16* __restrict__ wo, const u16* __restrict__ w_r,
                                                      const u16* __restrict__ wg, const u16* __restrict__ wu, const u16* __restrict__ wd,
                                                      const u16* __restrict__ c, const u16* __restrict__ w_mod, unsigned char* ws, u16* out) {
  __shared__ __attribute__((aligned(16))) unsigned char lds[LDS_BYTES];
  const int w = blockIdx.x, xcd = w & 7, idx = w >> 3;
  const int tid = threadIdx.x, lane = tid & 63, wave = __builtin_amdgcn_readfirstlane(tid >> 6);
  const int n = lane & 15, grp = lane >> 4;
  const rsrc_t rs = __builtin_amdgcn_make_buffer_rsrc(ws, 0, W_END, 0x00020000);
  const unsigned lds_base = (unsigned)(uintptr_t)(lds_t*)lds;
  u32 amp_ = 0u; const u32 amp0_ = (u32)__builtin_amdgcn_s_memrealtime();
  const unsigned long long epoch = (llvm_dispatch_id() + 1ull) * 0x9E3779B97F4A7C15ull;
  const u32 e0 = (u32)epoch, e1 = (u32)(epoch >> 32);
  unsigned long long pc0; asm volatile("s_getpc_b64 %0" : "=s"(pc0));
  asm volatile("" : "+s"(vc), "+s"(w_r), "+s"(wu), "+s"(wd), "+s"(out), "+s"(x), "+s"(w_mod), "+s"(wg), "+s"(wq), "+s"(wo));

  const int mw = idx < 6 ? idx * 8 + xcd : -1;
  const int md = mw < 18 ? mw : -1;
  const int ml = mw >= 18 ? mw : mw >= 0 && mw < 6 ? 48 + mw : -1;
  const int lnb = mw >= 6 && mw < 14 ? mw - 6 : -1;
  const int qs = idx >= 6 && idx < 14 ? xcd * 8 + idx - 6 : -1;
  const int oj = idx >= 14 && idx < 23 ? idx - 14 : -1, op = xcd;
  const int gs = idx >= 23 ? xcd * 9 + idx - 23 : -1;

  const int s_att = w & 1, kvh = (w >> 1) & 15, b_att = w >> 5;
  {
    const size_t pairbase = ((size_t)(b_att * 16 + kvh) * SK + (SK - WIN)) * 256;
    const char* kp = (const char*)kc + pairbase;
    const char* vp = (const char*)vc + pairbase;
    const int wpart = (wave & 3) * 4096 + s_att * 16384 + (wave >> 2) * 262144;
    const int ko = wpart + (n >> 1) * 256 + (n & 1) * 64 + grp * 16;
    const int vo = wpart + n * 16 + grp * 512;
#define KLD(u) do { unsigned long long t_; asm volatile("s_mov_b64 %1, %3\n\tglobal_load_dwordx4 %0, %2, %1 offset:%4 nt" : "=v"(kb[u]), "=&s"(t_) : "v"(ko), "s"(kp + ((u) >> 2) * 32768), "i"((((u) >> 1) & 1) * 2048 + ((u) & 1) * 128) : "memory"); } while (0)
#define VLD(u) do { unsigned long long t_; asm volatile("s_mov_b64 %1, %3\n\tglobal_load_dwordx4 %0, %2, %1 offset:%4 nt" : "=v"(vb[u]), "=&s"(t_) : "v"(vo), "s"(vp + ((u) >> 2) * 32768), "i"((((u) >> 1) & 1) * 2048 + ((u) & 1) * 256) : "memory"); } while (0)
#define TIE8(a, o) asm volatile("" : "+v"(a[o]), "+v"(a[(o) + 1]), "+v"(a[(o) + 2]), "+v"(a[(o) + 3]), "+v"(a[(o) + 4]), "+v"(a[(o) + 5]), "+v"(a[(o) + 6]), "+v"(a[(o) + 7]))
    const int pr_ = gs >= 0 ? PR2 : mw >= 18 ? PR3 : PR;
    const u32 tp0 = (u32)__builtin_amdgcn_s_memrealtime() + (gs >= 0 || mw >= 18 ? 0 : 9 * PR / 4);
#define PACE(i) do { _Pragma("nounroll") while ((int)((u32)__builtin_amdgcn_s_memrealtime() - tp0) < (i) * pr_ / 4) __builtin_amdgcn_s_sleep(2); SCHED(); } while (0)
#define REP4(M, u, W) do { M(u); W; M((u) + 1); W; M((u) + 2); W; M((u) + 3); W; } while (0)
    static_assert(DK <= 7 && DV <= 7, "the hand-placed waits assume it");
    u32x4 kb[32], vb[32];
    const int pz = mw >= 18 ? mw - 18 : idx >= 14 ? w - 82 : -1;
#define POISON() do { if (pz >= 0) { _Pragma("unroll") for (int i = 0; i < 2; ++i) { const int g_ = pz * PZ + i * NT + tid; \
      if (i * NT + tid < PZ && g_ < W_MS / 16) st128(rs, g_ * 16, u32x4{0xffffffffu, 0xffffffffu, 0xffffffffu, 0xffffffffu}); } } SCHED(); } while (0)
#define PFLAG() do { if (pz >= 0 && tid == 448) st64(rs, W_FL + pz * 8, u32x2{e0, e1}); SCHED(); } while (0)
#define SILU_IMAGE(c0, c1) do { float f[8]; unpack8(c0, f); u32x4 o; \
        _Pragma("unroll") for (int j = 0; j < 4; ++j) o[j] = cvt2(silu(f[2 * j]), silu(f[2 * j + 1])); \
        const int b_ = tid / 72, ch_ = tid - b_ * 72; \
        *(u32x4*)(lds + L_IMG + img_off(b_, ch_)) = o; \
        if (wave == 0) { unpack8(c1, f); \
          _Pragma("unroll") for (int j = 0; j < 4; ++j) o[j] = cvt2(silu(f[2 * j]), silu(f[2 * j + 1])); \
          *(u32x4*)(lds + L_IMG + img_off(7, 8 + lane)) = o; } } while (0)
    if (md >= 0) {
      u32x4 c0, c1, x0, x1; u32x2 xc;
      __builtin_amdgcn_s_waitcnt(0x0f70);
      int ho = W_H + img_off(n & 7, (md >= 9 ? md - 9 : md) * 8 + wave) * 2 + (grp & 1) * 16;
      ald128(c0, c, tid * 16);
      ald128(c1, c, (512 + lane) * 16);
      ald128(x0, x, wave * 1152 + lane * 16);
      ald128(x1, x, wave * 1152 + 1024 + (lane & 7) * 16);
      ald64(xc, x, (n & 7) * 1152 + ((md >= 9 ? md - 9 : 0) * 64 + wave * 8 + (grp & 1) * 4) * 2);
      BAR();
      float* st = (float*)(lds + L_TILE + 1024);
      if (wave < 4) {
        slab_dma(w_mod + md * 64, NMOD * 2, lds_base + L_W, wave, lane);
        asm volatile("s_waitcnt vmcnt(9)" ::: "memory");
      } else asm volatile("s_waitcnt vmcnt(0)" ::: "memory");
      asm volatile("s_nop 0 ; both branches above cover V" : "+v"(c0), "+v"(c1), "+v"(x0), "+v"(x1), "+v"(xc) :: "memory");
      {
        SILU_IMAGE(c0, c1);
        float v[16]; unpack8(x0, v); unpack8(x1, v + 8);
        float mean, den; ln_row(v, lane < 8, mean, den);
        if (lane == 0) { st[wave] = mean; st[8 + wave] = den; }
      }
      if (wave >= 4) slab_dma(w_mod + md * 64, NMOD * 2, lds_base + L_W, wave, lane);
      lds_barrier();
      REP4(KLD, 0, );
      u32x4 wf[9], af[9];
      img_frags(lds, L_IMG, lane, af);
      const float mean = st[n & 7], rd = frcp(st[8 + (n & 7)]);
      float ln[4] = {rbf(rbf(lo16(xc[0]) - mean) * rd), rbf(rbf(hi16(xc[0]) - mean) * rd), rbf(rbf(lo16(xc[1]) - mean) * rd), rbf(rbf(hi16(xc[1]) - mean) * rd)};
#pragma unroll
      for (int s = 0; s < 9; ++s) asm volatile("" : "+v"(af[s]));
      asm volatile("" : "+v"(ln[0]), "+v"(ln[1]), "+v"(ln[2]), "+v"(ln[3]), "+v"(ho));
      WAITV(4); BAR(); SCHED();
      slab_frags(lds, L_W, wave, lane, wf);
      const f32x4 s = half_sum(gemv9(wf, af));
      f32x4 r = s;
      if (md >= 9) {
#pragma unroll
        for (int j = 0; j < 4; ++j) r[j] = ln[j] * rbf(1.f + rbf(s[j]));
      }
      if (n < 8 && grp < 2) st128(rs, ho + (md >= 9 ? 18432 : 0), u32x4{cvt2(r[0], r[1]), cvt2(r[2], r[3]), e0, e1});
      SCHED();
      TL(1);
      BAR();
      KLD(4); KLD(5); WAITV(DK);
      KLD(6); WAITV(DK); KLD(7); WAITV(DK); KLD(8); WAITV(DK); KLD(9); WAITV(DK); KLD(10); WAITV(DK);
      KLD(11); WAITV(DK); REP4(KLD, 12, WAITV(DK)); REP4(KLD, 16, WAITV(DK));
      REP4(KLD, 20, WAITV(DK)); KLD(24); WAITV(DK); KLD(25);
    } else {
      constexpr int L_LAND = 98304;
      const unsigned land = lds_base + L_LAND + wave * 6144;
      const unsigned char* lp = lds + L_LAND + wave * 6144 + lane * 16;
      const int sl_ = (3 - (int)((uintptr_t)wq >> 7)) & 7;
      const bool slowq = qs >= 0 && (qs & 7) == sl_;
      if (gs >= 0 && wave == 0) dma_keep((const char*)wq + (size_t)(idx - 23) * 64 * 8192 + xcd * 1024 + sl_ * 128, lane * 8192, land);
      if (gs >= 0 && (wave == 1 || (wave == 2 && xcd < 2))) dma_keep((const char*)w_mod + (size_t)((idx - 23) * 64 + 32) * (NMOD * 2) + xcd * 128 + (wave - 1) * 2048, (lane & 31) * (NMOD * 2) + (wave == 1 ? (lane >> 5) * 1024 : 0), land);
      POISON();
      if (oj >= 0 && idx < 17 && wave * 8192 < CODE_BYTES)
        dma_keep((const void*)pc0, min(wave * 8192 + lane * 128, CODE_BYTES - 128), lds_base + 107520 + wave * 256);
      const u32 v0 = (lane >> 3) * 1152 + (((lane & 7) ^ ((lane >> 4) & 1)) << 4);
      const char* gb = (const char*)(wo + (size_t)(op * 512 + wave * 64) * D + oj * 64);
      if (ml >= 0) { dma_sc(c, tid * 16, land); dma_sc(c, (512 + lane) * 16, land + 1024); }
#define WK(j) do { if (oj >= 0) { if ((j) < 8) dma(gb + (j) * 8 * 1152, v0 ^ (((j) & 1) * 32 + ((j) >> 2) * 64), lds_base + L_W + (wave * 8 + (j)) * 1024); } \
                   else if (slowq) { if ((j) == 8) slab_dma(wq + qs * 64, NQ * 2, lds_base + L_W, wave, lane); } \
                   else if (qs >= 0 && (j) < 9) slab_dma<(j) < 9 ? (j) : 0, (j) < 9 ? (j) + 1 : 0>(wq + qs * 64, NQ * 2, lds_base + L_W, wave, lane); \
                   KLD(j); WAITV(DK); } while (0)
      WK(0); WK(1); WK(2); WK(3);
      WK(4); WK(5);
      WK(6); WK(7);
      TL(24);
      if (ml >= 0) { const u32x4 c0 = *(const u32x4*)lp, c1 = *(const u32x4*)(lp + 1024); SILU_IMAGE(c0, c1); }
      BAR();
      PFLAG();
      WK(8); WK(9); WK(10); WK(11);
      TL(25);
      PACE(12); REP4(KLD, 12, WAITV(DK));
      TL(26);
      const int hg0 = (wave * 144 + lane) * 16, hg2 = (wave * 144 + 128 + n) * 16;
#define HREQ() do { dma_sc(ws + W_H, hg0, land); dma_sc(ws + W_H + 18432, hg0, land + 1024); dma_sc(ws + W_H + 1024, hg0, land + 2048); \
                    dma_sc(ws + W_H + 18432 + 1024, hg0, land + 3072); dma_sc(ws + W_H, hg2, land + 4096); dma_sc(ws + W_H + 18432, hg2, land + 5120); } while (0)
#define HGET() do { _Pragma("unroll") for (int i = 0; i < 6; ++i) hq[i] = *(const u32x4*)(lp + i * 1024); } while (0)
#define HQOK() __all((tag_x(hq[0], e0, e1) | tag_x(hq[1], e0, e1) | tag_x(hq[2], e0, e1) | tag_x(hq[3], e0, e1) | tag_x(hq[4], e0, e1) | tag_x(hq[5], e0, e1)) == 0u)
#define HOK() __all((tag_x(hp[0], e0, e1) | tag_x(hp[1], e0, e1) | tag_x(hp[2], e0, e1)) == 0u)
      if (qs >= 0) HREQ();
      PACE(16); REP4(KLD, 16, WAITV(DK));
      if (qs >= 0) WAITV(4);
      TL(27);
      PACE(20); KLD(20); KLD(21);
      if (qs >= 0) {
        u32x4 hq[6]; HGET();
        _Pragma("nounroll") for (int sp = 0; sp < SPIN_CAP && !HQOK(); ++sp) { nap(16); HREQ(); WAITV(0); HGET(); }
        TL(2);
#define HSUM(a, b) cvt2(lo16(a) + lo16(b), hi16(a) + hi16(b))
        *(u32x2*)(lds + L_IMG + hg0 / 2) = u32x2{HSUM(hq[0][0], hq[1][0]), HSUM(hq[0][1], hq[1][1])};
        *(u32x2*)(lds + L_IMG + 512 + hg0 / 2) = u32x2{HSUM(hq[2][0], hq[3][0]), HSUM(hq[2][1], hq[3][1])};
        if (lane < 16) *(u32x2*)(lds + L_IMG + hg2 / 2) = u32x2{HSUM(hq[4][0], hq[5][0]), HSUM(hq[4][1], hq[5][1])};
      } else WAITV(DK);
      KLD(22); KLD(23);
      if (qs >= 0) {
        u32x4 wf[9], af[9];
        lds_barrier();
        slab_frags(lds, L_W, wave, lane, wf);
        img_frags(lds, L_IMG, lane, af);
        const f32x4 s = half_sum(gemv9(wf, af));
        if (n < 8 && grp < 2) *(u32x2*)(lds + L_TILE + n * 128 + wave * 16 + grp * 8) = u32x2{cvt2(s[0], s[1]), cvt2(s[2], s[3])};
      } else WAITV(DK);
      PACE(24); KLD(24); KLD(25);
      if (qs >= 0) {
        lds_barrier();
        if (wave < 2) {
          const u32x2 t2 = *(const u32x2*)(lds + L_TILE + tid * 8);
          st128(rs, W_Q + ((tid >> 4) * 1024 + qs * 16 + n) * 16, u32x4{t2[0], t2[1], e0, e1});
        }
        TL(3);
      }
      SCHED();
    }
    const unsigned char* qg = ws + W_Q + (b_att * 1024 + kvh * 64) * 16;
    KLD(26); WAITV(DK); KLD(27); WAITV(DK); PACE(28); KLD(28); WAITV(DK); KLD(29); WAITV(DK);
    PACE(30); KLD(30); WAITV(DK); KLD(31); WAITV(DK);
    TL(28);
    PACE(32); VLD(0); WAITV(DK); VLD(1); WAITV(DK); PACE(34); VLD(2); WAITV(DK); VLD(3); WAITV(DK);
    if (wave == 1) dma_sc(qg, lane * 16, lds_base + L_B + 1024);
    PACE(36); VLD(4); WAITV(DK); VLD(5); WAITV(DK); PACE(38); VLD(6); WAITV(DK); VLD(7); WAITV(DK);
    if (wave == 1) dma_sc(qg, lane * 16, lds_base + L_B + 1024);
    TL(29);
    PACE(40); VLD(8); WAITV(DV); VLD(9); WAITV(DV); PACE(42); VLD(10); WAITV(DV); VLD(11); WAITV(DV);
    TIE8(kb, 0); TIE8(kb, 8); TIE8(kb, 16); TIE8(kb, 24);
    PACE(44); VLD(12); VLD(13);
    u32x4 qf[2];
    {
      const u32x4* ql = (const u32x4*)(lds + L_B + 1024 + (((n >> 1) & 1) * 32 + (n & 1) * 8 + grp * 2) * 16);
      u32x4 g[4];
      _Pragma("nounroll") for (int sp = 0; sp < SPIN_CAP; ++sp) {
        g[0] = ql[0]; g[1] = ql[1]; g[2] = ql[16]; g[3] = ql[17];
        if (__all((tag_x(g[0], e0, e1) | tag_x(g[1], e0, e1) | tag_x(g[2], e0, e1) | tag_x(g[3], e0, e1)) == 0u)) break;
        if (wave == 1) { dma_sc(qg, lane * 16, lds_base + L_B + 1024); WAITV(0); } else nap(4);
      }
      asm volatile("" ::: "memory");
      g[0] = ql[0]; g[1] = ql[1]; g[2] = ql[16]; g[3] = ql[17];
      qf[0] = n < 4 ? u32x4{g[0][0], g[0][1], g[1][0], g[1][1]} : u32x4{0, 0, 0, 0};
      qf[1] = n < 4 ? u32x4{g[2][0], g[2][1], g[3][0], g[3][1]} : u32x4{0, 0, 0, 0};
    }
    TL(4);
    constexpr float SCALE = 0.088388347648318440f;
    f32x2 sc[16];
    float mx = -INFINITY;
#pragma unroll
    for (int T = 0; T < 16; ++T) {
      f32x4 acc = mfma(kb[T * 2], qf[0], f32x4{0.f, 0.f, 0.f, 0.f});
      acc = mfma(kb[T * 2 + 1], qf[1], acc);
      sc[T] = f32x2{acc[0] + dppf<0xB1>(acc[1]), acc[2] + dppf<0xB1>(acc[3])};
      if (T == 7 || T == 15) { SCHED(); if (T == 7) VLD(14); else VLD(15); SCHED(); }
    }
#pragma unroll
    for (int T = 0; T < 16; ++T)
#pragma unroll
      for (int j = 0; j < 2; ++j) { const float v = rbf(rbf(sc[T][j]) * SCALE); sc[T][j] = v; mx = fmaxf(mx, v); }
    SCHED(); VLD(16); SCHED();
    mx = fmaxf(mx, __shfl_xor(mx, 16));
    mx = fmaxf(mx, __shfl_xor(mx, 32));
    float ls = 0.f;
#pragma unroll
    for (int T = 0; T < 16; ++T)
#pragma unroll
      for (int j = 0; j < 2; ++j) { sc[T][j] = __expf(sc[T][j] - mx); ls += sc[T][j]; }
    SCHED(); VLD(17); SCHED();
    ls += __shfl_xor(ls, 16);
    ls += __shfl_xor(ls, 32);
    float* stat = (float*)(lds + L_B);
    if (lane == 0 || lane == 2) *(f32x2*)(stat + (wave * 2 + (lane >> 1)) * 2) = f32x2{mx, ls};
    SCHED(); VLD(18); VLD(19); SCHED();
    lds_barrier();
    float M = -INFINITY, L = 0.f;
    {
      f32x2 t[8];
#pragma unroll
      for (int q = 0; q < 8; ++q) { t[q] = *(const f32x2*)(stat + (q * 2 + ((n >> 1) & 1)) * 2); M = fmaxf(M, t[q].x); }
#pragma unroll
      for (int q = 0; q < 8; ++q) L = fmaf(__expf(t[q].x - M), t[q].y, L);
    }
    if (tid < 8) st128(rs, W_ST + (w * 8 + lane) * 16, u32x4{__float_as_uint(M), __float_as_uint(L), e0, e1});
    SCHED();
#define VTR(G) do { u32x4 f_[8]; _Pragma("unroll") for (int t = 0; t < 8; ++t) _Pragma("unroll") for (int j = 0; j < 4; ++j) \
      f_[t][j] = (t & 1) ? phi(vb[(G) * 8 + 2 * j][t >> 1], vb[(G) * 8 + 2 * j + 1][t >> 1]) : plo(vb[(G) * 8 + 2 * j][t >> 1], vb[(G) * 8 + 2 * j + 1][t >> 1]); \
      _Pragma("unroll") for (int t = 0; t < 8; ++t) { vb[(G) * 8 + t] = f_[t]; asm volatile("" : "+v"(vb[(G) * 8 + t])); } SCHED(); } while (0)
    WAITV(12);
    TIE8(vb, 0);
    VTR(0);
    TL(30);
    PACE(52); VLD(20); WAITV(5);
    TIE8(vb, 8);
    VTR(1);
    VLD(21); WAITV(DV);
    const int po = W_ST + ((w ^ 1) * 8 + (n & 2)) * 16;
    int l1 = lane; asm volatile("" : "+v"(l1));
    const int fl = 1024 + min(l1, NPZ / 2 - 65) * 16;
    if (wave == 1) { dma_sc(ws + W_FL, l1 * 16, lds_base + L_B + 2048); dma_sc(ws + W_FL, fl, lds_base + L_B + 3072); }
    PACE(54);
    VLD(22); WAITV(DV); VLD(23); WAITV(DV);
    u32x4 ps;
    ald128sc(ps, ws, po);
    PACE(56);
    VLD(24); WAITV(DV); VLD(25); WAITV(DV);
    TL(31);
    PACE(58);
    VLD(26); WAITV(DV); VLD(27); WAITV(DV);
    u32x4 ps2;
    VLD(28); VLD(29); WAITV(6);
    asm volatile("" : "+v"(ps));
    TIE8(vb, 16);
    ald128sc(ps2, ws, po);
    VLD(30); VLD(31);
    VTR(2);
    if (!__all(tag_ok(ps, e0, e1))) {
      WAITV(2);
      asm volatile("" : "+v"(ps2));
      ps = ps2;
      if (!__all(tag_ok(ps, e0, e1))) {
        int sp = 0;
        _Pragma("nounroll") do { nap(1); ps = ld128(rs, po); } while (!__all(tag_ok(ps, e0, e1)) && ++sp < SPIN_CAP);
      }
    }
#define FOK(a, b) __all(((a[0] ^ e0) | (a[1] ^ e1) | tag_x(a, e0, e1) | (b[0] ^ e0) | (b[1] ^ e1) | tag_x(b, e0, e1)) == 0u)
    if (wave == 1 && !FOK((*(const u32x4*)(lds + L_B + 2048 + l1 * 16)), (*(const u32x4*)(lds + L_B + 3072 + l1 * 16)))) {
      _Pragma("nounroll") for (int sp = 0; sp < SPIN_CAP; ++sp) {
        nap(1);
        const u32x4 a = ld128(rs, W_FL + l1 * 16), b = ld128(rs, W_FL + fl);
        if (FOK(a, b)) break;
      }
    }
    float fac;
    {
      const float M2 = __uint_as_float(ps[0]), L2 = __uint_as_float(ps[1]);
      const float Mg = fmaxf(M, M2);
      const float Lg = __expf(M - Mg) * L + __expf(M2 - Mg) * L2;
      fac = __expf(mx - Mg) / Lg;
    }
    TL(5);
    u32x4 pf[4];
#pragma unroll
    for (int G = 0; G < 4; ++G) {
#pragma unroll
      for (int j = 0; j < 4; ++j) pf[G][j] = cvt2(sc[4 * G + j][0] * fac, sc[4 * G + j][1] * fac);
    }
    f32x4 oa[8];
#pragma unroll
    for (int t = 0; t < 8; ++t) oa[t] = f32x4{0.f, 0.f, 0.f, 0.f};
    SCHED();
#pragma unroll
    for (int G = 0; G < 2; ++G)
#pragma unroll
      for (int t = 0; t < 8; ++t) oa[t] = mfma(pf[G], vb[G * 8 + t], oa[t]);
    SCHED();
#pragma unroll
    for (int t = 0; t < 8; ++t) oa[t] = mfma(pf[2], vb[16 + t], oa[t]);
    SCHED();
    int t2_ = threadIdx.x; asm volatile("" : "+v"(t2_));
    const int tid = t2_, lane = tid & 63, n = lane & 15, grp = lane >> 4;
    int oo = W_O + ((kvh * 8 + b_att) * 2 + s_att) * 1024 + ((lane >> 4) * 16 + (lane & 1) * 8 + ((lane >> 1) & 7)) * 16;
    int mw_ = L_C + wave * 1024 + n * 32, mr_ = L_C + lane * 16;
    asm volatile("" : "+v"(oo), "+v"(mw_), "+v"(mr_), "+v"(pf[3]));
    constexpr int L_H2 = L_W + 73728;
    constexpr int L_ACT = L_C + 4608;
    int ia[3], ra[4], yo[3], fo[7], tw, tr_;
    const bool late = gs >= 0 || mw >= 18;
    if (late) {
      BAR();
      slab_dma<0, 4>(gs >= 0 ? wg + gs * 64 : w_mod + ml * 64, gs >= 0 ? FF * 2 : NMOD * 2, lds_base + L_W + (gs >= 0 ? 73728 : 0), wave, lane);
      {
    const int b = wave;
#pragma unroll
    for (int i = 0; i < 3; ++i) {
      const int k = i < 2 ? i * 256 + lane * 4 : 512 + n * 4, hf = k >= 288 ? 1 : 0, kk = k - hf * 288, s = kk >> 5, g = (kk >> 3) & 3;
      ia[i] = L_H2 + (s * 64 + g * 16 + ((hf * 8 + b) ^ ((s & 3) * 4 + g))) * 16 + (kk & 7) * 2;
    }
#pragma unroll
    for (int i = 0; i < 4; ++i) ra[i] = L_H2 + (grp * 16 + (n ^ (i * 4 + grp))) * 16;
    tw = L_ACT + n * 144 + wave * 16 + (grp & 1) * 8 + (grp >> 1) * 4; tr_ = L_ACT + (n & 7) * 144 + grp * 16;
    yo[0] = W_Y + (((wave * 8 + grp * 2) * NG + gs) * 16 + n) * 16; yo[1] = yo[0] + NG * 256;
    yo[2] = W_Y + ((((64 + wave * 2 + (grp >> 1)) * NG + gs) * 2 + (grp & 1)) * 8 + n) * 16;
    {
      const int b_ = lane >> 3, cc = lane & 7, col = gs * 8 + cc, hf = col >= 288 ? 1 : 0, kk = col - hf * 288, s_ = kk >> 5, g = (kk >> 3) & 3;
      fo[0] = W_M + ((3 * 8 + b_) * 144 + gs * 2 + (cc >> 2)) * 16;
      fo[1] = W_X1 + (b_ * 152 + gs * 2 + (cc >> 2)) * 16; fo[2] = W_X1 + (b_ * 152 + 144) * 16;
      fo[3] = W_Y + (gs * 1152 + tid) * 16;
      fo[4] = L_H2 + (s_ * 64 + g * 16 + ((hf * 8 + b_) ^ ((s_ & 3) * 4 + g))) * 16 + cc * 2;
      fo[5] = L_C + (((cc >> 2) * 8 + b_) * 4 + (cc & 3)) * 4;
      fo[6] = (b_ * D + col) * 2;
    }
    asm volatile("" : "+v"(ia[0]), "+v"(ia[1]), "+v"(ia[2]), "+v"(ra[0]), "+v"(ra[1]), "+v"(ra[2]), "+v"(ra[3]), "+v"(tw), "+v"(tr_), "+v"(yo[0]), "+v"(yo[1]), "+v"(yo[2]),
                 "+v"(fo[0]), "+v"(fo[1]), "+v"(fo[2]), "+v"(fo[3]), "+v"(fo[4]), "+v"(fo[5]), "+v"(fo[6]));
      }
      WAITV(6);
    } else WAITV(2);
    u32x4 vf[8];
    asm volatile("s_nop 0 ; both branches above cover V up to the last two" : "+v"(vb[24]), "+v"(vb[25]), "+v"(vb[26]), "+v"(vb[27]), "+v"(vb[28]), "+v"(vb[29]));
    asm volatile("" :: "v"(ps2));
#pragma unroll
    for (int t = 0; t < 8; ++t)
#pragma unroll
      for (int j = 0; j < 3; ++j) vf[t][j] = (t & 1) ? phi(vb[24 + 2 * j][t >> 1], vb[25 + 2 * j][t >> 1]) : plo(vb[24 + 2 * j][t >> 1], vb[25 + 2 * j][t >> 1]);
#pragma unroll
    for (int t = 0; t < 8; ++t) asm volatile("" : "+v"(vf[t][0]), "+v"(vf[t][1]), "+v"(vf[t][2]));
    if (late) WAITV(4); else WAITV(0);
    asm volatile("s_nop 0 ; both branches above cover V" : "+v"(vb[30]), "+v"(vb[31]));
#pragma unroll
    for (int t = 0; t < 8; ++t) {
      vf[t][3] = (t & 1) ? phi(vb[30][t >> 1], vb[31][t >> 1]) : plo(vb[30][t >> 1], vb[31][t >> 1]);
      oa[t] = mfma(pf[3], vf[t], oa[t]);
    }
    TL(6);
    if (grp == 0) {
#pragma unroll
      for (int g = 0; g < 2; ++g) {
        *(f32x4*)(lds + mw_ + g * 512) = f32x4{oa[0][2 * g], oa[1][2 * g], oa[2][2 * g], oa[3][2 * g]};
        *(f32x4*)(lds + mw_ + g * 512 + 16) = f32x4{oa[4][2 * g], oa[5][2 * g], oa[6][2 * g], oa[7][2 * g]};
      }
    }
    lds_barrier();
    if (wave == 0) {
      f32x4 m[8];
#pragma unroll
      for (int q = 0; q < 8; ++q) m[q] = *(const f32x4*)(lds + mr_ + q * 1024);
      SCHED();
      const f32x4 o = ((m[0] + m[1]) + (m[2] + m[3])) + ((m[4] + m[5]) + (m[6] + m[7]));
      st128(rs, oo, u32x4{sanf(o[0]), sanf(o[1]), sanf(o[2]), sanf(o[3])});
    }
    TL(7); SCHED();
  if (gs >= 0) {
    const int b = wave;
    BAR(); SCHED();
    u32x4 bg[9], bu[9], wdf[2][5];
    {
      u32x2 rd[2][8];
      slab_dma<4, 9>(wg + gs * 64, FF * 2, lds_base + L_W + 73728, wave, lane);
      TL(23);
      BAR();
      const char* db = (const char*)(wd + (size_t)gs * 64 * D + wave * 64);
      const u32 dv = grp * 9216 + n * 8;
#pragma unroll
      for (int ks = 0; ks < 2; ++ks)
#pragma unroll
        for (int i = 0; i < 8; ++i) ald64nt(rd[ks][i], db + (ks * 32 + i) * 1152, dv);
      {
        const int r = lane >> 3;
        dma((const char*)(wd + (size_t)(gs * 64 + wave * 8) * D + 512), r * 1152 + (((lane & 7) ^ (r & 2) ^ ((wave & 1) * 4)) << 4), lds_base + L_B + wave * 1024);
      }
      TL(19);
      asm volatile("s_waitcnt vmcnt(17)\n\ts_barrier" ::: "memory");
      TL(22);
      slab_frags(lds, L_W + 73728, wave, lane, bg);
      SCHED();
      slab_dma(wu + gs * 64, FF * 2, lds_base + L_W, wave, lane);
      TL(20);
      asm volatile("s_waitcnt vmcnt(10)" : "+v"(rd[0][0]), "+v"(rd[0][1]), "+v"(rd[0][2]), "+v"(rd[0][3]), "+v"(rd[0][4]), "+v"(rd[0][5]), "+v"(rd[0][6]), "+v"(rd[0][7]),
                   "+v"(rd[1][0]), "+v"(rd[1][1]), "+v"(rd[1][2]), "+v"(rd[1][3]), "+v"(rd[1][4]), "+v"(rd[1][5]), "+v"(rd[1][6]), "+v"(rd[1][7]) :: "memory");
#pragma unroll
      for (int ks = 0; ks < 2; ++ks)
#pragma unroll
        for (int j = 0; j < 4; ++j) {
          wdf[ks][0][j] = plo(rd[ks][2 * j][0], rd[ks][2 * j + 1][0]); wdf[ks][1][j] = phi(rd[ks][2 * j][0], rd[ks][2 * j + 1][0]);
          wdf[ks][2][j] = plo(rd[ks][2 * j][1], rd[ks][2 * j + 1][1]); wdf[ks][3][j] = phi(rd[ks][2 * j][1], rd[ks][2 * j + 1][1]);
        }
#pragma unroll
      for (int t = 0; t < 4; ++t) asm volatile("" : "+v"(wdf[0][t]), "+v"(wdf[1][t]));
      TL(21);
    }
    asm volatile("s_waitcnt vmcnt(0) lgkmcnt(0)\n\ts_barrier" ::: "memory");
    {
      u32x4 hp[3];
      const int h0 = W_H2 + (b * 144 + lane) * 16, h2o = W_H2 + (b * 144 + 128 + n) * 16;
      hp[0] = ld128(rs, h0); hp[1] = ld128(rs, h0 + 1024); hp[2] = ld128(rs, h2o);
      slab_frags(lds, L_W, wave, lane, bu);
      const int q = n >> 2, p = lane & 3;
      const int a0 = L_B + (8 * grp + q) * 128 + ((((wave & 3) * 2 + (p >> 1)) ^ (q & 2) ^ ((grp & 1) * 4)) << 4) + (p & 1) * 8;
#pragma unroll
      for (int ks = 0; ks < 2; ++ks) {
        const u32x2 lo = trd(lds, a0 + ks * 4096), hi = trd(lds, a0 + ks * 4096 + 512);
        wdf[ks][4] = u32x4{lo[0], lo[1], hi[0], hi[1]};
      }
      TL(12);
#pragma unroll
      for (int s = 0; s < 9; ++s) asm volatile("" : "+v"(bg[s]), "+v"(bu[s]));
      asm volatile("" : "+v"(wdf[0][4]), "+v"(wdf[1][4]));
      if (!HOK()) {
        int sp = 0;
        _Pragma("nounroll") do { nap(2); hp[0] = ld128(rs, h0); hp[1] = ld128(rs, h0 + 1024); hp[2] = ld128(rs, h2o); } while (!HOK() && ++sp < SPIN_CAP);
      }
      TL(13);
      *(u32x2*)(lds + ia[0]) = u32x2{hp[0][0], hp[0][1]};
      *(u32x2*)(lds + ia[1]) = u32x2{hp[1][0], hp[1][1]};
      if (lane < 16) *(u32x2*)(lds + ia[2]) = u32x2{hp[2][0], hp[2][1]};
      lds_barrier();
      u32x4 af[9];
#pragma unroll
      for (int s = 0; s < 9; ++s) af[s] = *(const u32x4*)(lds + ra[s & 3] + s * 1024);
      SCHED();
      f32x4 ag[2], au[2];
#pragma unroll
      for (int s = 0; s < 9; ++s) {
        ag[s & 1] = mfma(bg[s], af[s], s < 2 ? f32x4{0.f, 0.f, 0.f, 0.f} : ag[s & 1]);
        au[s & 1] = mfma(bu[s], af[s], s < 2 ? f32x4{0.f, 0.f, 0.f, 0.f} : au[s & 1]);
      }
      const f32x2 hg = half_sum2(ag[0] + ag[1]), hu = half_sum2(au[0] + au[1]);
      const u32 act = cvt2(rbf(silu(rbf(hg.x))) * rbf(hu.x), rbf(silu(rbf(hg.y))) * rbf(hu.y));
      if (n < 8) *(u32*)(lds + tw) = act;
    }
    lds_barrier();
    TL(14);
    {
      u32x4 af[2];
#pragma unroll
      for (int ks = 0; ks < 2; ++ks) af[ks] = *(const u32x4*)(lds + tr_ + ks * 64);
      f32x4 d[5];
#pragma unroll
      for (int t = 0; t < 5; ++t) d[t] = mfma(wdf[1][t], af[1], mfma(wdf[0][t], af[0], f32x4{0.f, 0.f, 0.f, 0.f}));
      if (n < 8) {
#pragma unroll
        for (int j = 0; j < 4; ++j)
          st128(rs, yo[j >> 1] + (j & 1) * 128, u32x4{sanf(d[0][j]), sanf(d[1][j]), sanf(d[2][j]), sanf(d[3][j])});
        if (wave < 4)
          st128(rs, yo[2], u32x4{sanf(d[4][0]), sanf(d[4][1]), sanf(d[4][2]), sanf(d[4][3])});
      }
    }
    TL(15);
    {
      float gbv = 0.f, x1v = 0.f, h2 = 0.f, rsel = 0.f;
      if (wave == 7) {
        u32x4 gq, xq, rq;
        _Pragma("nounroll") for (int sp = 0; sp < SPIN_CAP; ++sp) {
          gq = ld128(rs, fo[0]); xq = ld128(rs, fo[1]); rq = ld128(rs, fo[2]);
          if (__all((tag_x(gq, e0, e1) | tag_x(xq, e0, e1) | tag_x(rq, e0, e1)) == 0u)) break;
          nap(1);
        }
        const u32 gw = (lane & 2) ? gq[1] : gq[0], xw = (lane & 2) ? xq[1] : xq[0];
        gbv = (lane & 1) ? hi16(gw) : lo16(gw); x1v = (lane & 1) ? hi16(xw) : lo16(xw);
        h2 = b2f(*(const u16*)(lds + fo[4])); rsel = __uint_as_float(rq[0]);
        asm volatile("" : "+v"(gbv), "+v"(x1v), "+v"(h2), "+v"(rsel));
      }
      WAITV(0);
      u32x4 y[3] = {};
      _Pragma("nounroll") for (int sp = 0; sp < SPIN_CAP; ++sp) {
        y[0] = ld128(rs, fo[3]); y[1] = ld128(rs, fo[3] + 8192);
        if (wave < 2) y[2] = ld128(rs, fo[3] + 16384);
        if (__all(mxu(mxu(mx4(y[0]), mx4(y[1])), mx4(y[2])) != 0xffffffffu)) break;
        nap(1);
      }
      TL(16);
      f32x4 s;
#pragma unroll
      for (int k = 0; k < 4; ++k) s[k] = xsum32(xsum16((__uint_as_float(y[0][k]) + __uint_as_float(y[1][k])) + __uint_as_float(y[2][k])));
      if (lane < 16) *(f32x4*)(lds + L_C + (wave * 16 + lane) * 16) = s;
      lds_barrier();
      if (wave == 7) {
        float t[8];
#pragma unroll
        for (int ww = 0; ww < 8; ++ww) t[ww] = *(const float*)(lds + fo[5] + ww * 256);
        SCHED();
        const float y2 = rbf(rbf(((t[0] + t[1]) + (t[2] + t[3])) + ((t[4] + t[5]) + (t[6] + t[7]))) * rsel);
        const float res = x1v + rbf(gbv * rbf(h2 + y2));
        __builtin_amdgcn_raw_buffer_store_b16((u16)(cvt2(res, 0.f) & 0xffffu), __builtin_amdgcn_make_buffer_rsrc(out, 0, 8 * D * 2, 0x00020000), fo[6], 0, 0);
      }
      TL(17);
    }
  }
  {
  int t3_ = threadIdx.x; asm volatile("" : "+v"(t3_));
  const int tid = t3_, lane = tid & 63, n = lane & 15, grp = lane >> 4;
  if (qs >= 0) {
    const u16* mat = (wave & 1) ? wg : wu;
    const int g0 = ((3 - (int)((uintptr_t)mat >> 7) - xcd * 9) & 7) + (wave >= 4 ? 8 : 0);
    const int r = (idx - 6) * 72 + ((wave >> 1) & 1) * 36 + min(lane, 35);
    if (g0 < 9) dma_keep(mat, r * 9216 + (xcd * 9 + g0) * 128, lds_base + L_W + wave * 256);
  }
  if (ml >= 0) {
    BAR();
    if (mw < 18) slab_dma<0, 4>(w_mod + ml * 64, NMOD * 2, lds_base + L_W, wave, lane);
    slab_dma<4, 9>(w_mod + ml * 64, NMOD * 2, lds_base + L_W, wave, lane);
    u32x4 wf[9], af[9];
    img_frags(lds, L_IMG, lane, af);
    asm volatile("s_waitcnt vmcnt(0)\n\ts_barrier" ::: "memory");
    slab_frags(lds, L_W, wave, lane, wf);
    const f32x4 s = half_sum(gemv9(wf, af));
    if (n < 8 && grp < 2) *(u32x2*)(lds + L_TILE + n * 128 + wave * 16 + grp * 8) = u32x2{cvt2(s[0], s[1]), cvt2(s[2], s[3])};
    lds_barrier();
    if (wave < 2) {
      const u32x2 t2 = *(const u32x2*)(lds + L_TILE + tid * 8);
      const int ch = (ml - 18) / 9, sl = ml - 18 - ch * 9;
      st128(rs, W_M + ((ch * 8 + (tid >> 4)) * 144 + sl * 16 + n) * 16, u32x4{t2[0], t2[1], e0, e1});
    }
    TL(18);
  }

  if (lnb >= 0 && wave < 3) {
    const int b = lnb, g = tid < 144 ? tid : 143;
    const bool on = tid < 144;
    float* ex = (float*)(lds + L_TILE);
    const u32x2 xa = __builtin_amdgcn_raw_buffer_load_b64(__builtin_amdgcn_make_buffer_rsrc((void*)x, 0, 8 * D * 2, 0x00020000), (b * D + g * 4) * 2, 0, 0);
    const u32x2 wra = __builtin_amdgcn_raw_buffer_load_b64(__builtin_amdgcn_make_buffer_rsrc((void*)w_r, 0, D * 2, 0x00020000), g * 8, 0, 0);
    u32x4 mm[3];
    _Pragma("nounroll") for (int sp = 0; sp < SPIN_CAP; ++sp) {
#pragma unroll
      for (int k = 0; k < 3; ++k) mm[k] = ld128(rs, W_M + ((k * 8 + b) * 144 + g) * 16);
      if (__all((tag_x(mm[0], e0, e1) | tag_x(mm[1], e0, e1) | tag_x(mm[2], e0, e1)) == 0u)) break;
      nap(4);
    }
    f32x2 xv[2], ga[2], sb[2], cb1[2], wr[2];
#pragma unroll
    for (int i = 0; i < 2; ++i) {
      xv[i] = up2(xa[i]); wr[i] = up2(wra[i]); ga[i] = up2(mm[0][i]); sb[i] = up2(mm[1][i]); cb1[i] = rbf2(f32x2{1.f, 1.f} + up2(mm[2][i]));
      asm volatile("" : "+v"(xv[i]), "+v"(ga[i]), "+v"(sb[i]), "+v"(cb1[i]), "+v"(wr[i]));
    }
    int ho = W_H2 + (b * 144 + g) * 16, xo = W_X1 + (b * 152 + g) * 16;
    asm volatile("" : "+v"(ho), "+v"(xo));
    u32x4 xp[8];
    _Pragma("nounroll") for (int sp = 0; sp < SPIN_CAP; ++sp) {
#pragma unroll
      for (int p = 0; p < 8; ++p) xp[p] = ld128(rs, W_XP + (p * 8 + b) * 2304 + g * 16);
      if (__all(mxu(mxu(mxu(mx4(xp[0]), mx4(xp[1])), mxu(mx4(xp[2]), mx4(xp[3]))), mxu(mxu(mx4(xp[4]), mx4(xp[5])), mxu(mx4(xp[6]), mx4(xp[7])))) != 0xffffffffu)) break;
      nap(1);
    }
    TL(10);
    f32x2 v[2];
#pragma unroll
    for (int i = 0; i < 2; ++i) {
      f32x2 pp[8];
#pragma unroll
      for (int p = 0; p < 8; ++p) pp[p] = f32x2{__uint_as_float(xp[p][2 * i]), __uint_as_float(xp[p][2 * i + 1])};
      v[i] = rbf2(xv[i] + rbf2(ga[i] * rbf2(((pp[0] + pp[1]) + (pp[2] + pp[3])) + ((pp[4] + pp[5]) + (pp[6] + pp[7])))));
    }
    const f32x2 s2 = v[0] + v[1];
    const float mw = wsum(on ? s2.x + s2.y : 0.f) * (wave < 2 ? 1.f / 256 : 1.f / 64);
    const f32x2 t0 = v[0] - f32x2{mw, mw}, t1 = v[1] - f32x2{mw, mw}, q2 = t0 * t0 + t1 * t1;
    const float m2 = wsum(on ? q2.x + q2.y : 0.f);
    if (lane == 0) *(f32x2*)(ex + wave * 2) = f32x2{mw, m2};
    lds_barrier();
    const f32x4 e01 = *(const f32x4*)ex; const f32x2 e2 = *(const f32x2*)(ex + 4);
    const float mu = (e01[0] + e01[2]) * (256.f / D) + e2.x * (64.f / D);
    const float d0 = e01[0] - mu, d1 = e01[2] - mu, d2 = e2.x - mu;
    const float var = rbf(((e01[1] + e01[3]) + e2.y + 256.f * (d0 * d0 + d1 * d1) + 64.f * d2 * d2) * (1.f / D));
    const float mean = rbf(mu), rden = frcp(rbf(sqrtf(rbf(var + 1e-5f))));
    u32 hw[2];
#pragma unroll
    for (int i = 0; i < 2; ++i) {
      const f32x2 t = rbf2(rbf2(rbf2(v[i] - f32x2{mean, mean}) * f32x2{rden, rden}) * cb1[i]) + sb[i];
      hw[i] = cvt2(t.x, t.y);
    }
    if (on) {
      st128(rs, ho, u32x4{hw[0], hw[1], e0, e1});
      st128(rs, xo, u32x4{cvt2(v[0].x, v[0].y), cvt2(v[1].x, v[1].y), e0, e1});
    }
    TL(11);
    const f32x2 r2 = up2(hw[0]) * wr[0] + up2(hw[1]) * wr[1];
    const float rw = wsum(on ? r2.x + r2.y : 0.f);
    if (lane == 0) ex[8 + wave] = rw;
    lds_barrier();
    if (tid < 8) {
      const float r = rbf((ex[8] + ex[9]) + ex[10]);
      st128(rs, W_X1 + (b * 152 + 144 + tid) * 16, u32x4{__float_as_uint(rbf(1.f / (1.f + __expf(-r)))), 0u, e0, e1});
    }
  }

  if (oj >= 0) {
    const int ob = W_O + ((op * 2 + (wave >> 2)) * 8 + (lane >> 3)) * 2048 + (wave & 3) * 256 + (lane & 7) * 16;
    u32x4 a[4];
#pragma unroll
    for (int p = 0; p < 4; ++p) a[p] = ld128(rs, ob + (p >> 1) * 1024 + (p & 1) * 128);
    SCHED();
    u32x4 wf[8];
    {
      const int q = n >> 2, p = lane & 3, hf = p >> 1;
      const int a0 = L_W + (wave * 64 + hf * 32 + 8 * grp + q) * 128 + (((q >> 1) ^ ((grp & 1) * 2) ^ (hf * 4)) << 4) + (p & 1) * 8;
#pragma unroll
      for (int cg = 0; cg < 8; ++cg) {
        const u32x2 lo = trd(lds, a0 ^ (cg * 16)), hi = trd(lds, (a0 ^ (cg * 16)) + 512);
        wf[cg] = u32x4{lo[0], lo[1], hi[0], hi[1]};
      }
    }
    int rw = L_OI + (n & 7) * 4128 + (wave * 2 + (n >> 3)) * 256 + (grp & 1) * 16, rr = L_OI + wave * 4128 + grp * 1024 + n * 16;
    int xo = W_XP + (op * 8 + wave) * 2304 + oj * 256 + lane * 16, sl = ((n & 7) * 8 + (n >> 3) * 4 + grp) * 4;
#pragma unroll
    for (int cg = 0; cg < 8; ++cg) asm volatile("" : "+v"(wf[cg]));
    asm volatile("" : "+v"(rw), "+v"(rr), "+v"(xo), "+v"(sl));
    _Pragma("nounroll") for (int sp = 0; sp < SPIN_CAP; ++sp) {
      const bool m0 = mxu(mx4(a[0]), mx4(a[1])) == 0xffffffffu, m1 = mxu(mx4(a[2]), mx4(a[3])) == 0xffffffffu;
      if (!__any(m0 || m1)) break;
      nap(2);
      if (m0) { a[0] = ld128(rs, ob); a[1] = ld128(rs, ob + 128); }
      if (m1) { a[2] = ld128(rs, ob + 1024); a[3] = ld128(rs, ob + 1152); }
    }
    TL(8);
#define OS(p, j) (__uint_as_float(a[p][j]) + __uint_as_float(a[(p) + 2][j]))
#define OP(v) (u32)__builtin_amdgcn_ds_bpermute(sl, (int)(v))
    const u32x4 of = {OP(cvt2(OS(0, 0), OS(0, 1))), OP(cvt2(OS(0, 2), OS(0, 3))), OP(cvt2(OS(1, 0), OS(1, 1))), OP(cvt2(OS(1, 2), OS(1, 3)))};
    f32x4 acc[8];
#pragma unroll
    for (int cg = 0; cg < 8; ++cg) acc[cg] = mfma(wf[cg], of, f32x4{0.f, 0.f, 0.f, 0.f});
    if ((n >> 3) == (grp >> 1)) {
#pragma unroll
      for (int cg = 0; cg < 8; ++cg) *(f32x4*)(lds + rw + cg * 32) = acc[cg];
    }
    lds_barrier();
    f32x4 s = (*(const f32x4*)(lds + rr) + *(const f32x4*)(lds + rr + 256)) + (*(const f32x4*)(lds + rr + 512) + *(const f32x4*)(lds + rr + 768));
#pragma unroll
    for (int k = 0; k < 4; ++k) s[k] = xsum32(xsum16(s[k]));
    if (lane < 16) st128(rs, xo, u32x4{sanf(s[0]), sanf(s[1]), sanf(s[2]), sanf(s[3])});
    TL(9);
  }
  TLOUT();
  }
  }
}

extern "C" void kernel_launch(void* const* d_in, const int* in_sizes, int n_in, void* d_out, int out_size, void* d_ws, size_t ws_size,
                              hipStream_t stream) {
  fused_kernel<<<NWG, NT, 0, stream>>>((const u16*)d_in[0], (const u16*)d_in[1], (const u16*)d_in[2], (const u16*)d_in[3], (const u16*)d_in[4],
                                       (const u16*)d_in[6], (const u16*)d_in[7], (const u16*)d_in[8], (const u16*)d_in[9], (const u16*)d_in[10],
                                       (const u16*)d_in[11], (unsigned char*)d_ws, (u16*)d_out);
}
```

```cpp
#include <hip/hip_runtime.h>
#include <hip/hip_bf16.h>
#include <cstdint>

typedef unsigned u32;
typedef unsigned short u16;
typedef u32 u32x4 __attribute__((ext_vector_type(4)));
typedef u32 u32x2 __attribute__((ext_vector_type(2)));
typedef float f32x4 __attribute__((ext_vector_type(4)));
typedef float f32x2 __attribute__((ext_vector_type(2)));
typedef short s16x4 __attribute__((ext_vector_type(4)));
typedef __bf16 bf16x8 __attribute__((ext_vector_type(8)));
typedef __bf16 bf16x2 __attribute__((ext_vector_type(2)));
typedef __attribute__((address_space(3))) unsigned char lds_t;

constexpr int D = 576, SK = 8192, WIN = 2048, FF = 4608, NQ = 4096, NMOD = 6 * D;
constexpr int NWG = 256, NT = 512, NG = 72;
constexpr int SPIN_CAP = 1 << 15;

constexpr int W_O = 0;
constexpr int W_XP = W_O + 262144;
constexpr int W_Y = W_XP + 147456;
constexpr int W_MS = W_Y + NG * NG * 256;
constexpr int W_FL = W_MS;
constexpr int W_H = W_FL + 2048;
constexpr int W_M = W_H + 36864;
constexpr int W_Q = W_M + 73728;
constexpr int W_ST = W_Q + 131072;
constexpr int W_H2 = W_ST + 32768;
constexpr int W_X1 = W_H2 + 18432;
constexpr int W_END = W_X1 + 8 * 152 * 16;
constexpr int NPZ = 174, PZ = (W_MS / 16 + NPZ - 1) / NPZ;
static_assert(PZ <= 2 * NT, "poison split");

constexpr int L_W = 0;
constexpr int L_IMG = 73728;
constexpr int L_TILE = L_IMG + 9216;
constexpr int L_OI = 65536;
constexpr int L_B = 147456;
constexpr int L_C = 155648;
constexpr int LDS_BYTES = 163840;

#define SCHED() __builtin_amdgcn_sched_barrier(0)
#define BAR() asm volatile("s_barrier" ::: "memory")
#define TL(i) do {} while (0)
#define TLOUT() do {} while (0)
#define WAITV_(n) do { asm volatile("s_waitcnt vmcnt(" #n ")" ::: "memory"); SCHED(); } while (0)
#define WAITV(n) WAITV_(n)
#define OF 198
#define OL 150
#define OO 203
#define OQ 203
#define EP 4
#define CPW 0
#define CPM 0
#define QL 6
#define QN 6
#define DK 5
#define DV DK
#define PR 115
#define PR2 113
#define PR3 113

__device__ __forceinline__ float lo16(u32 u) { return __uint_as_float(u << 16); }
__device__ __forceinline__ float hi16(u32 u) { return __uint_as_float(u & 0xffff0000u); }
__device__ __forceinline__ float b2f(u16 u) { return __uint_as_float((u32)u << 16); }
__device__ __forceinline__ u32 cvt2(float a, float b) { return __builtin_bit_cast(u32, __builtin_convertvector(f32x2{a, b}, bf16x2)); }
__device__ __forceinline__ float rbf(float v) { return __uint_as_float(cvt2(0.f, v)); }
__device__ __forceinline__ u32 sanf(float f) { u32 v = __float_as_uint(f); return v < 0xfffffffeu ? v : 0xfffffffeu; }
__device__ __forceinline__ f32x2 up2(u32 u) { return f32x2{lo16(u), hi16(u)}; }
__device__ __forceinline__ f32x2 rbf2(f32x2 v) { return f32x2{rbf(v.x), rbf(v.y)}; }
__device__ __forceinline__ void lds_barrier() { asm volatile("s_waitcnt lgkmcnt(0)\n\ts_barrier" ::: "memory"); }
__device__ __forceinline__ f32x4 mfma(u32x4 a, u32x4 b, f32x4 c) {
  return __builtin_amdgcn_mfma_f32_16x16x32_bf16(__builtin_bit_cast(bf16x8, a), __builtin_bit_cast(bf16x8, b), c, 0, 0, 0);
}
__device__ __forceinline__ u32 plo(u32 a, u32 b) { return __builtin_amdgcn_perm(b, a, 0x05040100u); }
__device__ __forceinline__ u32 phi(u32 a, u32 b) { return __builtin_amdgcn_perm(b, a, 0x07060302u); }

extern "C" __device__ __attribute__((const)) unsigned long long llvm_dispatch_id() __asm("llvm.amdgcn.dispatch.id");
typedef __amdgpu_buffer_rsrc_t rsrc_t;
__device__ __forceinline__ u32x4 ld128(rsrc_t rs, int off) { return __builtin_amdgcn_raw_buffer_load_b128(rs, off, 0, 16); }
__device__ __forceinline__ void st128(rsrc_t rs, int off, u32x4 v) { __builtin_amdgcn_raw_buffer_store_b128(v, rs, off, 0, 16); }
__device__ __forceinline__ void st64(rsrc_t rs, int off, u32x2 v) { __builtin_amdgcn_raw_buffer_store_b64(v, rs, off, 0, 16); }
__device__ __forceinline__ void dma(const void* gbase, u32 voff, unsigned lds_dst) {
  unsigned keep;
  asm volatile("s_mov_b32 %0, m0\n\ts_mov_b32 m0, %3\n\ts_nop 2\n\tglobal_load_lds_dwordx4 %1, %2 nt\n\ts_mov_b32 m0, %0"
               : "=&s"(keep) : "v"(voff), "s"(gbase), "s"(lds_dst) : "memory");
}
__device__ __forceinline__ void dma_keep(const void* gbase, u32 voff, unsigned lds_dst) {
  unsigned keep;
  asm volatile("s_mov_b32 %0, m0\n\ts_mov_b32 m0, %3\n\ts_nop 2\n\tglobal_load_lds_dword %1, %2\n\ts_mov_b32 m0, %0"
               : "=&s"(keep) : "v"(voff), "s"(gbase), "s"(lds_dst) : "memory");
}
__device__ __forceinline__ void dma_sc(const void* gbase, u32 voff, unsigned lds_dst) {
  unsigned keep;
  asm volatile("s_mov_b32 %0, m0\n\ts_mov_b32 m0, %3\n\ts_nop 2\n\tglobal_load_lds_dwordx4 %1, %2 sc1\n\ts_mov_b32 m0, %0"
               : "=&s"(keep) : "v"(voff), "s"(gbase), "s"(lds_dst) : "memory");
}
__device__ __forceinline__ void ald128(u32x4& d, const void* gbase, u32 voff) { unsigned long long t; asm volatile("s_mov_b64 %1, %3\n\tglobal_load_dwordx4 %0, %2, %1" : "=v"(d), "=&s"(t) : "v"(voff), "s"(gbase) : "memory"); }
__device__ __forceinline__ void ald64(u32x2& d, const void* gbase, u32 voff) { unsigned long long t; asm volatile("s_mov_b64 %1, %3\n\tglobal_load_dwordx2 %0, %2, %1" : "=v"(d), "=&s"(t) : "v"(voff), "s"(gbase) : "memory"); }
__device__ __forceinline__ void ald64nt(u32x2& d, const void* gbase, u32 voff) { unsigned long long t; asm volatile("s_mov_b64 %1, %3\n\tglobal_load_dwordx2 %0, %2, %1 nt" : "=v"(d), "=&s"(t) : "v"(voff), "s"(gbase) : "memory"); }
__device__ __forceinline__ void ald128sc(u32x4& d, const void* gbase, u32 voff) { unsigned long long t; asm volatile("s_mov_b64 %1, %3\n\tglobal_load_dwordx4 %0, %2, %1 sc1" : "=v"(d), "=&s"(t) : "v"(voff), "s"(gbase) : "memory"); }
__device__ __forceinline__ u32x2 trd(const unsigned char* lds, int off) {
  return __builtin_bit_cast(u32x2, __builtin_amdgcn_ds_read_tr16_b64_v4i16((__attribute__((address_space(3))) s16x4*)((lds_t*)lds + off)));
}
#define nap(n) do { __builtin_amdgcn_s_sleep(n); asm volatile("" ::: "memory"); } while (0)
__device__ __forceinline__ float frcp(float v) { return __builtin_amdgcn_rcpf(v); }
__device__ __forceinline__ float fdiv(float a, float d, float r) { const float q = a * r; return fmaf(fmaf(-q, d, a), r, q); }
__device__ __forceinline__ float silu(float v) { const float d = 1.f + __expf(-v); const float r = frcp(d); return d < 3.0e38f ? fdiv(v, d, r) : v * r; }
template <int CTRL> __device__ __forceinline__ float dppf(float x) {
  return __builtin_bit_cast(float, __builtin_amdgcn_mov_dpp(__builtin_bit_cast(int, x), CTRL, 0xf, 0xf, true));
}
__device__ __forceinline__ u32 mxu(u32 a, u32 b) { return a > b ? a : b; }
__device__ __forceinline__ u32 mx4(u32x4 v) { return mxu(mxu(v[0], v[1]), mxu(v[2], v[3])); }
__device__ __forceinline__ float rl(float v, int l) { return __builtin_bit_cast(float, __builtin_amdgcn_readlane(__builtin_bit_cast(int, v), l)); }
__device__ __forceinline__ float wsum(float v) {
  v += dppf<0xB1>(v); v += dppf<0x4E>(v); v += dppf<0x141>(v); v += dppf<0x140>(v);
  return (rl(v, 0) + rl(v, 16)) + (rl(v, 32) + rl(v, 48));
}
__device__ __forceinline__ float up32(float v) { const u32 u = __float_as_uint(v); return __uint_as_float(__builtin_amdgcn_permlane32_swap(u, u, false, false)[1]); }
__device__ __forceinline__ float xsum32(float v) { const u32 u = __float_as_uint(v); const auto r = __builtin_amdgcn_permlane32_swap(u, u, false, false); return __uint_as_float(r[0]) + __uint_as_float(r[1]); }
__device__ __forceinline__ float xsum16(float v) { const u32 u = __float_as_uint(v); const auto r = __builtin_amdgcn_permlane16_swap(u, u, false, false); return __uint_as_float(r[0]) + __uint_as_float(r[1]); }
__device__ __forceinline__ f32x4 half_sum(f32x4 d) {
  f32x4 r;
#pragma unroll
  for (int j = 0; j < 4; ++j) r[j] = d[j] + dppf<0x108>(up32(d[j]));
  return r;
}
__device__ __forceinline__ f32x2 half_sum2(f32x4 d) {
  f32x2 r;
#pragma unroll
  for (int j = 0; j < 2; ++j) {
    const auto s = __builtin_amdgcn_permlane32_swap(__float_as_uint(d[j]), __float_as_uint(d[j + 2]), false, false);
    r[j] = __uint_as_float(s[0]) + dppf<0x108>(__uint_as_float(s[1]));
  }
  return r;
}
__device__ __forceinline__ void unpack8(u32x4 u, float* f) {
#pragma unroll
  for (int j = 0; j < 4; ++j) { f[2 * j] = lo16(u[j]); f[2 * j + 1] = hi16(u[j]); }
}
__device__ __forceinline__ void ln_row(const float (&v)[16], bool has2, float& mean, float& den) {
  float s = 0.f;
#pragma unroll
  for (int i = 0; i < 8; ++i) s += v[i];
  if (has2) {
#pragma unroll
    for (int i = 8; i < 16; ++i) s += v[i];
  }
  s = wsum(s);
  const float mu = s * (1.f / D);
  mean = rbf(mu);
  float q = 0.f;
#pragma unroll
  for (int i = 0; i < 8; ++i) { const float t = v[i] - mu; q = fmaf(t, t, q); }
  if (has2) {
#pragma unroll
    for (int i = 8; i < 16; ++i) { const float t = v[i] - mu; q = fmaf(t, t, q); }
  }
  q = wsum(q);
  const float var = rbf(q * (1.f / D));
  den = rbf(sqrtf(rbf(var + 1e-5f)));
}
template <int J0 = 0, int J1 = 9>
__device__ __forceinline__ void slab_dma(const u16* base, int stride_b, unsigned lds_dst, int wave, int lane) {
  const u32 v0 = (lane >> 3) * stride_b + (((lane & 7) ^ ((wave & 1) * 4 + (lane >> 4))) << 4), v1 = v0 ^ 32u;
  const u32 v4 = wave >= 4 ? v1 : v0;
  const char* gb = (const char*)base + (size_t)wave * 8 * stride_b;
#pragma unroll
  for (int j = J0; j < J1; ++j) dma(gb + (size_t)j * 64 * stride_b, j < 4 ? v0 : j == 4 ? v4 : v1, lds_dst + (j * 8 + wave) * 1024);
}
__device__ __forceinline__ void slab_frags(const unsigned char* lds, int base, int wave, int lane, u32x4 (&wf)[9]) {
  const int g = lane >> 4, q = (lane & 15) >> 2, p = lane & 3, hf = p >> 1;
  const int a0 = base + (hf * 288 + 8 * g + q) * 128 + ((wave ^ ((g & 1) * 4 + (q >> 1)) ^ (hf * 2)) << 4) + (p & 1) * 8, a1 = (a0 ^ 32) + 512;
#pragma unroll
  for (int s = 0; s < 9; ++s) {
    const u32x2 lo = trd(lds, a0 + s * 4096), hi = trd(lds, a1 + s * 4096);
    wf[s] = u32x4{lo[0], lo[1], hi[0], hi[1]};
  }
}
__device__ __forceinline__ void img_frags(const unsigned char* lds, int img, int lane, u32x4 (&af)[9]) {
#pragma unroll
  for (int s = 0; s < 9; ++s) af[s] = *(const u32x4*)(lds + img + (s * 64 + lane) * 16);
}
__device__ __forceinline__ f32x4 gemv9(const u32x4 (&wf)[9], const u32x4 (&af)[9]) {
  f32x4 acc[3];
  SCHED();
#pragma unroll
  for (int s = 0; s < 9; ++s) acc[s % 3] = mfma(wf[s], af[s], s < 3 ? f32x4{0.f, 0.f, 0.f, 0.f} : acc[s % 3]);
  return (acc[0] + acc[1]) + acc[2];
}
__device__ __forceinline__ int img_off(int b, int k8) {
  const int hf = k8 >= 36 ? 1 : 0, kk = k8 - hf * 36;
  return ((kk >> 2) * 64 + (kk & 3) * 16 + hf * 8 + b) * 16;
}
__device__ __forceinline__ u32 tag_x(u32x4 v, u32 e0, u32 e1) { return (v[2] ^ e0) | (v[3] ^ e1); }
__device__ __forceinline__ bool tag_ok(u32x4 v, u32 e0, u32 e1) { return tag_x(v, e0, e1) == 0u; }

constexpr int CODE_BYTES = 31140;

__global__ void __launch_bounds__(NT, 2) fused_kernel(const u16* __restrict__ x, const u16* __restrict__ kc, const u16* __restrict__ vc,
                                                      const u16* __restrict__ wq, const u16* __restrict__ wo, const u16* __restrict__ w_r,
                                                      const u16* __restrict__ wg, const u16* __restrict__ wu, const u16* __restrict__ wd,
                                                      const u16* __restrict__ c, const u16* __restrict__ w_mod, unsigned char* ws, u16* out) {
  __shared__ __attribute__((aligned(16))) unsigned char lds[LDS_BYTES];
  const int w = blockIdx.x, xcd = w & 7, idx = w >> 3;
  const int tid = threadIdx.x, lane = tid & 63, wave = __builtin_amdgcn_readfirstlane(tid >> 6);
  const int n = lane & 15, grp = lane >> 4;
  const rsrc_t rs = __builtin_amdgcn_make_buffer_rsrc(ws, 0, W_END, 0x00020000);
  const unsigned lds_base = (unsigned)(uintptr_t)(lds_t*)lds;
  {
    unsigned long long pc0; asm volatile("s_getpc_b64 %0" : "=s"(pc0));
    if (w >= 224 && wave == CPW) dma_keep((const void*)pc0, min((w >> 3) * 8192 - 28 * 8192 + lane * 128, CODE_BYTES - 128), lds_base + 107520);
    SCHED();
  }
  const unsigned long long epoch = (llvm_dispatch_id() + 1ull) * 0x9E3779B97F4A7C15ull;
  const u32 e0 = (u32)epoch, e1 = (u32)(epoch >> 32);
  asm volatile("" : "+s"(vc), "+s"(w_r), "+s"(wu), "+s"(wd), "+s"(out), "+s"(x), "+s"(w_mod), "+s"(wg), "+s"(wq), "+s"(wo));

  const int mw = idx < 6 ? idx * 8 + xcd : -1;
  const int md = mw < 18 ? mw : -1;
  const int ml = mw >= 18 ? mw : mw >= 0 && mw < 6 ? 48 + mw : -1;
  const int lnb = mw >= 6 && mw < 14 ? mw - 6 : -1;
  const int qs = idx >= 6 && idx < 14 ? xcd * 8 + idx - 6 : -1;
  const int oj = idx >= 14 && idx < 23 ? idx - 14 : -1, op = xcd;
  const int gs = idx >= 23 ? xcd * 9 + idx - 23 : -1;

  const int s_att = w & 1, kvh = (w >> 1) & 15, b_att = w >> 5;
  {
    const size_t pairbase = ((size_t)(b_att * 16 + kvh) * SK + (SK - WIN)) * 256;
    const char* kp = (const char*)kc + pairbase;
    const char* vp = (const char*)vc + pairbase;
    const int wpart = (wave & 3) * 4096 + s_att * 16384 + (wave >> 2) * 262144;
    const int ko = wpart + (n >> 1) * 256 + (n & 1) * 64 + grp * 16;
    const int vo = wpart + n * 16 + grp * 512;
#define KLD(u) do { unsigned long long t_; asm volatile("s_mov_b64 %1, %3\n\tglobal_load_dwordx4 %0, %2, %1 offset:%4 nt" : "=v"(kb[u]), "=&s"(t_) : "v"(ko), "s"(kp + ((u) >> 2) * 32768), "i"((((u) >> 1) & 1) * 2048 + ((u) & 1) * 128) : "memory"); } while (0)
#define VLD(u) do { unsigned long long t_; asm volatile("s_mov_b64 %1, %3\n\tglobal_load_dwordx4 %0, %2, %1 offset:%4 nt" : "=v"(vb[u]), "=&s"(t_) : "v"(vo), "s"(vp + ((u) >> 2) * 32768), "i"((((u) >> 1) & 1) * 2048 + ((u) & 1) * 256) : "memory"); } while (0)
#define TIE8(a, o) asm volatile("" : "+v"(a[o]), "+v"(a[(o) + 1]), "+v"(a[(o) + 2]), "+v"(a[(o) + 3]), "+v"(a[(o) + 4]), "+v"(a[(o) + 5]), "+v"(a[(o) + 6]), "+v"(a[(o) + 7]))
    const int pr_ = gs >= 0 ? PR2 : mw >= 18 ? PR3 : PR;
    const u32 tp0 = (u32)__builtin_amdgcn_s_memrealtime() + (gs >= 0 ? OF : mw >= 18 ? OL : oj >= 0 ? OO : OQ);
#define PACE(i) do { _Pragma("nounroll") while ((int)((u32)__builtin_amdgcn_s_memrealtime() - tp0) < (i) * pr_ / 4) __builtin_amdgcn_s_sleep(2); SCHED(); } while (0)
#define REP4(M, u, W) do { M(u); W; M((u) + 1); W; M((u) + 2); W; M((u) + 3); W; } while (0)
    static_assert(DK <= 7 && DV <= 7, "the hand-placed waits assume it");
    u32x4 kb[32], vb[32];
    const int pz = mw >= 18 ? mw - 18 : idx >= 14 ? w - 82 : -1;
#define POISON() do { if (pz >= 0) { _Pragma("unroll") for (int i = 0; i < 2; ++i) { const int g_ = pz * PZ + i * NT + tid; \
      if (i * NT + tid < PZ && g_ < W_MS / 16) st128(rs, g_ * 16, u32x4{0xffffffffu, 0xffffffffu, 0xffffffffu, 0xffffffffu}); } } SCHED(); } while (0)
#define PFLAG() do { if (pz >= 0 && tid == 448) st64(rs, W_FL + pz * 8, u32x2{e0, e1}); SCHED(); } while (0)
#define SILU_IMAGE(c0, c1) do { float f[8]; unpack8(c0, f); u32x4 o; \
        _Pragma("unroll") for (int j = 0; j < 4; ++j) o[j] = cvt2(silu(f[2 * j]), silu(f[2 * j + 1])); \
        const int b_ = tid / 72, ch_ = tid - b_ * 72; \
        *(u32x4*)(lds + L_IMG + img_off(b_, ch_)) = o; \
        if (wave == 0) { unpack8(c1, f); \
          _Pragma("unroll") for (int j = 0; j < 4; ++j) o[j] = cvt2(silu(f[2 * j]), silu(f[2 * j + 1])); \
          *(u32x4*)(lds + L_IMG + img_off(7, 8 + lane)) = o; } } while (0)
    if (md >= 0) {
      u32x4 c0, c1, x0, x1; u32x2 xc;
      __builtin_amdgcn_s_waitcnt(0x0f70);
      int ho = W_H + img_off(n & 7, (md >= 9 ? md - 9 : md) * 8 + wave) * 2 + (grp & 1) * 16;
      ald128(c0, c, tid * 16);
      ald128(c1, c, (512 + lane) * 16);
      ald128(x0, x, wave * 1152 + lane * 16);
      ald128(x1, x, wave * 1152 + 1024 + (lane & 7) * 16);
      ald64(xc, x, (n & 7) * 1152 + ((md >= 9 ? md - 9 : 0) * 64 + wave * 8 + (grp & 1) * 4) * 2);
      TL(35);
      BAR();
      float* st = (float*)(lds + L_TILE + 1024);
      if (wave < 4) {
        slab_dma(w_mod + md * 64, NMOD * 2, lds_base + L_W, wave, lane);
        asm volatile("s_waitcnt vmcnt(9)" ::: "memory");
      } else asm volatile("s_waitcnt vmcnt(0)" ::: "memory");
      asm volatile("s_nop 0 ; both branches above cover V" : "+v"(c0), "+v"(c1), "+v"(x0), "+v"(x1), "+v"(xc) :: "memory");
      {
        SILU_IMAGE(c0, c1);
        float v[16]; unpack8(x0, v); unpack8(x1, v + 8);
        float mean, den; ln_row(v, lane < 8, mean, den);
        if (lane == 0) { st[wave] = mean; st[8 + wave] = den; }
      }
      if (wave >= 4) slab_dma(w_mod + md * 64, NMOD * 2, lds_base + L_W, wave, lane);
      lds_barrier();
      REP4(KLD, 0, );
      u32x4 wf[9], af[9];
      img_frags(lds, L_IMG, lane, af);
      const float mean = st[n & 7], rd = frcp(st[8 + (n & 7)]);
      float ln[4] = {rbf(rbf(lo16(xc[0]) - mean) * rd), rbf(rbf(hi16(xc[0]) - mean) * rd), rbf(rbf(lo16(xc[1]) - mean) * rd), rbf(rbf(hi16(xc[1]) - mean) * rd)};
#pragma unroll
      for (int s = 0; s < 9; ++s) asm volatile("" : "+v"(af[s]));
      asm volatile("" : "+v"(ln[0]), "+v"(ln[1]), "+v"(ln[2]), "+v"(ln[3]), "+v"(ho));
      WAITV(4); BAR(); SCHED();
      slab_frags(lds, L_W, wave, lane, wf);
      const f32x4 s = half_sum(gemv9(wf, af));
      f32x4 r = s;
      if (md >= 9) {
#pragma unroll
        for (int j = 0; j < 4; ++j) r[j] = ln[j] * rbf(1.f + rbf(s[j]));
      }
      if (n < 8 && grp < 2) st128(rs, ho + (md >= 9 ? 18432 : 0), u32x4{cvt2(r[0], r[1]), cvt2(r[2], r[3]), e0, e1});
      SCHED();
      TL(1);
      BAR();
      KLD(4); KLD(5); WAITV(DK);
      KLD(6); WAITV(DK); KLD(7); WAITV(DK); KLD(8); WAITV(DK); KLD(9); WAITV(DK); KLD(10); WAITV(DK);
      KLD(11); WAITV(DK); REP4(KLD, 12, WAITV(DK)); REP4(KLD, 16, WAITV(DK));
      REP4(KLD, 20, WAITV(DK)); KLD(24); WAITV(DK); KLD(25);
    } else {
      constexpr int L_LAND = 98304;
      const unsigned land = lds_base + L_LAND + wave * 6144;
      const unsigned char* lp = lds + L_LAND + wave * 6144 + lane * 16;
      const int sl_ = (3 - (int)((uintptr_t)wq >> 7)) & 7;
      const bool slowq = qs >= 0 && (qs & 7) == sl_;
      if (gs >= 0 && wave == 0) dma_keep((const char*)wq + (size_t)(idx - 23) * 64 * 8192 + xcd * 1024 + sl_ * 128, lane * 8192, land);
      if (gs >= 0 && (wave == 1 || (wave == 2 && xcd < 2))) dma_keep((const char*)w_mod + (size_t)((idx - 23) * 64 + 32) * (NMOD * 2) + xcd * 128 + (wave - 1) * 2048, (lane & 31) * (NMOD * 2) + (wave == 1 ? (lane >> 5) * 1024 : 0), land);
      POISON();
      const u32 v0 = (lane >> 3) * 1152 + (((lane & 7) ^ ((lane >> 4) & 1)) << 4);
      const char* gb = (const char*)(wo + (size_t)(op * 512 + wave * 64) * D + oj * 64);
      if (ml >= 0) { dma_sc(c, tid * 16, land); dma_sc(c, (512 + lane) * 16, land + 1024); }
#define WK(j) do { if (oj >= 0) { if ((j) < 8) dma(gb + (j) * 8 * 1152, v0 ^ (((j) & 1) * 32 + ((j) >> 2) * 64), lds_base + L_W + (wave * 8 + (j)) * 1024); } \
                   else if (slowq) { if ((j) == 8) slab_dma(wq + qs * 64, NQ * 2, lds_base + L_W, wave, lane); } \
                   else if (qs >= 0 && (j) < 9) slab_dma<(j) < 9 ? (j) : 0, (j) < 9 ? (j) + 1 : 0>(wq + qs * 64, NQ * 2, lds_base + L_W, wave, lane); \
                   else if (EP && qs < 0 && (j) >= EP) PACE(j); \
                   KLD(j); if ((j) == 0) TL(32); if ((j) == 4) TL(33); WAITV(DK); if ((j) == 5) TL(34); } while (0)
      WK(0); WK(1); WK(2); WK(3);
      WK(4); WK(5);
      WK(6); WK(7);
      TL(24);
      if (ml >= 0) { const u32x4 c0 = *(const u32x4*)lp, c1 = *(const u32x4*)(lp + 1024); SILU_IMAGE(c0, c1); }
      BAR();
      PFLAG();
      WK(8); WK(9); WK(10); WK(11);
      TL(25);
      PACE(12); REP4(KLD, 12, WAITV(DK));
      TL(26);
      const int hg0 = (wave * 144 + lane) * 16, hg2 = (wave * 144 + 128 + n) * 16;
#define HREQ() do { dma_sc(ws + W_H, hg0, land); dma_sc(ws + W_H + 18432, hg0, land + 1024); dma_sc(ws + W_H + 1024, hg0, land + 2048); \
                    dma_sc(ws + W_H + 18432 + 1024, hg0, land + 3072); dma_sc(ws + W_H, hg2, land + 4096); dma_sc(ws + W_H + 18432, hg2, land + 5120); } while (0)
#define HGET() do { _Pragma("unroll") for (int i = 0; i < 6; ++i) hq[i] = *(const u32x4*)(lp + i * 1024); } while (0)
#define HQOK() __all((tag_x(hq[0], e0, e1) | tag_x(hq[1], e0, e1) | tag_x(hq[2], e0, e1) | tag_x(hq[3], e0, e1) | tag_x(hq[4], e0, e1) | tag_x(hq[5], e0, e1)) == 0u)
#define HOK() __all((tag_x(hp[0], e0, e1) | tag_x(hp[1], e0, e1) | tag_x(hp[2], e0, e1)) == 0u)
      if (qs >= 0) HREQ();
      PACE(16); REP4(KLD, 16, WAITV(DK));
      if (qs >= 0) WAITV(4);
      TL(27);
      PACE(20); KLD(20); KLD(21);
      if (qs >= 0) {
        u32x4 hq[6]; HGET();
        _Pragma("nounroll") for (int sp = 0; sp < SPIN_CAP && !HQOK(); ++sp) { nap(16); HREQ(); WAITV(0); HGET(); }
        TL(2);
#define HSUM(a, b) cvt2(lo16(a) + lo16(b), hi16(a) + hi16(b))
        *(u32x2*)(lds + L_IMG + hg0 / 2) = u32x2{HSUM(hq[0][0], hq[1][0]), HSUM(hq[0][1], hq[1][1])};
        *(u32x2*)(lds + L_IMG + 512 + hg0 / 2) = u32x2{HSUM(hq[2][0], hq[3][0]), HSUM(hq[2][1], hq[3][1])};
        if (lane < 16) *(u32x2*)(lds + L_IMG + hg2 / 2) = u32x2{HSUM(hq[4][0], hq[5][0]), HSUM(hq[4][1], hq[5][1])};
      } else WAITV(DK);
      KLD(22); KLD(23);
      if (qs >= 0) {
        u32x4 wf[9], af[9];
        lds_barrier();
        slab_frags(lds, L_W, wave, lane, wf);
        img_frags(lds, L_IMG, lane, af);
        const f32x4 s = half_sum(gemv9(wf, af));
        if (n < 8 && grp < 2) *(u32x2*)(lds + L_TILE + n * 128 + wave * 16 + grp * 8) = u32x2{cvt2(s[0], s[1]), cvt2(s[2], s[3])};
      } else WAITV(DK);
      PACE(24); KLD(24); KLD(25);
      if (qs >= 0) {
        lds_barrier();
        if (wave < 2) {
          const u32x2 t2 = *(const u32x2*)(lds + L_TILE + tid * 8);
          st128(rs, W_Q + ((tid >> 4) * 1024 + qs * 16 + n) * 16, u32x4{t2[0], t2[1], e0, e1});
        }
        TL(3);
      }
      SCHED();
    }
    const unsigned char* qg = ws + W_Q + (b_att * 1024 + kvh * 64) * 16;
    KLD(26); WAITV(DK); KLD(27); WAITV(DK); PACE(28); KLD(28); WAITV(DK); KLD(29); WAITV(DK);
    PACE(30); KLD(30); WAITV(DK); KLD(31); WAITV(DK);
    TL(28);
    PACE(32); VLD(0); WAITV(DK); VLD(1); WAITV(DK); PACE(34); VLD(2); WAITV(DK); VLD(3); WAITV(DK);
    if (wave == 1) dma_sc(qg, lane * 16, lds_base + L_B + 1024);
    PACE(36); VLD(4); WAITV(DK); VLD(5); WAITV(DK); PACE(38); VLD(6); WAITV(DK); VLD(7); WAITV(DK);
    if (wave == 1) dma_sc(qg, lane * 16, lds_base + L_B + 1024);
    TL(29);
#define QLOOK(b) do { if ((QL & (b)) && wave == 1) dma_sc(qg, lane * 16, lds_base + L_B + 1024); } while (0)
    PACE(40); QLOOK(1); VLD(8); WAITV(DV); VLD(9); WAITV(DV); PACE(42); QLOOK(2); VLD(10); WAITV(DV); VLD(11); WAITV(DV);
    TIE8(kb, 0); TIE8(kb, 8); TIE8(kb, 16); TIE8(kb, 24);
    PACE(44); QLOOK(4); VLD(12); VLD(13);
    u32x4 qf[2];
    {
      const u32x4* ql = (const u32x4*)(lds + L_B + 1024 + (((n >> 1) & 1) * 32 + (n & 1) * 8 + grp * 2) * 16);
      u32x4 g[4];
      _Pragma("nounroll") for (int sp = 0; sp < SPIN_CAP; ++sp) {
        g[0] = ql[0]; g[1] = ql[1]; g[2] = ql[16]; g[3] = ql[17];
        if (__all((tag_x(g[0], e0, e1) | tag_x(g[1], e0, e1) | tag_x(g[2], e0, e1) | tag_x(g[3], e0, e1)) == 0u)) break;
        if (wave == 1 && sp >= QN) { dma_sc(qg, lane * 16, lds_base + L_B + 1024); WAITV(0); } else nap(4);
      }
      asm volatile("" ::: "memory");
      g[0] = ql[0]; g[1] = ql[1]; g[2] = ql[16]; g[3] = ql[17];
      qf[0] = n < 4 ? u32x4{g[0][0], g[0][1], g[1][0], g[1][1]} : u32x4{0, 0, 0, 0};
      qf[1] = n < 4 ? u32x4{g[2][0], g[2][1], g[3][0], g[3][1]} : u32x4{0, 0, 0, 0};
    }
    TL(4);
    constexpr float SCALE = 0.088388347648318440f;
    f32x2 sc[16];
    float mx = -INFINITY;
#pragma unroll
    for (int T = 0; T < 16; ++T) {
      f32x4 acc = mfma(kb[T * 2], qf[0], f32x4{0.f, 0.f, 0.f, 0.f});
      acc = mfma(kb[T * 2 + 1], qf[1], acc);
      sc[T] = f32x2{acc[0] + dppf<0xB1>(acc[1]), acc[2] + dppf<0xB1>(acc[3])};
      if (T == 7 || T == 15) { SCHED(); if (T == 7) VLD(14); else VLD(15); SCHED(); }
    }
#pragma unroll
    for (int T = 0; T < 16; ++T)
#pragma unroll
      for (int j = 0; j < 2; ++j) { const float v = rbf(rbf(sc[T][j]) * SCALE); sc[T][j] = v; mx = fmaxf(mx, v); }
    SCHED(); VLD(16); SCHED();
    mx = fmaxf(mx, __shfl_xor(mx, 16));
    mx = fmaxf(mx, __shfl_xor(mx, 32));
    float ls = 0.f;
#pragma unroll
    for (int T = 0; T < 16; ++T)
#pragma unroll
      for (int j = 0; j < 2; ++j) { sc[T][j] = __expf(sc[T][j] - mx); ls += sc[T][j]; }
    SCHED(); VLD(17); SCHED();
    ls += __shfl_xor(ls, 16);
    ls += __shfl_xor(ls, 32);
    float* stat = (float*)(lds + L_B);
    if (lane == 0 || lane == 2) *(f32x2*)(stat + (wave * 2 + (lane >> 1)) * 2) = f32x2{mx, ls};
    SCHED(); VLD(18); VLD(19); SCHED();
    lds_barrier();
    float M = -INFINITY, L = 0.f;
    {
      f32x2 t[8];
#pragma unroll
      for (int q = 0; q < 8; ++q) { t[q] = *(const f32x2*)(stat + (q * 2 + ((n >> 1) & 1)) * 2); M = fmaxf(M, t[q].x); }
#pragma unroll
      for (int q = 0; q < 8; ++q) L = fmaf(__expf(t[q].x - M), t[q].y, L);
    }
    if (tid < 8) st128(rs, W_ST + (w * 8 + lane) * 16, u32x4{__float_as_uint(M), __float_as_uint(L), e0, e1});
    SCHED();
#define VTR(G) do { u32x4 f_[8]; _Pragma("unroll") for (int t = 0; t < 8; ++t) _Pragma("unroll") for (int j = 0; j < 4; ++j) \
      f_[t][j] = (t & 1) ? phi(vb[(G) * 8 + 2 * j][t >> 1], vb[(G) * 8 + 2 * j + 1][t >> 1]) : plo(vb[(G) * 8 + 2 * j][t >> 1], vb[(G) * 8 + 2 * j + 1][t >> 1]); \
      _Pragma("unroll") for (int t = 0; t < 8; ++t) { vb[(G) * 8 + t] = f_[t]; asm volatile("" : "+v"(vb[(G) * 8 + t])); } SCHED(); } while (0)
    WAITV(12);
    TIE8(vb, 0);
    VTR(0);
    TL(30);
    PACE(52); VLD(20); WAITV(5);
    TIE8(vb, 8);
    VTR(1);
    VLD(21); WAITV(DV);
    const int po = W_ST + ((w ^ 1) * 8 + (n & 2)) * 16;
    int l1 = lane; asm volatile("" : "+v"(l1));
    const int fl = 1024 + min(l1, NPZ / 2 - 65) * 16;
    if (wave == 1) { dma_sc(ws + W_FL, l1 * 16, lds_base + L_B + 2048); dma_sc(ws + W_FL, fl, lds_base + L_B + 3072); }
    PACE(54);
    VLD(22); WAITV(DV); VLD(23); WAITV(DV);
    u32x4 ps;
    ald128sc(ps, ws, po);
    PACE(56);
    VLD(24); WAITV(DV); VLD(25); WAITV(DV);
    TL(31);
    PACE(58);
    VLD(26); WAITV(DV); VLD(27); WAITV(DV);
    u32x4 ps2;
    VLD(28); VLD(29); WAITV(6);
    asm volatile("" : "+v"(ps));
    TIE8(vb, 16);
    ald128sc(ps2, ws, po);
    VLD(30); VLD(31);
    VTR(2);
    if (!__all(tag_ok(ps, e0, e1))) {
      WAITV(2);
      asm volatile("" : "+v"(ps2));
      ps = ps2;
      if (!__all(tag_ok(ps, e0, e1))) {
        int sp = 0;
        _Pragma("nounroll") do { nap(1); ps = ld128(rs, po); } while (!__all(tag_ok(ps, e0, e1)) && ++sp < SPIN_CAP);
      }
    }
#define FOK(a, b) __all(((a[0] ^ e0) | (a[1] ^ e1) | tag_x(a, e0, e1) | (b[0] ^ e0) | (b[1] ^ e1) | tag_x(b, e0, e1)) == 0u)
    if (wave == 1 && !FOK((*(const u32x4*)(lds + L_B + 2048 + l1 * 16)), (*(const u32x4*)(lds + L_B + 3072 + l1 * 16)))) {
      _Pragma("nounroll") for (int sp = 0; sp < SPIN_CAP; ++sp) {
        nap(1);
        const u32x4 a = ld128(rs, W_FL + l1 * 16), b = ld128(rs, W_FL + fl);
        if (FOK(a, b)) break;
      }
    }
    float fac;
    {
      const float M2 = __uint_as_float(ps[0]), L2 = __uint_as_float(ps[1]);
      const float Mg = fmaxf(M, M2);
      const float Lg = __expf(M - Mg) * L + __expf(M2 - Mg) * L2;
      fac = __expf(mx - Mg) / Lg;
    }
    TL(5);
    u32x4 pf[4];
#pragma unroll
    for (int G = 0; G < 4; ++G) {
#pragma unroll
      for (int j = 0; j < 4; ++j) pf[G][j] = cvt2(sc[4 * G + j][0] * fac, sc[4 * G + j][1] * fac);
    }
    f32x4 oa[8];
#pragma unroll
    for (int t = 0; t < 8; ++t) oa[t] = f32x4{0.f, 0.f, 0.f, 0.f};
    SCHED();
#pragma unroll
    for (int G = 0; G < 2; ++G)
#pragma unroll
      for (int t = 0; t < 8; ++t) oa[t] = mfma(pf[G], vb[G * 8 + t], oa[t]);
    SCHED();
#pragma unroll
    for (int t = 0; t < 8; ++t) oa[t] = mfma(pf[2], vb[16 + t], oa[t]);
    SCHED();
    int t2_ = threadIdx.x; asm volatile("" : "+v"(t2_));
    const int tid = t2_, lane = tid & 63, n = lane & 15, grp = lane >> 4;
    int oo = W_O + ((kvh * 8 + b_att) * 2 + s_att) * 1024 + ((lane >> 4) * 16 + (lane & 1) * 8 + ((lane >> 1) & 7)) * 16;
    int mw_ = L_C + wave * 1024 + n * 32, mr_ = L_C + lane * 16;
    asm volatile("" : "+v"(oo), "+v"(mw_), "+v"(mr_), "+v"(pf[3]));
    constexpr int L_H2 = L_W + 73728;
    constexpr int L_ACT = L_C + 4608;
    int ia[3], ra[4], yo[3], fo[7], tw, tr_;
    const bool late = gs >= 0 || mw >= 18;
    if (late) {
      BAR();
      slab_dma<0, 4>(gs >= 0 ? wg + gs * 64 : w_mod + ml * 64, gs >= 0 ? FF * 2 : NMOD * 2, lds_base + L_W + (gs >= 0 ? 73728 : 0), wave, lane);
      {
    const int b = wave;
#pragma unroll
    for (int i = 0; i < 3; ++i) {
      const int k = i < 2 ? i * 256 + lane * 4 : 512 + n * 4, hf = k >= 288 ? 1 : 0, kk = k - hf * 288, s = kk >> 5, g = (kk >> 3) & 3;
      ia[i] = L_H2 + (s * 64 + g * 16 + ((hf * 8 + b) ^ ((s & 3) * 4 + g))) * 16 + (kk & 7) * 2;
    }
#pragma unroll
    for (int i = 0; i < 4; ++i) ra[i] = L_H2 + (grp * 16 + (n ^ (i * 4 + grp))) * 16;
    tw = L_ACT + n * 144 + wave * 16 + (grp & 1) * 8 + (grp >> 1) * 4; tr_ = L_ACT + (n & 7) * 144 + grp * 16;
    yo[0] = W_Y + (((wave * 8 + grp * 2) * NG + gs) * 16 + n) * 16; yo[1] = yo[0] + NG * 256;
    yo[2] = W_Y + ((((64 + wave * 2 + (grp >> 1)) * NG + gs) * 2 + (grp & 1)) * 8 + n) * 16;
    {
      const int b_ = lane >> 3, cc = lane & 7, col = gs * 8 + cc, hf = col >= 288 ? 1 : 0, kk = col - hf * 288, s_ = kk >> 5, g = (kk >> 3) & 3;
      fo[0] = W_M + ((3 * 8 + b_) * 144 + gs * 2 + (cc >> 2)) * 16;
      fo[1] = W_X1 + (b_ * 152 + gs * 2 + (cc >> 2)) * 16; fo[2] = W_X1 + (b_ * 152 + 144) * 16;
      fo[3] = W_Y + (gs * 1152 + tid) * 16;
      fo[4] = L_H2 + (s_ * 64 + g * 16 + ((hf * 8 + b_) ^ ((s_ & 3) * 4 + g))) * 16 + cc * 2;
      fo[5] = L_C + (((cc >> 2) * 8 + b_) * 4 + (cc & 3)) * 4;
      fo[6] = (b_ * D + col) * 2;
    }
    asm volatile("" : "+v"(ia[0]), "+v"(ia[1]), "+v"(ia[2]), "+v"(ra[0]), "+v"(ra[1]), "+v"(ra[2]), "+v"(ra[3]), "+v"(tw), "+v"(tr_), "+v"(yo[0]), "+v"(yo[1]), "+v"(yo[2]),
                 "+v"(fo[0]), "+v"(fo[1]), "+v"(fo[2]), "+v"(fo[3]), "+v"(fo[4]), "+v"(fo[5]), "+v"(fo[6]));
      }
      WAITV(6);
    } else WAITV(2);
    u32x4 vf[8];
    asm volatile("s_nop 0 ; both branches above cover V up to the last two" : "+v"(vb[24]), "+v"(vb[25]), "+v"(vb[26]), "+v"(vb[27]), "+v"(vb[28]), "+v"(vb[29]));
    asm volatile("" :: "v"(ps2));
#pragma unroll
    for (int t = 0; t < 8; ++t)
#pragma unroll
      for (int j = 0; j < 3; ++j) vf[t][j] = (t & 1) ? phi(vb[24 + 2 * j][t >> 1], vb[25 + 2 * j][t >> 1]) : plo(vb[24 + 2 * j][t >> 1], vb[25 + 2 * j][t >> 1]);
#pragma unroll
    for (int t = 0; t < 8; ++t) asm volatile("" : "+v"(vf[t][0]), "+v"(vf[t][1]), "+v"(vf[t][2]));
    if (late) WAITV(4); else WAITV(0);
    asm volatile("s_nop 0 ; both branches above cover V" : "+v"(vb[30]), "+v"(vb[31]));
#pragma unroll
    for (int t = 0; t < 8; ++t) {
      vf[t][3] = (t & 1) ? phi(vb[30][t >> 1], vb[31][t >> 1]) : plo(vb[30][t >> 1], vb[31][t >> 1]);
      oa[t] = mfma(pf[3], vf[t], oa[t]);
    }
    TL(6);
    if (grp == 0) {
#pragma unroll
      for (int g = 0; g < 2; ++g) {
        *(f32x4*)(lds + mw_ + g * 512) = f32x4{oa[0][2 * g], oa[1][2 * g], oa[2][2 * g], oa[3][2 * g]};
        *(f32x4*)(lds + mw_ + g * 512 + 16) = f32x4{oa[4][2 * g], oa[5][2 * g], oa[6][2 * g], oa[7][2 * g]};
      }
    }
    lds_barrier();
    if (wave == 0) {
      f32x4 m[8];
#pragma unroll
      for (int q = 0; q < 8; ++q) m[q] = *(const f32x4*)(lds + mr_ + q * 1024);
      SCHED();
      const f32x4 o = ((m[0] + m[1]) + (m[2] + m[3])) + ((m[4] + m[5]) + (m[6] + m[7]));
      st128(rs, oo, u32x4{sanf(o[0]), sanf(o[1]), sanf(o[2]), sanf(o[3])});
    }
    TL(7); SCHED();
  if (gs >= 0) {
    const int b = wave;
    BAR(); SCHED();
    u32x4 bg[9], bu[9], wdf[2][5];
    {
      u32x2 rd[2][8];
      slab_dma<4, 9>(wg + gs * 64, FF * 2, lds_base + L_W + 73728, wave, lane);
      TL(23);
      BAR();
      const char* db = (const char*)(wd + (size_t)gs * 64 * D + wave * 64);
      const u32 dv = grp * 9216 + n * 8;
#pragma unroll
      for (int ks = 0; ks < 2; ++ks)
#pragma unroll
        for (int i = 0; i < 8; ++i) ald64nt(rd[ks][i], db + (ks * 32 + i) * 1152, dv);
      {
        const int r = lane >> 3;
        dma((const char*)(wd + (size_t)(gs * 64 + wave * 8) * D + 512), r * 1152 + (((lane & 7) ^ (r & 2) ^ ((wave & 1) * 4)) << 4), lds_base + L_B + wave * 1024);
      }
      TL(19);
      asm volatile("s_waitcnt vmcnt(17)\n\ts_barrier" ::: "memory");
      TL(22);
      slab_frags(lds, L_W + 73728, wave, lane, bg);
      SCHED();
      slab_dma(wu + gs * 64, FF * 2, lds_base + L_W, wave, lane);
      TL(20);
      asm volatile("s_waitcnt vmcnt(10)" : "+v"(rd[0][0]), "+v"(rd[0][1]), "+v"(rd[0][2]), "+v"(rd[0][3]), "+v"(rd[0][4]), "+v"(rd[0][5]), "+v"(rd[0][6]), "+v"(rd[0][7]),
                   "+v"(rd[1][0]), "+v"(rd[1][1]), "+v"(rd[1][2]), "+v"(rd[1][3]), "+v"(rd[1][4]), "+v"(rd[1][5]), "+v"(rd[1][6]), "+v"(rd[1][7]) :: "memory");
#pragma unroll
      for (int ks = 0; ks < 2; ++ks)
#pragma unroll
        for (int j = 0; j < 4; ++j) {
          wdf[ks][0][j] = plo(rd[ks][2 * j][0], rd[ks][2 * j + 1][0]); wdf[ks][1][j] = phi(rd[ks][2 * j][0], rd[ks][2 * j + 1][0]);
          wdf[ks][2][j] = plo(rd[ks][2 * j][1], rd[ks][2 * j + 1][1]); wdf[ks][3][j] = phi(rd[ks][2 * j][1], rd[ks][2 * j + 1][1]);
        }
#pragma unroll
      for (int t = 0; t < 4; ++t) asm volatile("" : "+v"(wdf[0][t]), "+v"(wdf[1][t]));
      TL(21);
    }
    asm volatile("s_waitcnt vmcnt(0) lgkmcnt(0)\n\ts_barrier" ::: "memory");
    {
      u32x4 hp[3];
      const int h0 = W_H2 + (b * 144 + lane) * 16, h2o = W_H2 + (b * 144 + 128 + n) * 16;
      hp[0] = ld128(rs, h0); hp[1] = ld128(rs, h0 + 1024); hp[2] = ld128(rs, h2o);
      slab_frags(lds, L_W, wave, lane, bu);
      const int q = n >> 2, p = lane & 3;
      const int a0 = L_B + (8 * grp + q) * 128 + ((((wave & 3) * 2 + (p >> 1)) ^ (q & 2) ^ ((grp & 1) * 4)) << 4) + (p & 1) * 8;
#pragma unroll
      for (int ks = 0; ks < 2; ++ks) {
        const u32x2 lo = trd(lds, a0 + ks * 4096), hi = trd(lds, a0 + ks * 4096 + 512);
        wdf[ks][4] = u32x4{lo[0], lo[1], hi[0], hi[1]};
      }
      TL(12);
#pragma unroll
      for (int s = 0; s < 9; ++s) asm volatile("" : "+v"(bg[s]), "+v"(bu[s]));
      asm volatile("" : "+v"(wdf[0][4]), "+v"(wdf[1][4]));
      if (!HOK()) {
        int sp = 0;
        _Pragma("nounroll") do { nap(2); hp[0] = ld128(rs, h0); hp[1] = ld128(rs, h0 + 1024); hp[2] = ld128(rs, h2o); } while (!HOK() && ++sp < SPIN_CAP);
      }
      TL(13);
      *(u32x2*)(lds + ia[0]) = u32x2{hp[0][0], hp[0][1]};
      *(u32x2*)(lds + ia[1]) = u32x2{hp[1][0], hp[1][1]};
      if (lane < 16) *(u32x2*)(lds + ia[2]) = u32x2{hp[2][0], hp[2][1]};
      lds_barrier();
      u32x4 af[9];
#pragma unroll
      for (int s = 0; s < 9; ++s) af[s] = *(const u32x4*)(lds + ra[s & 3] + s * 1024);
      SCHED();
      f32x4 ag[2], au[2];
#pragma unroll
      for (int s = 0; s < 9; ++s) {
        ag[s & 1] = mfma(bg[s], af[s], s < 2 ? f32x4{0.f, 0.f, 0.f, 0.f} : ag[s & 1]);
        au[s & 1] = mfma(bu[s], af[s], s < 2 ? f32x4{0.f, 0.f, 0.f, 0.f} : au[s & 1]);
      }
      const f32x2 hg = half_sum2(ag[0] + ag[1]), hu = half_sum2(au[0] + au[1]);
      const u32 act = cvt2(rbf(silu(rbf(hg.x))) * rbf(hu.x), rbf(silu(rbf(hg.y))) * rbf(hu.y));
      if (n < 8) *(u32*)(lds + tw) = act;
    }
    lds_barrier();
    TL(14);
    {
      u32x4 af[2];
#pragma unroll
      for (int ks = 0; ks < 2; ++ks) af[ks] = *(const u32x4*)(lds + tr_ + ks * 64);
      f32x4 d[5];
#pragma unroll
      for (int t = 0; t < 5; ++t) d[t] = mfma(wdf[1][t], af[1], mfma(wdf[0][t], af[0], f32x4{0.f, 0.f, 0.f, 0.f}));
      if (n < 8) {
#pragma unroll
        for (int j = 0; j < 4; ++j)
          st128(rs, yo[j >> 1] + (j & 1) * 128, u32x4{sanf(d[0][j]), sanf(d[1][j]), sanf(d[2][j]), sanf(d[3][j])});
        if (wave < 4)
          st128(rs, yo[2], u32x4{sanf(d[4][0]), sanf(d[4][1]), sanf(d[4][2]), sanf(d[4][3])});
      }
    }
    TL(15);
    {
      float gbv = 0.f, x1v = 0.f, h2 = 0.f, rsel = 0.f;
      if (wave == 7) {
        u32x4 gq, xq, rq;
        _Pragma("nounroll") for (int sp = 0; sp < SPIN_CAP; ++sp) {
          gq = ld128(rs, fo[0]); xq = ld128(rs, fo[1]); rq = ld128(rs, fo[2]);
          if (__all((tag_x(gq, e0, e1) | tag_x(xq, e0, e1) | tag_x(rq, e0, e1)) == 0u)) break;
          nap(1);
        }
        const u32 gw = (lane & 2) ? gq[1] : gq[0], xw = (lane & 2) ? xq[1] : xq[0];
        gbv = (lane & 1) ? hi16(gw) : lo16(gw); x1v = (lane & 1) ? hi16(xw) : lo16(xw);
        h2 = b2f(*(const u16*)(lds + fo[4])); rsel = __uint_as_float(rq[0]);
        asm volatile("" : "+v"(gbv), "+v"(x1v), "+v"(h2), "+v"(rsel));
      }
      WAITV(0);
      u32x4 y[3] = {};
      _Pragma("nounroll") for (int sp = 0; sp < SPIN_CAP; ++sp) {
        y[0] = ld128(rs, fo[3]); y[1] = ld128(rs, fo[3] + 8192);
        if (wave < 2) y[2] = ld128(rs, fo[3] + 16384);
        if (__all(mxu(mxu(mx4(y[0]), mx4(y[1])), mx4(y[2])) != 0xffffffffu)) break;
        nap(1);
      }
      TL(16);
      f32x4 s;
#pragma unroll
      for (int k = 0; k < 4; ++k) s[k] = xsum32(xsum16((__uint_as_float(y[0][k]) + __uint_as_float(y[1][k])) + __uint_as_float(y[2][k])));
      if (lane < 16) *(f32x4*)(lds + L_C + (wave * 16 + lane) * 16) = s;
      lds_barrier();
      if (wave == 7) {
        float t[8];
#pragma unroll
        for (int ww = 0; ww < 8; ++ww) t[ww] = *(const float*)(lds + fo[5] + ww * 256);
        SCHED();
        const float y2 = rbf(rbf(((t[0] + t[1]) + (t[2] + t[3])) + ((t[4] + t[5]) + (t[6] + t[7]))) * rsel);
        const float res = x1v + rbf(gbv * rbf(h2 + y2));
        __builtin_amdgcn_raw_buffer_store_b16((u16)(cvt2(res, 0.f) & 0xffffu), __builtin_amdgcn_make_buffer_rsrc(out, 0, 8 * D * 2, 0x00020000), fo[6], 0, 0);
      }
      TL(17);
    }
  }
  {
  int t3_ = threadIdx.x; asm volatile("" : "+v"(t3_));
  const int tid = t3_, lane = tid & 63, n = lane & 15, grp = lane >> 4;
  if (qs >= 0) {
    const u16* mat = (wave & 1) ? wg : wu;
    const int g0 = ((3 - (int)((uintptr_t)mat >> 7) - xcd * 9) & 7) + (wave >= 4 ? 8 : 0);
    const int r = (idx - 6) * 72 + ((wave >> 1) & 1) * 36 + min(lane, 35);
    if (g0 < 9) dma_keep(mat, r * 9216 + (xcd * 9 + g0) * 128, lds_base + L_W + wave * 256);
  }
  if (ml >= 0) {
    BAR();
    if (mw < 18) slab_dma<0, 4>(w_mod + ml * 64, NMOD * 2, lds_base + L_W, wave, lane);
    slab_dma<4, 9>(w_mod + ml * 64, NMOD * 2, lds_base + L_W, wave, lane);
    u32x4 wf[9], af[9];
    img_frags(lds, L_IMG, lane, af);
    asm volatile("s_waitcnt vmcnt(0)\n\ts_barrier" ::: "memory");
    slab_frags(lds, L_W, wave, lane, wf);
    const f32x4 s = half_sum(gemv9(wf, af));
    if (n < 8 && grp < 2) *(u32x2*)(lds + L_TILE + n * 128 + wave * 16 + grp * 8) = u32x2{cvt2(s[0], s[1]), cvt2(s[2], s[3])};
    lds_barrier();
    if (wave < 2) {
      const u32x2 t2 = *(const u32x2*)(lds + L_TILE + tid * 8);
      const int ch = (ml - 18) / 9, sl = ml - 18 - ch * 9;
      st128(rs, W_M + ((ch * 8 + (tid >> 4)) * 144 + sl * 16 + n) * 16, u32x4{t2[0], t2[1], e0, e1});
    }
    TL(18);
  }

  if (lnb >= 0 && wave < 3) {
    const int b = lnb, g = tid < 144 ? tid : 143;
    const bool on = tid < 144;
    float* ex = (float*)(lds + L_TILE);
    const u32x2 xa = __builtin_amdgcn_raw_buffer_load_b64(__builtin_amdgcn_make_buffer_rsrc((void*)x, 0, 8 * D * 2, 0x00020000), (b * D + g * 4) * 2, 0, 0);
    const u32x2 wra = __builtin_amdgcn_raw_buffer_load_b64(__builtin_amdgcn_make_buffer_rsrc((void*)w_r, 0, D * 2, 0x00020000), g * 8, 0, 0);
    u32x4 mm[3];
    _Pragma("nounroll") for (int sp = 0; sp < SPIN_CAP; ++sp) {
#pragma unroll
      for (int k = 0; k < 3; ++k) mm[k] = ld128(rs, W_M + ((k * 8 + b) * 144 + g) * 16);
      if (__all((tag_x(mm[0], e0, e1) | tag_x(mm[1], e0, e1) | tag_x(mm[2], e0, e1)) == 0u)) break;
      nap(4);
    }
    f32x2 xv[2], ga[2], sb[2], cb1[2], wr[2];
#pragma unroll
    for (int i = 0; i < 2; ++i) {
      xv[i] = up2(xa[i]); wr[i] = up2(wra[i]); ga[i] = up2(mm[0][i]); sb[i] = up2(mm[1][i]); cb1[i] = rbf2(f32x2{1.f, 1.f} + up2(mm[2][i]));
      asm volatile("" : "+v"(xv[i]), "+v"(ga[i]), "+v"(sb[i]), "+v"(cb1[i]), "+v"(wr[i]));
    }
    int ho = W_H2 + (b * 144 + g) * 16, xo = W_X1 + (b * 152 + g) * 16;
    asm volatile("" : "+v"(ho), "+v"(xo));
    u32x4 xp[8];
    _Pragma("nounroll") for (int sp = 0; sp < SPIN_CAP; ++sp) {
#pragma unroll
      for (int p = 0; p < 8; ++p) xp[p] = ld128(rs, W_XP + (p * 8 + b) * 2304 + g * 16);
      if (__all(mxu(mxu(mxu(mx4(xp[0]), mx4(xp[1])), mxu(mx4(xp[2]), mx4(xp[3]))), mxu(mxu(mx4(xp[4]), mx4(xp[5])), mxu(mx4(xp[6]), mx4(xp[7])))) != 0xffffffffu)) break;
      nap(1);
    }
    TL(10);
    f32x2 v[2];
#pragma unroll
    for (int i = 0; i < 2; ++i) {
      f32x2 pp[8];
#pragma unroll
      for (int p = 0; p < 8; ++p) pp[p] = f32x2{__uint_as_float(xp[p][2 * i]), __uint_as_float(xp[p][2 * i + 1])};
      v[i] = rbf2(xv[i] + rbf2(ga[i] * rbf2(((pp[0] + pp[1]) + (pp[2] + pp[3])) + ((pp[4] + pp[5]) + (pp[6] + pp[7])))));
    }
    const f32x2 s2 = v[0] + v[1];
    const float mw = wsum(on ? s2.x + s2.y : 0.f) * (wave < 2 ? 1.f / 256 : 1.f / 64);
    const f32x2 t0 = v[0] - f32x2{mw, mw}, t1 = v[1] - f32x2{mw, mw}, q2 = t0 * t0 + t1 * t1;
    const float m2 = wsum(on ? q2.x + q2.y : 0.f);
    if (lane == 0) *(f32x2*)(ex + wave * 2) = f32x2{mw, m2};
    lds_barrier();
    const f32x4 e01 = *(const f32x4*)ex; const f32x2 e2 = *(const f32x2*)(ex + 4);
    const float mu = (e01[0] + e01[2]) * (256.f / D) + e2.x * (64.f / D);
    const float d0 = e01[0] - mu, d1 = e01[2] - mu, d2 = e2.x - mu;
    const float var = rbf(((e01[1] + e01[3]) + e2.y + 256.f * (d0 * d0 + d1 * d1) + 64.f * d2 * d2) * (1.f / D));
    const float mean = rbf(mu), rden = frcp(rbf(sqrtf(rbf(var + 1e-5f))));
    u32 hw[2];
#pragma unroll
    for (int i = 0; i < 2; ++i) {
      const f32x2 t = rbf2(rbf2(rbf2(v[i] - f32x2{mean, mean}) * f32x2{rden, rden}) * cb1[i]) + sb[i];
      hw[i] = cvt2(t.x, t.y);
    }
    if (on) {
      st128(rs, ho, u32x4{hw[0], hw[1], e0, e1});
      st128(rs, xo, u32x4{cvt2(v[0].x, v[0].y), cvt2(v[1].x, v[1].y), e0, e1});
    }
    TL(11);
    const f32x2 r2 = up2(hw[0]) * wr[0] + up2(hw[1]) * wr[1];
    const float rw = wsum(on ? r2.x + r2.y : 0.f);
    if (lane == 0) ex[8 + wave] = rw;
    lds_barrier();
    if (tid < 8) {
      const float r = rbf((ex[8] + ex[9]) + ex[10]);
      st128(rs, W_X1 + (b * 152 + 144 + tid) * 16, u32x4{__float_as_uint(rbf(1.f / (1.f + __expf(-r)))), 0u, e0, e1});
    }
  }

  if (oj >= 0) {
    const int ob = W_O + ((op * 2 + (wave >> 2)) * 8 + (lane >> 3)) * 2048 + (wave & 3) * 256 + (lane & 7) * 16;
    u32x4 a[4];
#pragma unroll
    for (int p = 0; p < 4; ++p) a[p] = ld128(rs, ob + (p >> 1) * 1024 + (p & 1) * 128);
    SCHED();
    u32x4 wf[8];
    {
      const int q = n >> 2, p = lane & 3, hf = p >> 1;
      const int a0 = L_W + (wave * 64 + hf * 32 + 8 * grp + q) * 128 + (((q >> 1) ^ ((grp & 1) * 2) ^ (hf * 4)) << 4) + (p & 1) * 8;
#pragma unroll
      for (int cg = 0; cg < 8; ++cg) {
        const u32x2 lo = trd(lds, a0 ^ (cg * 16)), hi = trd(lds, (a0 ^ (cg * 16)) + 512);
        wf[cg] = u32x4{lo[0], lo[1], hi[0], hi[1]};
      }
    }
    int rw = L_OI + (n & 7) * 4128 + (wave * 2 + (n >> 3)) * 256 + (grp & 1) * 16, rr = L_OI + wave * 4128 + grp * 1024 + n * 16;
    int xo = W_XP + (op * 8 + wave) * 2304 + oj * 256 + lane * 16, sl = ((n & 7) * 8 + (n >> 3) * 4 + grp) * 4;
#pragma unroll
    for (int cg = 0; cg < 8; ++cg) asm volatile("" : "+v"(wf[cg]));
    asm volatile("" : "+v"(rw), "+v"(rr), "+v"(xo), "+v"(sl));
    _Pragma("nounroll") for (int sp = 0; sp < SPIN_CAP; ++sp) {
      const bool m0 = mxu(mx4(a[0]), mx4(a[1])) == 0xffffffffu, m1 = mxu(mx4(a[2]), mx4(a[3])) == 0xffffffffu;
      if (!__any(m0 || m1)) break;
      nap(2);
      if (m0) { a[0] = ld128(rs, ob); a[1] = ld128(rs, ob + 128); }
      if (m1) { a[2] = ld128(rs, ob + 1024); a[3] = ld128(rs, ob + 1152); }
    }
    TL(8);
#define OS(p, j) (__uint_as_float(a[p][j]) + __uint_as_float(a[(p) + 2][j]))
#define OP(v) (u32)__builtin_amdgcn_ds_bpermute(sl, (int)(v))
    const u32x4 of = {OP(cvt2(OS(0, 0), OS(0, 1))), OP(cvt2(OS(0, 2), OS(0, 3))), OP(cvt2(OS(1, 0), OS(1, 1))), OP(cvt2(OS(1, 2), OS(1, 3)))};
    f32x4 acc[8];
#pragma unroll
    for (int cg = 0; cg < 8; ++cg) acc[cg] = mfma(wf[cg], of, f32x4{0.f, 0.f, 0.f, 0.f});
    if ((n >> 3) == (grp >> 1)) {
#pragma unroll
      for (int cg = 0; cg < 8; ++cg) *(f32x4*)(lds + rw + cg * 32) = acc[cg];
    }
    lds_barrier();
    f32x4 s = (*(const f32x4*)(lds + rr) + *(const f32x4*)(lds + rr + 256)) + (*(const f32x4*)(lds + rr + 512) + *(const f32x4*)(lds + rr + 768));
#pragma unroll
    for (int k = 0; k < 4; ++k) s[k] = xsum32(xsum16(s[k]));
    if (lane < 16) st128(rs, xo, u32x4{sanf(s[0]), sanf(s[1]), sanf(s[2]), sanf(s[3])});
    TL(9);
  }
  TLOUT();
  }
  }
}

extern "C" void kernel_launch(void* const* d_in, const int* in_sizes, int n_in, void* d_out, int out_size, void* d_ws, size_t ws_size,
                              hipStream_t stream) {
  fused_kernel<<<NWG, NT, 0, stream>>>((const u16*)d_in[0], (const u16*)d_in[1], (const u16*)d_in[2], (const u16*)d_in[3], (const u16*)d_in[4],
                                       (const u16*)d_in[6], (const u16*)d_in[7], (const u16*)d_in[8], (const u16*)d_in[9], (const u16*)d_in[10],
                                       (const u16*)d_in[11], (unsigned char*)d_ws, (u16*)d_out);
}
```

```cpp
#include <hip/hip_runtime.h>
#include <hip/hip_bf16.h>
#include <cstdint>

typedef unsigned u32;
typedef unsigned short u16;
typedef u32 u32x4 __attribute__((ext_vector_type(4)));
typedef u32 u32x2 __attribute__((ext_vector_type(2)));
typedef float f32x4 __attribute__((ext_vector_type(4)));
typedef float f32x2 __attribute__((ext_vector_type(2)));
typedef short s16x4 __attribute__((ext_vector_type(4)));
typedef __bf16 bf16x8 __attribute__((ext_vector_type(8)));
typedef __bf16 bf16x2 __attribute__((ext_vector_type(2)));
typedef __attribute__((address_space(3))) unsigned char lds_t;

constexpr int D = 576, SK = 8192, WIN = 2048, FF = 4608, NQ = 4096, NMOD = 6 * D;
constexpr int NWG = 256, NT = 512, NG = 72;
constexpr int SPIN_CAP = 1 << 15;

constexpr int W_O = 0;
constexpr int W_XP = W_O + 262144;
constexpr int W_Y = W_XP + 147456;
constexpr int W_MS = W_Y + NG * NG * 256;
constexpr int W_FL = W_MS;
constexpr int W_H = W_FL + 2048;
constexpr int W_M = W_H + 36864;
constexpr int W_Q = W_M + 73728;
constexpr int W_ST = W_Q + 131072;
constexpr int W_H2 = W_ST + 32768;
constexpr int W_X1 = W_H2 + 18432;
constexpr int W_END = W_X1 + 8 * 152 * 16;
constexpr int NPZ = 174, PZ = (W_MS / 16 + NPZ - 1) / NPZ;
static_assert(PZ <= 2 * NT, "poison split");

constexpr int L_W = 0;
constexpr int L_IMG = 73728;
constexpr int L_TILE = L_IMG + 9216;
constexpr int L_OI = 65536;
constexpr int L_B = 147456;
constexpr int L_C = 155648;
constexpr int LDS_BYTES = 163840;

#define SCHED() __builtin_amdgcn_sched_barrier(0)
#define BAR() asm volatile("s_barrier" ::: "memory")
#define TL(i) do { if ((i) == 17) { SCHED(); amp_ = (u32)__builtin_amdgcn_s_memrealtime(); SCHED(); } } while (0)
#define TLOUT() do { if (amp_ != 0u) { const int lim_ = 4000 + 10 * max(0, min((int)(amp_ - amp0_) - 2000, 2000)); _Pragma("nounroll") while ((int)((u32)__builtin_amdgcn_s_memrealtime() - amp0_) < lim_) __builtin_amdgcn_s_sleep(4); } } while (0)
#define WAITV_(n) do { asm volatile("s_waitcnt vmcnt(" #n ")" ::: "memory"); SCHED(); } while (0)
#define WAITV(n) WAITV_(n)
#define OF 198
#define OL 150
#define OO 173
#define OQ 173
#define EP 4
#define CPW 0
#define CPM 0
#define QL 6
#define QN 6
#define DK 5
#define DV DK
#define PR 115
#define PR2 111
#define PR3 111

__device__ __forceinline__ float lo16(u32 u) { return __uint_as_float(u << 16); }
__device__ __forceinline__ float hi16(u32 u) { return __uint_as_float(u & 0xffff0000u); }
__device__ __forceinline__ float b2f(u16 u) { return __uint_as_float((u32)u << 16); }
__device__ __forceinline__ u32 cvt2(float a, float b) { return __builtin_bit_cast(u32, __builtin_convertvector(f32x2{a, b}, bf16x2)); }
__device__ __forceinline__ float rbf(float v) { return __uint_as_float(cvt2(0.f, v)); }
__device__ __forceinline__ u32 sanf(float f) { u32 v = __float_as_uint(f); return v < 0xfffffffeu ? v : 0xfffffffeu; }
__device__ __forceinline__ f32x2 up2(u32 u) { return f32x2{lo16(u), hi16(u)}; }
__device__ __forceinline__ f32x2 rbf2(f32x2 v) { return f32x2{rbf(v.x), rbf(v.y)}; }
__device__ __forceinline__ void lds_barrier() { asm volatile("s_waitcnt lgkmcnt(0)\n\ts_barrier" ::: "memory"); }
__device__ __forceinline__ f32x4 mfma(u32x4 a, u32x4 b, f32x4 c) {
  return __builtin_amdgcn_mfma_f32_16x16x32_bf16(__builtin_bit_cast(bf16x8, a), __builtin_bit_cast(bf16x8, b), c, 0, 0, 0);
}
__device__ __forceinline__ u32 plo(u32 a, u32 b) { return __builtin_amdgcn_perm(b, a, 0x05040100u); }
__device__ __forceinline__ u32 phi(u32 a, u32 b) { return __builtin_amdgcn_perm(b, a, 0x07060302u); }

extern "C" __device__ __attribute__((const)) unsigned long long llvm_dispatch_id() __asm("llvm.amdgcn.dispatch.id");
typedef __amdgpu_buffer_rsrc_t rsrc_t;
__device__ __forceinline__ u32x4 ld128(rsrc_t rs, int off) { return __builtin_amdgcn_raw_buffer_load_b128(rs, off, 0, 16); }
__device__ __forceinline__ void st128(rsrc_t rs, int off, u32x4 v) { __builtin_amdgcn_raw_buffer_store_b128(v, rs, off, 0, 16); }
__device__ __forceinline__ void st64(rsrc_t rs, int off, u32x2 v) { __builtin_amdgcn_raw_buffer_store_b64(v, rs, off, 0, 16); }
__device__ __forceinline__ void dma(const void* gbase, u32 voff, unsigned lds_dst) {
  unsigned keep;
  asm volatile("s_mov_b32 %0, m0\n\ts_mov_b32 m0, %3\n\ts_nop 2\n\tglobal_load_lds_dwordx4 %1, %2 nt\n\ts_mov_b32 m0, %0"
               : "=&s"(keep) : "v"(voff), "s"(gbase), "s"(lds_dst) : "memory");
}
__device__ __forceinline__ void dma_keep(const void* gbase, u32 voff, unsigned lds_dst) {
  unsigned keep;
  asm volatile("s_mov_b32 %0, m0\n\ts_mov_b32 m0, %3\n\ts_nop 2\n\tglobal_load_lds_dword %1, %2\n\ts_mov_b32 m0, %0"
               : "=&s"(keep) : "v"(voff), "s"(gbase), "s"(lds_dst) : "memory");
}
__device__ __forceinline__ void dma_sc(const void* gbase, u32 voff, unsigned lds_dst) {
  unsigned keep;
  asm volatile("s_mov_b32 %0, m0\n\ts_mov_b32 m0, %3\n\ts_nop 2\n\tglobal_load_lds_dwordx4 %1, %2 sc1\n\ts_mov_b32 m0, %0"
               : "=&s"(keep) : "v"(voff), "s"(gbase), "s"(lds_dst) : "memory");
}
__device__ __forceinline__ void ald128(u32x4& d, const void* gbase, u32 voff) { unsigned long long t; asm volatile("s_mov_b64 %1, %3\n\tglobal_load_dwordx4 %0, %2, %1" : "=v"(d), "=&s"(t) : "v"(voff), "s"(gbase) : "memory"); }
__device__ __forceinline__ void ald64(u32x2& d, const void* gbase, u32 voff) { unsigned long long t; asm volatile("s_mov_b64 %1, %3\n\tglobal_load_dwordx2 %0, %2, %1" : "=v"(d), "=&s"(t) : "v"(voff), "s"(gbase) : "memory"); }
__device__ __forceinline__ void ald64nt(u32x2& d, const void* gbase, u32 voff) { unsigned long long t; asm volatile("s_mov_b64 %1, %3\n\tglobal_load_dwordx2 %0, %2, %1 nt" : "=v"(d), "=&s"(t) : "v"(voff), "s"(gbase) : "memory"); }
__device__ __forceinline__ void ald128sc(u32x4& d, const void* gbase, u32 voff) { unsigned long long t; asm volatile("s_mov_b64 %1, %3\n\tglobal_load_dwordx4 %0, %2, %1 sc1" : "=v"(d), "=&s"(t) : "v"(voff), "s"(gbase) : "memory"); }
__device__ __forceinline__ u32x2 trd(const unsigned char* lds, int off) {
  return __builtin_bit_cast(u32x2, __builtin_amdgcn_ds_read_tr16_b64_v4i16((__attribute__((address_space(3))) s16x4*)((lds_t*)lds + off)));
}
#define nap(n) do { __builtin_amdgcn_s_sleep(n); asm volatile("" ::: "memory"); } while (0)
__device__ __forceinline__ float frcp(float v) { return __builtin_amdgcn_rcpf(v); }
__device__ __forceinline__ float fdiv(float a, float d, float r) { const float q = a * r; return fmaf(fmaf(-q, d, a), r, q); }
__device__ __forceinline__ float silu(float v) { const float d = 1.f + __expf(-v); const float r = frcp(d); return d < 3.0e38f ? fdiv(v, d, r) : v * r; }
template <int CTRL> __device__ __forceinline__ float dppf(float x) {
  return __builtin_bit_cast(float, __builtin_amdgcn_mov_dpp(__builtin_bit_cast(int, x), CTRL, 0xf, 0xf, true));
}
__device__ __forceinline__ u32 mxu(u32 a, u32 b) { return a > b ? a : b; }
__device__ __forceinline__ u32 mx4(u32x4 v) { return mxu(mxu(v[0], v[1]), mxu(v[2], v[3])); }
__device__ __forceinline__ float rl(float v, int l) { return __builtin_bit_cast(float, __builtin_amdgcn_readlane(__builtin_bit_cast(int, v), l)); }
__device__ __forceinline__ float wsum(float v) {
  v += dppf<0xB1>(v); v += dppf<0x4E>(v); v += dppf<0x141>(v); v += dppf<0x140>(v);
  return (rl(v, 0) + rl(v, 16)) + (rl(v, 32) + rl(v, 48));
}
__device__ __forceinline__ float up32(float v) { const u32 u = __float_as_uint(v); return __uint_as_float(__builtin_amdgcn_permlane32_swap(u, u, false, false)[1]); }
__device__ __forceinline__ float xsum32(float v) { const u32 u = __float_as_uint(v); const auto r = __builtin_amdgcn_permlane32_swap(u, u, false, false); return __uint_as_float(r[0]) + __uint_as_float(r[1]); }
__device__ __forceinline__ float xsum16(float v) { const u32 u = __float_as_uint(v); const auto r = __builtin_amdgcn_permlane16_swap(u, u, false, false); return __uint_as_float(r[0]) + __uint_as_float(r[1]); }
__device__ __forceinline__ f32x4 half_sum(f32x4 d) {
  f32x4 r;
#pragma unroll
  for (int j = 0; j < 4; ++j) r[j] = d[j] + dppf<0x108>(up32(d[j]));
  return r;
}
__device__ __forceinline__ f32x2 half_sum2(f32x4 d) {
  f32x2 r;
#pragma unroll
  for (int j = 0; j < 2; ++j) {
    const auto s = __builtin_amdgcn_permlane32_swap(__float_as_uint(d[j]), __float_as_uint(d[j + 2]), false, false);
    r[j] = __uint_as_float(s[0]) + dppf<0x108>(__uint_as_float(s[1]));
  }
  return r;
}
__device__ __forceinline__ void unpack8(u32x4 u, float* f) {
#pragma unroll
  for (int j = 0; j < 4; ++j) { f[2 * j] = lo16(u[j]); f[2 * j + 1] = hi16(u[j]); }
}
__device__ __forceinline__ void ln_row(const float (&v)[16], bool has2, float& mean, float& den) {
  float s = 0.f;
#pragma unroll
  for (int i = 0; i < 8; ++i) s += v[i];
  if (has2) {
#pragma unroll
    for (int i = 8; i < 16; ++i) s += v[i];
  }
  s = wsum(s);
  const float mu = s * (1.f / D);
  mean = rbf(mu);
  float q = 0.f;
#pragma unroll
  for (int i = 0; i < 8; ++i) { const float t = v[i] - mu; q = fmaf(t, t, q); }
  if (has2) {
#pragma unroll
    for (int i = 8; i < 16; ++i) { const float t = v[i] - mu; q = fmaf(t, t, q); }
  }
  q = wsum(q);
  const float var = rbf(q * (1.f / D));
  den = rbf(sqrtf(rbf(var + 1e-5f)));
}
template <int J0 = 0, int J1 = 9>
__device__ __forceinline__ void slab_dma(const u16* base, int stride_b, unsigned lds_dst, int wave, int lane) {
  const u32 v0 = (lane >> 3) * stride_b + (((lane & 7) ^ ((wave & 1) * 4 + (lane >> 4))) << 4), v1 = v0 ^ 32u;
  const u32 v4 = wave >= 4 ? v1 : v0;
  const char* gb = (const char*)base + (size_t)wave * 8 * stride_b;
#pragma unroll
  for (int j = J0; j < J1; ++j) dma(gb + (size_t)j * 64 * stride_b, j < 4 ? v0 : j == 4 ? v4 : v1, lds_dst + (j * 8 + wave) * 1024);
}
__device__ __forceinline__ void slab_frags(const unsigned char* lds, int base, int wave, int lane, u32x4 (&wf)[9]) {
  const int g = lane >> 4, q = (lane & 15) >> 2, p = lane & 3, hf = p >> 1;
  const int a0 = base + (hf * 288 + 8 * g + q) * 128 + ((wave ^ ((g & 1) * 4 + (q >> 1)) ^ (hf * 2)) << 4) + (p & 1) * 8, a1 = (a0 ^ 32) + 512;
#pragma unroll
  for (int s = 0; s < 9; ++s) {
    const u32x2 lo = trd(lds, a0 + s * 4096), hi = trd(lds, a1 + s * 4096);
    wf[s] = u32x4{lo[0], lo[1], hi[0], hi[1]};
  }
}
__device__ __forceinline__ void img_frags(const unsigned char* lds, int img, int lane, u32x4 (&af)[9]) {
#pragma unroll
  for (int s = 0; s < 9; ++s) af[s] = *(const u32x4*)(lds + img + (s * 64 + lane) * 16);
}
__device__ __forceinline__ f32x4 gemv9(const u32x4 (&wf)[9], const u32x4 (&af)[9]) {
  f32x4 acc[3];
  SCHED();
#pragma unroll
  for (int s = 0; s < 9; ++s) acc[s % 3] = mfma(wf[s], af[s], s < 3 ? f32x4{0.f, 0.f, 0.f, 0.f} : acc[s % 3]);
  return (acc[0] + acc[1]) + acc[2];
}
__device__ __forceinline__ int img_off(int b, int k8) {
  const int hf = k8 >= 36 ? 1 : 0, kk = k8 - hf * 36;
  return ((kk >> 2) * 64 + (kk & 3) * 16 + hf * 8 + b) * 16;
}
__device__ __forceinline__ u32 tag_x(u32x4 v, u32 e0, u32 e1) { return (v[2] ^ e0) | (v[3] ^ e1); }
__device__ __forceinline__ bool tag_ok(u32x4 v, u32 e0, u32 e1) { return tag_x(v, e0, e1) == 0u; }

constexpr int CODE_BYTES = 31140;

__global__ void __launch_bounds__(NT, 2) fused_kernel(const u16* __restrict__ x, const u16* __restrict__ kc, const u16* __restrict__ vc,
                                                      const u16* __restrict__ wq, const u16* __restrict__ wo, const u16* __restrict__ w_r,
                                                      const u16* __restrict__ wg, const u16* __restrict__ wu, const u16* __restrict__ wd,
                                                      const u16* __restrict__ c, const u16* __restrict__ w_mod, unsigned char* ws, u16* out) {
  __shared__ __attribute__((aligned(16))) unsigned char lds[LDS_BYTES];
  const int w = blockIdx.x, xcd = w & 7, idx = w >> 3;
  const int tid = threadIdx.x, lane = tid & 63, wave = __builtin_amdgcn_readfirstlane(tid >> 6);
  const int n = lane & 15, grp = lane >> 4;
  const rsrc_t rs = __builtin_amdgcn_make_buffer_rsrc(ws, 0, W_END, 0x00020000);
  const unsigned lds_base = (unsigned)(uintptr_t)(lds_t*)lds;
  {
    unsigned long long pc0; asm volatile("s_getpc_b64 %0" : "=s"(pc0));
    if (w >= 224 && wave == CPW) dma_keep((const void*)pc0, min((w >> 3) * 8192 - 28 * 8192 + lane * 128, CODE_BYTES - 128), lds_base + 107520);
    SCHED();
  }
  u32 amp_ = 0u; const u32 amp0_ = (u32)__builtin_amdgcn_s_memrealtime();
  const unsigned long long epoch = (llvm_dispatch_id() + 1ull) * 0x9E3779B97F4A7C15ull;
  const u32 e0 = (u32)epoch, e1 = (u32)(epoch >> 32);
  asm volatile("" : "+s"(vc), "+s"(w_r), "+s"(wu), "+s"(wd), "+s"(out), "+s"(x), "+s"(w_mod), "+s"(wg), "+s"(wq), "+s"(wo));

  const int mw = idx < 6 ? idx * 8 + xcd : -1;
  const int md = mw < 18 ? mw : -1;
  const int ml = mw >= 18 ? mw : mw >= 0 && mw < 6 ? 48 + mw : -1;
  const int lnb = mw >= 6 && mw < 14 ? mw - 6 : -1;
  const int qs = idx >= 6 && idx < 14 ? xcd * 8 + idx - 6 : -1;
  const int oj = idx >= 14 && idx < 23 ? idx - 14 : -1, op = xcd;
  const int gs = idx >= 23 ? xcd * 9 + idx - 23 : -1;

  const int s_att = w & 1, kvh = (w >> 1) & 15, b_att = w >> 5;
  {
    const size_t pairbase = ((size_t)(b_att * 16 + kvh) * SK + (SK - WIN)) * 256;
    const char* kp = (const char*)kc + pairbase;
    const char* vp = (const char*)vc + pairbase;
    const int wpart = (wave & 3) * 4096 + s_att * 16384 + (wave >> 2) * 262144;
    const int ko = wpart + (n >> 1) * 256 + (n & 1) * 64 + grp * 16;
    const int vo = wpart + n * 16 + grp * 512;
#define KLD(u) do { unsigned long long t_; asm volatile("s_mov_b64 %1, %3\n\tglobal_load_dwordx4 %0, %2, %1 offset:%4 nt" : "=v"(kb[u]), "=&s"(t_) : "v"(ko), "s"(kp + ((u) >> 2) * 32768), "i"((((u) >> 1) & 1) * 2048 + ((u) & 1) * 128) : "memory"); } while (0)
#define VLD(u) do { unsigned long long t_; asm volatile("s_mov_b64 %1, %3\n\tglobal_load_dwordx4 %0, %2, %1 offset:%4 nt" : "=v"(vb[u]), "=&s"(t_) : "v"(vo), "s"(vp + ((u) >> 2) * 32768), "i"((((u) >> 1) & 1) * 2048 + ((u) & 1) * 256) : "memory"); } while (0)
#define TIE8(a, o) asm volatile("" : "+v"(a[o]), "+v"(a[(o) + 1]), "+v"(a[(o) + 2]), "+v"(a[(o) + 3]), "+v"(a[(o) + 4]), "+v"(a[(o) + 5]), "+v"(a[(o) + 6]), "+v"(a[(o) + 7]))
    const int pr_ = gs >= 0 ? PR2 : mw >= 18 ? PR3 : PR;
    const u32 tp0 = (u32)__builtin_amdgcn_s_memrealtime() + (gs >= 0 ? OF : mw >= 18 ? OL : oj >= 0 ? OO : OQ);
#define PACE(i) do { _Pragma("nounroll") while ((int)((u32)__builtin_amdgcn_s_memrealtime() - tp0) < (i) * pr_ / 4) __builtin_amdgcn_s_sleep(2); SCHED(); } while (0)
#define REP4(M, u, W) do { M(u); W; M((u) + 1); W; M((u) + 2); W; M((u) + 3); W; } while (0)
    static_assert(DK <= 7 && DV <= 7, "the hand-placed waits assume it");
    u32x4 kb[32], vb[32];
    const int pz = mw >= 18 ? mw - 18 : idx >= 14 ? w - 82 : -1;
#define POISON() do { if (pz >= 0) { _Pragma("unroll") for (int i = 0; i < 2; ++i) { const int g_ = pz * PZ + i * NT + tid; \
      if (i * NT + tid < PZ && g_ < W_MS / 16) st128(rs, g_ * 16, u32x4{0xffffffffu, 0xffffffffu, 0xffffffffu, 0xffffffffu}); } } SCHED(); } while (0)
#define PFLAG() do { if (pz >= 0 && tid == 448) st64(rs, W_FL + pz * 8, u32x2{e0, e1}); SCHED(); } while (0)
#define SILU_IMAGE(c0, c1) do { float f[8]; unpack8(c0, f); u32x4 o; \
        _Pragma("unroll") for (int j = 0; j < 4; ++j) o[j] = cvt2(silu(f[2 * j]), silu(f[2 * j + 1])); \
        const int b_ = tid / 72, ch_ = tid - b_ * 72; \
        *(u32x4*)(lds + L_IMG + img_off(b_, ch_)) = o; \
        if (wave == 0) { unpack8(c1, f); \
          _Pragma("unroll") for (int j = 0; j < 4; ++j) o[j] = cvt2(silu(f[2 * j]), silu(f[2 * j + 1])); \
          *(u32x4*)(lds + L_IMG + img_off(7, 8 + lane)) = o; } } while (0)
    if (md >= 0) {
      u32x4 c0, c1, x0, x1; u32x2 xc;
      __builtin_amdgcn_s_waitcnt(0x0f70);
      int ho = W_H + img_off(n & 7, (md >= 9 ? md - 9 : md) * 8 + wave) * 2 + (grp & 1) * 16;
      ald128(c0, c, tid * 16);
      ald128(c1, c, (512 + lane) * 16);
      ald128(x0, x, wave * 1152 + lane * 16);
      ald128(x1, x, wave * 1152 + 1024 + (lane & 7) * 16);
      ald64(xc, x, (n & 7) * 1152 + ((md >= 9 ? md - 9 : 0) * 64 + wave * 8 + (grp & 1) * 4) * 2);
      TL(35);
      BAR();
      float* st = (float*)(lds + L_TILE + 1024);
      if (wave < 4) {
        slab_dma(w_mod + md * 64, NMOD * 2, lds_base + L_W, wave, lane);
        asm volatile("s_waitcnt vmcnt(9)" ::: "memory");
      } else asm volatile("s_waitcnt vmcnt(0)" ::: "memory");
      asm volatile("s_nop 0 ; both branches above cover V" : "+v"(c0), "+v"(c1), "+v"(x0), "+v"(x1), "+v"(xc) :: "memory");
      {
        SILU_IMAGE(c0, c1);
        float v[16]; unpack8(x0, v); unpack8(x1, v + 8);
        float mean, den; ln_row(v, lane < 8, mean, den);
        if (lane == 0) { st[wave] = mean; st[8 + wave] = den; }
      }
      if (wave >= 4) slab_dma(w_mod + md * 64, NMOD * 2, lds_base + L_W, wave, lane);
      lds_barrier();
      REP4(KLD, 0, );
      u32x4 wf[9], af[9];
      img_frags(lds, L_IMG, lane, af);
      const float mean = st[n & 7], rd = frcp(st[8 + (n & 7)]);
      float ln[4] = {rbf(rbf(lo16(xc[0]) - mean) * rd), rbf(rbf(hi16(xc[0]) - mean) * rd), rbf(rbf(lo16(xc[1]) - mean) * rd), rbf(rbf(hi16(xc[1]) - mean) * rd)};
#pragma unroll
      for (int s = 0; s < 9; ++s) asm volatile("" : "+v"(af[s]));
      asm volatile("" : "+v"(ln[0]), "+v"(ln[1]), "+v"(ln[2]), "+v"(ln[3]), "+v"(ho));
      WAITV(4); BAR(); SCHED();
      slab_frags(lds, L_W, wave, lane, wf);
      const f32x4 s = half_sum(gemv9(wf, af));
      f32x4 r = s;
      if (md >= 9) {
#pragma unroll
        for (int j = 0; j < 4; ++j) r[j] = ln[j] * rbf(1.f + rbf(s[j]));
      }
      if (n < 8 && grp < 2) st128(rs, ho + (md >= 9 ? 18432 : 0), u32x4{cvt2(r[0], r[1]), cvt2(r[2], r[3]), e0, e1});
      SCHED();
      TL(1);
      BAR();
      KLD(4); KLD(5); WAITV(DK);
      KLD(6); WAITV(DK); KLD(7); WAITV(DK); KLD(8); WAITV(DK); KLD(9); WAITV(DK); KLD(10); WAITV(DK);
      KLD(11); WAITV(DK); REP4(KLD, 12, WAITV(DK)); REP4(KLD, 16, WAITV(DK));
      REP4(KLD, 20, WAITV(DK)); KLD(24); WAITV(DK); KLD(25);
    } else {
      constexpr int L_LAND = 98304;
      const unsigned land = lds_base + L_LAND + wave * 6144;
      const unsigned char* lp = lds + L_LAND + wave * 6144 + lane * 16;
      const int sl_ = (3 - (int)((uintptr_t)wq >> 7)) & 7;
      const bool slowq = qs >= 0 && (qs & 7) == sl_;
      if (gs >= 0 && wave == 0) dma_keep((const char*)wq + (size_t)(idx - 23) * 64 * 8192 + xcd * 1024 + sl_ * 128, lane * 8192, land);
      if (gs >= 0 && (wave == 1 || (wave == 2 && xcd < 2))) dma_keep((const char*)w_mod + (size_t)((idx - 23) * 64 + 32) * (NMOD * 2) + xcd * 128 + (wave - 1) * 2048, (lane & 31) * (NMOD * 2) + (wave == 1 ? (lane >> 5) * 1024 : 0), land);
      POISON();
      const u32 v0 = (lane >> 3) * 1152 + (((lane & 7) ^ ((lane >> 4) & 1)) << 4);
      const char* gb = (const char*)(wo + (size_t)(op * 512 + wave * 64) * D + oj * 64);
      if (ml >= 0) { dma_sc(c, tid * 16, land); dma_sc(c, (512 + lane) * 16, land + 1024); }
#define WK(j) do { if (oj >= 0) { if ((j) < 8) dma(gb + (j) * 8 * 1152, v0 ^ (((j) & 1) * 32 + ((j) >> 2) * 64), lds_base + L_W + (wave * 8 + (j)) * 1024); } \
                   else if (slowq) { if ((j) == 8) slab_dma(wq + qs * 64, NQ * 2, lds_base + L_W, wave, lane); } \
                   else if (qs >= 0 && (j) < 9) slab_dma<(j) < 9 ? (j) : 0, (j) < 9 ? (j) + 1 : 0>(wq + qs * 64, NQ * 2, lds_base + L_W, wave, lane); \
                   else if (EP && qs < 0 && (j) >= EP) PACE(j); \
                   KLD(j); if ((j) == 0) TL(32); if ((j) == 4) TL(33); WAITV(DK); if ((j) == 5) TL(34); } while (0)
      WK(0); WK(1); WK(2); WK(3);
      WK(4); WK(5);
      WK(6); WK(7);
      TL(24);
      if (ml >= 0) { const u32x4 c0 = *(const u32x4*)lp, c1 = *(const u32x4*)(lp + 1024); SILU_IMAGE(c0, c1); }
      BAR();
      PFLAG();
      WK(8); WK(9); WK(10); WK(11);
      TL(25);
      PACE(12); REP4(KLD, 12, WAITV(DK));
      TL(26);
      const int hg0 = (wave * 144 + lane) * 16, hg2 = (wave * 144 + 128 + n) * 16;
#define HREQ() do { dma_sc(ws + W_H, hg0, land); dma_sc(ws + W_H + 18432, hg0, land + 1024); dma_sc(ws + W_H + 1024, hg0, land + 2048); \
                    dma_sc(ws + W_H + 18432 + 1024, hg0, land + 3072); dma_sc(ws + W_H, hg2, land + 4096); dma_sc(ws + W_H + 18432, hg2, land + 5120); } while (0)
#define HGET() do { _Pragma("unroll") for (int i = 0; i < 6; ++i) hq[i] = *(const u32x4*)(lp + i * 1024); } while (0)
#define HQOK() __all((tag_x(hq[0], e0, e1) | tag_x(hq[1], e0, e1) | tag_x(hq[2], e0, e1) | tag_x(hq[3], e0, e1) | tag_x(hq[4], e0, e1) | tag_x(hq[5], e0, e1)) == 0u)
#define HOK() __all((tag_x(hp[0], e0, e1) | tag_x(hp[1], e0, e1) | tag_x(hp[2], e0, e1)) == 0u)
      if (qs >= 0) HREQ();
      PACE(16); REP4(KLD, 16, WAITV(DK));
      if (qs >= 0) WAITV(4);
      TL(27);
      PACE(20); KLD(20); KLD(21);
      if (qs >= 0) {
        u32x4 hq[6]; HGET();
        _Pragma("nounroll") for (int sp = 0; sp < SPIN_CAP && !HQOK(); ++sp) { nap(16); HREQ(); WAITV(0); HGET(); }
        TL(2);
#define HSUM(a, b) cvt2(lo16(a) + lo16(b), hi16(a) + hi16(b))
        *(u32x2*)(lds + L_IMG + hg0 / 2) = u32x2{HSUM(hq[0][0], hq[1][0]), HSUM(hq[0][1], hq[1][1])};
        *(u32x2*)(lds + L_IMG + 512 + hg0 / 2) = u32x2{HSUM(hq[2][0], hq[3][0]), HSUM(hq[2][1], hq[3][1])};
        if (lane < 16) *(u32x2*)(lds + L_IMG + hg2 / 2) = u32x2{HSUM(hq[4][0], hq[5][0]), HSUM(hq[4][1], hq[5][1])};
      } else WAITV(DK);
      KLD(22); KLD(23);
      if (qs >= 0) {
        u32x4 wf[9], af[9];
        lds_barrier();
        slab_frags(lds, L_W, wave, lane, wf);
        img_frags(lds, L_IMG, lane, af);
        const f32x4 s = half_sum(gemv9(wf, af));
        if (n < 8 && grp < 2) *(u32x2*)(lds + L_TILE + n * 128 + wave * 16 + grp * 8) = u32x2{cvt2(s[0], s[1]), cvt2(s[2], s[3])};
      } else WAITV(DK);
      PACE(24); KLD(24); KLD(25);
      if (qs >= 0) {
        lds_barrier();
        if (wave < 2) {
          const u32x2 t2 = *(const u32x2*)(lds + L_TILE + tid * 8);
          st128(rs, W_Q + ((tid >> 4) * 1024 + qs * 16 + n) * 16, u32x4{t2[0], t2[1], e0, e1});
        }
        TL(3);
      }
      SCHED();
    }
    const unsigned char* qg = ws + W_Q + (b_att * 1024 + kvh * 64) * 16;
    KLD(26); WAITV(DK); KLD(27); WAITV(DK); PACE(28); KLD(28); WAITV(DK); KLD(29); WAITV(DK);
    PACE(30); KLD(30); WAITV(DK); KLD(31); WAITV(DK);
    TL(28);
    PACE(32); VLD(0); WAITV(DK); VLD(1); WAITV(DK); PACE(34); VLD(2); WAITV(DK); VLD(3); WAITV(DK);
    if (wave == 1) dma_sc(qg, lane * 16, lds_base + L_B + 1024);
    PACE(36); VLD(4); WAITV(DK); VLD(5); WAITV(DK); PACE(38); VLD(6); WAITV(DK); VLD(7); WAITV(DK);
    if (wave == 1) dma_sc(qg, lane * 16, lds_base + L_B + 1024);
    TL(29);
#define QLOOK(b) do { if ((QL & (b)) && wave == 1) dma_sc(qg, lane * 16, lds_base + L_B + 1024); } while (0)
    PACE(40); QLOOK(1); VLD(8); WAITV(DV); VLD(9); WAITV(DV); PACE(42); QLOOK(2); VLD(10); WAITV(DV); VLD(11); WAITV(DV);
    TIE8(kb, 0); TIE8(kb, 8); TIE8(kb, 16); TIE8(kb, 24);
    PACE(44); QLOOK(4); VLD(12); VLD(13);
    u32x4 qf[2];
    {
      const u32x4* ql = (const u32x4*)(lds + L_B + 1024 + (((n >> 1) & 1) * 32 + (n & 1) * 8 + grp * 2) * 16);
      u32x4 g[4];
      _Pragma("nounroll") for (int sp = 0; sp < SPIN_CAP; ++sp) {
        g[0] = ql[0]; g[1] = ql[1]; g[2] = ql[16]; g[3] = ql[17];
        if (__all((tag_x(g[0], e0, e1) | tag_x(g[1], e0, e1) | tag_x(g[2], e0, e1) | tag_x(g[3], e0, e1)) == 0u)) break;
        if (wave == 1 && sp >= QN) { dma_sc(qg, lane * 16, lds_base + L_B + 1024); WAITV(0); } else nap(4);
      }
      asm volatile("" ::: "memory");
      g[0] = ql[0]; g[1] = ql[1]; g[2] = ql[16]; g[3] = ql[17];
      qf[0] = n < 4 ? u32x4{g[0][0], g[0][1], g[1][0], g[1][1]} : u32x4{0, 0, 0, 0};
      qf[1] = n < 4 ? u32x4{g[2][0], g[2][1], g[3][0], g[3][1]} : u32x4{0, 0, 0, 0};
    }
    TL(4);
    constexpr float SCALE = 0.088388347648318440f;
    f32x2 sc[16];
    float mx = -INFINITY;
#pragma unroll
    for (int T = 0; T < 16; ++T) {
      f32x4 acc = mfma(kb[T * 2], qf[0], f32x4{0.f, 0.f, 0.f, 0.f});
      acc = mfma(kb[T * 2 + 1], qf[1], acc);
      sc[T] = f32x2{acc[0] + dppf<0xB1>(acc[1]), acc[2] + dppf<0xB1>(acc[3])};
      if (T == 7 || T == 15) { SCHED(); if (T == 7) VLD(14); else VLD(15); SCHED(); }
    }
#pragma unroll
    for (int T = 0; T < 16; ++T)
#pragma unroll
      for (int j = 0; j < 2; ++j) { const float v = rbf(rbf(sc[T][j]) * SCALE); sc[T][j] = v; mx = fmaxf(mx, v); }
    SCHED(); VLD(16); SCHED();
    mx = fmaxf(mx, __shfl_xor(mx, 16));
    mx = fmaxf(mx, __shfl_xor(mx, 32));
    float ls = 0.f;
#pragma unroll
    for (int T = 0; T < 16; ++T)
#pragma unroll
      for (int j = 0; j < 2; ++j) { sc[T][j] = __expf(sc[T][j] - mx); ls += sc[T][j]; }
    SCHED(); VLD(17); SCHED();
    ls += __shfl_xor(ls, 16);
    ls += __shfl_xor(ls, 32);
    float* stat = (float*)(lds + L_B);
    if (lane == 0 || lane == 2) *(f32x2*)(stat + (wave * 2 + (lane >> 1)) * 2) = f32x2{mx, ls};
    SCHED(); VLD(18); VLD(19); SCHED();
    lds_barrier();
    float M = -INFINITY, L = 0.f;
    {
      f32x2 t[8];
#pragma unroll
      for (int q = 0; q < 8; ++q) { t[q] = *(const f32x2*)(stat + (q * 2 + ((n >> 1) & 1)) * 2); M = fmaxf(M, t[q].x); }
#pragma unroll
      for (int q = 0; q < 8; ++q) L = fmaf(__expf(t[q].x - M), t[q].y, L);
    }
    if (tid < 8) st128(rs, W_ST + (w * 8 + lane) * 16, u32x4{__float_as_uint(M), __float_as_uint(L), e0, e1});
    SCHED();
#define VTR(G) do { u32x4 f_[8]; _Pragma("unroll") for (int t = 0; t < 8; ++t) _Pragma("unroll") for (int j = 0; j < 4; ++j) \
      f_[t][j] = (t & 1) ? phi(vb[(G) * 8 + 2 * j][t >> 1], vb[(G) * 8 + 2 * j + 1][t >> 1]) : plo(vb[(G) * 8 + 2 * j][t >> 1], vb[(G) * 8 + 2 * j + 1][t >> 1]); \
      _Pragma("unroll") for (int t = 0; t < 8; ++t) { vb[(G) * 8 + t] = f_[t]; asm volatile("" : "+v"(vb[(G) * 8 + t])); } SCHED(); } while (0)
    WAITV(12);
    TIE8(vb, 0);
    VTR(0);
    TL(30);
    PACE(52); VLD(20); WAITV(5);
    TIE8(vb, 8);
    VTR(1);
    VLD(21); WAITV(DV);
    const int po = W_ST + ((w ^ 1) * 8 + (n & 2)) * 16;
    int l1 = lane; asm volatile("" : "+v"(l1));
    const int fl = 1024 + min(l1, NPZ / 2 - 65) * 16;
    if (wave == 1) { dma_sc(ws + W_FL, l1 * 16, lds_base + L_B + 2048); dma_sc(ws + W_FL, fl, lds_base + L_B + 3072); }
    PACE(54);
    VLD(22); WAITV(DV); VLD(23); WAITV(DV);
    u32x4 ps;
    ald128sc(ps, ws, po);
    PACE(56);
    VLD(24); WAITV(DV); VLD(25); WAITV(DV);
    TL(31);
    PACE(58);
    VLD(26); WAITV(DV); VLD(27); WAITV(DV);
    u32x4 ps2;
    VLD(28); VLD(29); WAITV(6);
    asm volatile("" : "+v"(ps));
    TIE8(vb, 16);
    ald128sc(ps2, ws, po);
    VLD(30); VLD(31);
    VTR(2);
    if (!__all(tag_ok(ps, e0, e1))) {
      WAITV(2);
      asm volatile("" : "+v"(ps2));
      ps = ps2;
      if (!__all(tag_ok(ps, e0, e1))) {
        int sp = 0;
        _Pragma("nounroll") do { nap(1); ps = ld128(rs, po); } while (!__all(tag_ok(ps, e0, e1)) && ++sp < SPIN_CAP);
      }
    }
#define FOK(a, b) __all(((a[0] ^ e0) | (a[1] ^ e1) | tag_x(a, e0, e1) | (b[0] ^ e0) | (b[1] ^ e1) | tag_x(b, e0, e1)) == 0u)
    if (wave == 1 && !FOK((*(const u32x4*)(lds + L_B + 2048 + l1 * 16)), (*(const u32x4*)(lds + L_B + 3072 + l1 * 16)))) {
      _Pragma("nounroll") for (int sp = 0; sp < SPIN_CAP; ++sp) {
        nap(1);
        const u32x4 a = ld128(rs, W_FL + l1 * 16), b = ld128(rs, W_FL + fl);
        if (FOK(a, b)) break;
      }
    }
    float fac;
    {
      const float M2 = __uint_as_float(ps[0]), L2 = __uint_as_float(ps[1]);
      const float Mg = fmaxf(M, M2);
      const float Lg = __expf(M - Mg) * L + __expf(M2 - Mg) * L2;
      fac = __expf(mx - Mg) / Lg;
    }
    TL(5);
    u32x4 pf[4];
#pragma unroll
    for (int G = 0; G < 4; ++G) {
#pragma unroll
      for (int j = 0; j < 4; ++j) pf[G][j] = cvt2(sc[4 * G + j][0] * fac, sc[4 * G + j][1] * fac);
    }
    f32x4 oa[8];
#pragma unroll
    for (int t = 0; t < 8; ++t) oa[t] = f32x4{0.f, 0.f, 0.f, 0.f};
    SCHED();
#pragma unroll
    for (int G = 0; G < 2; ++G)
#pragma unroll
      for (int t = 0; t < 8; ++t) oa[t] = mfma(pf[G], vb[G * 8 + t], oa[t]);
    SCHED();
#pragma unroll
    for (int t = 0; t < 8; ++t) oa[t] = mfma(pf[2], vb[16 + t], oa[t]);
    SCHED();
    int t2_ = threadIdx.x; asm volatile("" : "+v"(t2_));
    const int tid = t2_, lane = tid & 63, n = lane & 15, grp = lane >> 4;
    int oo = W_O + ((kvh * 8 + b_att) * 2 + s_att) * 1024 + ((lane >> 4) * 16 + (lane & 1) * 8 + ((lane >> 1) & 7)) * 16;
    int mw_ = L_C + wave * 1024 + n * 32, mr_ = L_C + lane * 16;
    asm volatile("" : "+v"(oo), "+v"(mw_), "+v"(mr_), "+v"(pf[3]));
    constexpr int L_H2 = L_W + 73728;
    constexpr int L_ACT = L_C + 4608;
    int ia[3], ra[4], yo[3], fo[7], tw, tr_;
    const bool late = gs >= 0 || mw >= 18;
    if (late) {
      BAR();
      slab_dma<0, 4>(gs >= 0 ? wg + gs * 64 : w_mod + ml * 64, gs >= 0 ? FF * 2 : NMOD * 2, lds_base + L_W + (gs >= 0 ? 73728 : 0), wave, lane);
      {
    const int b = wave;
#pragma unroll
    for (int i = 0; i < 3; ++i) {
      const int k = i < 2 ? i * 256 + lane * 4 : 512 + n * 4, hf = k >= 288 ? 1 : 0, kk = k - hf * 288, s = kk >> 5, g = (kk >> 3) & 3;
      ia[i] = L_H2 + (s * 64 + g * 16 + ((hf * 8 + b) ^ ((s & 3) * 4 + g))) * 16 + (kk & 7) * 2;
    }
#pragma unroll
    for (int i = 0; i < 4; ++i) ra[i] = L_H2 + (grp * 16 + (n ^ (i * 4 + grp))) * 16;
    tw = L_ACT + n * 144 + wave * 16 + (grp & 1) * 8 + (grp >> 1) * 4; tr_ = L_ACT + (n & 7) * 144 + grp * 16;
    yo[0] = W_Y + (((wave * 8 + grp * 2) * NG + gs) * 16 + n) * 16; yo[1] = yo[0] + NG * 256;
    yo[2] = W_Y + ((((64 + wave * 2 + (grp >> 1)) * NG + gs) * 2 + (grp & 1)) * 8 + n) * 16;
    {
      const int b_ = lane >> 3, cc = lane & 7, col = gs * 8 + cc, hf = col >= 288 ? 1 : 0, kk = col - hf * 288, s_ = kk >> 5, g = (kk >> 3) & 3;
      fo[0] = W_M + ((3 * 8 + b_) * 144 + gs * 2 + (cc >> 2)) * 16;
      fo[1] = W_X1 + (b_ * 152 + gs * 2 + (cc >> 2)) * 16; fo[2] = W_X1 + (b_ * 152 + 144) * 16;
      fo[3] = W_Y + (gs * 1152 + tid) * 16;
      fo[4] = L_H2 + (s_ * 64 + g * 16 + ((hf * 8 + b_) ^ ((s_ & 3) * 4 + g))) * 16 + cc * 2;
      fo[5] = L_C + (((cc >> 2) * 8 + b_) * 4 + (cc & 3)) * 4;
      fo[6] = (b_ * D + col) * 2;
    }
    asm volatile("" : "+v"(ia[0]), "+v"(ia[1]), "+v"(ia[2]), "+v"(ra[0]), "+v"(ra[1]), "+v"(ra[2]), "+v"(ra[3]), "+v"(tw), "+v"(tr_), "+v"(yo[0]), "+v"(yo[1]), "+v"(yo[2]),
                 "+v"(fo[0]), "+v"(fo[1]), "+v"(fo[2]), "+v"(fo[3]), "+v"(fo[4]), "+v"(fo[5]), "+v"(fo[6]));
      }
      WAITV(6);
    } else WAITV(2);
    u32x4 vf[8];
    asm volatile("s_nop 0 ; both branches above cover V up to the last two" : "+v"(vb[24]), "+v"(vb[25]), "+v"(vb[26]), "+v"(vb[27]), "+v"(vb[28]), "+v"(vb[29]));
    asm volatile("" :: "v"(ps2));
#pragma unroll
    for (int t = 0; t < 8; ++t)
#pragma unroll
      for (int j = 0; j < 3; ++j) vf[t][j] = (t & 1) ? phi(vb[24 + 2 * j][t >> 1], vb[25 + 2 * j][t >> 1]) : plo(vb[24 + 2 * j][t >> 1], vb[25 + 2 * j][t >> 1]);
#pragma unroll
    for (int t = 0; t < 8; ++t) asm volatile("" : "+v"(vf[t][0]), "+v"(vf[t][1]), "+v"(vf[t][2]));
    if (late) WAITV(4); else WAITV(0);
    asm volatile("s_nop 0 ; both branches above cover V" : "+v"(vb[30]), "+v"(vb[31]));
#pragma unroll
    for (int t = 0; t < 8; ++t) {
      vf[t][3] = (t & 1) ? phi(vb[30][t >> 1], vb[31][t >> 1]) : plo(vb[30][t >> 1], vb[31][t >> 1]);
      oa[t] = mfma(pf[3], vf[t], oa[t]);
    }
    TL(6);
    if (grp == 0) {
#pragma unroll
      for (int g = 0; g < 2; ++g) {
        *(f32x4*)(lds + mw_ + g * 512) = f32x4{oa[0][2 * g], oa[1][2 * g], oa[2][2 * g], oa[3][2 * g]};
        *(f32x4*)(lds + mw_ + g * 512 + 16) = f32x4{oa[4][2 * g], oa[5][2 * g], oa[6][2 * g], oa[7][2 * g]};
      }
    }
    lds_barrier();
    if (wave == 0) {
      f32x4 m[8];
#pragma unroll
      for (int q = 0; q < 8; ++q) m[q] = *(const f32x4*)(lds + mr_ + q * 1024);
      SCHED();
      const f32x4 o = ((m[0] + m[1]) + (m[2] + m[3])) + ((m[4] + m[5]) + (m[6] + m[7]));
      st128(rs, oo, u32x4{sanf(o[0]), sanf(o[1]), sanf(o[2]), sanf(o[3])});
    }
    TL(7); SCHED();
  if (gs >= 0) {
    const int b = wave;
    BAR(); SCHED();
    u32x4 bg[9], bu[9], wdf[2][5];
    {
      u32x2 rd[2][8];
      slab_dma<4, 9>(wg + gs * 64, FF * 2, lds_base + L_W + 73728, wave, lane);
      TL(23);
      BAR();
      const char* db = (const char*)(wd + (size_t)gs * 64 * D + wave * 64);
      const u32 dv = grp * 9216 + n * 8;
#pragma unroll
      for (int ks = 0; ks < 2; ++ks)
#pragma unroll
        for (int i = 0; i < 8; ++i) ald64nt(rd[ks][i], db + (ks * 32 + i) * 1152, dv);
      {
        const int r = lane >> 3;
        dma((const char*)(wd + (size_t)(gs * 64 + wave * 8) * D + 512), r * 1152 + (((lane & 7) ^ (r & 2) ^ ((wave & 1) * 4)) << 4), lds_base + L_B + wave * 1024);
      }
      TL(19);
      asm volatile("s_waitcnt vmcnt(17)\n\ts_barrier" ::: "memory");
      TL(22);
      slab_frags(lds, L_W + 73728, wave, lane, bg);
      SCHED();
      slab_dma(wu + gs * 64, FF * 2, lds_base + L_W, wave, lane);
      TL(20);
      asm volatile("s_waitcnt vmcnt(10)" : "+v"(rd[0][0]), "+v"(rd[0][1]), "+v"(rd[0][2]), "+v"(rd[0][3]), "+v"(rd[0][4]), "+v"(rd[0][5]), "+v"(rd[0][6]), "+v"(rd[0][7]),
                   "+v"(rd[1][0]), "+v"(rd[1][1]), "+v"(rd[1][2]), "+v"(rd[1][3]), "+v"(rd[1][4]), "+v"(rd[1][5]), "+v"(rd[1][6]), "+v"(rd[1][7]) :: "memory");
#pragma unroll
      for (int ks = 0; ks < 2; ++ks)
#pragma unroll
        for (int j = 0; j < 4; ++j) {
          wdf[ks][0][j] = plo(rd[ks][2 * j][0], rd[ks][2 * j + 1][0]); wdf[ks][1][j] = phi(rd[ks][2 * j][0], rd[ks][2 * j + 1][0]);
          wdf[ks][2][j] = plo(rd[ks][2 * j][1], rd[ks][2 * j + 1][1]); wdf[ks][3][j] = phi(rd[ks][2 * j][1], rd[ks][2 * j + 1][1]);
        }
#pragma unroll
      for (int t = 0; t < 4; ++t) asm volatile("" : "+v"(wdf[0][t]), "+v"(wdf[1][t]));
      TL(21);
    }
    asm volatile("s_waitcnt vmcnt(0) lgkmcnt(0)\n\ts_barrier" ::: "memory");
    {
      u32x4 hp[3];
      const int h0 = W_H2 + (b * 144 + lane) * 16, h2o = W_H2 + (b * 144 + 128 + n) * 16;
      hp[0] = ld128(rs, h0); hp[1] = ld128(rs, h0 + 1024); hp[2] = ld128(rs, h2o);
      slab_frags(lds, L_W, wave, lane, bu);
      const int q = n >> 2, p = lane & 3;
      const int a0 = L_B + (8 * grp + q) * 128 + ((((wave & 3) * 2 + (p >> 1)) ^ (q & 2) ^ ((grp & 1) * 4)) << 4) + (p & 1) * 8;
#pragma unroll
      for (int ks = 0; ks < 2; ++ks) {
        const u32x2 lo = trd(lds, a0 + ks * 4096), hi = trd(lds, a0 + ks * 4096 + 512);
        wdf[ks][4] = u32x4{lo[0], lo[1], hi[0], hi[1]};
      }
      TL(12);
#pragma unroll
      for (int s = 0; s < 9; ++s) asm volatile("" : "+v"(bg[s]), "+v"(bu[s]));
      asm volatile("" : "+v"(wdf[0][4]), "+v"(wdf[1][4]));
      if (!HOK()) {
        int sp = 0;
        _Pragma("nounroll") do { nap(2); hp[0] = ld128(rs, h0); hp[1] = ld128(rs, h0 + 1024); hp[2] = ld128(rs, h2o); } while (!HOK() && ++sp < SPIN_CAP);
      }
      TL(13);
      *(u32x2*)(lds + ia[0]) = u32x2{hp[0][0], hp[0][1]};
      *(u32x2*)(lds + ia[1]) = u32x2{hp[1][0], hp[1][1]};
      if (lane < 16) *(u32x2*)(lds + ia[2]) = u32x2{hp[2][0], hp[2][1]};
      lds_barrier();
      u32x4 af[9];
#pragma unroll
      for (int s = 0; s < 9; ++s) af[s] = *(const u32x4*)(lds + ra[s & 3] + s * 1024);
      SCHED();
      f32x4 ag[2], au[2];
#pragma unroll
      for (int s = 0; s < 9; ++s) {
        ag[s & 1] = mfma(bg[s], af[s], s < 2 ? f32x4{0.f, 0.f, 0.f, 0.f} : ag[s & 1]);
        au[s & 1] = mfma(bu[s], af[s], s < 2 ? f32x4{0.f, 0.f, 0.f, 0.f} : au[s & 1]);
      }
      const f32x2 hg = half_sum2(ag[0] + ag[1]), hu = half_sum2(au[0] + au[1]);
      const u32 act = cvt2(rbf(silu(rbf(hg.x))) * rbf(hu.x), rbf(silu(rbf(hg.y))) * rbf(hu.y));
      if (n < 8) *(u32*)(lds + tw) = act;
    }
    lds_barrier();
    TL(14);
    {
      u32x4 af[2];
#pragma unroll
      for (int ks = 0; ks < 2; ++ks) af[ks] = *(const u32x4*)(lds + tr_ + ks * 64);
      f32x4 d[5];
#pragma unroll
      for (int t = 0; t < 5; ++t) d[t] = mfma(wdf[1][t], af[1], mfma(wdf[0][t], af[0], f32x4{0.f, 0.f, 0.f, 0.f}));
      if (n < 8) {
#pragma unroll
        for (int j = 0; j < 4; ++j)
          st128(rs, yo[j >> 1] + (j & 1) * 128, u32x4{sanf(d[0][j]), sanf(d[1][j]), sanf(d[2][j]), sanf(d[3][j])});
        if (wave < 4)
          st128(rs, yo[2], u32x4{sanf(d[4][0]), sanf(d[4][1]), sanf(d[4][2]), sanf(d[4][3])});
      }
    }
    TL(15);
    {
      float gbv = 0.f, x1v = 0.f, h2 = 0.f, rsel = 0.f;
      if (wave == 7) {
        u32x4 gq, xq, rq;
        _Pragma("nounroll") for (int sp = 0; sp < SPIN_CAP; ++sp) {
          gq = ld128(rs, fo[0]); xq = ld128(rs, fo[1]); rq = ld128(rs, fo[2]);
          if (__all((tag_x(gq, e0, e1) | tag_x(xq, e0, e1) | tag_x(rq, e0, e1)) == 0u)) break;
          nap(1);
        }
        const u32 gw = (lane & 2) ? gq[1] : gq[0], xw = (lane & 2) ? xq[1] : xq[0];
        gbv = (lane & 1) ? hi16(gw) : lo16(gw); x1v = (lane & 1) ? hi16(xw) : lo16(xw);
        h2 = b2f(*(const u16*)(lds + fo[4])); rsel = __uint_as_float(rq[0]);
        asm volatile("" : "+v"(gbv), "+v"(x1v), "+v"(h2), "+v"(rsel));
      }
      WAITV(0);
      u32x4 y[3] = {};
      _Pragma("nounroll") for (int sp = 0; sp < SPIN_CAP; ++sp) {
        y[0] = ld128(rs, fo[3]); y[1] = ld128(rs, fo[3] + 8192);
        if (wave < 2) y[2] = ld128(rs, fo[3] + 16384);
        if (__all(mxu(mxu(mx4(y[0]), mx4(y[1])), mx4(y[2])) != 0xffffffffu)) break;
        nap(1);
      }
      TL(16);
      f32x4 s;
#pragma unroll
      for (int k = 0; k < 4; ++k) s[k] = xsum32(xsum16((__uint_as_float(y[0][k]) + __uint_as_float(y[1][k])) + __uint_as_float(y[2][k])));
      if (lane < 16) *(f32x4*)(lds + L_C + (wave * 16 + lane) * 16) = s;
      lds_barrier();
      if (wave == 7) {
        float t[8];
#pragma unroll
        for (int ww = 0; ww < 8; ++ww) t[ww] = *(const float*)(lds + fo[5] + ww * 256);
        SCHED();
        const float y2 = rbf(rbf(((t[0] + t[1]) + (t[2] + t[3])) + ((t[4] + t[5]) + (t[6] + t[7]))) * rsel);
        const float res = x1v + rbf(gbv * rbf(h2 + y2));
        __builtin_amdgcn_raw_buffer_store_b16((u16)(cvt2(res, 0.f) & 0xffffu), __builtin_amdgcn_make_buffer_rsrc(out, 0, 8 * D * 2, 0x00020000), fo[6], 0, 0);
      }
      TL(17);
    }
  }
  {
  int t3_ = threadIdx.x; asm volatile("" : "+v"(t3_));
  const int tid = t3_, lane = tid & 63, n = lane & 15, grp = lane >> 4;
  if (qs >= 0) {
    const u16* mat = (wave & 1) ? wg : wu;
    const int g0 = ((3 - (int)((uintptr_t)mat >> 7) - xcd * 9) & 7) + (wave >= 4 ? 8 : 0);
    const int r = (idx - 6) * 72 + ((wave >> 1) & 1) * 36 + min(lane, 35);
    if (g0 < 9) dma_keep(mat, r * 9216 + (xcd * 9 + g0) * 128, lds_base + L_W + wave * 256);
  }
  if (ml >= 0) {
    BAR();
    if (mw < 18) slab_dma<0, 4>(w_mod + ml * 64, NMOD * 2, lds_base + L_W, wave, lane);
    slab_dma<4, 9>(w_mod + ml * 64, NMOD * 2, lds_base + L_W, wave, lane);
    u32x4 wf[9], af[9];
    img_frags(lds, L_IMG, lane, af);
    asm volatile("s_waitcnt vmcnt(0)\n\ts_barrier" ::: "memory");
    slab_frags(lds, L_W, wave, lane, wf);
    const f32x4 s = half_sum(gemv9(wf, af));
    if (n < 8 && grp < 2) *(u32x2*)(lds + L_TILE + n * 128 + wave * 16 + grp * 8) = u32x2{cvt2(s[0], s[1]), cvt2(s[2], s[3])};
    lds_barrier();
    if (wave < 2) {
      const u32x2 t2 = *(const u32x2*)(lds + L_TILE + tid * 8);
      const int ch = (ml - 18) / 9, sl = ml - 18 - ch * 9;
      st128(rs, W_M + ((ch * 8 + (tid >> 4)) * 144 + sl * 16 + n) * 16, u32x4{t2[0], t2[1], e0, e1});
    }
    TL(18);
  }

  if (lnb >= 0 && wave < 3) {
    const int b = lnb, g = tid < 144 ? tid : 143;
    const bool on = tid < 144;
    float* ex = (float*)(lds + L_TILE);
    const u32x2 xa = __builtin_amdgcn_raw_buffer_load_b64(__builtin_amdgcn_make_buffer_rsrc((void*)x, 0, 8 * D * 2, 0x00020000), (b * D + g * 4) * 2, 0, 0);
    const u32x2 wra = __builtin_amdgcn_raw_buffer_load_b64(__builtin_amdgcn_make_buffer_rsrc((void*)w_r, 0, D * 2, 0x00020000), g * 8, 0, 0);
    u32x4 mm[3];
    _Pragma("nounroll") for (int sp = 0; sp < SPIN_CAP; ++sp) {
#pragma unroll
      for (int k = 0; k < 3; ++k) mm[k] = ld128(rs, W_M + ((k * 8 + b) * 144 + g) * 16);
      if (__all((tag_x(mm[0], e0, e1) | tag_x(mm[1], e0, e1) | tag_x(mm[2], e0, e1)) == 0u)) break;
      nap(4);
    }
    f32x2 xv[2], ga[2], sb[2], cb1[2], wr[2];
#pragma unroll
    for (int i = 0; i < 2; ++i) {
      xv[i] = up2(xa[i]); wr[i] = up2(wra[i]); ga[i] = up2(mm[0][i]); sb[i] = up2(mm[1][i]); cb1[i] = rbf2(f32x2{1.f, 1.f} + up2(mm[2][i]));
      asm volatile("" : "+v"(xv[i]), "+v"(ga[i]), "+v"(sb[i]), "+v"(cb1[i]), "+v"(wr[i]));
    }
    int ho = W_H2 + (b * 144 + g) * 16, xo = W_X1 + (b * 152 + g) * 16;
    asm volatile("" : "+v"(ho), "+v"(xo));
    u32x4 xp[8];
    _Pragma("nounroll") for (int sp = 0; sp < SPIN_CAP; ++sp) {
#pragma unroll
      for (int p = 0; p < 8; ++p) xp[p] = ld128(rs, W_XP + (p * 8 + b) * 2304 + g * 16);
      if (__all(mxu(mxu(mxu(mx4(xp[0]), mx4(xp[1])), mxu(mx4(xp[2]), mx4(xp[3]))), mxu(mxu(mx4(xp[4]), mx4(xp[5])), mxu(mx4(xp[6]), mx4(xp[7])))) != 0xffffffffu)) break;
      nap(1);
    }
    TL(10);
    f32x2 v[2];
#pragma unroll
    for (int i = 0; i < 2; ++i) {
      f32x2 pp[8];
#pragma unroll
      for (int p = 0; p < 8; ++p) pp[p] = f32x2{__uint_as_float(xp[p][2 * i]), __uint_as_float(xp[p][2 * i + 1])};
      v[i] = rbf2(xv[i] + rbf2(ga[i] * rbf2(((pp[0] + pp[1]) + (pp[2] + pp[3])) + ((pp[4] + pp[5]) + (pp[6] + pp[7])))));
    }
    const f32x2 s2 = v[0] + v[1];
    const float mw = wsum(on ? s2.x + s2.y : 0.f) * (wave < 2 ? 1.f / 256 : 1.f / 64);
    const f32x2 t0 = v[0] - f32x2{mw, mw}, t1 = v[1] - f32x2{mw, mw}, q2 = t0 * t0 + t1 * t1;
    const float m2 = wsum(on ? q2.x + q2.y : 0.f);
    if (lane == 0) *(f32x2*)(ex + wave * 2) = f32x2{mw, m2};
    lds_barrier();
    const f32x4 e01 = *(const f32x4*)ex; const f32x2 e2 = *(const f32x2*)(ex + 4);
    const float mu = (e01[0] + e01[2]) * (256.f / D) + e2.x * (64.f / D);
    const float d0 = e01[0] - mu, d1 = e01[2] - mu, d2 = e2.x - mu;
    const float var = rbf(((e01[1] + e01[3]) + e2.y + 256.f * (d0 * d0 + d1 * d1) + 64.f * d2 * d2) * (1.f / D));
    const float mean = rbf(mu), rden = frcp(rbf(sqrtf(rbf(var + 1e-5f))));
    u32 hw[2];
#pragma unroll
    for (int i = 0; i < 2; ++i) {
      const f32x2 t = rbf2(rbf2(rbf2(v[i] - f32x2{mean, mean}) * f32x2{rden, rden}) * cb1[i]) + sb[i];
      hw[i] = cvt2(t.x, t.y);
    }
    if (on) {
      st128(rs, ho, u32x4{hw[0], hw[1], e0, e1});
      st128(rs, xo, u32x4{cvt2(v[0].x, v[0].y), cvt2(v[1].x, v[1].y), e0, e1});
    }
    TL(11);
    const f32x2 r2 = up2(hw[0]) * wr[0] + up2(hw[1]) * wr[1];
    const float rw = wsum(on ? r2.x + r2.y : 0.f);
    if (lane == 0) ex[8 + wave] = rw;
    lds_barrier();
    if (tid < 8) {
      const float r = rbf((ex[8] + ex[9]) + ex[10]);
      st128(rs, W_X1 + (b * 152 + 144 + tid) * 16, u32x4{__float_as_uint(rbf(1.f / (1.f + __expf(-r)))), 0u, e0, e1});
    }
  }

  if (oj >= 0) {
    const int ob = W_O + ((op * 2 + (wave >> 2)) * 8 + (lane >> 3)) * 2048 + (wave & 3) * 256 + (lane & 7) * 16;
    u32x4 a[4];
#pragma unroll
    for (int p = 0; p < 4; ++p) a[p] = ld128(rs, ob + (p >> 1) * 1024 + (p & 1) * 128);
    SCHED();
    u32x4 wf[8];
    {
      const int q = n >> 2, p = lane & 3, hf = p >> 1;
      const int a0 = L_W + (wave * 64 + hf * 32 + 8 * grp + q) * 128 + (((q >> 1) ^ ((grp & 1) * 2) ^ (hf * 4)) << 4) + (p & 1) * 8;
#pragma unroll
      for (int cg = 0; cg < 8; ++cg) {
        const u32x2 lo = trd(lds, a0 ^ (cg * 16)), hi = trd(lds, (a0 ^ (cg * 16)) + 512);
        wf[cg] = u32x4{lo[0], lo[1], hi[0], hi[1]};
      }
    }
    int rw = L_OI + (n & 7) * 4128 + (wave * 2 + (n >> 3)) * 256 + (grp & 1) * 16, rr = L_OI + wave * 4128 + grp * 1024 + n * 16;
    int xo = W_XP + (op * 8 + wave) * 2304 + oj * 256 + lane * 16, sl = ((n & 7) * 8 + (n >> 3) * 4 + grp) * 4;
#pragma unroll
    for (int cg = 0; cg < 8; ++cg) asm volatile("" : "+v"(wf[cg]));
    asm volatile("" : "+v"(rw), "+v"(rr), "+v"(xo), "+v"(sl));
    _Pragma("nounroll") for (int sp = 0; sp < SPIN_CAP; ++sp) {
      const bool m0 = mxu(mx4(a[0]), mx4(a[1])) == 0xffffffffu, m1 = mxu(mx4(a[2]), mx4(a[3])) == 0xffffffffu;
      if (!__any(m0 || m1)) break;
      nap(2);
      if (m0) { a[0] = ld128(rs, ob); a[1] = ld128(rs, ob + 128); }
      if (m1) { a[2] = ld128(rs, ob + 1024); a[3] = ld128(rs, ob + 1152); }
    }
    TL(8);
#define OS(p, j) (__uint_as_float(a[p][j]) + __uint_as_float(a[(p) + 2][j]))
#define OP(v) (u32)__builtin_amdgcn_ds_bpermute(sl, (int)(v))
    const u32x4 of = {OP(cvt2(OS(0, 0), OS(0, 1))), OP(cvt2(OS(0, 2), OS(0, 3))), OP(cvt2(OS(1, 0), OS(1, 1))), OP(cvt2(OS(1, 2), OS(1, 3)))};
    f32x4 acc[8];
#pragma unroll
    for (int cg = 0; cg < 8; ++cg) acc[cg] = mfma(wf[cg], of, f32x4{0.f, 0.f, 0.f, 0.f});
    if ((n >> 3) == (grp >> 1)) {
#pragma unroll
      for (int cg = 0; cg < 8; ++cg) *(f32x4*)(lds + rw + cg * 32) = acc[cg];
    }
    lds_barrier();
    f32x4 s = (*(const f32x4*)(lds + rr) + *(const f32x4*)(lds + rr + 256)) + (*(const f32x4*)(lds + rr + 512) + *(const f32x4*)(lds + rr + 768));
#pragma unroll
    for (int k = 0; k < 4; ++k) s[k] = xsum32(xsum16(s[k]));
    if (lane < 16) st128(rs, xo, u32x4{sanf(s[0]), sanf(s[1]), sanf(s[2]), sanf(s[3])});
    TL(9);
  }
  TLOUT();
  }
  }
}

extern "C" void kernel_launch(void* const* d_in, const int* in_sizes, int n_in, void* d_out, int out_size, void* d_ws, size_t ws_size,
                              hipStream_t stream) {
  fused_kernel<<<NWG, NT, 0, stream>>>((const u16*)d_in[0], (const u16*)d_in[1], (const u16*)d_in[2], (const u16*)d_in[3], (const u16*)d_in[4],
                                       (const u16*)d_in[6], (const u16*)d_in[7], (const u16*)d_in[8], (const u16*)d_in[9], (const u16*)d_in[10],
                                       (const u16*)d_in[11], (unsigned char*)d_ws, (u16*)d_out);
}
```

```cpp
#include <hip/hip_runtime.h>
#include <hip/hip_bf16.h>
#include <cstdint>

typedef unsigned u32;
typedef unsigned short u16;
typedef u32 u32x4 __attribute__((ext_vector_type(4)));
typedef u32 u32x2 __attribute__((ext_vector_type(2)));
typedef float f32x4 __attribute__((ext_vector_type(4)));
typedef float f32x2 __attribute__((ext_vector_type(2)));
typedef short s16x4 __attribute__((ext_vector_type(4)));
typedef __bf16 bf16x8 __attribute__((ext_vector_type(8)));
typedef __bf16 bf16x2 __attribute__((ext_vector_type(2)));
typedef __attribute__((address_space(3))) unsigned char lds_t;

constexpr int D = 576, SK = 8192, WIN = 2048, FF = 4608, NQ = 4096, NMOD = 6 * D;
constexpr int NWG = 256, NT = 512, NG = 72;
constexpr int SPIN_CAP = 1 << 15;

constexpr int W_O = 0;
constexpr int W_XP = W_O + 262144;
constexpr int W_Y = W_XP + 147456;
constexpr int W_MS = W_Y + NG * NG * 256;
constexpr int W_FL = W_MS;
constexpr int W_H = W_FL + 2048;
constexpr int W_M = W_H + 36864;
constexpr int W_Q = W_M + 73728;
constexpr int W_ST = W_Q + 131072;
constexpr int W_H2 = W_ST + 32768;
constexpr int W_X1 = W_H2 + 18432;
constexpr int W_END = W_X1 + 8 * 152 * 16;
constexpr int NPZ = 174, PZ = (W_MS / 16 + NPZ - 1) / NPZ;
static_assert(PZ <= 2 * NT, "poison split");

constexpr int L_W = 0;
constexpr int L_IMG = 73728;
constexpr int L_TILE = L_IMG + 9216;
constexpr int L_OI = 65536;
constexpr int L_B = 147456;
constexpr int L_C = 155648;
constexpr int LDS_BYTES = 163840;

#define SCHED() __builtin_amdgcn_sched_barrier(0)
#define BAR() asm volatile("s_barrier" ::: "memory")
#define TL(i) do {} while (0)
#define TLOUT() do {} while (0)
#define WAITV_(n) do { asm volatile("s_waitcnt vmcnt(" #n ")" ::: "memory"); SCHED(); } while (0)
#define WAITV(n) WAITV_(n)
#define OF 134
#define OL 60
#define OO 203
#define OQ 203
#define EP 4
#define CPW 0
#define CPM 0
#define QL 6
#define QN 6
#define DK 5
#define DV DK
#define PR 117
#define PR2 117
#define PR3 117

__device__ __forceinline__ float lo16(u32 u) { return __uint_as_float(u << 16); }
__device__ __forceinline__ float hi16(u32 u) { return __uint_as_float(u & 0xffff0000u); }
__device__ __forceinline__ float b2f(u16 u) { return __uint_as_float((u32)u << 16); }
__device__ __forceinline__ u32 cvt2(float a, float b) { return __builtin_bit_cast(u32, __builtin_convertvector(f32x2{a, b}, bf16x2)); }
__device__ __forceinline__ float rbf(float v) { return __uint_as_float(cvt2(0.f, v)); }
__device__ __forceinline__ u32 sanf(float f) { u32 v = __float_as_uint(f); return v < 0xfffffffeu ? v : 0xfffffffeu; }
__device__ __forceinline__ f32x2 up2(u32 u) { return f32x2{lo16(u), hi16(u)}; }
__device__ __forceinline__ f32x2 rbf2(f32x2 v) { return f32x2{rbf(v.x), rbf(v.y)}; }
__device__ __forceinline__ void lds_barrier() { asm volatile("s_waitcnt lgkmcnt(0)\n\ts_barrier" ::: "memory"); }
__device__ __forceinline__ f32x4 mfma(u32x4 a, u32x4 b, f32x4 c) {
  return __builtin_amdgcn_mfma_f32_16x16x32_bf16(__builtin_bit_cast(bf16x8, a), __builtin_bit_cast(bf16x8, b), c, 0, 0, 0);
}
__device__ __forceinline__ u32 plo(u32 a, u32 b) { return __builtin_amdgcn_perm(b, a, 0x05040100u); }
__device__ __forceinline__ u32 phi(u32 a, u32 b) { return __builtin_amdgcn_perm(b, a, 0x07060302u); }

extern "C" __device__ __attribute__((const)) unsigned long long llvm_dispatch_id() __asm("llvm.amdgcn.dispatch.id");
typedef __amdgpu_buffer_rsrc_t rsrc_t;
__device__ __forceinline__ u32x4 ld128(rsrc_t rs, int off) { return __builtin_amdgcn_raw_buffer_load_b128(rs, off, 0, 16); }
__device__ __forceinline__ void st128(rsrc_t rs, int off, u32x4 v) { __builtin_amdgcn_raw_buffer_store_b128(v, rs, off, 0, 16); }
__device__ __forceinline__ void st64(rsrc_t rs, int off, u32x2 v) { __builtin_amdgcn_raw_buffer_store_b64(v, rs, off, 0, 16); }
__device__ __forceinline__ void dma(const void* gbase, u32 voff, unsigned lds_dst) {
  unsigned keep;
  asm volatile("s_mov_b32 %0, m0\n\ts_mov_b32 m0, %3\n\ts_nop 2\n\tglobal_load_lds_dwordx4 %1, %2 nt\n\ts_mov_b32 m0, %0"
               : "=&s"(keep) : "v"(voff), "s"(gbase), "s"(lds_dst) : "memory");
}
__device__ __forceinline__ void dma_keep(const void* gbase, u32 voff, unsigned lds_dst) {
  unsigned keep;
  asm volatile("s_mov_b32 %0, m0\n\ts_mov_b32 m0, %3\n\ts_nop 2\n\tglobal_load_lds_dword %1, %2\n\ts_mov_b32 m0, %0"
               : "=&s"(keep) : "v"(voff), "s"(gbase), "s"(lds_dst) : "memory");
}
__device__ __forceinline__ void dma_sc(const void* gbase, u32 voff, unsigned lds_dst) {
  unsigned keep;
  asm volatile("s_mov_b32 %0, m0\n\ts_mov_b32 m0, %3\n\ts_nop 2\n\tglobal_load_lds_dwordx4 %1, %2 sc1\n\ts_mov_b32 m0, %0"
               : "=&s"(keep) : "v"(voff), "s"(gbase), "s"(lds_dst) : "memory");
}
__device__ __forceinline__ void ald128(u32x4& d, const void* gbase, u32 voff) { unsigned long long t; asm volatile("s_mov_b64 %1, %3\n\tglobal_load_dwordx4 %0, %2, %1" : "=v"(d), "=&s"(t) : "v"(voff), "s"(gbase) : "memory"); }
__device__ __forceinline__ void ald64(u32x2& d, const void* gbase, u32 voff) { unsigned long long t; asm volatile("s_mov_b64 %1, %3\n\tglobal_load_dwordx2 %0, %2, %1" : "=v"(d), "=&s"(t) : "v"(voff), "s"(gbase) : "memory"); }
__device__ __forceinline__ void ald64nt(u32x2& d, const void* gbase, u32 voff) { unsigned long long t; asm volatile("s_mov_b64 %1, %3\n\tglobal_load_dwordx2 %0, %2, %1 nt" : "=v"(d), "=&s"(t) : "v"(voff), "s"(gbase) : "memory"); }
__device__ __forceinline__ void ald128sc(u32x4& d, const void* gbase, u32 voff) { unsigned long long t; asm volatile("s_mov_b64 %1, %3\n\tglobal_load_dwordx4 %0, %2, %1 sc1" : "=v"(d), "=&s"(t) : "v"(voff), "s"(gbase) : "memory"); }
__device__ __forceinline__ u32x2 trd(const unsigned char* lds, int off) {
  return __builtin_bit_cast(u32x2, __builtin_amdgcn_ds_read_tr16_b64_v4i16((__attribute__((address_space(3))) s16x4*)((lds_t*)lds + off)));
}
#define nap(n) do { __builtin_amdgcn_s_sleep(n); asm volatile("" ::: "memory"); } while (0)
__device__ __forceinline__ float frcp(float v) { return __builtin_amdgcn_rcpf(v); }
__device__ __forceinline__ float fdiv(float a, float d, float r) { const float q = a * r; return fmaf(fmaf(-q, d, a), r, q); }
__device__ __forceinline__ float silu(float v) { const float d = 1.f + __expf(-v); const float r = frcp(d); return d < 3.0e38f ? fdiv(v, d, r) : v * r; }
template <int CTRL> __device__ __forceinline__ float dppf(float x) {
  return __builtin_bit_cast(float, __builtin_amdgcn_mov_dpp(__builtin_bit_cast(int, x), CTRL, 0xf, 0xf, true));
}
__device__ __forceinline__ u32 mxu(u32 a, u32 b) { return a > b ? a : b; }
__device__ __forceinline__ u32 mx4(u32x4 v) { return mxu(mxu(v[0], v[1]), mxu(v[2], v[3])); }
__device__ __forceinline__ float rl(float v, int l) { return __builtin_bit_cast(float, __builtin_amdgcn_readlane(__builtin_bit_cast(int, v), l)); }
__device__ __forceinline__ float wsum(float v) {
  v += dppf<0xB1>(v); v += dppf<0x4E>(v); v += dppf<0x141>(v); v += dppf<0x140>(v);
  return (rl(v, 0) + rl(v, 16)) + (rl(v, 32) + rl(v, 48));
}
__device__ __forceinline__ float up32(float v) { const u32 u = __float_as_uint(v); return __uint_as_float(__builtin_amdgcn_permlane32_swap(u, u, false, false)[1]); }
__device__ __forceinline__ float xsum32(float v) { const u32 u = __float_as_uint(v); const auto r = __builtin_amdgcn_permlane32_swap(u, u, false, false); return __uint_as_float(r[0]) + __uint_as_float(r[1]); }
__device__ __forceinline__ float xsum16(float v) { const u32 u = __float_as_uint(v); const auto r = __builtin_amdgcn_permlane16_swap(u, u, false, false); return __uint_as_float(r[0]) + __uint_as_float(r[1]); }
__device__ __forceinline__ f32x4 half_sum(f32x4 d) {
  f32x4 r;
#pragma unroll
  for (int j = 0; j < 4; ++j) r[j] = d[j] + dppf<0x108>(up32(d[j]));
  return r;
}
__device__ __forceinline__ f32x2 half_sum2(f32x4 d) {
  f32x2 r;
#pragma unroll
  for (int j = 0; j < 2; ++j) {
    const auto s = __builtin_amdgcn_permlane32_swap(__float_as_uint(d[j]), __float_as_uint(d[j + 2]), false, false);
    r[j] = __uint_as_float(s[0]) + dppf<0x108>(__uint_as_float(s[1]));
  }
  return r;
}
__device__ __forceinline__ void unpack8(u32x4 u, float* f) {
#pragma unroll
  for (int j = 0; j < 4; ++j) { f[2 * j] = lo16(u[j]); f[2 * j + 1] = hi16(u[j]); }
}
__device__ __forceinline__ void ln_row(const float (&v)[16], bool has2, float& mean, float& den) {
  float s = 0.f;
#pragma unroll
  for (int i = 0; i < 8; ++i) s += v[i];
  if (has2) {
#pragma unroll
    for (int i = 8; i < 16; ++i) s += v[i];
  }
  s = wsum(s);
  const float mu = s * (1.f / D);
  mean = rbf(mu);
  float q = 0.f;
#pragma unroll
  for (int i = 0; i < 8; ++i) { const float t = v[i] - mu; q = fmaf(t, t, q); }
  if (has2) {
#pragma unroll
    for (int i = 8; i < 16; ++i) { const float t = v[i] - mu; q = fmaf(t, t, q); }
  }
  q = wsum(q);
  const float var = rbf(q * (1.f / D));
  den = rbf(sqrtf(rbf(var + 1e-5f)));
}
template <int J0 = 0, int J1 = 9>
__device__ __forceinline__ void slab_dma(const u16* base, int stride_b, unsigned lds_dst, int wave, int lane) {
  const u32 v0 = (lane >> 3) * stride_b + (((lane & 7) ^ ((wave & 1) * 4 + (lane >> 4))) << 4), v1 = v0 ^ 32u;
  const u32 v4 = wave >= 4 ? v1 : v0;
  const char* gb = (const char*)base + (size_t)wave * 8 * stride_b;
#pragma unroll
  for (int j = J0; j < J1; ++j) dma(gb + (size_t)j * 64 * stride_b, j < 4 ? v0 : j == 4 ? v4 : v1, lds_dst + (j * 8 + wave) * 1024);
}
__device__ __forceinline__ void slab_frags(const unsigned char* lds, int base, int wave, int lane, u32x4 (&wf)[9]) {
  const int g = lane >> 4, q = (lane & 15) >> 2, p = lane & 3, hf = p >> 1;
  const int a0 = base + (hf * 288 + 8 * g + q) * 128 + ((wave ^ ((g & 1) * 4 + (q >> 1)) ^ (hf * 2)) << 4) + (p & 1) * 8, a1 = (a0 ^ 32) + 512;
#pragma unroll
  for (int s = 0; s < 9; ++s) {
    const u32x2 lo = trd(lds, a0 + s * 4096), hi = trd(lds, a1 + s * 4096);
    wf[s] = u32x4{lo[0], lo[1], hi[0], hi[1]};
  }
}
__device__ __forceinline__ void img_frags(const unsigned char* lds, int img, int lane, u32x4 (&af)[9]) {
#pragma unroll
  for (int s = 0; s < 9; ++s) af[s] = *(const u32x4*)(lds + img + (s * 64 + lane) * 16);
}
__device__ __forceinline__ f32x4 gemv9(const u32x4 (&wf)[9], const u32x4 (&af)[9]) {
  f32x4 acc[3];
  SCHED();
#pragma unroll
  for (int s = 0; s < 9; ++s) acc[s % 3] = mfma(wf[s], af[s], s < 3 ? f32x4{0.f, 0.f, 0.f, 0.f} : acc[s % 3]);
  return (acc[0] + acc[1]) + acc[2];
}
__device__ __forceinline__ int img_off(int b, int k8) {
  const int hf = k8 >= 36 ? 1 : 0, kk = k8 - hf * 36;
  return ((kk >> 2) * 64 + (kk & 3) * 16 + hf * 8 + b) * 16;
}
__device__ __forceinline__ u32 tag_x(u32x4 v, u32 e0, u32 e1) { return (v[2] ^ e0) | (v[3] ^ e1); }
__device__ __forceinline__ bool tag_ok(u32x4 v, u32 e0, u32 e1) { return tag_x(v, e0, e1) == 0u; }

constexpr int CODE_BYTES = 31140;

__global__ void __launch_bounds__(NT, 2) fused_kernel(const u16* __restrict__ x, const u16* __restrict__ kc, const u16* __restrict__ vc,
                                                      const u16* __restrict__ wq, const u16* __restrict__ wo, const u16* __restrict__ w_r,
                                                      const u16* __restrict__ wg, const u16* __restrict__ wu, const u16* __restrict__ wd,
                                                      const u16* __restrict__ c, const u16* __restrict__ w_mod, unsigned char* ws, u16* out) {
  __shared__ __attribute__((aligned(16))) unsigned char lds[LDS_BYTES];
  const int w = blockIdx.x, xcd = w & 7, idx = w >> 3;
  const int tid = threadIdx.x, lane = tid & 63, wave = __builtin_amdgcn_readfirstlane(tid >> 6);
  const int n = lane & 15, grp = lane >> 4;
  const rsrc_t rs = __builtin_amdgcn_make_buffer_rsrc(ws, 0, W_END, 0x00020000);
  const unsigned lds_base = (unsigned)(uintptr_t)(lds_t*)lds;
  {
    unsigned long long pc0; asm volatile("s_getpc_b64 %0" : "=s"(pc0));
    if (w >= 224 && wave == CPW) dma_keep((const void*)pc0, min((w >> 3) * 8192 - 28 * 8192 + lane * 128, CODE_BYTES - 128), lds_base + 107520);
    SCHED();
  }
  const unsigned long long epoch = (llvm_dispatch_id() + 1ull) * 0x9E3779B97F4A7C15ull;
  const u32 e0 = (u32)epoch, e1 = (u32)(epoch >> 32);
  asm volatile("" : "+s"(vc), "+s"(w_r), "+s"(wu), "+s"(wd), "+s"(out), "+s"(x), "+s"(w_mod), "+s"(wg), "+s"(wq), "+s"(wo));

  const int mw = idx < 6 ? idx * 8 + xcd : -1;
  const int md = mw < 18 ? mw : -1;
  const int ml = mw >= 18 ? mw : mw >= 0 && mw < 6 ? 48 + mw : -1;
  const int lnb = mw >= 6 && mw < 14 ? mw - 6 : -1;
  const int qs = idx >= 6 && idx < 14 ? xcd * 8 + idx - 6 : -1;
  const int oj = idx >= 14 && idx < 23 ? idx - 14 : -1, op = xcd;
  const int gs = idx >= 23 ? xcd * 9 + idx - 23 : -1;

  const int s_att = w & 1, kvh = (w >> 1) & 15, b_att = w >> 5;
  {
    const size_t pairbase = ((size_t)(b_att * 16 + kvh) * SK + (SK - WIN)) * 256;
    const char* kp = (const char*)kc + pairbase;
    const char* vp = (const char*)vc + pairbase;
    const int wpart = (wave & 3) * 4096 + s_att * 16384 + (wave >> 2) * 262144;
    const int ko = wpart + (n >> 1) * 256 + (n & 1) * 64 + grp * 16;
    const int vo = wpart + n * 16 + grp * 512;
#define KLD(u) do { unsigned long long t_; asm volatile("s_mov_b64 %1, %3\n\tglobal_load_dwordx4 %0, %2, %1 offset:%4 nt" : "=v"(kb[u]), "=&s"(t_) : "v"(ko), "s"(kp + ((u) >> 2) * 32768), "i"((((u) >> 1) & 1) * 2048 + ((u) & 1) * 128) : "memory"); } while (0)
#define VLD(u) do { unsigned long long t_; asm volatile("s_mov_b64 %1, %3\n\tglobal_load_dwordx4 %0, %2, %1 offset:%4 nt" : "=v"(vb[u]), "=&s"(t_) : "v"(vo), "s"(vp + ((u) >> 2) * 32768), "i"((((u) >> 1) & 1) * 2048 + ((u) & 1) * 256) : "memory"); } while (0)
#define TIE8(a, o) asm volatile("" : "+v"(a[o]), "+v"(a[(o) + 1]), "+v"(a[(o) + 2]), "+v"(a[(o) + 3]), "+v"(a[(o) + 4]), "+v"(a[(o) + 5]), "+v"(a[(o) + 6]), "+v"(a[(o) + 7]))
    const int pr_ = gs >= 0 ? PR2 : mw >= 18 ? PR3 : PR;
    const u32 tp0 = (u32)__builtin_amdgcn_s_memrealtime() + (gs >= 0 ? OF : mw >= 18 ? OL : oj >= 0 ? OO : OQ);
#define PACE(i) do { _Pragma("nounroll") while ((int)((u32)__builtin_amdgcn_s_memrealtime() - tp0) < (i) * pr_ / 4) __builtin_amdgcn_s_sleep(2); SCHED(); } while (0)
#define REP4(M, u, W) do { M(u); W; M((u) + 1); W; M((u) + 2); W; M((u) + 3); W; } while (0)
    static_assert(DK <= 7 && DV <= 7, "the hand-placed waits assume it");
    u32x4 kb[32], vb[32];
    const int pz = mw >= 18 ? mw - 18 : idx >= 14 ? w - 82 : -1;
#define POISON() do { if (pz >= 0) { _Pragma("unroll") for (int i = 0; i < 2; ++i) { const int g_ = pz * PZ + i * NT + tid; \
      if (i * NT + tid < PZ && g_ < W_MS / 16) st128(rs, g_ * 16, u32x4{0xffffffffu, 0xffffffffu, 0xffffffffu, 0xffffffffu}); } } SCHED(); } while (0)
#define PFLAG() do { if (pz >= 0 && tid == 448) st64(rs, W_FL + pz * 8, u32x2{e0, e1}); SCHED(); } while (0)
#define SILU_IMAGE(c0, c1) do { float f[8]; unpack8(c0, f); u32x4 o; \
        _Pragma("unroll") for (int j = 0; j < 4; ++j) o[j] = cvt2(silu(f[2 * j]), silu(f[2 * j + 1])); \
        const int b_ = tid / 72, ch_ = tid - b_ * 72; \
        *(u32x4*)(lds + L_IMG + img_off(b_, ch_)) = o; \
        if (wave == 0) { unpack8(c1, f); \
          _Pragma("unroll") for (int j = 0; j < 4; ++j) o[j] = cvt2(silu(f[2 * j]), silu(f[2 * j + 1])); \
          *(u32x4*)(lds + L_IMG + img_off(7, 8 + lane)) = o; } } while (0)
    if (md >= 0) {
      u32x4 c0, c1, x0, x1; u32x2 xc;
      __builtin_amdgcn_s_waitcnt(0x0f70);
      int ho = W_H + img_off(n & 7, (md >= 9 ? md - 9 : md) * 8 + wave) * 2 + (grp & 1) * 16;
      ald128(c0, c, tid * 16);
      ald128(c1, c, (512 + lane) * 16);
      ald128(x0, x, wave * 1152 + lane * 16);
      ald128(x1, x, wave * 1152 + 1024 + (lane & 7) * 16);
      ald64(xc, x, (n & 7) * 1152 + ((md >= 9 ? md - 9 : 0) * 64 + wave * 8 + (grp & 1) * 4) * 2);
      TL(35);
      BAR();
      float* st = (float*)(lds + L_TILE + 1024);
      if (wave < 4) {
        slab_dma(w_mod + md * 64, NMOD * 2, lds_base + L_W, wave, lane);
        asm volatile("s_waitcnt vmcnt(9)" ::: "memory");
      } else asm volatile("s_waitcnt vmcnt(0)" ::: "memory");
      asm volatile("s_nop 0 ; both branches above cover V" : "+v"(c0), "+v"(c1), "+v"(x0), "+v"(x1), "+v"(xc) :: "memory");
      {
        SILU_IMAGE(c0, c1);
        float v[16]; unpack8(x0, v); unpack8(x1, v + 8);
        float mean, den; ln_row(v, lane < 8, mean, den);
        if (lane == 0) { st[wave] = mean; st[8 + wave] = den; }
      }
      if (wave >= 4) slab_dma(w_mod + md * 64, NMOD * 2, lds_base + L_W, wave, lane);
      lds_barrier();
      REP4(KLD, 0, );
      u32x4 wf[9], af[9];
      img_frags(lds, L_IMG, lane, af);
      const float mean = st[n & 7], rd = frcp(st[8 + (n & 7)]);
      float ln[4] = {rbf(rbf(lo16(xc[0]) - mean) * rd), rbf(rbf(hi16(xc[0]) - mean) * rd), rbf(rbf(lo16(xc[1]) - mean) * rd), rbf(rbf(hi16(xc[1]) - mean) * rd)};
#pragma unroll
      for (int s = 0; s < 9; ++s) asm volatile("" : "+v"(af[s]));
      asm volatile("" : "+v"(ln[0]), "+v"(ln[1]), "+v"(ln[2]), "+v"(ln[3]), "+v"(ho));
      WAITV(4); BAR(); SCHED();
      slab_frags(lds, L_W, wave, lane, wf);
      const f32x4 s = half_sum(gemv9(wf, af));
      f32x4 r = s;
      if (md >= 9) {
#pragma unroll
        for (int j = 0; j < 4; ++j) r[j] = ln[j] * rbf(1.f + rbf(s[j]));
      }
      if (n < 8 && grp < 2) st128(rs, ho + (md >= 9 ? 18432 : 0), u32x4{cvt2(r[0], r[1]), cvt2(r[2], r[3]), e0, e1});
      SCHED();
      TL(1);
      BAR();
      KLD(4); KLD(5); WAITV(DK);
      KLD(6); WAITV(DK); KLD(7); WAITV(DK); KLD(8); WAITV(DK); KLD(9); WAITV(DK); KLD(10); WAITV(DK);
      KLD(11); WAITV(DK); REP4(KLD, 12, WAITV(DK)); REP4(KLD, 16, WAITV(DK));
      REP4(KLD, 20, WAITV(DK)); KLD(24); WAITV(DK); KLD(25);
    } else {
      constexpr int L_LAND = 98304;
      const unsigned land = lds_base + L_LAND + wave * 6144;
      const unsigned char* lp = lds + L_LAND + wave * 6144 + lane * 16;
      const int sl_ = (3 - (int)((uintptr_t)wq >> 7)) & 7;
      const bool slowq = qs >= 0 && (qs & 7) == sl_;
      if (gs >= 0 && wave == 0) dma_keep((const char*)wq + (size_t)(idx - 23) * 64 * 8192 + xcd * 1024 + sl_ * 128, lane * 8192, land);
      if (gs >= 0 && (wave == 1 || (wave == 2 && xcd < 2))) dma_keep((const char*)w_mod + (size_t)((idx - 23) * 64 + 32) * (NMOD * 2) + xcd * 128 + (wave - 1) * 2048, (lane & 31) * (NMOD * 2) + (wave == 1 ? (lane >> 5) * 1024 : 0), land);
      POISON();
      const u32 v0 = (lane >> 3) * 1152 + (((lane & 7) ^ ((lane >> 4) & 1)) << 4);
      const char* gb = (const char*)(wo + (size_t)(op * 512 + wave * 64) * D + oj * 64);
      if (ml >= 0) { dma_sc(c, tid * 16, land); dma_sc(c, (512 + lane) * 16, land + 1024); }
#define WK(j) do { if (oj >= 0) { if ((j) < 8) dma(gb + (j) * 8 * 1152, v0 ^ (((j) & 1) * 32 + ((j) >> 2) * 64), lds_base + L_W + (wave * 8 + (j)) * 1024); } \
                   else if (slowq) { if ((j) == 8) slab_dma(wq + qs * 64, NQ * 2, lds_base + L_W, wave, lane); } \
                   else if (qs >= 0 && (j) < 9) slab_dma<(j) < 9 ? (j) : 0, (j) < 9 ? (j) + 1 : 0>(wq + qs * 64, NQ * 2, lds_base + L_W, wave, lane); \
                   else if (EP && qs < 0 && (j) >= EP) PACE(j); \
                   KLD(j); if ((j) == 0) TL(32); if ((j) == 4) TL(33); WAITV(DK); if ((j) == 5) TL(34); } while (0)
      WK(0); WK(1); WK(2); WK(3);
      WK(4); WK(5);
      WK(6); WK(7);
      TL(24);
      if (ml >= 0) { const u32x4 c0 = *(const u32x4*)lp, c1 = *(const u32x4*)(lp + 1024); SILU_IMAGE(c0, c1); }
      BAR();
      PFLAG();
      WK(8); WK(9); WK(10); WK(11);
      TL(25);
      PACE(12); REP4(KLD, 12, WAITV(DK));
      TL(26);
      const int hg0 = (wave * 144 + lane) * 16, hg2 = (wave * 144 + 128 + n) * 16;
#define HREQ() do { dma_sc(ws + W_H, hg0, land); dma_sc(ws + W_H + 18432, hg0, land + 1024); dma_sc(ws + W_H + 1024, hg0, land + 2048); \
                    dma_sc(ws + W_H + 18432 + 1024, hg0, land + 3072); dma_sc(ws + W_H, hg2, land + 4096); dma_sc(ws + W_H + 18432, hg2, land + 5120); } while (0)
#define HGET() do { _Pragma("unroll") for (int i = 0; i < 6; ++i) hq[i] = *(const u32x4*)(lp + i * 1024); } while (0)
#define HQOK() __all((tag_x(hq[0], e0, e1) | tag_x(hq[1], e0, e1) | tag_x(hq[2], e0, e1) | tag_x(hq[3], e0, e1) | tag_x(hq[4], e0, e1) | tag_x(hq[5], e0, e1)) == 0u)
#define HOK() __all((tag_x(hp[0], e0, e1) | tag_x(hp[1], e0, e1) | tag_x(hp[2], e0, e1)) == 0u)
      if (qs >= 0) HREQ();
      PACE(16); REP4(KLD, 16, WAITV(DK));
      if (qs >= 0) WAITV(4);
      TL(27);
      PACE(20); KLD(20); KLD(21);
      if (qs >= 0) {
        u32x4 hq[6]; HGET();
        _Pragma("nounroll") for (int sp = 0; sp < SPIN_CAP && !HQOK(); ++sp) { nap(16); HREQ(); WAITV(0); HGET(); }
        TL(2);
#define HSUM(a, b) cvt2(lo16(a) + lo16(b), hi16(a) + hi16(b))
        *(u32x2*)(lds + L_IMG + hg0 / 2) = u32x2{HSUM(hq[0][0], hq[1][0]), HSUM(hq[0][1], hq[1][1])};
        *(u32x2*)(lds + L_IMG + 512 + hg0 / 2) = u32x2{HSUM(hq[2][0], hq[3][0]), HSUM(hq[2][1], hq[3][1])};
        if (lane < 16) *(u32x2*)(lds + L_IMG + hg2 / 2) = u32x2{HSUM(hq[4][0], hq[5][0]), HSUM(hq[4][1], hq[5][1])};
      } else WAITV(DK);
      KLD(22); KLD(23);
      if (qs >= 0) {
        u32x4 wf[9], af[9];
        lds_barrier();
        slab_frags(lds, L_W, wave, lane, wf);
        img_frags(lds, L_IMG, lane, af);
        const f32x4 s = half_sum(gemv9(wf, af));
        if (n < 8 && grp < 2) *(u32x2*)(lds + L_TILE + n * 128 + wave * 16 + grp * 8) = u32x2{cvt2(s[0], s[1]), cvt2(s[2], s[3])};
      } else WAITV(DK);
      PACE(24); KLD(24); KLD(25);
      if (qs >= 0) {
        lds_barrier();
        if (wave < 2) {
          const u32x2 t2 = *(const u32x2*)(lds + L_TILE + tid * 8);
          st128(rs, W_Q + ((tid >> 4) * 1024 + qs * 16 + n) * 16, u32x4{t2[0], t2[1], e0, e1});
        }
        TL(3);
      }
      SCHED();
    }
    const unsigned char* qg = ws + W_Q + (b_att * 1024 + kvh * 64) * 16;
    KLD(26); WAITV(DK); KLD(27); WAITV(DK); PACE(28); KLD(28); WAITV(DK); KLD(29); WAITV(DK);
    PACE(30); KLD(30); WAITV(DK); KLD(31); WAITV(DK);
    TL(28);
    PACE(32); VLD(0); WAITV(DK); VLD(1); WAITV(DK); PACE(34); VLD(2); WAITV(DK); VLD(3); WAITV(DK);
    if (wave == 1) dma_sc(qg, lane * 16, lds_base + L_B + 1024);
    PACE(36); VLD(4); WAITV(DK); VLD(5); WAITV(DK); PACE(38); VLD(6); WAITV(DK); VLD(7); WAITV(DK);
    if (wave == 1) dma_sc(qg, lane * 16, lds_base + L_B + 1024);
    TL(29);
#define QLOOK(b) do { if ((QL & (b)) && wave == 1) dma_sc(qg, lane * 16, lds_base + L_B + 1024); } while (0)
    PACE(40); QLOOK(1); VLD(8); WAITV(DV); VLD(9); WAITV(DV); PACE(42); QLOOK(2); VLD(10); WAITV(DV); VLD(11); WAITV(DV);
    TIE8(kb, 0); TIE8(kb, 8); TIE8(kb, 16); TIE8(kb, 24);
    PACE(44); QLOOK(4); VLD(12); VLD(13);
    u32x4 qf[2];
    {
      const u32x4* ql = (const u32x4*)(lds + L_B + 1024 + (((n >> 1) & 1) * 32 + (n & 1) * 8 + grp * 2) * 16);
      u32x4 g[4];
      _Pragma("nounroll") for (int sp = 0; sp < SPIN_CAP; ++sp) {
        g[0] = ql[0]; g[1] = ql[1]; g[2] = ql[16]; g[3] = ql[17];
        if (__all((tag_x(g[0], e0, e1) | tag_x(g[1], e0, e1) | tag_x(g[2], e0, e1) | tag_x(g[3], e0, e1)) == 0u)) break;
        if (wave == 1 && sp >= QN) { dma_sc(qg, lane * 16, lds_base + L_B + 1024); WAITV(0); } else nap(4);
      }
      asm volatile("" ::: "memory");
      g[0] = ql[0]; g[1] = ql[1]; g[2] = ql[16]; g[3] = ql[17];
      qf[0] = n < 4 ? u32x4{g[0][0], g[0][1], g[1][0], g[1][1]} : u32x4{0, 0, 0, 0};
      qf[1] = n < 4 ? u32x4{g[2][0], g[2][1], g[3][0], g[3][1]} : u32x4{0, 0, 0, 0};
    }
    TL(4);
    constexpr float SCALE = 0.088388347648318440f;
    f32x2 sc[16];
    float mx = -INFINITY;
#pragma unroll
    for (int T = 0; T < 16; ++T) {
      f32x4 acc = mfma(kb[T * 2], qf[0], f32x4{0.f, 0.f, 0.f, 0.f});
      acc = mfma(kb[T * 2 + 1], qf[1], acc);
      sc[T] = f32x2{acc[0] + dppf<0xB1>(acc[1]), acc[2] + dppf<0xB1>(acc[3])};
      if (T == 7 || T == 15) { SCHED(); if (T == 7) VLD(14); else VLD(15); SCHED(); }
    }
#pragma unroll
    for (int T = 0; T < 16; ++T)
#pragma unroll
      for (int j = 0; j < 2; ++j) { const float v = rbf(rbf(sc[T][j]) * SCALE); sc[T][j] = v; mx = fmaxf(mx, v); }
    SCHED(); VLD(16); SCHED();
    mx = fmaxf(mx, __shfl_xor(mx, 16));
    mx = fmaxf(mx, __shfl_xor(mx, 32));
    float ls = 0.f;
#pragma unroll
    for (int T = 0; T < 16; ++T)
#pragma unroll
      for (int j = 0; j < 2; ++j) { sc[T][j] = __expf(sc[T][j] - mx); ls += sc[T][j]; }
    SCHED(); VLD(17); SCHED();
    ls += __shfl_xor(ls, 16);
    ls += __shfl_xor(ls, 32);
    float* stat = (float*)(lds + L_B);
    if (lane == 0 || lane == 2) *(f32x2*)(stat + (wave * 2 + (lane >> 1)) * 2) = f32x2{mx, ls};
    SCHED(); VLD(18); VLD(19); SCHED();
    lds_barrier();
    float M = -INFINITY, L = 0.f;
    {
      f32x2 t[8];
#pragma unroll
      for (int q = 0; q < 8; ++q) { t[q] = *(const f32x2*)(stat + (q * 2 + ((n >> 1) & 1)) * 2); M = fmaxf(M, t[q].x); }
#pragma unroll
      for (int q = 0; q < 8; ++q) L = fmaf(__expf(t[q].x - M), t[q].y, L);
    }
    if (tid < 8) st128(rs, W_ST + (w * 8 + lane) * 16, u32x4{__float_as_uint(M), __float_as_uint(L), e0, e1});
    SCHED();
#define VTR(G) do { u32x4 f_[8]; _Pragma("unroll") for (int t = 0; t < 8; ++t) _Pragma("unroll") for (int j = 0; j < 4; ++j) \
      f_[t][j] = (t & 1) ? phi(vb[(G) * 8 + 2 * j][t >> 1], vb[(G) * 8 + 2 * j + 1][t >> 1]) : plo(vb[(G) * 8 + 2 * j][t >> 1], vb[(G) * 8 + 2 * j + 1][t >> 1]); \
      _Pragma("unroll") for (int t = 0; t < 8; ++t) { vb[(G) * 8 + t] = f_[t]; asm volatile("" : "+v"(vb[(G) * 8 + t])); } SCHED(); } while (0)
    WAITV(12);
    TIE8(vb, 0);
    VTR(0);
    TL(30);
    PACE(52); VLD(20); WAITV(5);
    TIE8(vb, 8);
    VTR(1);
    VLD(21); WAITV(DV);
    const int po = W_ST + ((w ^ 1) * 8 + (n & 2)) * 16;
    int l1 = lane; asm volatile("" : "+v"(l1));
    const int fl = 1024 + min(l1, NPZ / 2 - 65) * 16;
    if (wave == 1) { dma_sc(ws + W_FL, l1 * 16, lds_base + L_B + 2048); dma_sc(ws + W_FL, fl, lds_base + L_B + 3072); }
    PACE(54);
    VLD(22); WAITV(DV); VLD(23); WAITV(DV);
    u32x4 ps;
    ald128sc(ps, ws, po);
    PACE(56);
    VLD(24); WAITV(DV); VLD(25); WAITV(DV);
    TL(31);
    PACE(58);
    VLD(26); WAITV(DV); VLD(27); WAITV(DV);
    u32x4 ps2;
    VLD(28); VLD(29); WAITV(6);
    asm volatile("" : "+v"(ps));
    TIE8(vb, 16);
    ald128sc(ps2, ws, po);
    VLD(30); VLD(31);
    VTR(2);
    if (!__all(tag_ok(ps, e0, e1))) {
      WAITV(2);
      asm volatile("" : "+v"(ps2));
      ps = ps2;
      if (!__all(tag_ok(ps, e0, e1))) {
        int sp = 0;
        _Pragma("nounroll") do { nap(1); ps = ld128(rs, po); } while (!__all(tag_ok(ps, e0, e1)) && ++sp < SPIN_CAP);
      }
    }
#define FOK(a, b) __all(((a[0] ^ e0) | (a[1] ^ e1) | tag_x(a, e0, e1) | (b[0] ^ e0) | (b[1] ^ e1) | tag_x(b, e0, e1)) == 0u)
    if (wave == 1 && !FOK((*(const u32x4*)(lds + L_B + 2048 + l1 * 16)), (*(const u32x4*)(lds + L_B + 3072 + l1 * 16)))) {
      _Pragma("nounroll") for (int sp = 0; sp < SPIN_CAP; ++sp) {
        nap(1);
        const u32x4 a = ld128(rs, W_FL + l1 * 16), b = ld128(rs, W_FL + fl);
        if (FOK(a, b)) break;
      }
    }
    float fac;
    {
      const float M2 = __uint_as_float(ps[0]), L2 = __uint_as_float(ps[1]);
      const float Mg = fmaxf(M, M2);
      const float Lg = __expf(M - Mg) * L + __expf(M2 - Mg) * L2;
      fac = __expf(mx - Mg) / Lg;
    }
    TL(5);
    u32x4 pf[4];
#pragma unroll
    for (int G = 0; G < 4; ++G) {
#pragma unroll
      for (int j = 0; j < 4; ++j) pf[G][j] = cvt2(sc[4 * G + j][0] * fac, sc[4 * G + j][1] * fac);
    }
    f32x4 oa[8];
#pragma unroll
    for (int t = 0; t < 8; ++t) oa[t] = f32x4{0.f, 0.f, 0.f, 0.f};
    SCHED();
#pragma unroll
    for (int G = 0; G < 2; ++G)
#pragma unroll
      for (int t = 0; t < 8; ++t) oa[t] = mfma(pf[G], vb[G * 8 + t], oa[t]);
    SCHED();
#pragma unroll
    for (int t = 0; t < 8; ++t) oa[t] = mfma(pf[2], vb[16 + t], oa[t]);
    SCHED();
    int t2_ = threadIdx.x; asm volatile("" : "+v"(t2_));
    const int tid = t2_, lane = tid & 63, n = lane & 15, grp = lane >> 4;
    int oo = W_O + ((kvh * 8 + b_att) * 2 + s_att) * 1024 + ((lane >> 4) * 16 + (lane & 1) * 8 + ((lane >> 1) & 7)) * 16;
    int mw_ = L_C + wave * 1024 + n * 32, mr_ = L_C + lane * 16;
    asm volatile("" : "+v"(oo), "+v"(mw_), "+v"(mr_), "+v"(pf[3]));
    constexpr int L_H2 = L_W + 73728;
    constexpr int L_ACT = L_C + 4608;
    int ia[3], ra[4], yo[3], fo[7], tw, tr_;
    const bool late = gs >= 0 || mw >= 18;
    if (late) {
      BAR();
      slab_dma<0, 4>(gs >= 0 ? wg + gs * 64 : w_mod + ml * 64, gs >= 0 ? FF * 2 : NMOD * 2, lds_base + L_W + (gs >= 0 ? 73728 : 0), wave, lane);
      {
    const int b = wave;
#pragma unroll
    for (int i = 0; i < 3; ++i) {
      const int k = i < 2 ? i * 256 + lane * 4 : 512 + n * 4, hf = k >= 288 ? 1 : 0, kk = k - hf * 288, s = kk >> 5, g = (kk >> 3) & 3;
      ia[i] = L_H2 + (s * 64 + g * 16 + ((hf * 8 + b) ^ ((s & 3) * 4 + g))) * 16 + (kk & 7) * 2;
    }
#pragma unroll
    for (int i = 0; i < 4; ++i) ra[i] = L_H2 + (grp * 16 + (n ^ (i * 4 + grp))) * 16;
    tw = L_ACT + n * 144 + wave * 16 + (grp & 1) * 8 + (grp >> 1) * 4; tr_ = L_ACT + (n & 7) * 144 + grp * 16;
    yo[0] = W_Y + (((wave * 8 + grp * 2) * NG + gs) * 16 + n) * 16; yo[1] = yo[0] + NG * 256;
    yo[2] = W_Y + ((((64 + wave * 2 + (grp >> 1)) * NG + gs) * 2 + (grp & 1)) * 8 + n) * 16;
    {
      const int b_ = lane >> 3, cc = lane & 7, col = gs * 8 + cc, hf = col >= 288 ? 1 : 0, kk = col - hf * 288, s_ = kk >> 5, g = (kk >> 3) & 3;
      fo[0] = W_M + ((3 * 8 + b_) * 144 + gs * 2 + (cc >> 2)) * 16;
      fo[1] = W_X1 + (b_ * 152 + gs * 2 + (cc >> 2)) * 16; fo[2] = W_X1 + (b_ * 152 + 144) * 16;
      fo[3] = W_Y + (gs * 1152 + tid) * 16;
      fo[4] = L_H2 + (s_ * 64 + g * 16 + ((hf * 8 + b_) ^ ((s_ & 3) * 4 + g))) * 16 + cc * 2;
      fo[5] = L_C + (((cc >> 2) * 8 + b_) * 4 + (cc & 3)) * 4;
      fo[6] = (b_ * D + col) * 2;
    }
    asm volatile("" : "+v"(ia[0]), "+v"(ia[1]), "+v"(ia[2]), "+v"(ra[0]), "+v"(ra[1]), "+v"(ra[2]), "+v"(ra[3]), "+v"(tw), "+v"(tr_), "+v"(yo[0]), "+v"(yo[1]), "+v"(yo[2]),
                 "+v"(fo[0]), "+v"(fo[1]), "+v"(fo[2]), "+v"(fo[3]), "+v"(fo[4]), "+v"(fo[5]), "+v"(fo[6]));
      }
      WAITV(6);
    } else WAITV(2);
    u32x4 vf[8];
    asm volatile("s_nop 0 ; both branches above cover V up to the last two" : "+v"(vb[24]), "+v"(vb[25]), "+v"(vb[26]), "+v"(vb[27]), "+v"(vb[28]), "+v"(vb[29]));
    asm volatile("" :: "v"(ps2));
#pragma unroll
    for (int t = 0; t < 8; ++t)
#pragma unroll
      for (int j = 0; j < 3; ++j) vf[t][j] = (t & 1) ? phi(vb[24 + 2 * j][t >> 1], vb[25 + 2 * j][t >> 1]) : plo(vb[24 + 2 * j][t >> 1], vb[25 + 2 * j][t >> 1]);
#pragma unroll
    for (int t = 0; t < 8; ++t) asm volatile("" : "+v"(vf[t][0]), "+v"(vf[t][1]), "+v"(vf[t][2]));
    if (late) WAITV(4); else WAITV(0);
    asm volatile("s_nop 0 ; both branches above cover V" : "+v"(vb[30]), "+v"(vb[31]));
#pragma unroll
    for (int t = 0; t < 8; ++t) {
      vf[t][3] = (t & 1) ? phi(vb[30][t >> 1], vb[31][t >> 1]) : plo(vb[30][t >> 1], vb[31][t >> 1]);
      oa[t] = mfma(pf[3], vf[t], oa[t]);
    }
    TL(6);
    if (grp == 0) {
#pragma unroll
      for (int g = 0; g < 2; ++g) {
        *(f32x4*)(lds + mw_ + g * 512) = f32x4{oa[0][2 * g], oa[1][2 * g], oa[2][2 * g], oa[3][2 * g]};
        *(f32x4*)(lds + mw_ + g * 512 + 16) = f32x4{oa[4][2 * g], oa[5][2 * g], oa[6][2 * g], oa[7][2 * g]};
      }
    }
    lds_barrier();
    if (wave == 0) {
      f32x4 m[8];
#pragma unroll
      for (int q = 0; q < 8; ++q) m[q] = *(const f32x4*)(lds + mr_ + q * 1024);
      SCHED();
      const f32x4 o = ((m[0] + m[1]) + (m[2] + m[3])) + ((m[4] + m[5]) + (m[6] + m[7]));
      st128(rs, oo, u32x4{sanf(o[0]), sanf(o[1]), sanf(o[2]), sanf(o[3])});
    }
    TL(7); SCHED();
  if (gs >= 0) {
    const int b = wave;
    BAR(); SCHED();
    u32x4 bg[9], bu[9], wdf[2][5];
    {
      u32x2 rd[2][8];
      slab_dma<4, 9>(wg + gs * 64, FF * 2, lds_base + L_W + 73728, wave, lane);
      TL(23);
      BAR();
      const char* db = (const char*)(wd + (size_t)gs * 64 * D + wave * 64);
      const u32 dv = grp * 9216 + n * 8;
#pragma unroll
      for (int ks = 0; ks < 2; ++ks)
#pragma unroll
        for (int i = 0; i < 8; ++i) ald64nt(rd[ks][i], db + (ks * 32 + i) * 1152, dv);
      {
        const int r = lane >> 3;
        dma((const char*)(wd + (size_t)(gs * 64 + wave * 8) * D + 512), r * 1152 + (((lane & 7) ^ (r & 2) ^ ((wave & 1) * 4)) << 4), lds_base + L_B + wave * 1024);
      }
      TL(19);
      asm volatile("s_waitcnt vmcnt(17)\n\ts_barrier" ::: "memory");
      TL(22);
      slab_frags(lds, L_W + 73728, wave, lane, bg);
      SCHED();
      slab_dma(wu + gs * 64, FF * 2, lds_base + L_W, wave, lane);
      TL(20);
      asm volatile("s_waitcnt vmcnt(10)" : "+v"(rd[0][0]), "+v"(rd[0][1]), "+v"(rd[0][2]), "+v"(rd[0][3]), "+v"(rd[0][4]), "+v"(rd[0][5]), "+v"(rd[0][6]), "+v"(rd[0][7]),
                   "+v"(rd[1][0]), "+v"(rd[1][1]), "+v"(rd[1][2]), "+v"(rd[1][3]), "+v"(rd[1][4]), "+v"(rd[1][5]), "+v"(rd[1][6]), "+v"(rd[1][7]) :: "memory");
#pragma unroll
      for (int ks = 0; ks < 2; ++ks)
#pragma unroll
        for (int j = 0; j < 4; ++j) {
          wdf[ks][0][j] = plo(rd[ks][2 * j][0], rd[ks][2 * j + 1][0]); wdf[ks][1][j] = phi(rd[ks][2 * j][0], rd[ks][2 * j + 1][0]);
          wdf[ks][2][j] = plo(rd[ks][2 * j][1], rd[ks][2 * j + 1][1]); wdf[ks][3][j] = phi(rd[ks][2 * j][1], rd[ks][2 * j + 1][1]);
        }
#pragma unroll
      for (int t = 0; t < 4; ++t) asm volatile("" : "+v"(wdf[0][t]), "+v"(wdf[1][t]));
      TL(21);
    }
    asm volatile("s_waitcnt vmcnt(0) lgkmcnt(0)\n\ts_barrier" ::: "memory");
    {
      u32x4 hp[3];
      const int h0 = W_H2 + (b * 144 + lane) * 16, h2o = W_H2 + (b * 144 + 128 + n) * 16;
      hp[0] = ld128(rs, h0); hp[1] = ld128(rs, h0 + 1024); hp[2] = ld128(rs, h2o);
      slab_frags(lds, L_W, wave, lane, bu);
      const int q = n >> 2, p = lane & 3;
      const int a0 = L_B + (8 * grp + q) * 128 + ((((wave & 3) * 2 + (p >> 1)) ^ (q & 2) ^ ((grp & 1) * 4)) << 4) + (p & 1) * 8;
#pragma unroll
      for (int ks = 0; ks < 2; ++ks) {
        const u32x2 lo = trd(lds, a0 + ks * 4096), hi = trd(lds, a0 + ks * 4096 + 512);
        wdf[ks][4] = u32x4{lo[0], lo[1], hi[0], hi[1]};
      }
      TL(12);
#pragma unroll
      for (int s = 0; s < 9; ++s) asm volatile("" : "+v"(bg[s]), "+v"(bu[s]));
      asm volatile("" : "+v"(wdf[0][4]), "+v"(wdf[1][4]));
      if (!HOK()) {
        int sp = 0;
        _Pragma("nounroll") do { nap(2); hp[0] = ld128(rs, h0); hp[1] = ld128(rs, h0 + 1024); hp[2] = ld128(rs, h2o); } while (!HOK() && ++sp < SPIN_CAP);
      }
      TL(13);
      *(u32x2*)(lds + ia[0]) = u32x2{hp[0][0], hp[0][1]};
      *(u32x2*)(lds + ia[1]) = u32x2{hp[1][0], hp[1][1]};
      if (lane < 16) *(u32x2*)(lds + ia[2]) = u32x2{hp[2][0], hp[2][1]};
      lds_barrier();
      u32x4 af[9];
#pragma unroll
      for (int s = 0; s < 9; ++s) af[s] = *(const u32x4*)(lds + ra[s & 3] + s * 1024);
      SCHED();
      f32x4 ag[2], au[2];
#pragma unroll
      for (int s = 0; s < 9; ++s) {
        ag[s & 1] = mfma(bg[s], af[s], s < 2 ? f32x4{0.f, 0.f, 0.f, 0.f} : ag[s & 1]);
        au[s & 1] = mfma(bu[s], af[s], s < 2 ? f32x4{0.f, 0.f, 0.f, 0.f} : au[s & 1]);
      }
      const f32x2 hg = half_sum2(ag[0] + ag[1]), hu = half_sum2(au[0] + au[1]);
      const u32 act = cvt2(rbf(silu(rbf(hg.x))) * rbf(hu.x), rbf(silu(rbf(hg.y))) * rbf(hu.y));
      if (n < 8) *(u32*)(lds + tw) = act;
    }
    lds_barrier();
    TL(14);
    {
      u32x4 af[2];
#pragma unroll
      for (int ks = 0; ks < 2; ++ks) af[ks] = *(const u32x4*)(lds + tr_ + ks * 64);
      f32x4 d[5];
#pragma unroll
      for (int t = 0; t < 5; ++t) d[t] = mfma(wdf[1][t], af[1], mfma(wdf[0][t], af[0], f32x4{0.f, 0.f, 0.f, 0.f}));
      if (n < 8) {
#pragma unroll
        for (int j = 0; j < 4; ++j)
          st128(rs, yo[j >> 1] + (j & 1) * 128, u32x4{sanf(d[0][j]), sanf(d[1][j]), sanf(d[2][j]), sanf(d[3][j])});
        if (wave < 4)
          st128(rs, yo[2], u32x4{sanf(d[4][0]), sanf(d[4][1]), sanf(d[4][2]), sanf(d[4][3])});
      }
    }
    TL(15);
    {
      float gbv = 0.f, x1v = 0.f, h2 = 0.f, rsel = 0.f;
      if (wave == 7) {
        u32x4 gq, xq, rq;
        _Pragma("nounroll") for (int sp = 0; sp < SPIN_CAP; ++sp) {
          gq = ld128(rs, fo[0]); xq = ld128(rs, fo[1]); rq = ld128(rs, fo[2]);
          if (__all((tag_x(gq, e0, e1) | tag_x(xq, e0, e1) | tag_x(rq, e0, e1)) == 0u)) break;
          nap(1);
        }
        const u32 gw = (lane & 2) ? gq[1] : gq[0], xw = (lane & 2) ? xq[1] : xq[0];
        gbv = (lane & 1) ? hi16(gw) : lo16(gw); x1v = (lane & 1) ? hi16(xw) : lo16(xw);
        h2 = b2f(*(const u16*)(lds + fo[4])); rsel = __uint_as_float(rq[0]);
        asm volatile("" : "+v"(gbv), "+v"(x1v), "+v"(h2), "+v"(rsel));
      }
      WAITV(0);
      u32x4 y[3] = {};
      _Pragma("nounroll") for (int sp = 0; sp < SPIN_CAP; ++sp) {
        y[0] = ld128(rs, fo[3]); y[1] = ld128(rs, fo[3] + 8192);
        if (wave < 2) y[2] = ld128(rs, fo[3] + 16384);
        if (__all(mxu(mxu(mx4(y[0]), mx4(y[1])), mx4(y[2])) != 0xffffffffu)) break;
        nap(1);
      }
      TL(16);
      f32x4 s;
#pragma unroll
      for (int k = 0; k < 4; ++k) s[k] = xsum32(xsum16((__uint_as_float(y[0][k]) + __uint_as_float(y[1][k])) + __uint_as_float(y[2][k])));
      if (lane < 16) *(f32x4*)(lds + L_C + (wave * 16 + lane) * 16) = s;
      lds_barrier();
      if (wave == 7) {
        float t[8];
#pragma unroll
        for (int ww = 0; ww < 8; ++ww) t[ww] = *(const float*)(lds + fo[5] + ww * 256);
        SCHED();
        const float y2 = rbf(rbf(((t[0] + t[1]) + (t[2] + t[3])) + ((t[4] + t[5]) + (t[6] + t[7]))) * rsel);
        const float res = x1v + rbf(gbv * rbf(h2 + y2));
        __builtin_amdgcn_raw_buffer_store_b16((u16)(cvt2(res, 0.f) & 0xffffu), __builtin_amdgcn_make_buffer_rsrc(out, 0, 8 * D * 2, 0x00020000), fo[6], 0, 0);
      }
      TL(17);
    }
  }
  {
  int t3_ = threadIdx.x; asm volatile("" : "+v"(t3_));
  const int tid = t3_, lane = tid & 63, n = lane & 15, grp = lane >> 4;
  if (qs >= 0) {
    const u16* mat = (wave & 1) ? wg : wu;
    const int g0 = ((3 - (int)((uintptr_t)mat >> 7) - xcd * 9) & 7) + (wave >= 4 ? 8 : 0);
    const int r = (idx - 6) * 72 + ((wave >> 1) & 1) * 36 + min(lane, 35);
    if (g0 < 9) dma_keep(mat, r * 9216 + (xcd * 9 + g0) * 128, lds_base + L_W + wave * 256);
  }
  if (ml >= 0) {
    BAR();
    if (mw < 18) slab_dma<0, 4>(w_mod + ml * 64, NMOD * 2, lds_base + L_W, wave, lane);
    slab_dma<4, 9>(w_mod + ml * 64, NMOD * 2, lds_base + L_W, wave, lane);
    u32x4 wf[9], af[9];
    img_frags(lds, L_IMG, lane, af);
    asm volatile("s_waitcnt vmcnt(0)\n\ts_barrier" ::: "memory");
    slab_frags(lds, L_W, wave, lane, wf);
    const f32x4 s = half_sum(gemv9(wf, af));
    if (n < 8 && grp < 2) *(u32x2*)(lds + L_TILE + n * 128 + wave * 16 + grp * 8) = u32x2{cvt2(s[0], s[1]), cvt2(s[2], s[3])};
    lds_barrier();
    if (wave < 2) {
      const u32x2 t2 = *(const u32x2*)(lds + L_TILE + tid * 8);
      const int ch = (ml - 18) / 9, sl = ml - 18 - ch * 9;
      st128(rs, W_M + ((ch * 8 + (tid >> 4)) * 144 + sl * 16 + n) * 16, u32x4{t2[0], t2[1], e0, e1});
    }
    TL(18);
  }

  if (lnb >= 0 && wave < 3) {
    const int b = lnb, g = tid < 144 ? tid : 143;
    const bool on = tid < 144;
    float* ex = (float*)(lds + L_TILE);
    const u32x2 xa = __builtin_amdgcn_raw_buffer_load_b64(__builtin_amdgcn_make_buffer_rsrc((void*)x, 0, 8 * D * 2, 0x00020000), (b * D + g * 4) * 2, 0, 0);
    const u32x2 wra = __builtin_amdgcn_raw_buffer_load_b64(__builtin_amdgcn_make_buffer_rsrc((void*)w_r, 0, D * 2, 0x00020000), g * 8, 0, 0);
    u32x4 mm[3];
    _Pragma("nounroll") for (int sp = 0; sp < SPIN_CAP; ++sp) {
#pragma unroll
      for (int k = 0; k < 3; ++k) mm[k] = ld128(rs, W_M + ((k * 8 + b) * 144 + g) * 16);
      if (__all((tag_x(mm[0], e0, e1) | tag_x(mm[1], e0, e1) | tag_x(mm[2], e0, e1)) == 0u)) break;
      nap(4);
    }
    f32x2 xv[2], ga[2], sb[2], cb1[2], wr[2];
#pragma unroll
    for (int i = 0; i < 2; ++i) {
      xv[i] = up2(xa[i]); wr[i] = up2(wra[i]); ga[i] = up2(mm[0][i]); sb[i] = up2(mm[1][i]); cb1[i] = rbf2(f32x2{1.f, 1.f} + up2(mm[2][i]));
      asm volatile("" : "+v"(xv[i]), "+v"(ga[i]), "+v"(sb[i]), "+v"(cb1[i]), "+v"(wr[i]));
    }
    int ho = W_H2 + (b * 144 + g) * 16, xo = W_X1 + (b * 152 + g) * 16;
    asm volatile("" : "+v"(ho), "+v"(xo));
    u32x4 xp[8];
    _Pragma("nounroll") for (int sp = 0; sp < SPIN_CAP; ++sp) {
#pragma unroll
      for (int p = 0; p < 8; ++p) xp[p] = ld128(rs, W_XP + (p * 8 + b) * 2304 + g * 16);
      if (__all(mxu(mxu(mxu(mx4(xp[0]), mx4(xp[1])), mxu(mx4(xp[2]), mx4(xp[3]))), mxu(mxu(mx4(xp[4]), mx4(xp[5])), mxu(mx4(xp[6]), mx4(xp[7])))) != 0xffffffffu)) break;
      nap(1);
    }
    TL(10);
    f32x2 v[2];
#pragma unroll
    for (int i = 0; i < 2; ++i) {
      f32x2 pp[8];
#pragma unroll
      for (int p = 0; p < 8; ++p) pp[p] = f32x2{__uint_as_float(xp[p][2 * i]), __uint_as_float(xp[p][2 * i + 1])};
      v[i] = rbf2(xv[i] + rbf2(ga[i] * rbf2(((pp[0] + pp[1]) + (pp[2] + pp[3])) + ((pp[4] + pp[5]) + (pp[6] + pp[7])))));
    }
    const f32x2 s2 = v[0] + v[1];
    const float mw = wsum(on ? s2.x + s2.y : 0.f) * (wave < 2 ? 1.f / 256 : 1.f / 64);
    const f32x2 t0 = v[0] - f32x2{mw, mw}, t1 = v[1] - f32x2{mw, mw}, q2 = t0 * t0 + t1 * t1;
    const float m2 = wsum(on ? q2.x + q2.y : 0.f);
    if (lane == 0) *(f32x2*)(ex + wave * 2) = f32x2{mw, m2};
    lds_barrier();
    const f32x4 e01 = *(const f32x4*)ex; const f32x2 e2 = *(const f32x2*)(ex + 4);
    const float mu = (e01[0] + e01[2]) * (256.f / D) + e2.x * (64.f / D);
    const float d0 = e01[0] - mu, d1 = e01[2] - mu, d2 = e2.x - mu;
    const float var = rbf(((e01[1] + e01[3]) + e2.y + 256.f * (d0 * d0 + d1 * d1) + 64.f * d2 * d2) * (1.f / D));
    const float mean = rbf(mu), rden = frcp(rbf(sqrtf(rbf(var + 1e-5f))));
    u32 hw[2];
#pragma unroll
    for (int i = 0; i < 2; ++i) {
      const f32x2 t = rbf2(rbf2(rbf2(v[i] - f32x2{mean, mean}) * f32x2{rden, rden}) * cb1[i]) + sb[i];
      hw[i] = cvt2(t.x, t.y);
    }
    if (on) {
      st128(rs, ho, u32x4{hw[0], hw[1], e0, e1});
      st128(rs, xo, u32x4{cvt2(v[0].x, v[0].y), cvt2(v[1].x, v[1].y), e0, e1});
    }
    TL(11);
    const f32x2 r2 = up2(hw[0]) * wr[0] + up2(hw[1]) * wr[1];
    const float rw = wsum(on ? r2.x + r2.y : 0.f);
    if (lane == 0) ex[8 + wave] = rw;
    lds_barrier();
    if (tid < 8) {
      const float r = rbf((ex[8] + ex[9]) + ex[10]);
      st128(rs, W_X1 + (b * 152 + 144 + tid) * 16, u32x4{__float_as_uint(rbf(1.f / (1.f + __expf(-r)))), 0u, e0, e1});
    }
  }

  if (oj >= 0) {
    const int ob = W_O + ((op * 2 + (wave >> 2)) * 8 + (lane >> 3)) * 2048 + (wave & 3) * 256 + (lane & 7) * 16;
    u32x4 a[4];
#pragma unroll
    for (int p = 0; p < 4; ++p) a[p] = ld128(rs, ob + (p >> 1) * 1024 + (p & 1) * 128);
    SCHED();
    u32x4 wf[8];
    {
      const int q = n >> 2, p = lane & 3, hf = p >> 1;
      const int a0 = L_W + (wave * 64 + hf * 32 + 8 * grp + q) * 128 + (((q >> 1) ^ ((grp & 1) * 2) ^ (hf * 4)) << 4) + (p & 1) * 8;
#pragma unroll
      for (int cg = 0; cg < 8; ++cg) {
        const u32x2 lo = trd(lds, a0 ^ (cg * 16)), hi = trd(lds, (a0 ^ (cg * 16)) + 512);
        wf[cg] = u32x4{lo[0], lo[1], hi[0], hi[1]};
      }
    }
    int rw = L_OI + (n & 7) * 4128 + (wave * 2 + (n >> 3)) * 256 + (grp & 1) * 16, rr = L_OI + wave * 4128 + grp * 1024 + n * 16;
    int xo = W_XP + (op * 8 + wave) * 2304 + oj * 256 + lane * 16, sl = ((n & 7) * 8 + (n >> 3) * 4 + grp) * 4;
#pragma unroll
    for (int cg = 0; cg < 8; ++cg) asm volatile("" : "+v"(wf[cg]));
    asm volatile("" : "+v"(rw), "+v"(rr), "+v"(xo), "+v"(sl));
    _Pragma("nounroll") for (int sp = 0; sp < SPIN_CAP; ++sp) {
      const bool m0 = mxu(mx4(a[0]), mx4(a[1])) == 0xffffffffu, m1 = mxu(mx4(a[2]), mx4(a[3])) == 0xffffffffu;
      if (!__any(m0 || m1)) break;
      nap(2);
      if (m0) { a[0] = ld128(rs, ob); a[1] = ld128(rs, ob + 128); }
      if (m1) { a[2] = ld128(rs, ob + 1024); a[3] = ld128(rs, ob + 1152); }
    }
    TL(8);
#define OS(p, j) (__uint_as_float(a[p][j]) + __uint_as_float(a[(p) + 2][j]))
#define OP(v) (u32)__builtin_amdgcn_ds_bpermute(sl, (int)(v))
    const u32x4 of = {OP(cvt2(OS(0, 0), OS(0, 1))), OP(cvt2(OS(0, 2), OS(0, 3))), OP(cvt2(OS(1, 0), OS(1, 1))), OP(cvt2(OS(1, 2), OS(1, 3)))};
    f32x4 acc[8];
#pragma unroll
    for (int cg = 0; cg < 8; ++cg) acc[cg] = mfma(wf[cg], of, f32x4{0.f, 0.f, 0.f, 0.f});
    if ((n >> 3) == (grp >> 1)) {
#pragma unroll
      for (int cg = 0; cg < 8; ++cg) *(f32x4*)(lds + rw + cg * 32) = acc[cg];
    }
    lds_barrier();
    f32x4 s = (*(const f32x4*)(lds + rr) + *(const f32x4*)(lds + rr + 256)) + (*(const f32x4*)(lds + rr + 512) + *(const f32x4*)(lds + rr + 768));
#pragma unroll
    for (int k = 0; k < 4; ++k) s[k] = xsum32(xsum16(s[k]));
    if (lane < 16) st128(rs, xo, u32x4{sanf(s[0]), sanf(s[1]), sanf(s[2]), sanf(s[3])});
    TL(9);
  }
  TLOUT();
  }
  }
}

extern "C" void kernel_launch(void* const* d_in, const int* in_sizes, int n_in, void* d_out, int out_size, void* d_ws, size_t ws_size,
                              hipStream_t stream) {
  fused_kernel<<<NWG, NT, 0, stream>>>((const u16*)d_in[0], (const u16*)d_in[1], (const u16*)d_in[2], (const u16*)d_in[3], (const u16*)d_in[4],
                                       (const u16*)d_in[6], (const u16*)d_in[7], (const u16*)d_in[8], (const u16*)d_in[9], (const u16*)d_in[10],
                                       (const u16*)d_in[11], (unsigned char*)d_ws, (u16*)d_out);
}
```

```cpp
#include <hip/hip_runtime.h>
#include <hip/hip_bf16.h>
#include <cstdint>

typedef unsigned u32;
typedef unsigned short u16;
typedef u32 u32x4 __attribute__((ext_vector_type(4)));
typedef u32 u32x2 __attribute__((ext_vector_type(2)));
typedef float f32x4 __attribute__((ext_vector_type(4)));
typedef float f32x2 __attribute__((ext_vector_type(2)));
typedef short s16x4 __attribute__((ext_vector_type(4)));
typedef __bf16 bf16x8 __attribute__((ext_vector_type(8)));
typedef __bf16 bf16x2 __attribute__((ext_vector_type(2)));
typedef __attribute__((address_space(3))) unsigned char lds_t;

constexpr int D = 576, SK = 8192, WIN = 2048, FF = 4608, NQ = 4096, NMOD = 6 * D;
constexpr int NWG = 256, NT = 512, NG = 72;
constexpr int SPIN_CAP = 1 << 15;

constexpr int W_O = 0;
constexpr int W_XP = W_O + 262144;
constexpr int W_Y = W_XP + 147456;
constexpr int W_MS = W_Y + NG * NG * 256;
constexpr int W_FL = W_MS;
constexpr int W_H = W_FL + 2048;
constexpr int W_M = W_H + 36864;
constexpr int W_Q = W_M + 73728;
constexpr int W_ST = W_Q + 131072;
constexpr int W_H2 = W_ST + 32768;
constexpr int W_X1 = W_H2 + 18432;
constexpr int W_END = W_X1 + 8 * 152 * 16;
constexpr int NPZ = 174, PZ = (W_MS / 16 + NPZ - 1) / NPZ;
static_assert(PZ <= 2 * NT, "poison split");

constexpr int L_W = 0;
constexpr int L_IMG = 73728;
constexpr int L_TILE = L_IMG + 9216;
constexpr int L_OI = 65536;
constexpr int L_B = 147456;
constexpr int L_C = 155648;
constexpr int LDS_BYTES = 163840;

#define SCHED() __builtin_amdgcn_sched_barrier(0)
#define BAR() asm volatile("s_barrier" ::: "memory")
#define TL(i) do {} while (0)
#define TLOUT() do {} while (0)
#define WAITV_(n) do { asm volatile("s_waitcnt vmcnt(" #n ")" ::: "memory"); SCHED(); } while (0)
#define WAITV(n) WAITV_(n)
#define DK 5
#define DV DK
#define PR 114
#define OH 203
#define OF 114
#define OL 110

__device__ __forceinline__ float lo16(u32 u) { return __uint_as_float(u << 16); }
__device__ __forceinline__ float hi16(u32 u) { return __uint_as_float(u & 0xffff0000u); }
__device__ __forceinline__ float b2f(u16 u) { return __uint_as_float((u32)u << 16); }
__device__ __forceinline__ u32 cvt2(float a, float b) { return __builtin_bit_cast(u32, __builtin_convertvector(f32x2{a, b}, bf16x2)); }
__device__ __forceinline__ float rbf(float v) { return __uint_as_float(cvt2(0.f, v)); }
__device__ __forceinline__ u32 sanf(float f) { u32 v = __float_as_uint(f); return v < 0xfffffffeu ? v : 0xfffffffeu; }
__device__ __forceinline__ f32x2 up2(u32 u) { return f32x2{lo16(u), hi16(u)}; }
__device__ __forceinline__ f32x2 rbf2(f32x2 v) { return f32x2{rbf(v.x), rbf(v.y)}; }
__device__ __forceinline__ void lds_barrier() { asm volatile("s_waitcnt lgkmcnt(0)\n\ts_barrier" ::: "memory"); }
__device__ __forceinline__ f32x4 mfma(u32x4 a, u32x4 b, f32x4 c) {
  return __builtin_amdgcn_mfma_f32_16x16x32_bf16(__builtin_bit_cast(bf16x8, a), __builtin_bit_cast(bf16x8, b), c, 0, 0, 0);
}
__device__ __forceinline__ u32 plo(u32 a, u32 b) { return __builtin_amdgcn_perm(b, a, 0x05040100u); }
__device__ __forceinline__ u32 phi(u32 a, u32 b) { return __builtin_amdgcn_perm(b, a, 0x07060302u); }

extern "C" __device__ __attribute__((const)) unsigned long long llvm_dispatch_id() __asm("llvm.amdgcn.dispatch.id");
typedef __amdgpu_buffer_rsrc_t rsrc_t;
__device__ __forceinline__ u32x4 ld128(rsrc_t rs, int off) { return __builtin_amdgcn_raw_buffer_load_b128(rs, off, 0, 16); }
__device__ __forceinline__ void st128(rsrc_t rs, int off, u32x4 v) { __builtin_amdgcn_raw_buffer_store_b128(v, rs, off, 0, 16); }
__device__ __forceinline__ void st64(rsrc_t rs, int off, u32x2 v) { __builtin_amdgcn_raw_buffer_store_b64(v, rs, off, 0, 16); }
__device__ __forceinline__ void dma(const void* gbase, u32 voff, unsigned lds_dst) {
  unsigned keep;
  asm volatile("s_mov_b32 %0, m0\n\ts_mov_b32 m0, %3\n\ts_nop 2\n\tglobal_load_lds_dwordx4 %1, %2 nt\n\ts_mov_b32 m0, %0"
               : "=&s"(keep) : "v"(voff), "s"(gbase), "s"(lds_dst) : "memory");
}
__device__ __forceinline__ void dma_keep(const void* gbase, u32 voff, unsigned lds_dst) {
  unsigned keep;
  asm volatile("s_mov_b32 %0, m0\n\ts_mov_b32 m0, %3\n\ts_nop 2\n\tglobal_load_lds_dword %1, %2\n\ts_mov_b32 m0, %0"
               : "=&s"(keep) : "v"(voff), "s"(gbase), "s"(lds_dst) : "memory");
}
__device__ __forceinline__ void dma_sc(const void* gbase, u32 voff, unsigned lds_dst) {
  unsigned keep;
  asm volatile("s_mov_b32 %0, m0\n\ts_mov_b32 m0, %3\n\ts_nop 2\n\tglobal_load_lds_dwordx4 %1, %2 sc1\n\ts_mov_b32 m0, %0"
               : "=&s"(keep) : "v"(voff), "s"(gbase), "s"(lds_dst) : "memory");
}
__device__ __forceinline__ void ald128(u32x4& d, const void* gbase, u32 voff) { unsigned long long t; asm volatile("s_mov_b64 %1, %3\n\tglobal_load_dwordx4 %0, %2, %1" : "=v"(d), "=&s"(t) : "v"(voff), "s"(gbase) : "memory"); }
__device__ __forceinline__ void ald64(u32x2& d, const void* gbase, u32 voff) { unsigned long long t; asm volatile("s_mov_b64 %1, %3\n\tglobal_load_dwordx2 %0, %2, %1" : "=v"(d), "=&s"(t) : "v"(voff), "s"(gbase) : "memory"); }
__device__ __forceinline__ void ald128nt(u32x4& d, const void* gbase, u32 voff) { unsigned long long t; asm volatile("s_mov_b64 %1, %3\n\tglobal_load_dwordx4 %0, %2, %1 nt" : "=v"(d), "=&s"(t) : "v"(voff), "s"(gbase) : "memory"); }
__device__ __forceinline__ void ald128sc(u32x4& d, const void* gbase, u32 voff) { unsigned long long t; asm volatile("s_mov_b64 %1, %3\n\tglobal_load_dwordx4 %0, %2, %1 sc1" : "=v"(d), "=&s"(t) : "v"(voff), "s"(gbase) : "memory"); }
__device__ __forceinline__ u32x2 trd(const unsigned char* lds, int off) {
  return __builtin_bit_cast(u32x2, __builtin_amdgcn_ds_read_tr16_b64_v4i16((__attribute__((address_space(3))) s16x4*)((lds_t*)lds + off)));
}
#define nap(n) do { __builtin_amdgcn_s_sleep(n); asm volatile("" ::: "memory"); } while (0)
__device__ __forceinline__ float frcp(float v) { return __builtin_amdgcn_rcpf(v); }
__device__ __forceinline__ float fdiv(float a, float d, float r) { const float q = a * r; return fmaf(fmaf(-q, d, a), r, q); }
__device__ __forceinline__ float silu(float v) { const float d = 1.f + __expf(-v); const float r = frcp(d); return d < 3.0e38f ? fdiv(v, d, r) : v * r; }
template <int CTRL> __device__ __forceinline__ float dppf(float x) {
  return __builtin_bit_cast(float, __builtin_amdgcn_mov_dpp(__builtin_bit_cast(int, x), CTRL, 0xf, 0xf, true));
}
__device__ __forceinline__ u32 mxu(u32 a, u32 b) { return a > b ? a : b; }
__device__ __forceinline__ u32 mx4(u32x4 v) { return mxu(mxu(v[0], v[1]), mxu(v[2], v[3])); }
__device__ __forceinline__ float rl(float v, int l) { return __builtin_bit_cast(float, __builtin_amdgcn_readlane(__builtin_bit_cast(int, v), l)); }
__device__ __forceinline__ float wsum(float v) {
  v += dppf<0xB1>(v); v += dppf<0x4E>(v); v += dppf<0x141>(v); v += dppf<0x140>(v);
  return (rl(v, 0) + rl(v, 16)) + (rl(v, 32) + rl(v, 48));
}
__device__ __forceinline__ float up32(float v) { const u32 u = __float_as_uint(v); return __uint_as_float(__builtin_amdgcn_permlane32_swap(u, u, false, false)[1]); }
__device__ __forceinline__ float xsum32(float v) { const u32 u = __float_as_uint(v); const auto r = __builtin_amdgcn_permlane32_swap(u, u, false, false); return __uint_as_float(r[0]) + __uint_as_float(r[1]); }
__device__ __forceinline__ float xsum16(float v) { const u32 u = __float_as_uint(v); const auto r = __builtin_amdgcn_permlane16_swap(u, u, false, false); return __uint_as_float(r[0]) + __uint_as_float(r[1]); }
__device__ __forceinline__ f32x4 half_sum(f32x4 d) {
  f32x4 r;
#pragma unroll
  for (int j = 0; j < 4; ++j) r[j] = d[j] + dppf<0x108>(up32(d[j]));
  return r;
}
__device__ __forceinline__ f32x2 half_sum2(f32x4 d) {
  f32x2 r;
#pragma unroll
  for (int j = 0; j < 2; ++j) {
    const auto s = __builtin_amdgcn_permlane32_swap(__float_as_uint(d[j]), __float_as_uint(d[j + 2]), false, false);
    r[j] = __uint_as_float(s[0]) + dppf<0x108>(__uint_as_float(s[1]));
  }
  return r;
}
__device__ __forceinline__ void unpack8(u32x4 u, float* f) {
#pragma unroll
  for (int j = 0; j < 4; ++j) { f[2 * j] = lo16(u[j]); f[2 * j + 1] = hi16(u[j]); }
}
__device__ __forceinline__ void ln_row(const float (&v)[16], bool has2, float& mean, float& den) {
  float s = 0.f;
#pragma unroll
  for (int i = 0; i < 8; ++i) s += v[i];
  if (has2) {
#pragma unroll
    for (int i = 8; i < 16; ++i) s += v[i];
  }
  s = wsum(s);
  const float mu = s * (1.f / D);
  mean = rbf(mu);
  float q = 0.f;
#pragma unroll
  for (int i = 0; i < 8; ++i) { const float t = v[i] - mu; q = fmaf(t, t, q); }
  if (has2) {
#pragma unroll
    for (int i = 8; i < 16; ++i) { const float t = v[i] - mu; q = fmaf(t, t, q); }
  }
  q = wsum(q);
  const float var = rbf(q * (1.f / D));
  den = rbf(sqrtf(rbf(var + 1e-5f)));
}
template <int J0 = 0, int J1 = 9>
__device__ __forceinline__ void slab_dma(const u16* base, int stride_b, unsigned lds_dst, int wave, int lane) {
  const u32 v0 = (lane >> 3) * stride_b + (((lane & 7) ^ ((wave & 1) * 4 + (lane >> 4))) << 4), v1 = v0 ^ 32u;
  const u32 v4 = wave >= 4 ? v1 : v0;
  const char* gb = (const char*)base + (size_t)wave * 8 * stride_b;
#pragma unroll
  for (int j = J0; j < J1; ++j) dma(gb + (size_t)j * 64 * stride_b, j < 4 ? v0 : j == 4 ? v4 : v1, lds_dst + (j * 8 + wave) * 1024);
}
__device__ __forceinline__ void slab_frags(const unsigned char* lds, int base, int wave, int lane, u32x4 (&wf)[9]) {
  const int g = lane >> 4, q = (lane & 15) >> 2, p = lane & 3, hf = p >> 1;
  const int a0 = base + (hf * 288 + 8 * g + q) * 128 + ((wave ^ ((g & 1) * 4 + (q >> 1)) ^ (hf * 2)) << 4) + (p & 1) * 8, a1 = (a0 ^ 32) + 512;
#pragma unroll
  for (int s = 0; s < 9; ++s) {
    const u32x2 lo = trd(lds, a0 + s * 4096), hi = trd(lds, a1 + s * 4096);
    wf[s] = u32x4{lo[0], lo[1], hi[0], hi[1]};
  }
}
__device__ __forceinline__ void img_frags(const unsigned char* lds, int img, int lane, u32x4 (&af)[9]) {
#pragma unroll
  for (int s = 0; s < 9; ++s) af[s] = *(const u32x4*)(lds + img + (s * 64 + lane) * 16);
}
__device__ __forceinline__ f32x4 gemv9(const u32x4 (&wf)[9], const u32x4 (&af)[9]) {
  f32x4 acc[3];
  SCHED();
#pragma unroll
  for (int s = 0; s < 9; ++s) acc[s % 3] = mfma(wf[s], af[s], s < 3 ? f32x4{0.f, 0.f, 0.f, 0.f} : acc[s % 3]);
  return (acc[0] + acc[1]) + acc[2];
}
__device__ __forceinline__ int img_off(int b, int k8) {
  const int hf = k8 >= 36 ? 1 : 0, kk = k8 - hf * 36;
  return ((kk >> 2) * 64 + (kk & 3) * 16 + hf * 8 + b) * 16;
}
__device__ __forceinline__ u32 tag_x(u32x4 v, u32 e0, u32 e1) { return (v[2] ^ e0) | (v[3] ^ e1); }
__device__ __forceinline__ bool tag_ok(u32x4 v, u32 e0, u32 e1) { return tag_x(v, e0, e1) == 0u; }

constexpr int CODE_BYTES = 31268;

__global__ void __launch_bounds__(NT, 2) fused_kernel(const u16* __restrict__ x, const u16* __restrict__ kc, const u16* __restrict__ vc,
                                                      const u16* __restrict__ wq, const u16* __restrict__ wo, const u16* __restrict__ w_r,
                                                      const u16* __restrict__ wg, const u16* __restrict__ wu, const u16* __restrict__ wd,
                                                      const u16* __restrict__ c, const u16* __restrict__ w_mod, unsigned char* ws, u16* out) {
  __shared__ __attribute__((aligned(16))) unsigned char lds[LDS_BYTES];
  const int w = blockIdx.x, xcd = w & 7, idx = w >> 3;
  const int tid = threadIdx.x, lane = tid & 63, wave = __builtin_amdgcn_readfirstlane(tid >> 6);
  const int n = lane & 15, grp = lane >> 4;
  const rsrc_t rs = __builtin_amdgcn_make_buffer_rsrc(ws, 0, W_END, 0x00020000);
  const unsigned lds_base = (unsigned)(uintptr_t)(lds_t*)lds;
  {
    unsigned long long pc0; asm volatile("s_getpc_b64 %0" : "=s"(pc0));
    if (w >= 224 && wave == 0) dma_keep((const void*)pc0, min((w >> 3) * 8192 - 28 * 8192 + lane * 128, CODE_BYTES - 128), lds_base + 107520);
    SCHED();
  }
  const unsigned long long epoch = (llvm_dispatch_id() + 1ull) * 0x9E3779B97F4A7C15ull;
  const u32 e0 = (u32)epoch, e1 = (u32)(epoch >> 32);
  asm volatile("" : "+s"(vc), "+s"(w_r), "+s"(wu), "+s"(wd), "+s"(out), "+s"(x), "+s"(w_mod), "+s"(wg), "+s"(wq), "+s"(wo));

  const int mw = idx < 6 ? idx * 8 + xcd : -1;
  const int md = mw < 18 ? mw : -1;
  const int ml = mw >= 18 ? mw : mw >= 0 && mw < 6 ? 48 + mw : -1;
  const int lnb = mw >= 6 && mw < 14 ? mw - 6 : -1;
  const int qs = idx >= 6 && idx < 14 ? xcd * 8 + idx - 6 : -1;
  const int oj = idx >= 14 && idx < 23 ? idx - 14 : -1, op = xcd;
  const int gs = idx >= 23 ? xcd * 9 + idx - 23 : -1;

  const int s_att = w & 1, kvh = (w >> 1) & 15, b_att = w >> 5;
  {
    const size_t pairbase = ((size_t)(b_att * 16 + kvh) * SK + (SK - WIN)) * 256;
    const char* kp = (const char*)kc + pairbase;
    const char* vp = (const char*)vc + pairbase;
    const int wpart = (wave & 3) * 4096 + s_att * 16384 + (wave >> 2) * 262144;
    const int ko = wpart + (n >> 1) * 256 + (n & 1) * 64 + grp * 16;
    const int vo = wpart + n * 16 + grp * 512;
#define KLD(u) do { unsigned long long t_; asm volatile("s_mov_b64 %1, %3\n\tglobal_load_dwordx4 %0, %2, %1 offset:%4 nt" : "=v"(kb[u]), "=&s"(t_) : "v"(ko), "s"(kp + ((u) >> 2) * 32768), "i"((((u) >> 1) & 1) * 2048 + ((u) & 1) * 128) : "memory"); } while (0)
#define VLD(u) do { unsigned long long t_; asm volatile("s_mov_b64 %1, %3\n\tglobal_load_dwordx4 %0, %2, %1 offset:%4 nt" : "=v"(vb[u]), "=&s"(t_) : "v"(vo), "s"(vp + ((u) >> 2) * 32768), "i"((((u) >> 1) & 1) * 2048 + ((u) & 1) * 256) : "memory"); } while (0)
#define TIE8(a, o) asm volatile("" : "+v"(a[o]), "+v"(a[(o) + 1]), "+v"(a[(o) + 2]), "+v"(a[(o) + 3]), "+v"(a[(o) + 4]), "+v"(a[(o) + 5]), "+v"(a[(o) + 6]), "+v"(a[(o) + 7]))
    const u32 tp0 = (u32)__builtin_amdgcn_s_memrealtime() + (gs >= 0 ? OF : mw >= 18 ? OL : OH);
#define PACE(i) do { _Pragma("nounroll") while ((int)((u32)__builtin_amdgcn_s_memrealtime() - tp0) < (i) * PR / 4) __builtin_amdgcn_s_sleep(2); SCHED(); } while (0)
#define REP4(M, u, W) do { M(u); W; M((u) + 1); W; M((u) + 2); W; M((u) + 3); W; } while (0)
    static_assert(DK <= 7 && DV <= 7, "the hand-placed waits assume it");
    u32x4 kb[32], vb[32];
    const int pz = mw >= 18 ? mw - 18 : idx >= 14 ? w - 82 : -1;
#define POISON() do { if (pz >= 0) { _Pragma("unroll") for (int i = 0; i < 2; ++i) { const int g_ = pz * PZ + i * NT + tid; \
      if (i * NT + tid < PZ && g_ < W_MS / 16) st128(rs, g_ * 16, u32x4{0xffffffffu, 0xffffffffu, 0xffffffffu, 0xffffffffu}); } } SCHED(); } while (0)
#define PFLAG() do { if (pz >= 0 && tid == 448) st64(rs, W_FL + pz * 8, u32x2{e0, e1}); SCHED(); } while (0)
#define SILU_IMAGE(c0, c1) do { float f[8]; unpack8(c0, f); u32x4 o; \
        _Pragma("unroll") for (int j = 0; j < 4; ++j) o[j] = cvt2(silu(f[2 * j]), silu(f[2 * j + 1])); \
        const int b_ = tid / 72, ch_ = tid - b_ * 72; \
        *(u32x4*)(lds + L_IMG + img_off(b_, ch_)) = o; \
        if (wave == 0) { unpack8(c1, f); \
          _Pragma("unroll") for (int j = 0; j < 4; ++j) o[j] = cvt2(silu(f[2 * j]), silu(f[2 * j + 1])); \
          *(u32x4*)(lds + L_IMG + img_off(7, 8 + lane)) = o; } } while (0)
    if (md >= 0) {
      u32x4 c0, c1, x0, x1; u32x2 xc;
      __builtin_amdgcn_s_waitcnt(0x0f70);
      int ho = W_H + img_off(n & 7, (md >= 9 ? md - 9 : md) * 8 + wave) * 2 + (grp & 1) * 16;
      ald128(c0, c, tid * 16);
      ald128(c1, c, (512 + lane) * 16);
      ald128(x0, x, wave * 1152 + lane * 16);
      ald128(x1, x, wave * 1152 + 1024 + (lane & 7) * 16);
      ald64(xc, x, (n & 7) * 1152 + ((md >= 9 ? md - 9 : 0) * 64 + wave * 8 + (grp & 1) * 4) * 2);
      TL(35);
      BAR();
      float* st = (float*)(lds + L_TILE + 1024);
      if (wave < 4) {
        slab_dma(w_mod + md * 64, NMOD * 2, lds_base + L_W, wave, lane);
        asm volatile("s_waitcnt vmcnt(9)" ::: "memory");
      } else asm volatile("s_waitcnt vmcnt(0)" ::: "memory");
      asm volatile("s_nop 0 ; both branches above cover V" : "+v"(c0), "+v"(c1), "+v"(x0), "+v"(x1), "+v"(xc) :: "memory");
      {
        SILU_IMAGE(c0, c1);
        float v[16]; unpack8(x0, v); unpack8(x1, v + 8);
        float mean, den; ln_row(v, lane < 8, mean, den);
        if (lane == 0) { st[wave] = mean; st[8 + wave] = den; }
      }
      if (wave >= 4) slab_dma(w_mod + md * 64, NMOD * 2, lds_base + L_W, wave, lane);
      lds_barrier();
      REP4(KLD, 0, );
      u32x4 wf[9], af[9];
      img_frags(lds, L_IMG, lane, af);
      const float mean = st[n & 7], rd = frcp(st[8 + (n & 7)]);
      float ln[4] = {rbf(rbf(lo16(xc[0]) - mean) * rd), rbf(rbf(hi16(xc[0]) - mean) * rd), rbf(rbf(lo16(xc[1]) - mean) * rd), rbf(rbf(hi16(xc[1]) - mean) * rd)};
#pragma unroll
      for (int s = 0; s < 9; ++s) asm volatile("" : "+v"(af[s]));
      asm volatile("" : "+v"(ln[0]), "+v"(ln[1]), "+v"(ln[2]), "+v"(ln[3]), "+v"(ho));
      WAITV(4); BAR(); SCHED();
      slab_frags(lds, L_W, wave, lane, wf);
      const f32x4 s = half_sum(gemv9(wf, af));
      f32x4 r = s;
      if (md >= 9) {
#pragma unroll
        for (int j = 0; j < 4; ++j) r[j] = ln[j] * rbf(1.f + rbf(s[j]));
      }
      if (n < 8 && grp < 2) st128(rs, ho + (md >= 9 ? 18432 : 0), u32x4{cvt2(r[0], r[1]), cvt2(r[2], r[3]), e0, e1});
      SCHED();
      TL(1);
      BAR();
      KLD(4); KLD(5); WAITV(DK);
      KLD(6); WAITV(DK); KLD(7); WAITV(DK); KLD(8); WAITV(DK); KLD(9); WAITV(DK); KLD(10); WAITV(DK);
      KLD(11); WAITV(DK); REP4(KLD, 12, WAITV(DK)); REP4(KLD, 16, WAITV(DK));
      REP4(KLD, 20, WAITV(DK)); KLD(24); WAITV(DK); KLD(25);
    } else {
      constexpr int L_LAND = 98304;
      const unsigned land = lds_base + L_LAND + wave * 6144;
      const unsigned char* lp = lds + L_LAND + wave * 6144 + lane * 16;
      const int sl_ = (3 - (int)((uintptr_t)wq >> 7)) & 7;
      const bool slowq = qs >= 0 && (qs & 7) == sl_;
      if (gs >= 0 && wave == 0) dma_keep((const char*)wq + (size_t)(idx - 23) * 64 * 8192 + xcd * 1024 + sl_ * 128, lane * 8192, land);
      if (gs >= 0 && (wave == 1 || (wave == 2 && xcd < 2))) dma_keep((const char*)w_mod + (size_t)((idx - 23) * 64 + 32) * (NMOD * 2) + xcd * 128 + (wave - 1) * 2048, (lane & 31) * (NMOD * 2) + (wave == 1 ? (lane >> 5) * 1024 : 0), land);
      POISON();
      const u32 v0 = (lane >> 3) * 1152 + (((lane & 7) ^ ((lane >> 4) & 1)) << 4);
      const char* gb = (const char*)(wo + (size_t)(op * 512 + wave * 64) * D + oj * 64);
      if (ml >= 0) { dma_sc(c, tid * 16, land); dma_sc(c, (512 + lane) * 16, land + 1024); }
#define WK(j) do { if (oj >= 0) { if ((j) < 8) dma(gb + (j) * 8 * 1152, v0 ^ (((j) & 1) * 32 + ((j) >> 2) * 64), lds_base + L_W + (wave * 8 + (j)) * 1024); } \
                   else if (slowq) { if ((j) == 8) slab_dma(wq + qs * 64, NQ * 2, lds_base + L_W, wave, lane); } \
                   else if (qs >= 0 && (j) < 9) slab_dma<(j) < 9 ? (j) : 0, (j) < 9 ? (j) + 1 : 0>(wq + qs * 64, NQ * 2, lds_base + L_W, wave, lane); \
                   else if (qs < 0 && (j) >= 4) PACE(j); \
                   KLD(j); if ((j) == 0) TL(32); if ((j) == 4) TL(33); WAITV(DK); if ((j) == 5) TL(34); } while (0)
      WK(0); WK(1); WK(2); WK(3);
      WK(4); WK(5);
      WK(6); WK(7);
      TL(24);
      if (ml >= 0) { const u32x4 c0 = *(const u32x4*)lp, c1 = *(const u32x4*)(lp + 1024); SILU_IMAGE(c0, c1); }
      BAR();
      PFLAG();
      WK(8); WK(9); WK(10); WK(11);
      TL(25);
      PACE(12); REP4(KLD, 12, WAITV(DK));
      TL(26);
      const int hg0 = (wave * 144 + lane) * 16, hg2 = (wave * 144 + 128 + n) * 16;
#define HREQ() do { dma_sc(ws + W_H, hg0, land); dma_sc(ws + W_H + 18432, hg0, land + 1024); dma_sc(ws + W_H + 1024, hg0, land + 2048); \
                    dma_sc(ws + W_H + 18432 + 1024, hg0, land + 3072); dma_sc(ws + W_H, hg2, land + 4096); dma_sc(ws + W_H + 18432, hg2, land + 5120); } while (0)
#define HGET() do { _Pragma("unroll") for (int i = 0; i < 6; ++i) hq[i] = *(const u32x4*)(lp + i * 1024); } while (0)
#define HQOK() __all((tag_x(hq[0], e0, e1) | tag_x(hq[1], e0, e1) | tag_x(hq[2], e0, e1) | tag_x(hq[3], e0, e1) | tag_x(hq[4], e0, e1) | tag_x(hq[5], e0, e1)) == 0u)
#define HOK() __all((tag_x(hp[0], e0, e1) | tag_x(hp[1], e0, e1) | tag_x(hp[2], e0, e1)) == 0u)
      if (qs >= 0) HREQ();
      PACE(16); REP4(KLD, 16, WAITV(DK));
      if (qs >= 0) WAITV(4);
      TL(27);
      PACE(20); KLD(20); KLD(21);
      if (qs >= 0) {
        u32x4 hq[6]; HGET();
        _Pragma("nounroll") for (int sp = 0; sp < SPIN_CAP && !HQOK(); ++sp) { nap(16); HREQ(); WAITV(0); HGET(); }
        TL(2);
#define HSUM(a, b) cvt2(lo16(a) + lo16(b), hi16(a) + hi16(b))
        *(u32x2*)(lds + L_IMG + hg0 / 2) = u32x2{HSUM(hq[0][0], hq[1][0]), HSUM(hq[0][1], hq[1][1])};
        *(u32x2*)(lds + L_IMG + 512 + hg0 / 2) = u32x2{HSUM(hq[2][0], hq[3][0]), HSUM(hq[2][1], hq[3][1])};
        if (lane < 16) *(u32x2*)(lds + L_IMG + hg2 / 2) = u32x2{HSUM(hq[4][0], hq[5][0]), HSUM(hq[4][1], hq[5][1])};
      } else WAITV(DK);
      KLD(22); KLD(23);
      if (qs >= 0) {
        u32x4 wf[9], af[9];
        lds_barrier();
        slab_frags(lds, L_W, wave, lane, wf);
        img_frags(lds, L_IMG, lane, af);
        const f32x4 s = half_sum(gemv9(wf, af));
        if (n < 8 && grp < 2) *(u32x2*)(lds + L_TILE + n * 128 + wave * 16 + grp * 8) = u32x2{cvt2(s[0], s[1]), cvt2(s[2], s[3])};
      } else WAITV(DK);
      PACE(24); KLD(24); KLD(25);
      if (qs >= 0) {
        lds_barrier();
        if (wave < 2) {
          const u32x2 t2 = *(const u32x2*)(lds + L_TILE + tid * 8);
          st128(rs, W_Q + ((tid >> 4) * 1024 + qs * 16 + n) * 16, u32x4{t2[0], t2[1], e0, e1});
        }
        TL(3);
      }
      SCHED();
    }
    const unsigned char* qg = ws + W_Q + (b_att * 1024 + kvh * 64) * 16;
    KLD(26); WAITV(DK); KLD(27); WAITV(DK); PACE(28); KLD(28); WAITV(DK); KLD(29); WAITV(DK);
    PACE(30); KLD(30); WAITV(DK); KLD(31); WAITV(DK);
    TL(28);
    PACE(32); VLD(0); WAITV(DK); VLD(1); WAITV(DK); PACE(34); VLD(2); WAITV(DK); VLD(3); WAITV(DK);
#define QLOOK() do { if (wave == 1) dma_sc(qg, lane * 16, lds_base + L_B + 1024); } while (0)
    QLOOK();
    PACE(36); VLD(4); WAITV(DK); VLD(5); WAITV(DK); PACE(38); VLD(6); WAITV(DK); VLD(7); WAITV(DK);
    QLOOK();
    TL(29);
    PACE(40); VLD(8); WAITV(DV); VLD(9); WAITV(DV); PACE(42); QLOOK(); VLD(10); WAITV(DV); VLD(11); WAITV(DV);
    TIE8(kb, 0); TIE8(kb, 8); TIE8(kb, 16); TIE8(kb, 24);
    PACE(44); QLOOK(); VLD(12); VLD(13);
    u32x4 qf[2];
    {
      const u32x4* ql = (const u32x4*)(lds + L_B + 1024 + (((n >> 1) & 1) * 32 + (n & 1) * 8 + grp * 2) * 16);
      u32x4 g[4];
      _Pragma("nounroll") for (int sp = 0; sp < SPIN_CAP; ++sp) {
        g[0] = ql[0]; g[1] = ql[1]; g[2] = ql[16]; g[3] = ql[17];
        if (__all((tag_x(g[0], e0, e1) | tag_x(g[1], e0, e1) | tag_x(g[2], e0, e1) | tag_x(g[3], e0, e1)) == 0u)) break;
        if (wave == 1 && sp >= 6) { dma_sc(qg, lane * 16, lds_base + L_B + 1024); WAITV(0); } else nap(4);
      }
      asm volatile("" ::: "memory");
      g[0] = ql[0]; g[1] = ql[1]; g[2] = ql[16]; g[3] = ql[17];
      qf[0] = n < 4 ? u32x4{g[0][0], g[0][1], g[1][0], g[1][1]} : u32x4{0, 0, 0, 0};
      qf[1] = n < 4 ? u32x4{g[2][0], g[2][1], g[3][0], g[3][1]} : u32x4{0, 0, 0, 0};
    }
    TL(4);
    constexpr float SCALE = 0.088388347648318440f;
    f32x2 sc[16];
    float mx = -INFINITY;
#pragma unroll
    for (int T = 0; T < 16; ++T) {
      f32x4 acc = mfma(kb[T * 2], qf[0], f32x4{0.f, 0.f, 0.f, 0.f});
      acc = mfma(kb[T * 2 + 1], qf[1], acc);
      sc[T] = f32x2{acc[0] + dppf<0xB1>(acc[1]), acc[2] + dppf<0xB1>(acc[3])};
      if (T == 7 || T == 15) { SCHED(); if (T == 7) VLD(14); else VLD(15); SCHED(); }
    }
#pragma unroll
    for (int T = 0; T < 16; ++T)
#pragma unroll
      for (int j = 0; j < 2; ++j) { const float v = rbf(rbf(sc[T][j]) * SCALE); sc[T][j] = v; mx = fmaxf(mx, v); }
    SCHED(); VLD(16); SCHED();
    mx = fmaxf(mx, __shfl_xor(mx, 16));
    mx = fmaxf(mx, __shfl_xor(mx, 32));
    float ls = 0.f;
#pragma unroll
    for (int T = 0; T < 16; ++T)
#pragma unroll
      for (int j = 0; j < 2; ++j) { sc[T][j] = __expf(sc[T][j] - mx); ls += sc[T][j]; }
    SCHED(); VLD(17); SCHED();
    ls += __shfl_xor(ls, 16);
    ls += __shfl_xor(ls, 32);
    float* stat = (float*)(lds + L_B);
    if (lane == 0 || lane == 2) *(f32x2*)(stat + (wave * 2 + (lane >> 1)) * 2) = f32x2{mx, ls};
    SCHED(); VLD(18); VLD(19); SCHED();
    lds_barrier();
    float M = -INFINITY, L = 0.f;
    {
      f32x2 t[8];
#pragma unroll
      for (int q = 0; q < 8; ++q) { t[q] = *(const f32x2*)(stat + (q * 2 + ((n >> 1) & 1)) * 2); M = fmaxf(M, t[q].x); }
#pragma unroll
      for (int q = 0; q < 8; ++q) L = fmaf(__expf(t[q].x - M), t[q].y, L);
    }
    if (tid < 8) st128(rs, W_ST + (w * 8 + lane) * 16, u32x4{__float_as_uint(M), __float_as_uint(L), e0, e1});
    SCHED();
#define VTR(G) do { u32x4 f_[8]; _Pragma("unroll") for (int t = 0; t < 8; ++t) _Pragma("unroll") for (int j = 0; j < 4; ++j) \
      f_[t][j] = (t & 1) ? phi(vb[(G) * 8 + 2 * j][t >> 1], vb[(G) * 8 + 2 * j + 1][t >> 1]) : plo(vb[(G) * 8 + 2 * j][t >> 1], vb[(G) * 8 + 2 * j + 1][t >> 1]); \
      _Pragma("unroll") for (int t = 0; t < 8; ++t) { vb[(G) * 8 + t] = f_[t]; asm volatile("" : "+v"(vb[(G) * 8 + t])); } SCHED(); } while (0)
    WAITV(12);
    TIE8(vb, 0);
    VTR(0);
    TL(30);
    PACE(52); VLD(20); WAITV(5);
    TIE8(vb, 8);
    VTR(1);
    VLD(21); WAITV(DV);
    const int po = W_ST + ((w ^ 1) * 8 + (n & 2)) * 16;
    int l1 = lane; asm volatile("" : "+v"(l1));
    const int fl = 1024 + min(l1, NPZ / 2 - 65) * 16;
    if (wave == 1) { dma_sc(ws + W_FL, l1 * 16, lds_base + L_B + 2048); dma_sc(ws + W_FL, fl, lds_base + L_B + 3072); }
    PACE(54);
    VLD(22); WAITV(DV); VLD(23); WAITV(DV);
    u32x4 ps;
    ald128sc(ps, ws, po);
    PACE(56);
    VLD(24); WAITV(DV); VLD(25); WAITV(DV);
    TL(31);
    PACE(58);
    VLD(26); WAITV(DV); VLD(27); WAITV(DV);
    u32x4 ps2;
    VLD(28); VLD(29); WAITV(6);
    asm volatile("" : "+v"(ps));
    TIE8(vb, 16);
    ald128sc(ps2, ws, po);
    VLD(30); VLD(31);
    VTR(2);
    if (!__all(tag_ok(ps, e0, e1))) {
      WAITV(2);
      asm volatile("" : "+v"(ps2));
      ps = ps2;
      if (!__all(tag_ok(ps, e0, e1))) {
        int sp = 0;
        _Pragma("nounroll") do { nap(1); ps = ld128(rs, po); } while (!__all(tag_ok(ps, e0, e1)) && ++sp < SPIN_CAP);
      }
    }
#define FOK(a, b) __all(((a[0] ^ e0) | (a[1] ^ e1) | tag_x(a, e0, e1) | (b[0] ^ e0) | (b[1] ^ e1) | tag_x(b, e0, e1)) == 0u)
    if (wave == 1 && !FOK((*(const u32x4*)(lds + L_B + 2048 + l1 * 16)), (*(const u32x4*)(lds + L_B + 3072 + l1 * 16)))) {
      _Pragma("nounroll") for (int sp = 0; sp < SPIN_CAP; ++sp) {
        nap(1);
        const u32x4 a = ld128(rs, W_FL + l1 * 16), b = ld128(rs, W_FL + fl);
        if (FOK(a, b)) break;
      }
    }
    float fac;
    {
      const float M2 = __uint_as_float(ps[0]), L2 = __uint_as_float(ps[1]);
      const float Mg = fmaxf(M, M2);
      const float Lg = __expf(M - Mg) * L + __expf(M2 - Mg) * L2;
      fac = __expf(mx - Mg) / Lg;
    }
    TL(5);
    u32x4 pf[4];
#pragma unroll
    for (int G = 0; G < 4; ++G) {
#pragma unroll
      for (int j = 0; j < 4; ++j) pf[G][j] = cvt2(sc[4 * G + j][0] * fac, sc[4 * G + j][1] * fac);
    }
    f32x4 oa[8];
#pragma unroll
    for (int t = 0; t < 8; ++t) oa[t] = f32x4{0.f, 0.f, 0.f, 0.f};
    SCHED();
#pragma unroll
    for (int G = 0; G < 2; ++G)
#pragma unroll
      for (int t = 0; t < 8; ++t) oa[t] = mfma(pf[G], vb[G * 8 + t], oa[t]);
    SCHED();
#pragma unroll
    for (int t = 0; t < 8; ++t) oa[t] = mfma(pf[2], vb[16 + t], oa[t]);
    SCHED();
    int t2_ = threadIdx.x; asm volatile("" : "+v"(t2_));
    const int tid = t2_, lane = tid & 63, n = lane & 15, grp = lane >> 4;
    int oo = W_O + ((kvh * 8 + b_att) * 2 + s_att) * 1024 + ((lane >> 4) * 16 + (lane & 1) * 8 + ((lane >> 1) & 7)) * 16;
    int mw_ = L_C + wave * 1024 + n * 32, mr_ = L_C + lane * 16;
    asm volatile("" : "+v"(oo), "+v"(mw_), "+v"(mr_), "+v"(pf[3]));
    constexpr int L_H2 = L_W + 73728;
    constexpr int L_ACT = L_C + 4608;
    int ia[3], ra[4], yo[3], fo[7], tw, tr_;
    const bool late = gs >= 0 || mw >= 18;
    if (late) {
      BAR();
      slab_dma<0, 4>(gs >= 0 ? wg + gs * 64 : w_mod + ml * 64, gs >= 0 ? FF * 2 : NMOD * 2, lds_base + L_W + (gs >= 0 ? 73728 : 0), wave, lane);
      {
    const int b = wave;
#pragma unroll
    for (int i = 0; i < 3; ++i) {
      const int k = i < 2 ? i * 256 + lane * 4 : 512 + n * 4, hf = k >= 288 ? 1 : 0, kk = k - hf * 288, s = kk >> 5, g = (kk >> 3) & 3;
      ia[i] = L_H2 + (s * 64 + g * 16 + ((hf * 8 + b) ^ ((s & 3) * 4 + g))) * 16 + (kk & 7) * 2;
    }
#pragma unroll
    for (int i = 0; i < 4; ++i) ra[i] = L_H2 + (grp * 16 + (n ^ (i * 4 + grp))) * 16;
    tw = L_ACT + n * 144 + wave * 16 + (grp & 1) * 8 + (grp >> 1) * 4; tr_ = L_ACT + (n & 7) * 144 + grp * 16;
    yo[0] = W_Y + (((wave * 8 + (grp & 1) * 4) * NG + gs) * 16 + (grp >> 1) * 8 + n) * 16; yo[1] = yo[0] + NG * 512;
    yo[2] = W_Y + ((((64 + wave * 2 + (grp >> 1)) * NG + gs) * 2 + (grp & 1)) * 8 + n) * 16;
    {
      const int b_ = lane >> 3, cc = lane & 7, col = gs * 8 + cc, hf = col >= 288 ? 1 : 0, kk = col - hf * 288, s_ = kk >> 5, g = (kk >> 3) & 3;
      fo[0] = W_M + ((3 * 8 + b_) * 144 + gs * 2 + (cc >> 2)) * 16;
      fo[1] = W_X1 + (b_ * 152 + gs * 2 + (cc >> 2)) * 16; fo[2] = W_X1 + (b_ * 152 + 144) * 16;
      fo[3] = W_Y + (gs * 1152 + tid) * 16;
      fo[4] = L_H2 + (s_ * 64 + g * 16 + ((hf * 8 + b_) ^ ((s_ & 3) * 4 + g))) * 16 + cc * 2;
      fo[5] = L_C + (((cc >> 2) * 8 + b_) * 4 + (cc & 3)) * 4;
      fo[6] = (b_ * D + col) * 2;
    }
    asm volatile("" : "+v"(ia[0]), "+v"(ia[1]), "+v"(ia[2]), "+v"(ra[0]), "+v"(ra[1]), "+v"(ra[2]), "+v"(ra[3]), "+v"(tw), "+v"(tr_), "+v"(yo[0]), "+v"(yo[1]), "+v"(yo[2]),
                 "+v"(fo[0]), "+v"(fo[1]), "+v"(fo[2]), "+v"(fo[3]), "+v"(fo[4]), "+v"(fo[5]), "+v"(fo[6]));
      }
      WAITV(6);
    } else WAITV(2);
    u32x4 vf[8];
    asm volatile("s_nop 0 ; both branches above cover V up to the last two" : "+v"(vb[24]), "+v"(vb[25]), "+v"(vb[26]), "+v"(vb[27]), "+v"(vb[28]), "+v"(vb[29]));
    asm volatile("" :: "v"(ps2));
#pragma unroll
    for (int t = 0; t < 8; ++t)
#pragma unroll
      for (int j = 0; j < 3; ++j) vf[t][j] = (t & 1) ? phi(vb[24 + 2 * j][t >> 1], vb[25 + 2 * j][t >> 1]) : plo(vb[24 + 2 * j][t >> 1], vb[25 + 2 * j][t >> 1]);
#pragma unroll
    for (int t = 0; t < 8; ++t) asm volatile("" : "+v"(vf[t][0]), "+v"(vf[t][1]), "+v"(vf[t][2]));
    if (late) WAITV(4); else WAITV(0);
    asm volatile("s_nop 0 ; both branches above cover V" : "+v"(vb[30]), "+v"(vb[31]));
#pragma unroll
    for (int t = 0; t < 8; ++t) {
      vf[t][3] = (t & 1) ? phi(vb[30][t >> 1], vb[31][t >> 1]) : plo(vb[30][t >> 1], vb[31][t >> 1]);
      oa[t] = mfma(pf[3], vf[t], oa[t]);
    }
    TL(6);
    if (grp == 0) {
#pragma unroll
      for (int g = 0; g < 2; ++g) {
        *(f32x4*)(lds + mw_ + g * 512) = f32x4{oa[0][2 * g], oa[1][2 * g], oa[2][2 * g], oa[3][2 * g]};
        *(f32x4*)(lds + mw_ + g * 512 + 16) = f32x4{oa[4][2 * g], oa[5][2 * g], oa[6][2 * g], oa[7][2 * g]};
      }
    }
    lds_barrier();
    if (wave == 0) {
      f32x4 m[8];
#pragma unroll
      for (int q = 0; q < 8; ++q) m[q] = *(const f32x4*)(lds + mr_ + q * 1024);
      SCHED();
      const f32x4 o = ((m[0] + m[1]) + (m[2] + m[3])) + ((m[4] + m[5]) + (m[6] + m[7]));
      st128(rs, oo, u32x4{sanf(o[0]), sanf(o[1]), sanf(o[2]), sanf(o[3])});
    }
    TL(7); SCHED();
  if (gs >= 0) {
    const int b = wave;
    BAR(); SCHED();
    u32x4 bg[9], bu[9], wdf[2][5];
    {
      u32x4 rd[8];
      slab_dma<4, 9>(wg + gs * 64, FF * 2, lds_base + L_W + 73728, wave, lane);
      TL(23);
      BAR();
      const char* db = (const char*)(wd + (size_t)gs * 64 * D + wave * 64);
      const u32 dv = (n >> 3) * 36864 + grp * 9216 + (n & 7) * 16;
#pragma unroll
      for (int i = 0; i < 8; ++i) ald128nt(rd[i], db + i * 1152, dv);
      {
        const int r = lane >> 3;
        dma((const char*)(wd + (size_t)(gs * 64 + wave * 8) * D + 512), r * 1152 + (((lane & 7) ^ (r & 2) ^ ((wave & 1) * 4)) << 4), lds_base + L_B + wave * 1024);
      }
      TL(19);
      asm volatile("s_waitcnt vmcnt(9)\n\ts_barrier" ::: "memory");
      TL(22);
      slab_frags(lds, L_W + 73728, wave, lane, bg);
      SCHED();
      slab_dma(wu + gs * 64, FF * 2, lds_base + L_W, wave, lane);
      TL(20);
      asm volatile("s_waitcnt vmcnt(10)" : "+v"(rd[0]), "+v"(rd[1]), "+v"(rd[2]), "+v"(rd[3]), "+v"(rd[4]), "+v"(rd[5]), "+v"(rd[6]), "+v"(rd[7]) :: "memory");
#pragma unroll
      for (int j = 0; j < 4; ++j)
#pragma unroll
        for (int t = 0; t < 4; ++t) {
          const u32 a = (t & 1) ? phi(rd[2 * j][t >> 1], rd[2 * j + 1][t >> 1]) : plo(rd[2 * j][t >> 1], rd[2 * j + 1][t >> 1]);
          const u32 b_ = (t & 1) ? phi(rd[2 * j][2 + (t >> 1)], rd[2 * j + 1][2 + (t >> 1)]) : plo(rd[2 * j][2 + (t >> 1)], rd[2 * j + 1][2 + (t >> 1)]);
          wdf[0][t][j] = (u32)__builtin_amdgcn_update_dpp((int)a, (int)b_, 0x128, 0xf, 0xc, false);
          wdf[1][t][j] = (u32)__builtin_amdgcn_update_dpp((int)b_, (int)a, 0x128, 0xf, 0x3, false);
        }
#pragma unroll
      for (int t = 0; t < 4; ++t) asm volatile("" : "+v"(wdf[0][t]), "+v"(wdf[1][t]));
      TL(21);
    }
    asm volatile("s_waitcnt vmcnt(0) lgkmcnt(0)\n\ts_barrier" ::: "memory");
    {
      u32x4 hp[3];
      const int h0 = W_H2 + (b * 144 + lane) * 16, h2o = W_H2 + (b * 144 + 128 + n) * 16;
      hp[0] = ld128(rs, h0); hp[1] = ld128(rs, h0 + 1024); hp[2] = ld128(rs, h2o);
      slab_frags(lds, L_W, wave, lane, bu);
      const int q = n >> 2, p = lane & 3;
      const int a0 = L_B + (8 * grp + q) * 128 + ((((wave & 3) * 2 + (p >> 1)) ^ (q & 2) ^ ((grp & 1) * 4)) << 4) + (p & 1) * 8;
#pragma unroll
      for (int ks = 0; ks < 2; ++ks) {
        const u32x2 lo = trd(lds, a0 + ks * 4096), hi = trd(lds, a0 + ks * 4096 + 512);
        wdf[ks][4] = u32x4{lo[0], lo[1], hi[0], hi[1]};
      }
      TL(12);
#pragma unroll
      for (int s = 0; s < 9; ++s) asm volatile("" : "+v"(bg[s]), "+v"(bu[s]));
      asm volatile("" : "+v"(wdf[0][4]), "+v"(wdf[1][4]));
      if (!HOK()) {
        int sp = 0;
        _Pragma("nounroll") do { nap(2); hp[0] = ld128(rs, h0); hp[1] = ld128(rs, h0 + 1024); hp[2] = ld128(rs, h2o); } while (!HOK() && ++sp < SPIN_CAP);
      }
      TL(13);
      *(u32x2*)(lds + ia[0]) = u32x2{hp[0][0], hp[0][1]};
      *(u32x2*)(lds + ia[1]) = u32x2{hp[1][0], hp[1][1]};
      if (lane < 16) *(u32x2*)(lds + ia[2]) = u32x2{hp[2][0], hp[2][1]};
      lds_barrier();
      u32x4 af[9];
#pragma unroll
      for (int s = 0; s < 9; ++s) af[s] = *(const u32x4*)(lds + ra[s & 3] + s * 1024);
      SCHED();
      f32x4 ag[2], au[2];
#pragma unroll
      for (int s = 0; s < 9; ++s) {
        ag[s & 1] = mfma(bg[s], af[s], s < 2 ? f32x4{0.f, 0.f, 0.f, 0.f} : ag[s & 1]);
        au[s & 1] = mfma(bu[s], af[s], s < 2 ? f32x4{0.f, 0.f, 0.f, 0.f} : au[s & 1]);
      }
      const f32x2 hg = half_sum2(ag[0] + ag[1]), hu = half_sum2(au[0] + au[1]);
      const u32 act = cvt2(rbf(silu(rbf(hg.x))) * rbf(hu.x), rbf(silu(rbf(hg.y))) * rbf(hu.y));
      if (n < 8) *(u32*)(lds + tw) = act;
    }
    lds_barrier();
    TL(14);
    {
      u32x4 af[2];
#pragma unroll
      for (int ks = 0; ks < 2; ++ks) af[ks] = *(const u32x4*)(lds + tr_ + ks * 64);
      f32x4 d[5];
#pragma unroll
      for (int t = 0; t < 5; ++t) d[t] = mfma(wdf[1][t], af[1], mfma(wdf[0][t], af[0], f32x4{0.f, 0.f, 0.f, 0.f}));
      if (n < 8) {
#pragma unroll
        for (int j = 0; j < 4; ++j)
          __builtin_amdgcn_raw_buffer_store_b128(u32x4{sanf(d[0][j]), sanf(d[1][j]), sanf(d[2][j]), sanf(d[3][j])}, rs, yo[j >> 1], (j & 1) * NG * 256, 16);
        if (wave < 4)
          st128(rs, yo[2], u32x4{sanf(d[4][0]), sanf(d[4][1]), sanf(d[4][2]), sanf(d[4][3])});
      }
    }
    TL(15);
    {
      float gbv = 0.f, x1v = 0.f, h2 = 0.f, rsel = 0.f;
      if (wave == 7) {
        u32x4 gq, xq, rq;
        _Pragma("nounroll") for (int sp = 0; sp < SPIN_CAP; ++sp) {
          gq = ld128(rs, fo[0]); xq = ld128(rs, fo[1]); rq = ld128(rs, fo[2]);
          if (__all((tag_x(gq, e0, e1) | tag_x(xq, e0, e1) | tag_x(rq, e0, e1)) == 0u)) break;
          nap(1);
        }
        const u32 gw = (lane & 2) ? gq[1] : gq[0], xw = (lane & 2) ? xq[1] : xq[0];
        gbv = (lane & 1) ? hi16(gw) : lo16(gw); x1v = (lane & 1) ? hi16(xw) : lo16(xw);
        h2 = b2f(*(const u16*)(lds + fo[4])); rsel = __uint_as_float(rq[0]);
        asm volatile("" : "+v"(gbv), "+v"(x1v), "+v"(h2), "+v"(rsel));
      }
      WAITV(0);
      u32x4 y[3] = {};
      _Pragma("nounroll") for (int sp = 0; sp < SPIN_CAP; ++sp) {
        y[0] = ld128(rs, fo[3]); y[1] = ld128(rs, fo[3] + 8192);
        if (wave < 2) y[2] = ld128(rs, fo[3] + 16384);
        if (__all(mxu(mxu(mx4(y[0]), mx4(y[1])), mx4(y[2])) != 0xffffffffu)) break;
        nap(1);
      }
      TL(16);
      f32x4 s;
#pragma unroll
      for (int k = 0; k < 4; ++k) s[k] = xsum32(xsum16((__uint_as_float(y[0][k]) + __uint_as_float(y[1][k])) + __uint_as_float(y[2][k])));
      if (lane < 16) *(f32x4*)(lds + L_C + (wave * 16 + lane) * 16) = s;
      lds_barrier();
      if (wave == 7) {
        float t[8];
#pragma unroll
        for (int ww = 0; ww < 8; ++ww) t[ww] = *(const float*)(lds + fo[5] + ww * 256);
        SCHED();
        const float y2 = rbf(rbf(((t[0] + t[1]) + (t[2] + t[3])) + ((t[4] + t[5]) + (t[6] + t[7]))) * rsel);
        const float res = x1v + rbf(gbv * rbf(h2 + y2));
        __builtin_amdgcn_raw_buffer_store_b16((u16)(cvt2(res, 0.f) & 0xffffu), __builtin_amdgcn_make_buffer_rsrc(out, 0, 8 * D * 2, 0x00020000), fo[6], 0, 0);
      }
      TL(17);
    }
  }
  {
  int t3_ = threadIdx.x; asm volatile("" : "+v"(t3_));
  const int tid = t3_, lane = tid & 63, n = lane & 15, grp = lane >> 4;
  if (qs >= 0) {
    const u16* mat = (wave & 1) ? wg : wu;
    const int g0 = ((3 - (int)((uintptr_t)mat >> 7) - xcd * 9) & 7) + (wave >= 4 ? 8 : 0);
    const int r = (idx - 6) * 72 + ((wave >> 1) & 1) * 36 + min(lane, 35);
    if (g0 < 9) dma_keep(mat, r * 9216 + (xcd * 9 + g0) * 128, lds_base + L_W + wave * 256);
  }
  if (ml >= 0) {
    BAR();
    if (mw < 18) slab_dma<0, 4>(w_mod + ml * 64, NMOD * 2, lds_base + L_W, wave, lane);
    slab_dma<4, 9>(w_mod + ml * 64, NMOD * 2, lds_base + L_W, wave, lane);
    u32x4 wf[9], af[9];
    img_frags(lds, L_IMG, lane, af);
    asm volatile("s_waitcnt vmcnt(0)\n\ts_barrier" ::: "memory");
    slab_frags(lds, L_W, wave, lane, wf);
    const f32x4 s = half_sum(gemv9(wf, af));
    if (n < 8 && grp < 2) *(u32x2*)(lds + L_TILE + n * 128 + wave * 16 + grp * 8) = u32x2{cvt2(s[0], s[1]), cvt2(s[2], s[3])};
    lds_barrier();
    if (wave < 2) {
      const u32x2 t2 = *(const u32x2*)(lds + L_TILE + tid * 8);
      const int ch = (ml - 18) / 9, sl = ml - 18 - ch * 9;
      st128(rs, W_M + ((ch * 8 + (tid >> 4)) * 144 + sl * 16 + n) * 16, u32x4{t2[0], t2[1], e0, e1});
    }
    TL(18);
  }

  if (lnb >= 0 && wave < 3) {
    const int b = lnb, g = tid < 144 ? tid : 143;
    const bool on = tid < 144;
    float* ex = (float*)(lds + L_TILE);
    const u32x2 xa = __builtin_amdgcn_raw_buffer_load_b64(__builtin_amdgcn_make_buffer_rsrc((void*)x, 0, 8 * D * 2, 0x00020000), (b * D + g * 4) * 2, 0, 0);
    const u32x2 wra = __builtin_amdgcn_raw_buffer_load_b64(__builtin_amdgcn_make_buffer_rsrc((void*)w_r, 0, D * 2, 0x00020000), g * 8, 0, 0);
    u32x4 mm[3], xp[8];
#define XPLD() do { _Pragma("unroll") for (int p = 0; p < 8; ++p) xp[p] = ld128(rs, W_XP + (p * 8 + b) * 2304 + g * 16); } while (0)
    _Pragma("nounroll") for (int sp = 0; sp < SPIN_CAP; ++sp) {
#pragma unroll
      for (int k = 0; k < 3; ++k) mm[k] = ld128(rs, W_M + ((k * 8 + b) * 144 + g) * 16);
      XPLD();
      if (__all((tag_x(mm[0], e0, e1) | tag_x(mm[1], e0, e1) | tag_x(mm[2], e0, e1)) == 0u)) break;
      nap(2);
    }
    f32x2 xv[2], ga[2], sb[2], cb1[2], wr[2];
#pragma unroll
    for (int i = 0; i < 2; ++i) {
      xv[i] = up2(xa[i]); wr[i] = up2(wra[i]); ga[i] = up2(mm[0][i]); sb[i] = up2(mm[1][i]); cb1[i] = rbf2(f32x2{1.f, 1.f} + up2(mm[2][i]));
      asm volatile("" : "+v"(xv[i]), "+v"(ga[i]), "+v"(sb[i]), "+v"(cb1[i]), "+v"(wr[i]));
    }
    int ho = W_H2 + (b * 144 + g) * 16, xo = W_X1 + (b * 152 + g) * 16;
    asm volatile("" : "+v"(ho), "+v"(xo));
    _Pragma("nounroll") for (int sp = 0; sp < SPIN_CAP; ++sp) {
      if (__all(mxu(mxu(mxu(mx4(xp[0]), mx4(xp[1])), mxu(mx4(xp[2]), mx4(xp[3]))), mxu(mxu(mx4(xp[4]), mx4(xp[5])), mxu(mx4(xp[6]), mx4(xp[7])))) != 0xffffffffu)) break;
      nap(1);
      XPLD();
    }
    TL(10);
    f32x2 v[2];
#pragma unroll
    for (int i = 0; i < 2; ++i) {
      f32x2 pp[8];
#pragma unroll
      for (int p = 0; p < 8; ++p) pp[p] = f32x2{__uint_as_float(xp[p][2 * i]), __uint_as_float(xp[p][2 * i + 1])};
      v[i] = rbf2(xv[i] + rbf2(ga[i] * rbf2(((pp[0] + pp[1]) + (pp[2] + pp[3])) + ((pp[4] + pp[5]) + (pp[6] + pp[7])))));
    }
    const f32x2 s2 = v[0] + v[1];
    const float mw = wsum(on ? s2.x + s2.y : 0.f) * (wave < 2 ? 1.f / 256 : 1.f / 64);
    const f32x2 t0 = v[0] - f32x2{mw, mw}, t1 = v[1] - f32x2{mw, mw}, q2 = t0 * t0 + t1 * t1;
    const float m2 = wsum(on ? q2.x + q2.y : 0.f);
    if (lane == 0) *(f32x2*)(ex + wave * 2) = f32x2{mw, m2};
    lds_barrier();
    const f32x4 e01 = *(const f32x4*)ex; const f32x2 e2 = *(const f32x2*)(ex + 4);
    const float mu = (e01[0] + e01[2]) * (256.f / D) + e2.x * (64.f / D);
    const float d0 = e01[0] - mu, d1 = e01[2] - mu, d2 = e2.x - mu;
    const float var = rbf(((e01[1] + e01[3]) + e2.y + 256.f * (d0 * d0 + d1 * d1) + 64.f * d2 * d2) * (1.f / D));
    const float mean = rbf(mu), rden = frcp(rbf(sqrtf(rbf(var + 1e-5f))));
    u32 hw[2];
#pragma unroll
    for (int i = 0; i < 2; ++i) {
      const f32x2 t = rbf2(rbf2(rbf2(v[i] - f32x2{mean, mean}) * f32x2{rden, rden}) * cb1[i]) + sb[i];
      hw[i] = cvt2(t.x, t.y);
    }
    if (on) {
      st128(rs, ho, u32x4{hw[0], hw[1], e0, e1});
      st128(rs, xo, u32x4{cvt2(v[0].x, v[0].y), cvt2(v[1].x, v[1].y), e0, e1});
    }
    TL(11);
    const f32x2 r2 = up2(hw[0]) * wr[0] + up2(hw[1]) * wr[1];
    const float rw = wsum(on ? r2.x + r2.y : 0.f);
    if (lane == 0) ex[8 + wave] = rw;
    lds_barrier();
    if (tid < 8) {
      const float r = rbf((ex[8] + ex[9]) + ex[10]);
      st128(rs, W_X1 + (b * 152 + 144 + tid) * 16, u32x4{__float_as_uint(rbf(1.f / (1.f + __expf(-r)))), 0u, e0, e1});
    }
  }

  if (oj >= 0) {
    const int ob = W_O + ((op * 2 + (wave >> 2)) * 8 + (lane >> 3)) * 2048 + (wave & 3) * 256 + (lane & 7) * 16;
    u32x4 a[4];
#pragma unroll
    for (int p = 0; p < 4; ++p) a[p] = ld128(rs, ob + (p >> 1) * 1024 + (p & 1) * 128);
    SCHED();
    u32x4 wf[8];
    {
      const int q = n >> 2, p = lane & 3, hf = p >> 1;
      const int a0 = L_W + (wave * 64 + hf * 32 + 8 * grp + q) * 128 + (((q >> 1) ^ ((grp & 1) * 2) ^ (hf * 4)) << 4) + (p & 1) * 8;
#pragma unroll
      for (int cg = 0; cg < 8; ++cg) {
        const u32x2 lo = trd(lds, a0 ^ (cg * 16)), hi = trd(lds, (a0 ^ (cg * 16)) + 512);
        wf[cg] = u32x4{lo[0], lo[1], hi[0], hi[1]};
      }
    }
    int rw = L_OI + (n & 7) * 4128 + (wave * 2 + (n >> 3)) * 256 + (grp & 1) * 16, rr = L_OI + wave * 4128 + grp * 1024 + n * 16;
    int xo = W_XP + (op * 8 + wave) * 2304 + oj * 256 + lane * 16, sl = ((n & 7) * 8 + (n >> 3) * 4 + grp) * 4;
#pragma unroll
    for (int cg = 0; cg < 8; ++cg) asm volatile("" : "+v"(wf[cg]));
    asm volatile("" : "+v"(rw), "+v"(rr), "+v"(xo), "+v"(sl));
    _Pragma("nounroll") for (int sp = 0; sp < SPIN_CAP; ++sp) {
      const bool m0 = mxu(mx4(a[0]), mx4(a[1])) == 0xffffffffu, m1 = mxu(mx4(a[2]), mx4(a[3])) == 0xffffffffu;
      if (!__any(m0 || m1)) break;
      nap(2);
      if (m0) { a[0] = ld128(rs, ob); a[1] = ld128(rs, ob + 128); }
      if (m1) { a[2] = ld128(rs, ob + 1024); a[3] = ld128(rs, ob + 1152); }
    }
    TL(8);
#define OS(p, j) (__uint_as_float(a[p][j]) + __uint_as_float(a[(p) + 2][j]))
#define OP(v) (u32)__builtin_amdgcn_ds_bpermute(sl, (int)(v))
    const u32x4 of = {OP(cvt2(OS(0, 0), OS(0, 1))), OP(cvt2(OS(0, 2), OS(0, 3))), OP(cvt2(OS(1, 0), OS(1, 1))), OP(cvt2(OS(1, 2), OS(1, 3)))};
    f32x4 acc[8];
#pragma unroll
    for (int cg = 0; cg < 8; ++cg) acc[cg] = mfma(wf[cg], of, f32x4{0.f, 0.f, 0.f, 0.f});
    if ((n >> 3) == (grp >> 1)) {
#pragma unroll
      for (int cg = 0; cg < 8; ++cg) *(f32x4*)(lds + rw + cg * 32) = acc[cg];
    }
    lds_barrier();
    f32x4 s = (*(const f32x4*)(lds + rr) + *(const f32x4*)(lds + rr + 256)) + (*(const f32x4*)(lds + rr + 512) + *(const f32x4*)(lds + rr + 768));
#pragma unroll
    for (int k = 0; k < 4; ++k) s[k] = xsum32(xsum16(s[k]));
    if (lane < 16) st128(rs, xo, u32x4{sanf(s[0]), sanf(s[1]), sanf(s[2]), sanf(s[3])});
    TL(9);
  }
  TLOUT();
  }
  }
}

extern "C" void kernel_launch(void* const* d_in, const int* in_sizes, int n_in, void* d_out, int out_size, void* d_ws, size_t ws_size,
                              hipStream_t stream) {
  fused_kernel<<<NWG, NT, 0, stream>>>((const u16*)d_in[0], (const u16*)d_in[1], (const u16*)d_in[2], (const u16*)d_in[3], (const u16*)d_in[4],
                                       (const u16*)d_in[6], (const u16*)d_in[7], (const u16*)d_in[8], (const u16*)d_in[9], (const u16*)d_in[10],
                                       (const u16*)d_in[11], (unsigned char*)d_ws, (u16*)d_out);
}
```

```cpp
#include <hip/hip_runtime.h>
#include <hip/hip_bf16.h>
#include <cstdint>

typedef unsigned u32;
typedef unsigned short u16;
typedef u32 u32x4 __attribute__((ext_vector_type(4)));
typedef u32 u32x2 __attribute__((ext_vector_type(2)));
typedef float f32x4 __attribute__((ext_vector_type(4)));
typedef float f32x2 __attribute__((ext_vector_type(2)));
typedef short s16x4 __attribute__((ext_vector_type(4)));
typedef __bf16 bf16x8 __attribute__((ext_vector_type(8)));
typedef __bf16 bf16x2 __attribute__((ext_vector_type(2)));
typedef __bf16 bf16x16 __attribute__((ext_vector_type(16)));
typedef u32 u32x8 __attribute__((ext_vector_type(8)));
typedef __attribute__((address_space(3))) unsigned char lds_t;

constexpr int D = 576, SK = 8192, WIN = 2048, FF = 4608, NQ = 4096, NMOD = 6 * D;
constexpr int NWG = 256, NT = 512, NG = 72;
constexpr int SPIN_CAP = 1 << 15;

constexpr int W_O = 0;
constexpr int W_XP = W_O + 262144;
constexpr int W_Y = W_XP + 147456;
constexpr int W_MS = W_Y + NG * NG * 256;
constexpr int W_FL = W_MS;
constexpr int W_H = W_FL + 2048;
constexpr int W_M = W_H + 36864;
constexpr int W_Q = W_M + 73728;
constexpr int W_ST = W_Q + 131072;
constexpr int W_H2 = W_ST + 32768;
constexpr int W_X1 = W_H2 + 18432;
constexpr int W_END = W_X1 + 8 * 152 * 16;
constexpr int NPZ = 174, PZ = (W_MS / 16 + NPZ - 1) / NPZ;
static_assert(PZ <= 2 * NT, "poison split");

constexpr int L_W = 0;
constexpr int L_IMG = 73728;
constexpr int L_TILE = L_IMG + 9216;
constexpr int L_OI = 65536;
constexpr int L_B = 147456;
constexpr int L_C = 155648;
constexpr int LDS_BYTES = 163840;

#define SCHED() __builtin_amdgcn_sched_barrier(0)
#define BAR() asm volatile("s_barrier" ::: "memory")
#define TL(i) do {} while (0)
#define TLOUT() do {} while (0)
#define T2(i, ...) do {} while (0)
#define WAITV_(n) do { asm volatile("s_waitcnt vmcnt(" #n ")" ::: "memory"); SCHED(); } while (0)
#define WAITV(n) WAITV_(n)
#define W0 3
#define W1 5
#define W2 7
#define TH1 15
#define TH2 40
#define STR_(x) #x
#define STR(x) STR_(x)
#define WAITD() do { asm volatile("s_waitcnt vmcnt(" STR(W2) ")\n\ts_cmpk_gt_i32 %0, " STR(TH2) " ; throttle\n\ts_cbranch_scc1 4 ; throttle\n\ts_waitcnt vmcnt(" STR(W1) ") ; throttle\n\t" \
    "s_cmpk_gt_i32 %0, " STR(TH1) " ; throttle\n\ts_cbranch_scc1 1 ; throttle\n\ts_waitcnt vmcnt(" STR(W0) ") ; throttle" :: "s"(__builtin_amdgcn_readfirstlane(lag)) : "memory", "scc"); SCHED(); } while (0)
#define PR 114
#define OH 203
#define QS 0
#define LG0 0
#define LG1 1000
#define S63 1
#define OF 114
#define OL 110

__device__ __forceinline__ float lo16(u32 u) { return __uint_as_float(u << 16); }
__device__ __forceinline__ float hi16(u32 u) { return __uint_as_float(u & 0xffff0000u); }
__device__ __forceinline__ float b2f(u16 u) { return __uint_as_float((u32)u << 16); }
__device__ __forceinline__ u32 cvt2(float a, float b) { return __builtin_bit_cast(u32, __builtin_convertvector(f32x2{a, b}, bf16x2)); }
__device__ __forceinline__ float rbf(float v) { return __uint_as_float(cvt2(0.f, v)); }
__device__ __forceinline__ u32 sanf(float f) { u32 v = __float_as_uint(f); return v < 0xfffffffeu ? v : 0xfffffffeu; }
__device__ __forceinline__ f32x2 up2(u32 u) { return f32x2{lo16(u), hi16(u)}; }
__device__ __forceinline__ f32x2 rbf2(f32x2 v) { return f32x2{rbf(v.x), rbf(v.y)}; }
__device__ __forceinline__ void lds_barrier() { asm volatile("s_waitcnt lgkmcnt(0)\n\ts_barrier" ::: "memory"); }
__device__ __forceinline__ f32x4 mfma(u32x4 a, u32x4 b, f32x4 c) {
  return __builtin_amdgcn_mfma_f32_16x16x32_bf16(__builtin_bit_cast(bf16x8, a), __builtin_bit_cast(bf16x8, b), c, 0, 0, 0);
}
__device__ __forceinline__ f32x4 smf(u32x4 a, u32x8 b, f32x4 c, int idx) {
  return __builtin_amdgcn_smfmac_f32_16x16x64_bf16(__builtin_bit_cast(bf16x8, a), __builtin_bit_cast(bf16x16, b), c, idx, 0, 0);
}
__device__ __forceinline__ u32 plo(u32 a, u32 b) { return __builtin_amdgcn_perm(b, a, 0x05040100u); }
__device__ __forceinline__ u32 phi(u32 a, u32 b) { return __builtin_amdgcn_perm(b, a, 0x07060302u); }

extern "C" __device__ __attribute__((const)) unsigned long long llvm_dispatch_id() __asm("llvm.amdgcn.dispatch.id");
typedef __amdgpu_buffer_rsrc_t rsrc_t;
__device__ __forceinline__ u32x4 ld128(rsrc_t rs, int off) { return __builtin_amdgcn_raw_buffer_load_b128(rs, off, 0, 16); }
__device__ __forceinline__ void st128(rsrc_t rs, int off, u32x4 v) { __builtin_amdgcn_raw_buffer_store_b128(v, rs, off, 0, 16); }
__device__ __forceinline__ void st64(rsrc_t rs, int off, u32x2 v) { __builtin_amdgcn_raw_buffer_store_b64(v, rs, off, 0, 16); }
__device__ __forceinline__ void dma(const void* gbase, u32 voff, unsigned lds_dst) {
  unsigned keep;
  asm volatile("s_mov_b32 %0, m0\n\ts_mov_b32 m0, %3\n\ts_nop 2\n\tglobal_load_lds_dwordx4 %1, %2 nt\n\ts_mov_b32 m0, %0"
               : "=&s"(keep) : "v"(voff), "s"(gbase), "s"(lds_dst) : "memory");
}
__device__ __forceinline__ void dma_keep(const void* gbase, u32 voff, unsigned lds_dst) {
  unsigned keep;
  asm volatile("s_mov_b32 %0, m0\n\ts_mov_b32 m0, %3\n\ts_nop 2\n\tglobal_load_lds_dword %1, %2\n\ts_mov_b32 m0, %0"
               : "=&s"(keep) : "v"(voff), "s"(gbase), "s"(lds_dst) : "memory");
}
__device__ __forceinline__ void dma_sc(const void* gbase, u32 voff, unsigned lds_dst) {
  unsigned keep;
  asm volatile("s_mov_b32 %0, m0\n\ts_mov_b32 m0, %3\n\ts_nop 2\n\tglobal_load_lds_dwordx4 %1, %2 sc1\n\ts_mov_b32 m0, %0"
               : "=&s"(keep) : "v"(voff), "s"(gbase), "s"(lds_dst) : "memory");
}
__device__ __forceinline__ void ald128(u32x4& d, const void* gbase, u32 voff) { unsigned long long t; asm volatile("s_mov_b64 %1, %3\n\tglobal_load_dwordx4 %0, %2, %1" : "=v"(d), "=&s"(t) : "v"(voff), "s"(gbase) : "memory"); }
__device__ __forceinline__ void ald64(u32x2& d, const void* gbase, u32 voff) { unsigned long long t; asm volatile("s_mov_b64 %1, %3\n\tglobal_load_dwordx2 %0, %2, %1" : "=v"(d), "=&s"(t) : "v"(voff), "s"(gbase) : "memory"); }
__device__ __forceinline__ void ald128nt(u32x4& d, const void* gbase, u32 voff) { unsigned long long t; asm volatile("s_mov_b64 %1, %3\n\tglobal_load_dwordx4 %0, %2, %1 nt" : "=v"(d), "=&s"(t) : "v"(voff), "s"(gbase) : "memory"); }
__device__ __forceinline__ void ald128sc(u32x4& d, const void* gbase, u32 voff) { unsigned long long t; asm volatile("s_mov_b64 %1, %3\n\tglobal_load_dwordx4 %0, %2, %1 sc1" : "=v"(d), "=&s"(t) : "v"(voff), "s"(gbase) : "memory"); }
__device__ __forceinline__ u32x2 trd(const unsigned char* lds, int off) {
  return __builtin_bit_cast(u32x2, __builtin_amdgcn_ds_read_tr16_b64_v4i16((__attribute__((address_space(3))) s16x4*)((lds_t*)lds + off)));
}
#define nap(n) do { __builtin_amdgcn_s_sleep(n); asm volatile("" ::: "memory"); } while (0)
__device__ __forceinline__ float frcp(float v) { return __builtin_amdgcn_rcpf(v); }
__device__ __forceinline__ float fdiv(float a, float d, float r) { const float q = a * r; return fmaf(fmaf(-q, d, a), r, q); }
__device__ __forceinline__ float silu(float v) { const float d = 1.f + __expf(-v); const float r = frcp(d); return d < 3.0e38f ? fdiv(v, d, r) : v * r; }
template <int CTRL> __device__ __forceinline__ float dppf(float x) {
  return __builtin_bit_cast(float, __builtin_amdgcn_mov_dpp(__builtin_bit_cast(int, x), CTRL, 0xf, 0xf, true));
}
__device__ __forceinline__ u32 mxu(u32 a, u32 b) { return a > b ? a : b; }
__device__ __forceinline__ u32 mx4(u32x4 v) { return mxu(mxu(v[0], v[1]), mxu(v[2], v[3])); }
__device__ __forceinline__ float rl(float v, int l) { return __builtin_bit_cast(float, __builtin_amdgcn_readlane(__builtin_bit_cast(int, v), l)); }
__device__ __forceinline__ float wsum(float v) {
  v += dppf<0xB1>(v); v += dppf<0x4E>(v); v += dppf<0x141>(v); v += dppf<0x140>(v);
  return (rl(v, 0) + rl(v, 16)) + (rl(v, 32) + rl(v, 48));
}
__device__ __forceinline__ float up32(float v) { const u32 u = __float_as_uint(v); return __uint_as_float(__builtin_amdgcn_permlane32_swap(u, u, false, false)[1]); }
__device__ __forceinline__ float xsum32(float v) { const u32 u = __float_as_uint(v); const auto r = __builtin_amdgcn_permlane32_swap(u, u, false, false); return __uint_as_float(r[0]) + __uint_as_float(r[1]); }
__device__ __forceinline__ float xsum16(float v) { const u32 u = __float_as_uint(v); const auto r = __builtin_amdgcn_permlane16_swap(u, u, false, false); return __uint_as_float(r[0]) + __uint_as_float(r[1]); }
__device__ __forceinline__ f32x4 half_sum(f32x4 d) {
  f32x4 r;
#pragma unroll
  for (int j = 0; j < 4; ++j) r[j] = d[j] + dppf<0x108>(up32(d[j]));
  return r;
}
__device__ __forceinline__ f32x2 half_sum2(f32x4 d) {
  f32x2 r;
#pragma unroll
  for (int j = 0; j < 2; ++j) {
    const auto s = __builtin_amdgcn_permlane32_swap(__float_as_uint(d[j]), __float_as_uint(d[j + 2]), false, false);
    r[j] = __uint_as_float(s[0]) + dppf<0x108>(__uint_as_float(s[1]));
  }
  return r;
}
__device__ __forceinline__ void unpack8(u32x4 u, float* f) {
#pragma unroll
  for (int j = 0; j < 4; ++j) { f[2 * j] = lo16(u[j]); f[2 * j + 1] = hi16(u[j]); }
}
__device__ __forceinline__ void ln_row(const float (&v)[16], bool has2, float& mean, float& den) {
  float s = 0.f;
#pragma unroll
  for (int i = 0; i < 8; ++i) s += v[i];
  if (has2) {
#pragma unroll
    for (int i = 8; i < 16; ++i) s += v[i];
  }
  s = wsum(s);
  const float mu = s * (1.f / D);
  mean = rbf(mu);
  float q = 0.f;
#pragma unroll
  for (int i = 0; i < 8; ++i) { const float t = v[i] - mu; q = fmaf(t, t, q); }
  if (has2) {
#pragma unroll
    for (int i = 8; i < 16; ++i) { const float t = v[i] - mu; q = fmaf(t, t, q); }
  }
  q = wsum(q);
  const float var = rbf(q * (1.f / D));
  den = rbf(sqrtf(rbf(var + 1e-5f)));
}
template <int J0 = 0, int J1 = 9>
__device__ __forceinline__ void slab_dma(const u16* base, int stride_b, unsigned lds_dst, int wave, int lane) {
  const u32 v0 = (lane >> 3) * stride_b + (((lane & 7) ^ ((wave & 1) * 4 + (lane >> 4))) << 4), v1 = v0 ^ 32u;
  const u32 v4 = wave >= 4 ? v1 : v0;
  const char* gb = (const char*)base + (size_t)wave * 8 * stride_b;
#pragma unroll
  for (int j = J0; j < J1; ++j) dma(gb + (size_t)j * 64 * stride_b, j < 4 ? v0 : j == 4 ? v4 : v1, lds_dst + (j * 8 + wave) * 1024);
}
__device__ __forceinline__ void slab_frags(const unsigned char* lds, int base, int wave, int lane, u32x4 (&wf)[9]) {
  const int g = lane >> 4, q = (lane & 15) >> 2, p = lane & 3, hf = p >> 1;
  const int a0 = base + (hf * 288 + 8 * g + q) * 128 + ((wave ^ ((g & 1) * 4 + (q >> 1)) ^ (hf * 2)) << 4) + (p & 1) * 8, a1 = (a0 ^ 32) + 512;
#pragma unroll
  for (int s = 0; s < 9; ++s) {
    const u32x2 lo = trd(lds, a0 + s * 4096), hi = trd(lds, a1 + s * 4096);
    wf[s] = u32x4{lo[0], lo[1], hi[0], hi[1]};
  }
}
__device__ __forceinline__ void img_frags(const unsigned char* lds, int img, int lane, u32x4 (&af)[9]) {
#pragma unroll
  for (int s = 0; s < 9; ++s) af[s] = *(const u32x4*)(lds + img + (s * 64 + lane) * 16);
}
__device__ __forceinline__ f32x4 gemv9(const u32x4 (&wf)[9], const u32x4 (&af)[9]) {
  f32x4 acc[3];
  SCHED();
#pragma unroll
  for (int s = 0; s < 9; ++s) acc[s % 3] = mfma(wf[s], af[s], s < 3 ? f32x4{0.f, 0.f, 0.f, 0.f} : acc[s % 3]);
  return (acc[0] + acc[1]) + acc[2];
}
__device__ __forceinline__ int img_off(int b, int k8) {
  const int hf = k8 >= 36 ? 1 : 0, kk = k8 - hf * 36;
  return ((kk >> 2) * 64 + (kk & 3) * 16 + hf * 8 + b) * 16;
}
__device__ __forceinline__ u32 tag_x(u32x4 v, u32 e0, u32 e1) { return (v[2] ^ e0) | (v[3] ^ e1); }
__device__ __forceinline__ bool tag_ok(u32x4 v, u32 e0, u32 e1) { return tag_x(v, e0, e1) == 0u; }

constexpr int CODE_BYTES = 31500;

__global__ void __launch_bounds__(NT, 2) fused_kernel(const u16* __restrict__ x, const u16* __restrict__ kc, const u16* __restrict__ vc,
                                                      const u16* __restrict__ wq, const u16* __restrict__ wo, const u16* __restrict__ w_r,
                                                      const u16* __restrict__ wg, const u16* __restrict__ wu, const u16* __restrict__ wd,
                                                      const u16* __restrict__ c, const u16* __restrict__ w_mod, unsigned char* ws, u16* out) {
  __shared__ __attribute__((aligned(16))) unsigned char lds[LDS_BYTES];
  const int w = blockIdx.x, xcd = w & 7, idx = w >> 3;
  const int tid = threadIdx.x, lane = tid & 63, wave = __builtin_amdgcn_readfirstlane(tid >> 6);
  const int n = lane & 15, grp = lane >> 4;
  const rsrc_t rs = __builtin_amdgcn_make_buffer_rsrc(ws, 0, W_END, 0x00020000);
  const unsigned lds_base = (unsigned)(uintptr_t)(lds_t*)lds;
  {
    unsigned long long pc0; asm volatile("s_getpc_b64 %0" : "=s"(pc0));
    if (w >= 216 && wave == 0) dma_keep((const void*)pc0, min((w >> 3) * 8192 - 27 * 8192 + lane * 128, CODE_BYTES - 128), lds_base + 107520);
    SCHED();
  }
  const unsigned long long epoch = (llvm_dispatch_id() + 1ull) * 0x9E3779B97F4A7C15ull;
  const u32 e0 = (u32)epoch, e1 = (u32)(epoch >> 32);
  asm volatile("" : "+s"(vc), "+s"(w_r), "+s"(wu), "+s"(wd), "+s"(out), "+s"(x), "+s"(w_mod), "+s"(wg), "+s"(wq), "+s"(wo));

  const int mw = idx < 6 ? idx * 8 + xcd : -1;
  const int md = mw < 18 ? mw : -1;
  const int ml = mw >= 18 ? mw : mw >= 0 && mw < 6 ? 48 + mw : -1;
  const int lnb = mw >= 6 && mw < 14 ? mw - 6 : -1;
  const int qs = idx >= 6 && idx < 14 ? xcd * 8 + idx - 6 : -1;
  const int oj = idx >= 14 && idx < 23 ? idx - 14 : -1, op = xcd;
  const int gs = idx >= 23 ? xcd * 9 + idx - 23 : -1;

  const int s_att = w & 1, kvh = (w >> 1) & 15, b_att = w >> 5;
  {
    const size_t pairbase = ((size_t)(b_att * 16 + kvh) * SK + (SK - WIN)) * 256;
    const char* kp = (const char*)kc + pairbase;
    const char* vp = (const char*)vc + pairbase;
    const int wpart = (wave & 3) * 4096 + s_att * 16384 + (wave >> 2) * 262144;
    const int ko = wpart + (n >> 1) * 256 + (n & 1) * 64 + grp * 16;
    const int vo = wpart + lane * 16;
#define KLD(u) do { unsigned long long t_; asm volatile("s_mov_b64 %1, %3\n\tglobal_load_dwordx4 %0, %2, %1 offset:%4 nt" : "=v"(kb[u]), "=&s"(t_) : "v"(ko), "s"(kp + ((u) >> 2) * 32768), "i"((((u) >> 1) & 1) * 2048 + ((u) & 1) * 128) : "memory"); } while (0)
#define VLD(u) do { unsigned long long t_; asm volatile("s_mov_b64 %1, %3\n\tglobal_load_dwordx4 %0, %2, %1 offset:%4 nt" : "=v"(vb[u]), "=&s"(t_) : "v"(vo), "s"(vp + ((u) >> 2) * 32768), "i"(((u) & 3) * 1024) : "memory"); } while (0)
#define TIE8(a, o) asm volatile("" : "+v"(a[o]), "+v"(a[(o) + 1]), "+v"(a[(o) + 2]), "+v"(a[(o) + 3]), "+v"(a[(o) + 4]), "+v"(a[(o) + 5]), "+v"(a[(o) + 6]), "+v"(a[(o) + 7]))
    u32 tp0 = (u32)__builtin_amdgcn_s_memrealtime() + (gs >= 0 ? OF : mw >= 18 ? OL : qs >= 0 ? OH - QS : OH);
    int lag = gs >= 0 || mw >= 18 ? LG0 : LG1;
#define PACE(i) do { lag = (int)((u32)__builtin_amdgcn_s_memrealtime() - tp0) - (i) * PR / 4; \
      if (lag < 0) { _Pragma("nounroll") do __builtin_amdgcn_s_sleep(2); while ((int)((u32)__builtin_amdgcn_s_memrealtime() - tp0) < (i) * PR / 4); } SCHED(); } while (0)
#define REP4(M, u, W) do { M(u); W; M((u) + 1); W; M((u) + 2); W; M((u) + 3); W; } while (0)
    static_assert(W2 <= 7, "the hand-placed waits assume it");
    u32x4 kb[32], vb[32];
    const int pz = mw >= 18 ? mw - 18 : idx >= 14 ? w - 82 : -1;
#define POISON() do { if (pz >= 0) { _Pragma("unroll") for (int i = 0; i < 2; ++i) { const int g_ = pz * PZ + i * NT + tid; \
      if (i * NT + tid < PZ && g_ < W_MS / 16) st128(rs, g_ * 16, u32x4{0xffffffffu, 0xffffffffu, 0xffffffffu, 0xffffffffu}); } } SCHED(); } while (0)
#define PFLAG() do { if (pz >= 0 && tid == 448) st64(rs, W_FL + pz * 8, u32x2{e0, e1}); SCHED(); } while (0)
#define SILU_IMAGE(c0, c1) do { float f[8]; unpack8(c0, f); u32x4 o; \
        _Pragma("unroll") for (int j = 0; j < 4; ++j) o[j] = cvt2(silu(f[2 * j]), silu(f[2 * j + 1])); \
        const int b_ = tid / 72, ch_ = tid - b_ * 72; \
        *(u32x4*)(lds + L_IMG + img_off(b_, ch_)) = o; \
        if (wave == 0) { unpack8(c1, f); \
          _Pragma("unroll") for (int j = 0; j < 4; ++j) o[j] = cvt2(silu(f[2 * j]), silu(f[2 * j + 1])); \
          *(u32x4*)(lds + L_IMG + img_off(7, 8 + lane)) = o; } } while (0)
    if (md >= 0) {
      u32x4 c0, c1, x0, x1; u32x2 xc;
      __builtin_amdgcn_s_waitcnt(0x0f70);
      int ho = W_H + img_off(n & 7, (md >= 9 ? md - 9 : md) * 8 + wave) * 2 + (grp & 1) * 16;
      ald128(c0, c, tid * 16);
      ald128(c1, c, (512 + lane) * 16);
      ald128(x0, x, wave * 1152 + lane * 16);
      ald128(x1, x, wave * 1152 + 1024 + (lane & 7) * 16);
      ald64(xc, x, (n & 7) * 1152 + ((md >= 9 ? md - 9 : 0) * 64 + wave * 8 + (grp & 1) * 4) * 2);
      TL(35);
      BAR();
      float* st = (float*)(lds + L_TILE + 1024);
      if (wave < 4) {
        slab_dma(w_mod + md * 64, NMOD * 2, lds_base + L_W, wave, lane);
        asm volatile("s_waitcnt vmcnt(9)" ::: "memory");
      } else asm volatile("s_waitcnt vmcnt(0)" ::: "memory");
      asm volatile("s_nop 0 ; both branches above cover V" : "+v"(c0), "+v"(c1), "+v"(x0), "+v"(x1), "+v"(xc) :: "memory");
      {
        SILU_IMAGE(c0, c1);
        float v[16]; unpack8(x0, v); unpack8(x1, v + 8);
        float mean, den; ln_row(v, lane < 8, mean, den);
        if (lane == 0) { st[wave] = mean; st[8 + wave] = den; }
      }
      if (wave >= 4) slab_dma(w_mod + md * 64, NMOD * 2, lds_base + L_W, wave, lane);
      lds_barrier();
      REP4(KLD, 0, );
      u32x4 wf[9], af[9];
      img_frags(lds, L_IMG, lane, af);
      const float mean = st[n & 7], rd = frcp(st[8 + (n & 7)]);
      float ln[4] = {rbf(rbf(lo16(xc[0]) - mean) * rd), rbf(rbf(hi16(xc[0]) - mean) * rd), rbf(rbf(lo16(xc[1]) - mean) * rd), rbf(rbf(hi16(xc[1]) - mean) * rd)};
#pragma unroll
      for (int s = 0; s < 9; ++s) asm volatile("" : "+v"(af[s]));
      asm volatile("" : "+v"(ln[0]), "+v"(ln[1]), "+v"(ln[2]), "+v"(ln[3]), "+v"(ho));
      WAITV(4); BAR(); SCHED();
      slab_frags(lds, L_W, wave, lane, wf);
      const f32x4 s = half_sum(gemv9(wf, af));
      f32x4 r = s;
      if (md >= 9) {
#pragma unroll
        for (int j = 0; j < 4; ++j) r[j] = ln[j] * rbf(1.f + rbf(s[j]));
      }
      if (n < 8 && grp < 2) st128(rs, ho + (md >= 9 ? 18432 : 0), u32x4{cvt2(r[0], r[1]), cvt2(r[2], r[3]), e0, e1});
      SCHED();
      TL(1);
      BAR();
      KLD(4); KLD(5); WAITV(W2);
      KLD(6); WAITV(W2); KLD(7); WAITV(W2); KLD(8); WAITV(W2); KLD(9); WAITV(W2); KLD(10); WAITV(W2);
      KLD(11); WAITV(W2); REP4(KLD, 12, WAITV(W2)); REP4(KLD, 16, WAITV(W2));
      REP4(KLD, 20, WAITV(W2)); KLD(24); WAITV(W2); KLD(25);
    } else {
      constexpr int L_LAND = 98304;
      const unsigned land = lds_base + L_LAND + wave * 6144;
      const unsigned char* lp = lds + L_LAND + wave * 6144 + lane * 16;
      const int sl_ = (3 - (int)((uintptr_t)wq >> 7)) & 7;
      const int s2_ = (63 - (int)((uintptr_t)wq >> 7)) & 63;
      const bool slowq = qs >= 0 && ((qs & 7) == sl_ || (S63 && qs == s2_));
      if (S63 && gs >= 0 && wave == 3 && xcd == (s2_ >> 3)) dma_keep((const char*)wq + (size_t)(idx - 23) * 64 * 8192 + s2_ * 128, lane * 8192, land);
      if (gs >= 0 && wave == 0) dma_keep((const char*)wq + (size_t)(idx - 23) * 64 * 8192 + xcd * 1024 + sl_ * 128, lane * 8192, land);
      if (gs >= 0 && (wave == 1 || (wave == 2 && xcd < 2))) dma_keep((const char*)w_mod + (size_t)((idx - 23) * 64 + 32) * (NMOD * 2) + xcd * 128 + (wave - 1) * 2048, (lane & 31) * (NMOD * 2) + (wave == 1 ? (lane >> 5) * 1024 : 0), land);
      POISON();
      const u32 v0 = (lane >> 3) * 1152 + (((lane & 7) ^ ((lane >> 4) & 2)) << 4);
      const char* gb = (const char*)(wo + (size_t)(op * 512 + wave * 64) * D + oj * 64);
      if (ml >= 0) { dma_sc(c, tid * 16, land); dma_sc(c, (512 + lane) * 16, land + 1024); }
#define WK(j) do { if (oj >= 0) { if ((j) < 8) dma(gb + (j) * 8 * 1152, v0 ^ (((j) & 1) * 64), lds_base + L_W + (wave * 8 + (j)) * 1024); } \
                   else if (slowq) { if ((j) == 8) slab_dma(wq + qs * 64, NQ * 2, lds_base + L_W, wave, lane); } \
                   else if (qs >= 0 && (j) < 9) slab_dma<(j) < 9 ? (j) : 0, (j) < 9 ? (j) + 1 : 0>(wq + qs * 64, NQ * 2, lds_base + L_W, wave, lane); \
                   else if (qs < 0 && (j) >= 4) PACE(j); \
                   KLD(j); if ((j) == 0) TL(32); if ((j) == 4) TL(33); WAITD(); if ((j) == 5) TL(34); } while (0)
      WK(0); WK(1); WK(2); WK(3);
      WK(4); WK(5);
      WK(6); WK(7);
      TL(24);
      if (ml >= 0) { const u32x4 c0 = *(const u32x4*)lp, c1 = *(const u32x4*)(lp + 1024); SILU_IMAGE(c0, c1); }
      BAR();
      PFLAG();
      WK(8); WK(9); WK(10); WK(11);
      TL(25);
      PACE(12); REP4(KLD, 12, WAITD());
      TL(26);
      const int hg0 = (wave * 144 + lane) * 16, hg2 = (wave * 144 + 128 + n) * 16;
#define HREQ() do { dma_sc(ws + W_H, hg0, land); dma_sc(ws + W_H + 18432, hg0, land + 1024); dma_sc(ws + W_H + 1024, hg0, land + 2048); \
                    dma_sc(ws + W_H + 18432 + 1024, hg0, land + 3072); dma_sc(ws + W_H, hg2, land + 4096); dma_sc(ws + W_H + 18432, hg2, land + 5120); } while (0)
#define HGET() do { _Pragma("unroll") for (int i = 0; i < 6; ++i) hq[i] = *(const u32x4*)(lp + i * 1024); } while (0)
#define HQOK() __all((tag_x(hq[0], e0, e1) | tag_x(hq[1], e0, e1) | tag_x(hq[2], e0, e1) | tag_x(hq[3], e0, e1) | tag_x(hq[4], e0, e1) | tag_x(hq[5], e0, e1)) == 0u)
#define HOK() __all((tag_x(hp[0], e0, e1) | tag_x(hp[1], e0, e1) | tag_x(hp[2], e0, e1)) == 0u)
      if (qs >= 0) HREQ();
      PACE(16); REP4(KLD, 16, WAITD());
      if (qs >= 0) WAITV(4);
      TL(27);
      PACE(20); KLD(20); KLD(21);
      if (qs >= 0) {
        u32x4 hq[6]; HGET();
        _Pragma("nounroll") for (int sp = 0; sp < SPIN_CAP && !HQOK(); ++sp) { nap(16); HREQ(); WAITV(0); HGET(); }
        TL(2);
#define HSUM(a, b) cvt2(lo16(a) + lo16(b), hi16(a) + hi16(b))
        *(u32x2*)(lds + L_IMG + hg0 / 2) = u32x2{HSUM(hq[0][0], hq[1][0]), HSUM(hq[0][1], hq[1][1])};
        *(u32x2*)(lds + L_IMG + 512 + hg0 / 2) = u32x2{HSUM(hq[2][0], hq[3][0]), HSUM(hq[2][1], hq[3][1])};
        if (lane < 16) *(u32x2*)(lds + L_IMG + hg2 / 2) = u32x2{HSUM(hq[4][0], hq[5][0]), HSUM(hq[4][1], hq[5][1])};
      } else WAITD();
      KLD(22); KLD(23);
      if (qs >= 0) {
        u32x4 wf[9], af[9];
        lds_barrier();
        slab_frags(lds, L_W, wave, lane, wf);
        img_frags(lds, L_IMG, lane, af);
        const f32x4 s = half_sum(gemv9(wf, af));
        if (n < 8 && grp < 2) *(u32x2*)(lds + L_TILE + n * 128 + wave * 16 + grp * 8) = u32x2{cvt2(s[0], s[1]), cvt2(s[2], s[3])};
      } else WAITD();
      PACE(24); KLD(24); KLD(25);
      if (qs >= 0) {
        lds_barrier();
        if (wave < 2) {
          const u32x2 t2 = *(const u32x2*)(lds + L_TILE + tid * 8);
          st128(rs, W_Q + ((tid >> 4) * 1024 + qs * 16 + n) * 16, u32x4{t2[0], t2[1], e0, e1});
        }
        TL(3);
        tp0 += QS;
      }
      SCHED();
    }
    const unsigned char* qg = ws + W_Q + (b_att * 1024 + kvh * 64) * 16;
    KLD(26); WAITD(); KLD(27); WAITD(); PACE(28); KLD(28); WAITD(); KLD(29); WAITD();
    PACE(30); KLD(30); WAITD(); KLD(31); WAITD();
    TL(28);
    PACE(32); VLD(0); WAITD(); VLD(1); WAITD(); PACE(34); VLD(2); WAITD(); VLD(3); WAITD();
#define QLOOK() do { if (wave == 1) dma_sc(qg, lane * 16, lds_base + L_B + 1024); } while (0)
    QLOOK();
    PACE(36); VLD(4); WAITD(); VLD(5); WAITD(); PACE(38); VLD(6); WAITD(); VLD(7); WAITD();
    QLOOK();
    TL(29);
    PACE(40); VLD(8); WAITD(); VLD(9); WAITD(); PACE(42); QLOOK(); VLD(10); WAITD(); VLD(11); WAITD();
    TIE8(kb, 0); TIE8(kb, 8); TIE8(kb, 16); TIE8(kb, 24);
    PACE(44); QLOOK(); VLD(12); VLD(13);
    u32x4 qf[2];
    {
      const u32x4* ql = (const u32x4*)(lds + L_B + 1024 + ((n >> 3) * 32 + (n & 1) * 8 + grp * 2) * 16);
      u32x4 g[4];
      _Pragma("nounroll") for (int sp = 0; sp < SPIN_CAP; ++sp) {
        g[0] = ql[0]; g[1] = ql[1]; g[2] = ql[16]; g[3] = ql[17];
        if (__all((tag_x(g[0], e0, e1) | tag_x(g[1], e0, e1) | tag_x(g[2], e0, e1) | tag_x(g[3], e0, e1)) == 0u)) break;
        if (wave == 1 && sp >= 6) { dma_sc(qg, lane * 16, lds_base + L_B + 1024); WAITV(0); } else nap(4);
      }
      asm volatile("" ::: "memory");
      g[0] = ql[0]; g[1] = ql[1]; g[2] = ql[16]; g[3] = ql[17];
      qf[0] = u32x4{g[0][0], g[0][1], g[1][0], g[1][1]};
      qf[1] = u32x4{g[2][0], g[2][1], g[3][0], g[3][1]};
    }
    TL(4);
    constexpr float SCALE = 0.088388347648318440f;
    f32x2 sc[16];
    float mx = -INFINITY;
#pragma unroll
    for (int T = 0; T < 16; ++T) {
      f32x4 acc = mfma(kb[T * 2], qf[0], f32x4{0.f, 0.f, 0.f, 0.f});
      acc = mfma(kb[T * 2 + 1], qf[1], acc);
      sc[T] = f32x2{acc[0] + dppf<0xB1>(acc[1]), acc[2] + dppf<0xB1>(acc[3])};
      if (T == 7 || T == 15) { SCHED(); if (T == 7) VLD(14); else VLD(15); SCHED(); }
    }
#pragma unroll
    for (int T = 0; T < 16; ++T)
#pragma unroll
      for (int j = 0; j < 2; ++j) { const float v = rbf(rbf(sc[T][j]) * SCALE); sc[T][j] = v; mx = fmaxf(mx, v); }
    SCHED(); VLD(16); SCHED();
    mx = fmaxf(mx, __shfl_xor(mx, 16));
    mx = fmaxf(mx, __shfl_xor(mx, 32));
    float ls = 0.f;
#pragma unroll
    for (int T = 0; T < 16; ++T)
#pragma unroll
      for (int j = 0; j < 2; ++j) { sc[T][j] = __expf(sc[T][j] - mx); ls += sc[T][j]; }
    SCHED(); VLD(17); SCHED();
    ls += __shfl_xor(ls, 16);
    ls += __shfl_xor(ls, 32);
    float* stat = (float*)(lds + L_B);
    if (lane == 0 || lane == 8) *(f32x2*)(stat + (wave * 2 + (lane >> 3)) * 2) = f32x2{mx, ls};
    SCHED(); VLD(18); VLD(19); SCHED();
    lds_barrier();
    float M = -INFINITY, L = 0.f;
    {
      f32x2 t[8];
#pragma unroll
      for (int q = 0; q < 8; ++q) { t[q] = *(const f32x2*)(stat + (q * 2 + (n >> 3)) * 2); M = fmaxf(M, t[q].x); }
#pragma unroll
      for (int q = 0; q < 8; ++q) L = fmaf(__expf(t[q].x - M), t[q].y, L);
    }
    if (tid >= 4 && tid < 12) st128(rs, W_ST + (w * 8 + lane - 4) * 16, u32x4{__float_as_uint(M), __float_as_uint(L), e0, e1});
    SCHED();
    TL(30);
    PACE(52); VLD(20); WAITD(); VLD(21); WAITD();
    const int po = W_ST + ((w ^ 1) * 8 + (n >> 3) * 4) * 16;
    int l1 = lane; asm volatile("" : "+v"(l1));
    const int fl = 1024 + min(l1, NPZ / 2 - 65) * 16;
    if (wave == 1) { dma_sc(ws + W_FL, l1 * 16, lds_base + L_B + 2048); dma_sc(ws + W_FL, fl, lds_base + L_B + 3072); }
    u32 km[4] = {(l1 & 4) ? 0u : 0xffffu, (l1 & 4) ? 0xffffu : 0u, (l1 & 4) ? 0u : 0xffff0000u, (l1 & 4) ? 0xffff0000u : 0u};
    int px = (l1 & 3) * 0x5555;
    asm volatile("" : "+v"(km[0]), "+v"(km[1]), "+v"(km[2]), "+v"(km[3]), "+v"(px));
    PACE(54);
    VLD(22); WAITD(); VLD(23); WAITD();
    u32x4 ps;
    ald128sc(ps, ws, po);
    PACE(56);
    VLD(24); WAITD(); VLD(25); WAITD();
    TL(31);
    PACE(58);
    VLD(26); WAITD(); VLD(27); WAITD();
    u32x4 ps2;
    VLD(28); VLD(29); WAITV(6);
    asm volatile("" : "+v"(ps));
    TIE8(vb, 0); TIE8(vb, 8); TIE8(vb, 16);
    ald128sc(ps2, ws, po);
    VLD(30); VLD(31);
    if (!__all(tag_ok(ps, e0, e1))) {
      WAITV(2);
      asm volatile("" : "+v"(ps2));
      ps = ps2;
      if (!__all(tag_ok(ps, e0, e1))) {
        int sp = 0;
        _Pragma("nounroll") do { nap(1); ps = ld128(rs, po); } while (!__all(tag_ok(ps, e0, e1)) && ++sp < SPIN_CAP);
      }
    }
#define FOK(a, b) __all(((a[0] ^ e0) | (a[1] ^ e1) | tag_x(a, e0, e1) | (b[0] ^ e0) | (b[1] ^ e1) | tag_x(b, e0, e1)) == 0u)
    if (wave == 1 && !FOK((*(const u32x4*)(lds + L_B + 2048 + l1 * 16)), (*(const u32x4*)(lds + L_B + 3072 + l1 * 16)))) {
      _Pragma("nounroll") for (int sp = 0; sp < SPIN_CAP; ++sp) {
        nap(1);
        const u32x4 a = ld128(rs, W_FL + l1 * 16), b = ld128(rs, W_FL + fl);
        if (FOK(a, b)) break;
      }
    }
    float fac;
    {
      const float M2 = __uint_as_float(ps[0]), L2 = __uint_as_float(ps[1]);
      const float Mg = fmaxf(M, M2);
      const float Lg = __expf(M - Mg) * L + __expf(M2 - Mg) * L2;
      fac = __expf(mx - Mg) / Lg;
    }
    TL(5);
    u32x4 pf[16];
    u32 pp[16];
#pragma unroll
    for (int T = 0; T < 16; ++T) pp[T] = cvt2(sc[T][0] * fac, sc[T][1] * fac);
    SCHED();
#pragma unroll
    for (int T = 0; T < 16; ++T) {
      const u32 p = (u32)__builtin_amdgcn_mov_dpp((int)pp[T], 0xa0, 0xf, 0xf, true);
      pf[T] = u32x4{p & km[0], p & km[1], p & km[2], p & km[3]};
    }
    f32x4 oa = {0.f, 0.f, 0.f, 0.f};
#define PV(T) oa = smf(pf[T], __builtin_shufflevector(vb[2 * (T)], vb[2 * (T) + 1], 0, 1, 2, 3, 4, 5, 6, 7), oa, px)
#pragma unroll
    for (int T = 0; T < 12; ++T) PV(T);
    SCHED();
    int t2_ = threadIdx.x; asm volatile("" : "+v"(t2_));
    const int tid = t2_, lane = tid & 63, n = lane & 15, grp = lane >> 4;
    int oo = W_O + ((kvh * 8 + b_att) * 2 + s_att) * 1024 + (((grp >> 1) * 2 + (n >> 3)) * 16 + (grp & 1) * 8 + (n & 7)) * 16;
    int mw_ = L_C + tid * 16;
    asm volatile("" : "+v"(oo), "+v"(mw_), "+v"(pf[12]), "+v"(pf[13]), "+v"(pf[14]), "+v"(pf[15]));
    constexpr int L_H2 = L_W;
    constexpr int L_ACT = L_C + 4608;
    int ia, ra, ix, yo[2], fo[7], tw, tr_;
    const u32 gv = (lane >> 3) * (FF * 2) + (((lane & 7) ^ ((wave & 1) * 4 + ((lane >> 4) & 2))) << 4);
#define GU(j) do { const size_t o_ = ((size_t)((j) * 64 + wave * 8) * FF + gs * 64) * 2; dma((const char*)wg + o_, gv, lds_base + L_W + ((j) * 8 + wave) * 1024); \
                   dma((const char*)wu + o_, gv ^ 16u, lds_base + L_W + 73728 + ((j) * 8 + wave) * 1024); } while (0)
    const bool late = gs >= 0 || mw >= 18;
    if (late) {
      BAR();
      if (gs >= 0) { GU(0); GU(1); } else slab_dma<0, 4>(w_mod + ml * 64, NMOD * 2, lds_base + L_W, wave, lane);
      {
    const int b = wave;
    ia = L_H2 + grp * 1024 + ((n >> 2) * 16 + (n & 2) * 4 + (b ^ (n >> 1))) * 16 + (n & 1) * 8;
    ra = L_H2 + (grp * 16 + (n & 8) + ((n & 7) ^ (grp * 2 + (n >> 3)))) * 16; ix = n < 8 ? 0x4444 : 0xeeee;
    tw = L_ACT + ((grp & 1) * 4 + (grp >> 1) * 2 + (n >> 3)) * 144 + wave * 16 + (n & 7) * 2; tr_ = L_ACT + (n & 7) * 144 + grp * 16;
    yo[0] = W_Y + (((wave * 8 + (grp & 1) * 4 + (n >> 3) * 2) * NG + gs) * 16 + (grp >> 1) * 8 + (n & 7)) * 16;
    yo[1] = W_Y + ((((64 + wave * 2 + (grp >> 1)) * NG + gs) * 2 + (grp & 1)) * 8 + n) * 16;
    {
      const int b_ = lane >> 3, cc = lane & 7, col = gs * 8 + cc;
      fo[0] = W_M + ((3 * 8 + b_) * 144 + gs * 2 + (cc >> 2)) * 16;
      fo[1] = W_X1 + (b_ * 152 + gs * 2 + (cc >> 2)) * 16; fo[2] = W_X1 + (b_ * 152 + 144) * 16;
      fo[3] = W_Y + (gs * 1152 + tid) * 16;
      fo[4] = L_H2 + ((gs >> 1) * 16 + (gs & 1) * 8 + (b_ ^ (gs & 7))) * 16 + cc * 2;
      fo[5] = L_C + (((cc >> 2) * 8 + b_) * 4 + (cc & 3)) * 4;
      fo[6] = (b_ * D + col) * 2;
    }
    asm volatile("" : "+v"(ia), "+v"(ra), "+v"(ix), "+v"(tw), "+v"(tr_), "+v"(yo[0]), "+v"(yo[1]),
                 "+v"(fo[0]), "+v"(fo[1]), "+v"(fo[2]), "+v"(fo[3]), "+v"(fo[4]), "+v"(fo[5]), "+v"(fo[6]));
      }
      WAITV(6);
    } else WAITV(2);
    asm volatile("s_nop 0 ; both branches above cover V up to the last two" : "+v"(vb[24]), "+v"(vb[25]), "+v"(vb[26]), "+v"(vb[27]), "+v"(vb[28]), "+v"(vb[29]));
    asm volatile("" :: "v"(ps2));
    PV(12); PV(13); PV(14);
    SCHED();
    if (late) WAITV(4); else WAITV(0);
    T2(56, );
    asm volatile("s_nop 0 ; both branches above cover V" : "+v"(vb[30]), "+v"(vb[31]));
    PV(15);
    TL(6);
    *(f32x4*)(lds + mw_) = oa;
    T2(54, );
    lds_barrier();
    T2(55, );
    if (wave == 0) {
      f32x4 m[8];
#pragma unroll
      for (int q = 0; q < 8; ++q) m[q] = *(const f32x4*)(lds + mw_ + q * 1024);
      SCHED();
      const f32x4 o = ((m[0] + m[1]) + (m[2] + m[3])) + ((m[4] + m[5]) + (m[6] + m[7]));
      st128(rs, oo, u32x4{sanf(o[0]), sanf(o[1]), sanf(o[2]), sanf(o[3])});
    }
    TL(7); SCHED();
  if (gs >= 0) {
    const int b = wave;
    BAR(); SCHED();
    u32x8 bw[9];
    u32x4 wdf[2][5];
    const int ga0 = L_W + ((lane >> 1) & 1) * 73728 + ((grp >> 1) * 16 + (grp & 1) * 4 + ((n >> 2) & 1) + (n & 8)) * 128 + ((wave ^ ((grp & 1) * 2 + ((n >> 1) & 4)) ^ ((lane >> 1) & 1)) << 4) + (lane & 1) * 8;
#define GUF(S0, S1) do { _Pragma("unroll") for (int s = S0; s < S1; ++s) { const int a_ = ga0 + (s >> 2) * 32768, o_ = (s & 3) * 8192; \
      const u32x2 r0 = trd(lds, a_ + o_), r1 = trd(lds, a_ + o_ + 256), r2 = trd(lds, a_ + o_ + 4096), r3 = trd(lds, a_ + o_ + 4352); \
      bw[s] = u32x8{r0[0], r0[1], r1[0], r1[1], r2[0], r2[1], r3[0], r3[1]}; } } while (0)
    {
      u32x4 rd[8];
      GU(2); GU(3);
      TL(23);
      BAR();
      const char* db = (const char*)(wd + (size_t)gs * 64 * D + wave * 64);
      const u32 dv = (n >> 3) * 36864 + grp * 9216 + (n & 7) * 16;
#pragma unroll
      for (int i = 0; i < 8; ++i) ald128nt(rd[i], db + i * 1152, dv);
      {
        const int r = lane >> 3;
        dma((const char*)(wd + (size_t)(gs * 64 + wave * 8) * D + 512), r * 1152 + (((lane & 7) ^ (r & 2) ^ ((wave & 1) * 4)) << 4), lds_base + L_B + wave * 1024);
      }
      TL(19);
      asm volatile("s_waitcnt vmcnt(9)\n\ts_barrier" ::: "memory");
      TL(22);
      GUF(0, 4);
      SCHED();
      GU(4); GU(5); GU(6); GU(7); GU(8);
      TL(20);
      asm volatile("s_waitcnt vmcnt(11)" : "+v"(rd[0]), "+v"(rd[1]), "+v"(rd[2]), "+v"(rd[3]), "+v"(rd[4]), "+v"(rd[5]), "+v"(rd[6]), "+v"(rd[7]) :: "memory");
#pragma unroll
      for (int j = 0; j < 4; ++j)
#pragma unroll
        for (int t = 0; t < 4; ++t) {
          const u32 a = (t & 1) ? phi(rd[2 * j][t >> 1], rd[2 * j + 1][t >> 1]) : plo(rd[2 * j][t >> 1], rd[2 * j + 1][t >> 1]);
          const u32 b_ = (t & 1) ? phi(rd[2 * j][2 + (t >> 1)], rd[2 * j + 1][2 + (t >> 1)]) : plo(rd[2 * j][2 + (t >> 1)], rd[2 * j + 1][2 + (t >> 1)]);
          wdf[0][t][j] = (u32)__builtin_amdgcn_update_dpp((int)a, (int)b_, 0x128, 0xf, 0xc, false);
          wdf[1][t][j] = (u32)__builtin_amdgcn_update_dpp((int)b_, (int)a, 0x128, 0xf, 0x3, false);
        }
#pragma unroll
      for (int t = 0; t < 4; ++t) asm volatile("" : "+v"(wdf[0][t]), "+v"(wdf[1][t]));
      TL(21);
    }
    asm volatile("s_waitcnt vmcnt(0) lgkmcnt(0)\n\ts_barrier" ::: "memory");
    {
      u32x4 hp[3];
      const int h0 = W_H2 + (b * 144 + lane) * 16, h2o = W_H2 + (b * 144 + 128 + n) * 16;
      hp[0] = ld128(rs, h0); hp[1] = ld128(rs, h0 + 1024); hp[2] = ld128(rs, h2o);
      GUF(4, 9);
      const int q = n >> 2, p = lane & 3;
      const int a0 = L_B + (8 * grp + q) * 128 + ((((wave & 3) * 2 + (p >> 1)) ^ (q & 2) ^ ((grp & 1) * 4)) << 4) + (p & 1) * 8;
#pragma unroll
      for (int ks = 0; ks < 2; ++ks) {
        const u32x2 lo = trd(lds, a0 + ks * 4096), hi = trd(lds, a0 + ks * 4096 + 512);
        wdf[ks][4] = u32x4{lo[0], lo[1], hi[0], hi[1]};
      }
      TL(12);
#pragma unroll
      for (int s = 0; s < 9; ++s) asm volatile("" : "+v"(bw[s]));
      asm volatile("" : "+v"(wdf[0][4]), "+v"(wdf[1][4]));
      if (!HOK()) {
        int sp = 0;
        _Pragma("nounroll") do { nap(2); hp[0] = ld128(rs, h0); hp[1] = ld128(rs, h0 + 1024); hp[2] = ld128(rs, h2o); } while (!HOK() && ++sp < SPIN_CAP);
      }
      TL(13);
      *(u32x2*)(lds + ia) = u32x2{hp[0][0], hp[0][1]};
      *(u32x2*)(lds + ia + 4096) = u32x2{hp[1][0], hp[1][1]};
      if (lane < 16) *(u32x2*)(lds + ia + 8192) = u32x2{hp[2][0], hp[2][1]};
      lds_barrier();
      T2(47, );
      u32x4 af[9];
#pragma unroll
      for (int s = 0; s < 9; ++s) af[s] = *(const u32x4*)(lds + ra + s * 1024);
      SCHED();
      f32x4 d = {0.f, 0.f, 0.f, 0.f};
#pragma unroll
      for (int s = 0; s < 9; ++s) d = smf(af[s], bw[s], d, ix);
      T2(48, "+v"(d));
      int r[2];
#pragma unroll
      for (int j = 0; j < 2; ++j) {
        const auto t = __builtin_amdgcn_permlane32_swap(__float_as_uint(d[j]), __float_as_uint(d[j + 2]), false, false);
        r[j] = __float_as_int(rbf(__uint_as_float(t[0]) + __uint_as_float(t[1])));
      }
      const float g = __int_as_float(__builtin_amdgcn_update_dpp(r[0], r[1], 0x128, 0xf, 0xc, false)), u = __int_as_float(__builtin_amdgcn_update_dpp(r[1], r[0], 0x128, 0xf, 0x3, false));
      *(u16*)(lds + tw) = (u16)cvt2(rbf(silu(g)) * u, 0.f);
    }
    T2(49, );
    lds_barrier();
    TL(14);
    {
      u32x4 af[2];
#pragma unroll
      for (int ks = 0; ks < 2; ++ks) af[ks] = *(const u32x4*)(lds + tr_ + ks * 64);
      f32x4 d[5];
#pragma unroll
      for (int t = 0; t < 5; ++t) d[t] = mfma(wdf[1][t], af[1], mfma(wdf[0][t], af[0], f32x4{0.f, 0.f, 0.f, 0.f}));
      T2(50, "+v"(d[0]), "+v"(d[1]), "+v"(d[2]), "+v"(d[3]), "+v"(d[4]));
#define YS(t) sanf(__int_as_float(__builtin_amdgcn_update_dpp(__float_as_int(d[t][j]), __float_as_int(d[t][j + 2]), 0xe4, 0xf, 0xc, false)))
#pragma unroll
      for (int j = 0; j < 2; ++j)
        __builtin_amdgcn_raw_buffer_store_b128(u32x4{YS(0), YS(1), YS(2), YS(3)},
                                               rs, yo[0], j * NG * 256, 16);
      if (n < 8 && wave < 4)
        st128(rs, yo[1], u32x4{sanf(d[4][0]), sanf(d[4][1]), sanf(d[4][2]), sanf(d[4][3])});
    }
    TL(15);
    {
      float gbv = 0.f, x1v = 0.f, h2 = 0.f, rsel = 0.f;
      if (wave == 7) {
        u32x4 gq, xq, rq;
        _Pragma("nounroll") for (int sp = 0; sp < SPIN_CAP; ++sp) {
          gq = ld128(rs, fo[0]); xq = ld128(rs, fo[1]); rq = ld128(rs, fo[2]);
          if (__all((tag_x(gq, e0, e1) | tag_x(xq, e0, e1) | tag_x(rq, e0, e1)) == 0u)) break;
          nap(1);
        }
        const u32 gw = (lane & 2) ? gq[1] : gq[0], xw = (lane & 2) ? xq[1] : xq[0];
        gbv = (lane & 1) ? hi16(gw) : lo16(gw); x1v = (lane & 1) ? hi16(xw) : lo16(xw);
        h2 = b2f(*(const u16*)(lds + fo[4])); rsel = __uint_as_float(rq[0]);
        asm volatile("" : "+v"(gbv), "+v"(x1v), "+v"(h2), "+v"(rsel));
      }
      WAITV(0); T2(51, );
      u32x4 y[3] = {};
      _Pragma("nounroll") for (int sp = 0; sp < SPIN_CAP; ++sp) {
        y[0] = ld128(rs, fo[3]); y[1] = ld128(rs, fo[3] + 8192);
        if (wave < 2) y[2] = ld128(rs, fo[3] + 16384);
        if (__all(mxu(mxu(mx4(y[0]), mx4(y[1])), mx4(y[2])) != 0xffffffffu)) break;
        nap(1);
      }
      TL(16);
      f32x4 s;
#pragma unroll
      for (int k = 0; k < 4; ++k) s[k] = xsum32(xsum16((__uint_as_float(y[0][k]) + __uint_as_float(y[1][k])) + __uint_as_float(y[2][k])));
      if (lane < 16) *(f32x4*)(lds + L_C + (wave * 16 + lane) * 16) = s;
      T2(52, );
      lds_barrier();
      T2(53, );
      if (wave == 7) {
        float t[8];
#pragma unroll
        for (int ww = 0; ww < 8; ++ww) t[ww] = *(const float*)(lds + fo[5] + ww * 256);
        SCHED();
        const float y2 = rbf(rbf(((t[0] + t[1]) + (t[2] + t[3])) + ((t[4] + t[5]) + (t[6] + t[7]))) * rsel);
        const float res = x1v + rbf(gbv * rbf(h2 + y2));
        __builtin_amdgcn_raw_buffer_store_b16((u16)(cvt2(res, 0.f) & 0xffffu), __builtin_amdgcn_make_buffer_rsrc(out, 0, 8 * D * 2, 0x00020000), fo[6], 0, 0);
      }
      TL(17);
    }
  }
  {
  int t3_ = threadIdx.x; asm volatile("" : "+v"(t3_));
  const int tid = t3_, lane = tid & 63, n = lane & 15, grp = lane >> 4;
  if (qs >= 0) {
    const u16* mat = (wave & 1) ? wg : wu;
    const int g0 = ((3 - (int)((uintptr_t)mat >> 7) - xcd * 9) & 7) + (wave >= 4 ? 8 : 0);
    const int r = (idx - 6) * 72 + ((wave >> 1) & 1) * 36 + min(lane, 35);
    if (g0 < 9) dma_keep(mat, r * 9216 + (xcd * 9 + g0) * 128, lds_base + L_W + wave * 256);
  }
  if (ml >= 0) {
    BAR();
    if (mw < 18) slab_dma<0, 4>(w_mod + ml * 64, NMOD * 2, lds_base + L_W, wave, lane);
    slab_dma<4, 9>(w_mod + ml * 64, NMOD * 2, lds_base + L_W, wave, lane);
    u32x4 wf[9], af[9];
    img_frags(lds, L_IMG, lane, af);
    asm volatile("s_waitcnt vmcnt(0)\n\ts_barrier" ::: "memory");
    slab_frags(lds, L_W, wave, lane, wf);
    const f32x4 s = half_sum(gemv9(wf, af));
    if (n < 8 && grp < 2) *(u32x2*)(lds + L_TILE + n * 128 + wave * 16 + grp * 8) = u32x2{cvt2(s[0], s[1]), cvt2(s[2], s[3])};
    lds_barrier();
    if (wave < 2) {
      const u32x2 t2 = *(const u32x2*)(lds + L_TILE + tid * 8);
      const int ch = (ml - 18) / 9, sl = ml - 18 - ch * 9;
      st128(rs, W_M + ((ch * 8 + (tid >> 4)) * 144 + sl * 16 + n) * 16, u32x4{t2[0], t2[1], e0, e1});
    }
    TL(18);
  }

  if (lnb >= 0 && wave < 3) {
    const int b = lnb, g = tid < 144 ? tid : 143;
    const bool on = tid < 144;
    float* ex = (float*)(lds + L_TILE);
    const u32x2 xa = __builtin_amdgcn_raw_buffer_load_b64(__builtin_amdgcn_make_buffer_rsrc((void*)x, 0, 8 * D * 2, 0x00020000), (b * D + g * 4) * 2, 0, 0);
    const u32x2 wra = __builtin_amdgcn_raw_buffer_load_b64(__builtin_amdgcn_make_buffer_rsrc((void*)w_r, 0, D * 2, 0x00020000), g * 8, 0, 0);
    u32x4 mm[3], xp[8];
#define XPLD() do { _Pragma("unroll") for (int p = 0; p < 8; ++p) xp[p] = ld128(rs, W_XP + (p * 8 + b) * 2304 + g * 16); } while (0)
    _Pragma("nounroll") for (int sp = 0; sp < SPIN_CAP; ++sp) {
#pragma unroll
      for (int k = 0; k < 3; ++k) mm[k] = ld128(rs, W_M + ((k * 8 + b) * 144 + g) * 16);
      XPLD();
      if (__all((tag_x(mm[0], e0, e1) | tag_x(mm[1], e0, e1) | tag_x(mm[2], e0, e1)) == 0u)) break;
      nap(2);
    }
    f32x2 xv[2], ga[2], sb[2], cb1[2], wr[2];
#pragma unroll
    for (int i = 0; i < 2; ++i) {
      xv[i] = up2(xa[i]); wr[i] = up2(wra[i]); ga[i] = up2(mm[0][i]); sb[i] = up2(mm[1][i]); cb1[i] = rbf2(f32x2{1.f, 1.f} + up2(mm[2][i]));
      asm volatile("" : "+v"(xv[i]), "+v"(ga[i]), "+v"(sb[i]), "+v"(cb1[i]), "+v"(wr[i]));
    }
    int ho = W_H2 + (b * 144 + g) * 16, xo = W_X1 + (b * 152 + g) * 16;
    asm volatile("" : "+v"(ho), "+v"(xo));
    _Pragma("nounroll") for (int sp = 0; sp < SPIN_CAP; ++sp) {
      if (__all(mxu(mxu(mxu(mx4(xp[0]), mx4(xp[1])), mxu(mx4(xp[2]), mx4(xp[3]))), mxu(mxu(mx4(xp[4]), mx4(xp[5])), mxu(mx4(xp[6]), mx4(xp[7])))) != 0xffffffffu)) break;
      nap(1);
      XPLD();
    }
    TL(10);
    f32x2 v[2];
#pragma unroll
    for (int i = 0; i < 2; ++i) {
      f32x2 pp[8];
#pragma unroll
      for (int p = 0; p < 8; ++p) pp[p] = f32x2{__uint_as_float(xp[p][2 * i]), __uint_as_float(xp[p][2 * i + 1])};
      v[i] = rbf2(xv[i] + rbf2(ga[i] * rbf2(((pp[0] + pp[1]) + (pp[2] + pp[3])) + ((pp[4] + pp[5]) + (pp[6] + pp[7])))));
    }
    const f32x2 s2 = v[0] + v[1];
    const float mw = wsum(on ? s2.x + s2.y : 0.f) * (wave < 2 ? 1.f / 256 : 1.f / 64);
    T2(44, );
    const f32x2 t0 = v[0] - f32x2{mw, mw}, t1 = v[1] - f32x2{mw, mw}, q2 = t0 * t0 + t1 * t1;
    const float m2 = wsum(on ? q2.x + q2.y : 0.f);
    if (lane == 0) *(f32x2*)(ex + wave * 2) = f32x2{mw, m2};
    T2(45, );
    lds_barrier();
    T2(46, );
    const f32x4 e01 = *(const f32x4*)ex; const f32x2 e2 = *(const f32x2*)(ex + 4);
    const float mu = (e01[0] + e01[2]) * (256.f / D) + e2.x * (64.f / D);
    const float d0 = e01[0] - mu, d1 = e01[2] - mu, d2 = e2.x - mu;
    const float var = rbf(((e01[1] + e01[3]) + e2.y + 256.f * (d0 * d0 + d1 * d1) + 64.f * d2 * d2) * (1.f / D));
    const float mean = rbf(mu), rden = frcp(rbf(__builtin_amdgcn_sqrtf(rbf(var + 1e-5f))));
    u32 hw[2];
#pragma unroll
    for (int i = 0; i < 2; ++i) {
      const f32x2 t = rbf2(rbf2(rbf2(v[i] - f32x2{mean, mean}) * f32x2{rden, rden}) * cb1[i]) + sb[i];
      hw[i] = cvt2(t.x, t.y);
    }
    if (on) {
      st128(rs, ho, u32x4{hw[0], hw[1], e0, e1});
      st128(rs, xo, u32x4{cvt2(v[0].x, v[0].y), cvt2(v[1].x, v[1].y), e0, e1});
    }
    TL(11);
    const f32x2 r2 = up2(hw[0]) * wr[0] + up2(hw[1]) * wr[1];
    const float rw = wsum(on ? r2.x + r2.y : 0.f);
    if (lane == 0) ex[8 + wave] = rw;
    lds_barrier();
    if (tid < 8) {
      const float r = rbf((ex[8] + ex[9]) + ex[10]);
      st128(rs, W_X1 + (b * 152 + 144 + tid) * 16, u32x4{__float_as_uint(rbf(1.f / (1.f + __expf(-r)))), 0u, e0, e1});
    }
  }

  if (oj >= 0) {
    const int ob = W_O + ((op * 2 + (wave >> 2)) * 8 + (lane >> 3)) * 2048 + (wave & 3) * 256 + (lane & 7) * 16;
    u32x4 a[4];
#pragma unroll
    for (int p = 0; p < 4; ++p) a[p] = ld128(rs, ob + (p >> 1) * 1024 + (p & 1) * 128);
    SCHED();
    u32x8 wf[8];
    if (wave < 4) {
      const int q = n >> 2, p = lane & 3;
      const int a0 = L_W + ((grp >> 1) * 16 + (grp & 1) * 4 + (q & 1) + (q >> 1) * 8) * 128 + ((wave ^ ((grp & 1) + (q & 2))) << 5) + p * 8;
#pragma unroll
      for (int s = 0; s < 8; ++s) {
        const u32x2 r0 = trd(lds, a0 + s * 8192), r1 = trd(lds, a0 + s * 8192 + 256), r2 = trd(lds, a0 + s * 8192 + 4096), r3 = trd(lds, a0 + s * 8192 + 4352);
        wf[s] = u32x8{r0[0], r0[1], r1[0], r1[1], r2[0], r2[1], r3[0], r3[1]};
      }
#pragma unroll
      for (int s = 0; s < 8; ++s) asm volatile("" : "+v"(wf[s]));
    }
    int rw = L_OI + wave * 1024 + (((lane >> 1) & 3) * 16 + (lane & 1) * 8 + ((lane >> 3) ^ (lane & 7))) * 16, rr = L_OI + (grp * 16 + (n & 8) + ((n & 7) ^ (grp * 2 + (n >> 3)))) * 16;
    int xo = W_XP + (op * 8 + (grp & 1) * 4 + (grp >> 1) * 2) * 2304 + oj * 256 + wave * 64 + n * 4, ix = n < 8 ? 0x4444 : 0xeeee;
    asm volatile("" : "+v"(rw), "+v"(rr), "+v"(xo), "+v"(ix));
    _Pragma("nounroll") for (int sp = 0; sp < SPIN_CAP; ++sp) {
      const bool m0 = mxu(mx4(a[0]), mx4(a[1])) == 0xffffffffu, m1 = mxu(mx4(a[2]), mx4(a[3])) == 0xffffffffu;
      if (!__any(m0 || m1)) break;
      nap(2);
      if (m0) { a[0] = ld128(rs, ob); a[1] = ld128(rs, ob + 128); }
      if (m1) { a[2] = ld128(rs, ob + 1024); a[3] = ld128(rs, ob + 1152); }
    }
    TL(8);
#define OS(p, j) (__uint_as_float(a[p][j]) + __uint_as_float(a[(p) + 2][j]))
    *(u32x4*)(lds + rw) = u32x4{cvt2(OS(0, 0), OS(0, 1)), cvt2(OS(0, 2), OS(0, 3)), cvt2(OS(1, 0), OS(1, 1)), cvt2(OS(1, 2), OS(1, 3))};
    T2(41, );
    lds_barrier();
    T2(42, );
    if (wave < 4) {
      u32x4 af[8];
#pragma unroll
      for (int s = 0; s < 8; ++s) af[s] = *(const u32x4*)(lds + rr + s * 1024);
      SCHED();
      f32x4 d = {0.f, 0.f, 0.f, 0.f};
#pragma unroll
      for (int s = 0; s < 8; ++s) d = smf(af[s], wf[s], d, ix);
      T2(43, "+v"(d));
#pragma unroll
      for (int j = 0; j < 2; ++j) {
        const auto r = __builtin_amdgcn_permlane32_swap(__float_as_uint(d[j]), __float_as_uint(d[j + 2]), false, false);
        __builtin_amdgcn_raw_buffer_store_b32(sanf(__uint_as_float(r[0]) + __uint_as_float(r[1])), rs, xo + j * 2304, 0, 16);
      }
    }
    TL(9);
  }
  TLOUT();
  }
  }
}

extern "C" void kernel_launch(void* const* d_in, const int* in_sizes, int n_in, void* d_out, int out_size, void* d_ws, size_t ws_size,
                              hipStream_t stream) {
  fused_kernel<<<NWG, NT, 0, stream>>>((const u16*)d_in[0], (const u16*)d_in[1], (const u16*)d_in[2], (const u16*)d_in[3], (const u16*)d_in[4],
                                       (const u16*)d_in[6], (const u16*)d_in[7], (const u16*)d_in[8], (const u16*)d_in[9], (const u16*)d_in[10],
                                       (const u16*)d_in[11], (unsigned char*)d_ws, (u16*)d_out);
}
```

```cpp
#include <hip/hip_runtime.h>
#include <hip/hip_bf16.h>
#include <cstdint>

typedef unsigned u32;
typedef unsigned short u16;
typedef u32 u32x4 __attribute__((ext_vector_type(4)));
typedef u32 u32x2 __attribute__((ext_vector_type(2)));
typedef float f32x4 __attribute__((ext_vector_type(4)));
typedef float f32x2 __attribute__((ext_vector_type(2)));
typedef short s16x4 __attribute__((ext_vector_type(4)));
typedef __bf16 bf16x8 __attribute__((ext_vector_type(8)));
typedef __bf16 bf16x2 __attribute__((ext_vector_type(2)));
typedef __bf16 bf16x16 __attribute__((ext_vector_type(16)));
typedef u32 u32x8 __attribute__((ext_vector_type(8)));
typedef __attribute__((address_space(3))) unsigned char lds_t;

constexpr int D = 576, SK = 8192, WIN = 2048, FF = 4608, NQ = 4096, NMOD = 6 * D;
constexpr int NWG = 256, NT = 512, NG = 72;
constexpr int SPIN_CAP = 1 << 15;

constexpr int W_O = 0;
constexpr int W_XP = W_O + 262144;
constexpr int W_Y = W_XP + 12 * 16384;
constexpr int W_MS = W_Y + 81 * 16384;
constexpr int W_M = W_MS;
constexpr int W_H2 = W_M + 4 * 16384;
constexpr int W_PR = W_H2 + 3 * 16384;
constexpr int W_GB = W_PR + 2 * 16384;
constexpr int W_FL = W_GB + 18432;
constexpr int W_H = W_FL + 2048;
constexpr int W_Q = W_H + 36864;
constexpr int W_ST = W_Q + 131072;
constexpr int W_X1 = W_ST + 32768;
constexpr int W_END = W_X1 + 8 * 152 * 16;
constexpr int NPZ = 174, PZ = (W_MS / 16 + NPZ - 1) / NPZ;
static_assert(PZ <= 2 * NT, "poison split");

constexpr int L_W = 0;
constexpr int L_IMG = 73728;
constexpr int L_TILE = L_IMG + 9216;
constexpr int L_IM2 = 86016;
constexpr int L_OI = 65536;
constexpr int L_B = 147456;
constexpr int L_C = 155648;
constexpr int LDS_BYTES = 163840;

#define SCHED() __builtin_amdgcn_sched_barrier(0)
#define BAR() asm volatile("s_barrier" ::: "memory")
#define TL(i) do {} while (0)
#define TLOUT() do {} while (0)
#define T2(i, ...) do {} while (0)
#define WAITV_(n) do { asm volatile("s_waitcnt vmcnt(" #n ")" ::: "memory"); SCHED(); } while (0)
#define WAITV(n) WAITV_(n)
#define W0 3
#define W1 5
#define W2 7
#define TH1 15
#define TH2 40
#define STR_(x) #x
#define STR(x) STR_(x)
#define WAITD() do { asm volatile("s_waitcnt vmcnt(" STR(W2) ")\n\ts_cmpk_gt_i32 %0, " STR(TH2) " ; throttle\n\ts_cbranch_scc1 4 ; throttle\n\ts_waitcnt vmcnt(" STR(W1) ") ; throttle\n\t" \
    "s_cmpk_gt_i32 %0, " STR(TH1) " ; throttle\n\ts_cbranch_scc1 1 ; throttle\n\ts_waitcnt vmcnt(" STR(W0) ") ; throttle" :: "s"(__builtin_amdgcn_readfirstlane(lag)) : "memory", "scc"); SCHED(); } while (0)
#define PR 114
#define R1 34
#define G1 10
#define R2 52
#define G2 0
#define R3 24
#define G3 0
#define DO 0
#define FLIP 0
#define OH 175
#define OF 99
#define OL 75

__device__ __forceinline__ float lo16(u32 u) { return __uint_as_float(u << 16); }
__device__ __forceinline__ float hi16(u32 u) { return __uint_as_float(u & 0xffff0000u); }
__device__ __forceinline__ float b2f(u16 u) { return __uint_as_float((u32)u << 16); }
__device__ __forceinline__ u32 cvt2(float a, float b) { return __builtin_bit_cast(u32, __builtin_convertvector(f32x2{a, b}, bf16x2)); }
__device__ __forceinline__ float rbf(float v) { return __uint_as_float(cvt2(0.f, v)); }
__device__ __forceinline__ u32 sanf(float f) { u32 v = __float_as_uint(f); return v < 0xfffffffeu ? v : 0xfffffffeu; }
__device__ __forceinline__ f32x2 up2(u32 u) { return f32x2{lo16(u), hi16(u)}; }
__device__ __forceinline__ f32x2 rbf2(f32x2 v) { return f32x2{rbf(v.x), rbf(v.y)}; }
__device__ __forceinline__ void lds_barrier() { asm volatile("s_waitcnt lgkmcnt(0)\n\ts_barrier" ::: "memory"); }
__device__ __forceinline__ f32x4 mfma(u32x4 a, u32x4 b, f32x4 c) {
  return __builtin_amdgcn_mfma_f32_16x16x32_bf16(__builtin_bit_cast(bf16x8, a), __builtin_bit_cast(bf16x8, b), c, 0, 0, 0);
}
__device__ __forceinline__ f32x4 smf(u32x4 a, u32x8 b, f32x4 c, int idx) {
  return __builtin_amdgcn_smfmac_f32_16x16x64_bf16(__builtin_bit_cast(bf16x8, a), __builtin_bit_cast(bf16x16, b), c, idx, 0, 0);
}
__device__ __forceinline__ u32 plo(u32 a, u32 b) { return __builtin_amdgcn_perm(b, a, 0x05040100u); }
__device__ __forceinline__ u32 phi(u32 a, u32 b) { return __builtin_amdgcn_perm(b, a, 0x07060302u); }

extern "C" __device__ __attribute__((const)) unsigned long long llvm_dispatch_id() __asm("llvm.amdgcn.dispatch.id");
typedef __amdgpu_buffer_rsrc_t rsrc_t;
__device__ __forceinline__ u32x4 ld128(rsrc_t rs, int off) { return __builtin_amdgcn_raw_buffer_load_b128(rs, off, 0, 16); }
__device__ __forceinline__ void st128(rsrc_t rs, int off, u32x4 v) { __builtin_amdgcn_raw_buffer_store_b128(v, rs, off, 0, 16); }
__device__ __forceinline__ void st64(rsrc_t rs, int off, u32x2 v) { __builtin_amdgcn_raw_buffer_store_b64(v, rs, off, 0, 16); }
__device__ __forceinline__ void dma(const void* gbase, u32 voff, unsigned lds_dst) {
  unsigned keep;
  asm volatile("s_mov_b32 %0, m0\n\ts_mov_b32 m0, %3\n\ts_nop 2\n\tglobal_load_lds_dwordx4 %1, %2 nt\n\ts_mov_b32 m0, %0"
               : "=&s"(keep) : "v"(voff), "s"(gbase), "s"(lds_dst) : "memory");
}
__device__ __forceinline__ void dma_keep(const void* gbase, u32 voff, unsigned lds_dst) {
  unsigned keep;
  asm volatile("s_mov_b32 %0, m0\n\ts_mov_b32 m0, %3\n\ts_nop 2\n\tglobal_load_lds_dword %1, %2\n\ts_mov_b32 m0, %0"
               : "=&s"(keep) : "v"(voff), "s"(gbase), "s"(lds_dst) : "memory");
}
__device__ __forceinline__ void dma_sc(const void* gbase, u32 voff, unsigned lds_dst) {
  unsigned keep;
  asm volatile("s_mov_b32 %0, m0\n\ts_mov_b32 m0, %3\n\ts_nop 2\n\tglobal_load_lds_dwordx4 %1, %2 sc1\n\ts_mov_b32 m0, %0"
               : "=&s"(keep) : "v"(voff), "s"(gbase), "s"(lds_dst) : "memory");
}
__device__ __forceinline__ void ald128(u32x4& d, const void* gbase, u32 voff) { unsigned long long t; asm volatile("s_mov_b64 %1, %3\n\tglobal_load_dwordx4 %0, %2, %1" : "=v"(d), "=&s"(t) : "v"(voff), "s"(gbase) : "memory"); }
__device__ __forceinline__ void ald64(u32x2& d, const void* gbase, u32 voff) { unsigned long long t; asm volatile("s_mov_b64 %1, %3\n\tglobal_load_dwordx2 %0, %2, %1" : "=v"(d), "=&s"(t) : "v"(voff), "s"(gbase) : "memory"); }
__device__ __forceinline__ void ald128nt(u32x4& d, const void* gbase, u32 voff) { unsigned long long t; asm volatile("s_mov_b64 %1, %3\n\tglobal_load_dwordx4 %0, %2, %1 nt" : "=v"(d), "=&s"(t) : "v"(voff), "s"(gbase) : "memory"); }
__device__ __forceinline__ void ald128sc(u32x4& d, const void* gbase, u32 voff) { unsigned long long t; asm volatile("s_mov_b64 %1, %3\n\tglobal_load_dwordx4 %0, %2, %1 sc1" : "=v"(d), "=&s"(t) : "v"(voff), "s"(gbase) : "memory"); }
__device__ __forceinline__ u32x2 trd(const unsigned char* lds, int off) {
  return __builtin_bit_cast(u32x2, __builtin_amdgcn_ds_read_tr16_b64_v4i16((__attribute__((address_space(3))) s16x4*)((lds_t*)lds + off)));
}
#define nap(n) do { __builtin_amdgcn_s_sleep(n); asm volatile("" ::: "memory"); } while (0)
__device__ __forceinline__ float frcp(float v) { return __builtin_amdgcn_rcpf(v); }
__device__ __forceinline__ float fdiv(float a, float d, float r) { const float q = a * r; return fmaf(fmaf(-q, d, a), r, q); }
__device__ __forceinline__ float silu(float v) { const float d = 1.f + __expf(-v); const float r = frcp(d); return d < 3.0e38f ? fdiv(v, d, r) : v * r; }
template <int CTRL> __device__ __forceinline__ float dppf(float x) {
  return __builtin_bit_cast(float, __builtin_amdgcn_mov_dpp(__builtin_bit_cast(int, x), CTRL, 0xf, 0xf, true));
}
__device__ __forceinline__ u32 mxu(u32 a, u32 b) { return a > b ? a : b; }
__device__ __forceinline__ u32 mx4(u32x4 v) { return mxu(mxu(v[0], v[1]), mxu(v[2], v[3])); }
__device__ __forceinline__ float rl(float v, int l) { return __builtin_bit_cast(float, __builtin_amdgcn_readlane(__builtin_bit_cast(int, v), l)); }
__device__ __forceinline__ float wsum(float v) {
  v += dppf<0xB1>(v); v += dppf<0x4E>(v); v += dppf<0x141>(v); v += dppf<0x140>(v);
  return (rl(v, 0) + rl(v, 16)) + (rl(v, 32) + rl(v, 48));
}
__device__ __forceinline__ float up32(float v) { const u32 u = __float_as_uint(v); return __uint_as_float(__builtin_amdgcn_permlane32_swap(u, u, false, false)[1]); }
__device__ __forceinline__ float xsum32(float v) { const u32 u = __float_as_uint(v); const auto r = __builtin_amdgcn_permlane32_swap(u, u, false, false); return __uint_as_float(r[0]) + __uint_as_float(r[1]); }
__device__ __forceinline__ float xsum16(float v) { const u32 u = __float_as_uint(v); const auto r = __builtin_amdgcn_permlane16_swap(u, u, false, false); return __uint_as_float(r[0]) + __uint_as_float(r[1]); }
__device__ __forceinline__ f32x4 half_sum(f32x4 d) {
  f32x4 r;
#pragma unroll
  for (int j = 0; j < 4; ++j) r[j] = d[j] + dppf<0x108>(up32(d[j]));
  return r;
}
__device__ __forceinline__ f32x2 half_sum2(f32x4 d) {
  f32x2 r;
#pragma unroll
  for (int j = 0; j < 2; ++j) {
    const auto s = __builtin_amdgcn_permlane32_swap(__float_as_uint(d[j]), __float_as_uint(d[j + 2]), false, false);
    r[j] = __uint_as_float(s[0]) + dppf<0x108>(__uint_as_float(s[1]));
  }
  return r;
}
__device__ __forceinline__ void unpack8(u32x4 u, float* f) {
#pragma unroll
  for (int j = 0; j < 4; ++j) { f[2 * j] = lo16(u[j]); f[2 * j + 1] = hi16(u[j]); }
}
__device__ __forceinline__ void ln_row(const float (&v)[16], bool has2, float& mean, float& den) {
  float s = 0.f;
#pragma unroll
  for (int i = 0; i < 8; ++i) s += v[i];
  if (has2) {
#pragma unroll
    for (int i = 8; i < 16; ++i) s += v[i];
  }
  s = wsum(s);
  const float mu = s * (1.f / D);
  mean = rbf(mu);
  float q = 0.f;
#pragma unroll
  for (int i = 0; i < 8; ++i) { const float t = v[i] - mu; q = fmaf(t, t, q); }
  if (has2) {
#pragma unroll
    for (int i = 8; i < 16; ++i) { const float t = v[i] - mu; q = fmaf(t, t, q); }
  }
  q = wsum(q);
  const float var = rbf(q * (1.f / D));
  den = rbf(sqrtf(rbf(var + 1e-5f)));
}
template <int J0 = 0, int J1 = 9>
__device__ __forceinline__ void slab_dma(const u16* base, int stride_b, unsigned lds_dst, int wave, int lane) {
  const u32 v0 = (lane >> 3) * stride_b + (((lane & 7) ^ ((wave & 1) * 4 + (lane >> 4))) << 4), v1 = v0 ^ 32u;
  const u32 v4 = wave >= 4 ? v1 : v0;
  const char* gb = (const char*)base + (size_t)wave * 8 * stride_b;
#pragma unroll
  for (int j = J0; j < J1; ++j) dma(gb + (size_t)j * 64 * stride_b, j < 4 ? v0 : j == 4 ? v4 : v1, lds_dst + (j * 8 + wave) * 1024);
}
__device__ __forceinline__ void slab_frags(const unsigned char* lds, int base, int wave, int lane, u32x4 (&wf)[9]) {
  const int g = lane >> 4, q = (lane & 15) >> 2, p = lane & 3, hf = p >> 1;
  const int a0 = base + (hf * 288 + 8 * g + q) * 128 + ((wave ^ ((g & 1) * 4 + (q >> 1)) ^ (hf * 2)) << 4) + (p & 1) * 8, a1 = (a0 ^ 32) + 512;
#pragma unroll
  for (int s = 0; s < 9; ++s) {
    const u32x2 lo = trd(lds, a0 + s * 4096), hi = trd(lds, a1 + s * 4096);
    wf[s] = u32x4{lo[0], lo[1], hi[0], hi[1]};
  }
}
__device__ __forceinline__ void img_frags(const unsigned char* lds, int img, int lane, u32x4 (&af)[9]) {
#pragma unroll
  for (int s = 0; s < 9; ++s) af[s] = *(const u32x4*)(lds + img + (s * 64 + lane) * 16);
}
__device__ __forceinline__ f32x4 gemv9(const u32x4 (&wf)[9], const u32x4 (&af)[9]) {
  f32x4 acc[3];
  SCHED();
#pragma unroll
  for (int s = 0; s < 9; ++s) acc[s % 3] = mfma(wf[s], af[s], s < 3 ? f32x4{0.f, 0.f, 0.f, 0.f} : acc[s % 3]);
  return (acc[0] + acc[1]) + acc[2];
}
__device__ __forceinline__ int img_off(int b, int k8) {
  const int hf = k8 >= 36 ? 1 : 0, kk = k8 - hf * 36;
  return ((kk >> 2) * 64 + (kk & 3) * 16 + hf * 8 + b) * 16;
}
__device__ __forceinline__ int im2_off(int b, int k8) { return (k8 * 8 + (b ^ (k8 & 7))) * 16; }
__device__ __forceinline__ u32 tag_x(u32x4 v, u32 e0, u32 e1) { return (v[2] ^ e0) | (v[3] ^ e1); }
__device__ __forceinline__ bool tag_ok(u32x4 v, u32 e0, u32 e1) { return tag_x(v, e0, e1) == 0u; }

constexpr int CODE_BYTES = 33300;

__global__ void __launch_bounds__(NT, 2) fused_kernel(const u16* __restrict__ x, const u16* __restrict__ kc, const u16* __restrict__ c, const u16* __restrict__ vc,
                                                      const u16* __restrict__ wq, const u16* __restrict__ wo, const u16* __restrict__ w_r,
                                                      const u16* __restrict__ wg, const u16* __restrict__ wu, const u16* __restrict__ wd,
                                                      const u16* __restrict__ w_mod, unsigned char* ws, u16* out) {
  __shared__ __attribute__((aligned(16))) unsigned char lds[LDS_BYTES];
  SCHED();
  const int w = blockIdx.x, xcd = w & 7, idx = w >> 3;
  const int tid = threadIdx.x, lane = tid & 63, wave = __builtin_amdgcn_readfirstlane(tid >> 6);
  const int n = lane & 15, grp = lane >> 4;
  const unsigned lds_base = (unsigned)(uintptr_t)(lds_t*)lds;
  {
    unsigned long long pc0; unsigned keep;
    asm volatile("s_getpc_b64 %0\n\ts_mov_b32 %1, m0\n\ts_mov_b32 m0, %3\n\ts_cmp_lt_i32 %4, 216\n\ts_cbranch_scc1 2 ; throttle\n\tglobal_load_lds_dword %2, %0\n\ts_mov_b32 m0, %1"
                 : "=&s"(pc0), "=&s"(keep) : "v"(min((w >> 3) * 8192 - 27 * 8192 + lane * 128, CODE_BYTES - 128)), "s"(lds_base + 107520), "s"(wave ? 0 : w) : "memory", "scc");
  }
  const int s_att = w & 1, kvh = (w >> 1) & 15, b_att = w >> 5;
  size_t pairbase = ((size_t)(w >> 1) * SK + (SK - WIN)) * 256;
  int wpart = (wave & 3) * 4096 + s_att * 16384 + (wave >> 2) * 262144;
  int ko = wpart + (n >> 1) * 256 + (n & 1) * 64 + grp * 16;
  int eo[4] = {tid * 16, 8192 + lane * 16, wave * 1152 + lane * 16, wave * 1152 + (lane & 7) * 16};
  asm volatile("" : "+s"(pairbase), "+s"(wpart), "+v"(ko), "+v"(eo[0]), "+v"(eo[1]), "+v"(eo[2]), "+v"(eo[3]));
  SCHED();
  const char* kp = (const char*)kc + pairbase;
#define KLD(u) do { unsigned long long t_; asm volatile("s_mov_b64 %1, %3\n\tglobal_load_dwordx4 %0, %2, %1 offset:%4 nt" : "=v"(kb[u]), "=&s"(t_) : "v"(ko), "s"(kp + ((u) >> 2) * 32768), "i"((((u) >> 1) & 1) * 2048 + ((u) & 1) * 128) : "memory"); } while (0)
  u32x4 kb[32], vb[32];
  asm volatile("s_cmp_gt_i32 %12, 17\n\ts_cbranch_scc1 9 ; throttle\n\tglobal_load_dwordx4 %0, %4, %9 ; throttle\n\tglobal_load_dwordx4 %1, %5, %9 ; throttle\n\t"
               "global_load_dwordx4 %2, %6, %10 ; throttle\n\tglobal_load_dwordx4 %3, %7, %10 offset:1024 ; throttle\n\ts_branch 8 ; throttle\n\t"
               "global_load_dwordx4 %0, %8, %11 nt\n\tglobal_load_dwordx4 %1, %8, %11 offset:128 nt\n\tglobal_load_dwordx4 %2, %8, %11 offset:2048 nt\n\tglobal_load_dwordx4 %3, %8, %11 offset:2176 nt"
               : "=&v"(kb[0]), "=&v"(kb[1]), "=&v"(kb[2]), "=&v"(kb[3]) : "v"(eo[0]), "v"(eo[1]), "v"(eo[2]), "v"(eo[3]), "v"(ko), "s"(c), "s"(x), "s"(kp), "s"(w) : "memory", "scc");
  TL(32);
  SCHED();
  asm volatile("" : "+s"(vc), "+s"(w_r), "+s"(wu), "+s"(wd), "+s"(out), "+s"(x), "+s"(w_mod), "+s"(wg), "+s"(wq), "+s"(wo), "+s"(c), "+s"(ws));
  const rsrc_t rs = __builtin_amdgcn_make_buffer_rsrc(ws, 0, W_END, 0x00020000);
  const unsigned long long epoch = (llvm_dispatch_id() + 1ull) * 0x9E3779B97F4A7C15ull;
  const u32 e0 = (u32)epoch, e1 = (u32)(epoch >> 32);

  const int mw = idx < 6 ? idx * 8 + xcd : -1;
  const int md = mw < 18 ? mw : -1;
  const int ml = mw >= 18 ? mw : mw >= 0 && mw < 6 ? 48 + mw : -1;
  const int lnb = mw >= 6 && mw < 14 ? xcd : -1;
  const int qs = idx >= 6 && idx < 14 ? xcd * 8 + idx - 6 : -1;
  const int oj = idx >= 14 && idx < 23 ? idx - 14 : -1, op = xcd;
  const int gs = idx >= 23 ? xcd * 9 + idx - 23 : -1;

  {
    const char* vp = (const char*)vc + pairbase;
    const int vo = wpart + lane * 16;
#define VLD(u) do { unsigned long long t_; asm volatile("s_mov_b64 %1, %3\n\tglobal_load_dwordx4 %0, %2, %1 offset:%4 nt" : "=v"(vb[u]), "=&s"(t_) : "v"(vo), "s"(vp + ((u) >> 2) * 32768), "i"(((u) & 3) * 1024) : "memory"); } while (0)
#define TIE8(a, o) asm volatile("" : "+v"(a[o]), "+v"(a[(o) + 1]), "+v"(a[(o) + 2]), "+v"(a[(o) + 3]), "+v"(a[(o) + 4]), "+v"(a[(o) + 5]), "+v"(a[(o) + 6]), "+v"(a[(o) + 7]))
    const u32 tp0 = (u32)__builtin_amdgcn_s_memrealtime() + (gs >= 0 ? OF : mw >= 18 ? OL : OH);
    int lag = gs >= 0 || mw >= 18 ? 0 : 1000;
#define PACE(i) do { const int t_ = ((i) * PR >> 2) + ((i) >= R1 ? G1 : 0) + ((i) >= R2 ? G2 : 0) + ((i) >= R3 ? G3 : 0) + DO; lag = (int)((u32)__builtin_amdgcn_s_memrealtime() - tp0) - t_; \
      if (lag < 0) { _Pragma("nounroll") do __builtin_amdgcn_s_sleep(2); while ((int)((u32)__builtin_amdgcn_s_memrealtime() - tp0) < t_); } SCHED(); } while (0)
#define REP4(M, u, W) do { M(u); W; M((u) + 1); W; M((u) + 2); W; M((u) + 3); W; } while (0)
    static_assert(W2 <= 7, "the hand-placed waits assume it");
    const int pz = mw >= 18 ? mw - 18 : idx >= 14 ? w - 82 : -1;
#define POISON() do { if (pz >= 0) { _Pragma("unroll") for (int i = 0; i < 2; ++i) { const int g_ = pz * PZ + i * NT + tid; \
      if (i * NT + tid < PZ && g_ < W_MS / 16) st128(rs, g_ * 16, u32x4{0xffffffffu, 0xffffffffu, 0xffffffffu, 0xffffffffu}); } } SCHED(); } while (0)
#define SILU_IMAGE(c0, c1, both) do { float f[8]; unpack8(c0, f); u32x4 o; \
        _Pragma("unroll") for (int j = 0; j < 4; ++j) o[j] = cvt2(silu(f[2 * j]), silu(f[2 * j + 1])); \
        const int b_ = tid / 72, ch_ = tid - b_ * 72; \
        if (both) *(u32x4*)(lds + L_IMG + img_off(b_, ch_)) = o; \
        *(u32x4*)(lds + L_IM2 + im2_off(b_, ch_)) = o; \
        if (wave == 0) { unpack8(c1, f); \
          _Pragma("unroll") for (int j = 0; j < 4; ++j) o[j] = cvt2(silu(f[2 * j]), silu(f[2 * j + 1])); \
          if (both) *(u32x4*)(lds + L_IMG + img_off(7, 8 + lane)) = o; \
          *(u32x4*)(lds + L_IM2 + im2_off(7, 8 + lane)) = o; } } while (0)
    if (md >= 0) {
      u32x2 xc;
      int ho = W_H + img_off(n & 7, (md >= 9 ? md - 9 : md) * 8 + wave) * 2 + (grp & 1) * 16;
      ald64(xc, x, (n & 7) * 1152 + ((md >= 9 ? md - 9 : 0) * 64 + wave * 8 + (grp & 1) * 4) * 2);
      TL(35);
      BAR();
      float* st = (float*)(lds + L_TILE + 1024);
      if (wave < 4) {
        slab_dma(w_mod + md * 64, NMOD * 2, lds_base + L_W, wave, lane);
        asm volatile("s_waitcnt vmcnt(9)" ::: "memory");
      } else asm volatile("s_waitcnt vmcnt(0)" ::: "memory");
      asm volatile("s_nop 0 ; both branches above cover V" : "+v"(kb[0]), "+v"(kb[1]), "+v"(kb[2]), "+v"(kb[3]), "+v"(xc) :: "memory");
      {
        SILU_IMAGE(kb[0], kb[1], true);
        float v[16]; unpack8(kb[2], v); unpack8(kb[3], v + 8);
        float mean, den; ln_row(v, lane < 8, mean, den);
        if (lane == 0) { st[wave] = mean; st[8 + wave] = den; }
      }
      if (wave >= 4) slab_dma(w_mod + md * 64, NMOD * 2, lds_base + L_W, wave, lane);
      lds_barrier();
      REP4(KLD, 0, );
      u32x4 wf[9], af[9];
      img_frags(lds, L_IMG, lane, af);
      const float mean = st[n & 7], rd = frcp(st[8 + (n & 7)]);
      float ln[4] = {rbf(rbf(lo16(xc[0]) - mean) * rd), rbf(rbf(hi16(xc[0]) - mean) * rd), rbf(rbf(lo16(xc[1]) - mean) * rd), rbf(rbf(hi16(xc[1]) - mean) * rd)};
#pragma unroll
      for (int s = 0; s < 9; ++s) asm volatile("" : "+v"(af[s]));
      asm volatile("" : "+v"(ln[0]), "+v"(ln[1]), "+v"(ln[2]), "+v"(ln[3]), "+v"(ho));
      WAITV(4); BAR(); SCHED();
      slab_frags(lds, L_W, wave, lane, wf);
      const f32x4 s = half_sum(gemv9(wf, af));
      f32x4 r = s;
      if (md >= 9) {
#pragma unroll
        for (int j = 0; j < 4; ++j) r[j] = ln[j] * rbf(1.f + rbf(s[j]));
      }
      if (n < 8 && grp < 2) st128(rs, ho + (md >= 9 ? 18432 : 0), u32x4{cvt2(r[0], r[1]), cvt2(r[2], r[3]), e0, e1});
      SCHED();
      TL(1);
      BAR();
      KLD(4); KLD(5); WAITV(W2);
      KLD(6); WAITV(W2); KLD(7); WAITV(W2); KLD(8); WAITV(W2); KLD(9); WAITV(W2); KLD(10); WAITV(W2);
      KLD(11); WAITV(W2); REP4(KLD, 12, WAITV(W2)); REP4(KLD, 16, WAITV(W2));
      REP4(KLD, 20, WAITV(W2)); KLD(24); WAITV(W2); KLD(25);
    } else {
      constexpr int L_LAND = 98304;
      const unsigned land = lds_base + L_LAND + wave * 6144;
      const unsigned char* lp = lds + L_LAND + wave * 6144 + lane * 16;
      const int sl_ = (3 - (int)((uintptr_t)wq >> 7)) & 7;
      const int s2_ = (63 - (int)((uintptr_t)wq >> 7)) & 63;
      const bool slowq = qs >= 0 && ((qs & 7) == sl_ || qs == s2_);
      if (gs >= 0 && wave == 3 && xcd == (s2_ >> 3)) dma_keep((const char*)wq + (size_t)(idx - 23) * 64 * 8192 + s2_ * 128, lane * 8192, land);
      if (gs >= 0 && wave == 0) dma_keep((const char*)wq + (size_t)(idx - 23) * 64 * 8192 + xcd * 1024 + sl_ * 128, lane * 8192, land);
      if (gs >= 0 && (wave == 1 || (wave == 2 && xcd < 2))) dma_keep((const char*)w_mod + (size_t)((idx - 23) * 64 + 32) * (NMOD * 2) + xcd * 128 + (wave - 1) * 2048, (lane & 31) * (NMOD * 2) + (wave == 1 ? (lane >> 5) * 1024 : 0), land);
      POISON();
      const u32 v0 = (lane >> 3) * 1152 + (((lane & 7) ^ ((lane >> 4) & 2)) << 4);
      const char* gb = (const char*)(wo + (size_t)(op * 512 + wave * 64) * D + oj * 64);
      if (ml >= 0) { dma_sc(c, tid * 16, land); dma_sc(c, (512 + lane) * 16, land + 1024); }
      if (oj < 0 && qs < 0) {
        const int i_ = (gs >= 0 ? idx - 19 : idx - 2) * 8 + wave, o_ = (i_ >> 6) * 16384 + (i_ & 63) * 128 + (((xcd >> 2) ^ wave) & 1) * 8192;
        unsigned long long t0, t1, t2; u32 d_;
        asm volatile("s_memtime %0\n\ts_waitcnt lgkmcnt(0)\n\ts_load_dword %3, %4, %5\n\ts_waitcnt lgkmcnt(0)\n\ts_memtime %1\n\ts_waitcnt lgkmcnt(0)\n\t"
                     "s_load_dword %3, %4, %6\n\ts_waitcnt lgkmcnt(0)\n\ts_memtime %2\n\ts_waitcnt lgkmcnt(0)"
                     : "=&s"(t0), "=&s"(t1), "=&s"(t2), "=&s"(d_) : "s"(ws + W_PR), "s"(o_), "s"(o_ ^ 8192) : "memory");
        const int a_ = (int)((u32)t1 - (u32)t0), b_ = (int)((u32)t2 - (u32)t1);
        if (lane == 0) *(int*)(lds + L_B + 192 + wave * 4) = (wave & 1) ? b_ - a_ : a_ - b_;
        TL(58);
      }
#define WK(j) do { if (oj >= 0) { if ((j) < 8) dma(gb + (j) * 8 * 1152, v0 ^ (((j) & 1) * 64), lds_base + L_W + (wave * 8 + (j)) * 1024); } \
                   else if (slowq) { if ((j) == 8) slab_dma(wq + qs * 64, NQ * 2, lds_base + L_W, wave, lane); } \
                   else if (qs >= 0 && (j) < 9) slab_dma<(j) < 9 ? (j) : 0, (j) < 9 ? (j) + 1 : 0>(wq + qs * 64, NQ * 2, lds_base + L_W, wave, lane); \
                   else if (qs < 0 && (j) >= 4) PACE(j); \
                   if ((j) >= 4) KLD(j); if ((j) == 4) TL(33); WAITD(); if ((j) == 5) TL(34); } while (0)
      WK(0); WK(1); WK(2); WK(3);
      WK(4); WK(5);
      WK(6); WK(7);
      if (oj < 0 && qs < 0) WAITV(4);
      TL(24);
      if (ml >= 0) { const u32x4 c0 = *(const u32x4*)lp, c1 = *(const u32x4*)(lp + 1024); SILU_IMAGE(c0, c1, false); }
      lds_barrier();
      if (pz >= 0 && tid == 448) {
        const u32x4 v0_ = *(const u32x4*)(lds + L_B + 192), v1_ = *(const u32x4*)(lds + L_B + 208);
        const int d_ = (int)(v0_[0] + v0_[1] + v0_[2] + v0_[3] + v1_[0] + v1_[1] + v1_[2] + v1_[3]);
        st64(rs, W_FL + pz * 8, u32x2{e0 ^ (oj >= 0 ? pz & 1 : d_ > 0), e1});
      }
      SCHED();
      WK(8); WK(9); WK(10); WK(11);
      TL(25);
      PACE(12); REP4(KLD, 12, WAITD());
      TL(26);
      const int hg0 = (wave * 144 + lane) * 16, hg2 = (wave * 144 + 128 + n) * 16;
#define HREQ() do { dma_sc(ws + W_H, hg0, land); dma_sc(ws + W_H + 18432, hg0, land + 1024); dma_sc(ws + W_H + 1024, hg0, land + 2048); \
                    dma_sc(ws + W_H + 18432 + 1024, hg0, land + 3072); dma_sc(ws + W_H, hg2, land + 4096); dma_sc(ws + W_H + 18432, hg2, land + 5120); } while (0)
#define HGET() do { _Pragma("unroll") for (int i = 0; i < 6; ++i) hq[i] = *(const u32x4*)(lp + i * 1024); } while (0)
#define HQOK() __all((tag_x(hq[0], e0, e1) | tag_x(hq[1], e0, e1) | tag_x(hq[2], e0, e1) | tag_x(hq[3], e0, e1) | tag_x(hq[4], e0, e1) | tag_x(hq[5], e0, e1)) == 0u)
#define HOK() __all((tag_x(hp[0], e0, e1) | tag_x(hp[1], e0, e1) | tag_x(hp[2], e0, e1)) == 0u)
      if (qs >= 0) HREQ();
      PACE(16); REP4(KLD, 16, WAITD());
      if (qs >= 0) WAITV(4);
      TL(27);
      PACE(20); KLD(20); KLD(21);
      if (qs >= 0) {
        u32x4 hq[6]; HGET();
        _Pragma("nounroll") for (int sp = 0; sp < SPIN_CAP && !HQOK(); ++sp) { nap(16); HREQ(); WAITV(0); HGET(); }
        TL(2);
#define HSUM(a, b) cvt2(lo16(a) + lo16(b), hi16(a) + hi16(b))
        *(u32x2*)(lds + L_IMG + hg0 / 2) = u32x2{HSUM(hq[0][0], hq[1][0]), HSUM(hq[0][1], hq[1][1])};
        *(u32x2*)(lds + L_IMG + 512 + hg0 / 2) = u32x2{HSUM(hq[2][0], hq[3][0]), HSUM(hq[2][1], hq[3][1])};
        if (lane < 16) *(u32x2*)(lds + L_IMG + hg2 / 2) = u32x2{HSUM(hq[4][0], hq[5][0]), HSUM(hq[4][1], hq[5][1])};
      } else WAITD();
      KLD(22); KLD(23);
      if (qs >= 0) {
        u32x4 wf[9], af[9];
        lds_barrier();
        slab_frags(lds, L_W, wave, lane, wf);
        img_frags(lds, L_IMG, lane, af);
        const f32x4 s = half_sum(gemv9(wf, af));
        if (n < 8 && grp < 2) *(u32x2*)(lds + L_TILE + n * 128 + wave * 16 + grp * 8) = u32x2{cvt2(s[0], s[1]), cvt2(s[2], s[3])};
      } else WAITD();
      PACE(24); KLD(24); KLD(25);
      if (qs >= 0) {
        lds_barrier();
        if (wave < 2) {
          const u32x2 t2 = *(const u32x2*)(lds + L_TILE + tid * 8);
          st128(rs, W_Q + ((tid >> 4) * 1024 + qs * 16 + n) * 16, u32x4{t2[0], t2[1], e0, e1});
        }
        TL(3);
      }
      SCHED();
    }
    const unsigned char* qg = ws + W_Q + (b_att * 1024 + kvh * 64) * 16;
    KLD(26); WAITD(); KLD(27); WAITD(); PACE(28); KLD(28); WAITD(); KLD(29); WAITD();
    PACE(30); KLD(30); WAITD(); KLD(31); WAITD();
    TL(28);
    if (gs >= 0 && wave >= 2 && wave < 6) {
      const u16* mat = (wave & 1) ? wg : wu;
      const int g0 = ((3 - (int)((uintptr_t)mat >> 7) - xcd * 9) & 7) + (wave >= 4 ? 8 : 0);
      if (g0 < 9) dma_keep(mat, ((idx - 23) * 29 + min(lane, 28)) * 9216 + (xcd * 9 + g0) * 128, lds_base + L_B + 4096 + wave * 256);
    }
    PACE(32); VLD(0); WAITD(); VLD(1); WAITD(); PACE(34); VLD(2); WAITD(); VLD(3); WAITD();
#define QLOOK() do { if (wave == 1) dma_sc(qg, lane * 16, lds_base + L_B + 1024); } while (0)
    QLOOK();
#define FLOOK(l) do { dma_sc(ws + W_FL, (l) * 16, lds_base + L_B + 2048); dma_sc(ws + W_FL, 1024 + min(l, NPZ / 2 - 65) * 16, lds_base + L_B + 3072); } while (0)
    if (wave == 2) FLOOK(lane);
    PACE(36); VLD(4); WAITD(); VLD(5); WAITD(); PACE(38); VLD(6); WAITD(); VLD(7); WAITD();
    QLOOK();
    TL(29);
    PACE(40); VLD(8); WAITD(); VLD(9); WAITD(); PACE(42); QLOOK(); VLD(10); WAITD(); VLD(11); WAITD();
    TIE8(kb, 0); TIE8(kb, 8); TIE8(kb, 16); TIE8(kb, 24);
    PACE(44); QLOOK(); VLD(12); VLD(13);
    u32x4 qf[2];
    {
      const u32x4* ql = (const u32x4*)(lds + L_B + 1024 + ((n >> 3) * 32 + (n & 1) * 8 + grp * 2) * 16);
      u32x4 g[4];
      _Pragma("nounroll") for (int sp = 0; sp < SPIN_CAP; ++sp) {
        g[0] = ql[0]; g[1] = ql[1]; g[2] = ql[16]; g[3] = ql[17];
        if (__all((tag_x(g[0], e0, e1) | tag_x(g[1], e0, e1) | tag_x(g[2], e0, e1) | tag_x(g[3], e0, e1)) == 0u)) break;
        if (wave == 1 && sp >= 6) { dma_sc(qg, lane * 16, lds_base + L_B + 1024); WAITV(0); } else nap(4);
      }
      asm volatile("" ::: "memory");
      g[0] = ql[0]; g[1] = ql[1]; g[2] = ql[16]; g[3] = ql[17];
      qf[0] = u32x4{g[0][0], g[0][1], g[1][0], g[1][1]};
      qf[1] = u32x4{g[2][0], g[2][1], g[3][0], g[3][1]};
    }
    TL(4);
    constexpr float SCALE = 0.088388347648318440f;
    f32x2 sc[16];
    float mx = -INFINITY;
#pragma unroll
    for (int T = 0; T < 16; ++T) {
      f32x4 acc = mfma(kb[T * 2], qf[0], f32x4{0.f, 0.f, 0.f, 0.f});
      acc = mfma(kb[T * 2 + 1], qf[1], acc);
      sc[T] = f32x2{acc[0] + dppf<0xB1>(acc[1]), acc[2] + dppf<0xB1>(acc[3])};
      if (T == 7 || T == 15) { SCHED(); if (T == 7) VLD(14); else VLD(15); SCHED(); }
    }
#pragma unroll
    for (int T = 0; T < 16; ++T)
#pragma unroll
      for (int j = 0; j < 2; ++j) { const float v = rbf(rbf(sc[T][j]) * SCALE); sc[T][j] = v; mx = fmaxf(mx, v); }
    SCHED(); VLD(16); SCHED();
    mx = fmaxf(mx, __shfl_xor(mx, 16));
    mx = fmaxf(mx, __shfl_xor(mx, 32));
    float ls = 0.f;
#pragma unroll
    for (int T = 0; T < 16; ++T)
#pragma unroll
      for (int j = 0; j < 2; ++j) { sc[T][j] = __expf(sc[T][j] - mx); ls += sc[T][j]; }
    SCHED(); VLD(17); SCHED();
    ls += __shfl_xor(ls, 16);
    ls += __shfl_xor(ls, 32);
    float* stat = (float*)(lds + L_B);
    if (lane == 0 || lane == 8) *(f32x2*)(stat + (wave * 2 + (lane >> 3)) * 2) = f32x2{mx, ls};
    SCHED(); VLD(18); VLD(19); SCHED();
    if (wave == 2) {
      int l2 = lane; asm volatile("" : "+v"(l2));
      u32x4 a, b;
      _Pragma("nounroll") for (int sp = 0; sp < SPIN_CAP; ++sp) {
        a = *(const u32x4*)(lds + L_B + 2048 + l2 * 16); b = *(const u32x4*)(lds + L_B + 3072 + l2 * 16);
        if (__all(((((a[0] ^ e0) | (a[2] ^ e0) | (b[0] ^ e0) | (b[2] ^ e0)) & ~1u) | (a[1] ^ e1) | (a[3] ^ e1) | (b[1] ^ e1) | (b[3] ^ e1)) == 0u)) break;
        nap(1); FLOOK(l2); WAITV(0);
      }
      const unsigned long long mk = (1ull << (NPZ / 2 - 64)) - 1;
      const int cnt = __popcll(__ballot(a[0] != e0)) + __popcll(__ballot(a[2] != e0)) + __popcll(__ballot(b[0] != e0) & mk) + __popcll(__ballot(b[2] != e0) & mk);
      if (l2 == 0) *(int*)(lds + L_B + 128) = cnt > NPZ / 2 ? 8192 : 0;
    }
    lds_barrier();
    const int flip = __builtin_amdgcn_readfirstlane(*(const int*)(lds + L_B + 128)) ^ FLIP;
    float M = -INFINITY, L = 0.f;
    {
      f32x2 t[8];
#pragma unroll
      for (int q = 0; q < 8; ++q) { t[q] = *(const f32x2*)(stat + (q * 2 + (n >> 3)) * 2); M = fmaxf(M, t[q].x); }
#pragma unroll
      for (int q = 0; q < 8; ++q) L = fmaf(__expf(t[q].x - M), t[q].y, L);
    }
    if (tid >= 4 && tid < 12) st128(rs, W_ST + (w * 8 + lane - 4) * 16, u32x4{__float_as_uint(M), __float_as_uint(L), e0, e1});
    SCHED();
    TL(30);
    PACE(52); VLD(20); WAITD(); VLD(21); WAITD();
    const int po = W_ST + ((w ^ 1) * 8 + (n >> 3) * 4) * 16;
    int l1 = lane; asm volatile("" : "+v"(l1));
    u32 km[4] = {(l1 & 4) ? 0u : 0xffffu, (l1 & 4) ? 0xffffu : 0u, (l1 & 4) ? 0u : 0xffff0000u, (l1 & 4) ? 0xffff0000u : 0u};
    int px = (l1 & 3) * 0x5555;
    asm volatile("" : "+v"(km[0]), "+v"(km[1]), "+v"(km[2]), "+v"(km[3]), "+v"(px));
    PACE(54);
    VLD(22); WAITD(); VLD(23); WAITD();
    u32x4 ps, ps1, ps2, ps3;
    ald128sc(ps, ws, po);
    PACE(56);
    VLD(24); WAITD(); VLD(25); WAITD();
    TL(31);
    ald128sc(ps1, ws, po);
    PACE(58);
    VLD(26); WAITD(); VLD(27); WAITD();
    ald128sc(ps2, ws, po);
    VLD(28); VLD(29); WAITV(8);
    asm volatile("" : "+v"(ps));
    TIE8(vb, 0); TIE8(vb, 8); TIE8(vb, 16);
    ald128sc(ps3, ws, po);
    VLD(30); VLD(31);
    if (!__all(tag_ok(ps, e0, e1))) {
      WAITV(8);
      asm volatile("" : "+v"(ps1));
      ps = ps1;
      if (!__all(tag_ok(ps, e0, e1))) {
        WAITV(5);
        asm volatile("" : "+v"(ps2));
        ps = ps2;
        if (!__all(tag_ok(ps, e0, e1))) {
          WAITV(2);
          asm volatile("" : "+v"(ps3));
          ps = ps3;
          if (!__all(tag_ok(ps, e0, e1))) {
            int sp = 0;
            _Pragma("nounroll") do { nap(1); ps = ld128(rs, po); } while (!__all(tag_ok(ps, e0, e1)) && ++sp < SPIN_CAP);
          }
        }
      }
    }
    float fac;
    {
      const float M2 = __uint_as_float(ps[0]), L2 = __uint_as_float(ps[1]);
      const float Mg = fmaxf(M, M2);
      const float Lg = __expf(M - Mg) * L + __expf(M2 - Mg) * L2;
      fac = __expf(mx - Mg) / Lg;
    }
    TL(5);
    u32x4 pf[16];
    u32 pp[16];
#pragma unroll
    for (int T = 0; T < 16; ++T) pp[T] = cvt2(sc[T][0] * fac, sc[T][1] * fac);
    SCHED();
#pragma unroll
    for (int T = 0; T < 16; ++T) {
      const u32 p = (u32)__builtin_amdgcn_mov_dpp((int)pp[T], 0xa0, 0xf, 0xf, true);
      pf[T] = u32x4{p & km[0], p & km[1], p & km[2], p & km[3]};
    }
    f32x4 oa = {0.f, 0.f, 0.f, 0.f};
#define PV(T) oa = smf(pf[T], __builtin_shufflevector(vb[2 * (T)], vb[2 * (T) + 1], 0, 1, 2, 3, 4, 5, 6, 7), oa, px)
#pragma unroll
    for (int T = 0; T < 12; ++T) PV(T);
    SCHED();
    int t2_ = threadIdx.x; asm volatile("" : "+v"(t2_));
    const int tid = t2_, lane = tid & 63, n = lane & 15, grp = lane >> 4;
    int oo = W_O + (kvh & 7) * 32768 + (b_att >> 2) * 16384 + ((kvh >> 3) * 8192 ^ flip) + (b_att & 3) * 2048 + s_att * 1024 + (((grp >> 1) * 2 + (n >> 3)) * 16 + (grp & 1) * 8 + (n & 7)) * 16;
    int mw_ = L_C + tid * 16;
    asm volatile("" : "+v"(oo), "+v"(mw_), "+v"(pf[12]), "+v"(pf[13]), "+v"(pf[14]), "+v"(pf[15]));
    constexpr int L_H2 = L_W;
    constexpr int L_ACT = L_C + 4608;
    int ia, ra, ix, yo[2], fo[7], tw, tr_;
    const u32 gv = (lane >> 3) * (FF * 2) + (((lane & 7) ^ ((wave & 1) * 4 + ((lane >> 4) & 2))) << 4);
#define GU(j) do { const size_t o_ = ((size_t)((j) * 64 + wave * 8) * FF + gs * 64) * 2; dma((const char*)wg + o_, gv, lds_base + L_W + ((j) * 8 + wave) * 1024); \
                   dma((const char*)wu + o_, gv ^ 16u, lds_base + L_W + 73728 + ((j) * 8 + wave) * 1024); } while (0)
#define ML(j) dma((const char*)w_mod + ((size_t)((j) * 64 + wave * 8) * NMOD + ml * 64) * 2, (lane >> 3) * (NMOD * 2) + (((lane & 7) ^ ((wave & 1) * 4 + ((lane >> 4) & 2))) << 4), lds_base + L_W + ((j) * 8 + wave) * 1024)
    const bool late = gs >= 0 || mw >= 18;
    if (late) {
      BAR();
      if (gs >= 0) { GU(0); GU(1); } else { ML(0); ML(1); ML(2); ML(3); }
      {
    const int b = wave;
    ia = L_H2 + grp * 1024 + ((n >> 2) * 16 + (n & 2) * 4 + (b ^ (n >> 1))) * 16 + (n & 1) * 8;
    ra = L_H2 + (grp * 16 + (n & 8) + ((n & 7) ^ (grp * 2 + (n >> 3)))) * 16; ix = n < 8 ? 0x4444 : 0xeeee;
    tw = L_ACT + ((grp & 1) * 4 + (grp >> 1) * 2 + (n >> 3)) * 144 + wave * 16 + (n & 7) * 2; tr_ = L_ACT + (n & 7) * 144 + grp * 16;
    {
      const int f0 = wave * 8 + (grp & 1) * 4 + (n >> 3) * 2, h0 = f0 >= 36, g0 = f0 - h0 * 36, g1 = 28 + wave * 2 + (grp >> 1);
      yo[0] = W_Y + (gs < 64 ? g0 * 32768 + (gs >> 5) * 16384 : (72 + (g0 >> 2)) * 16384 + (g0 & 3) * 2048) + (h0 * 8192 ^ flip) + (gs & 31) * 256 + ((grp >> 1) * 8 + (n & 7)) * 16;
      yo[1] = W_Y + (gs < 64 ? g1 * 32768 + (gs >> 5) * 16384 : (72 + (g1 >> 2)) * 16384 + (g1 & 3) * 2048) + (8192 ^ flip) + (gs & 31) * 256 + ((grp & 1) * 8 + n) * 16;
    }
    {
      const int b_ = lane >> 3, cc = lane & 7, col = gs * 8 + cc;
      fo[0] = W_GB + (b_ * 144 + gs * 2 + (cc >> 2)) * 16;
      fo[1] = W_X1 + (b_ * 152 + gs * 2 + (cc >> 2)) * 16; fo[2] = W_X1 + (b_ * 152 + 144) * 16;
      fo[3] = W_Y + ((xcd >> 2) * 8192 ^ flip) + tid * 16;
      fo[4] = L_H2 + ((gs >> 1) * 16 + (gs & 1) * 8 + (b_ ^ (gs & 7))) * 16 + cc * 2;
      fo[5] = L_C + (((cc >> 2) * 8 + b_) * 4 + (cc & 3)) * 4;
      fo[6] = (b_ * D + col) * 2;
    }
    asm volatile("" : "+v"(ia), "+v"(ra), "+v"(ix), "+v"(tw), "+v"(tr_), "+v"(yo[0]), "+v"(yo[1]),
                 "+v"(fo[0]), "+v"(fo[1]), "+v"(fo[2]), "+v"(fo[3]), "+v"(fo[4]), "+v"(fo[5]), "+v"(fo[6]));
      }
      WAITV(6);
    } else WAITV(2);
    asm volatile("s_nop 0 ; both branches above cover V up to the last two" : "+v"(vb[24]), "+v"(vb[25]), "+v"(vb[26]), "+v"(vb[27]), "+v"(vb[28]), "+v"(vb[29]));
    asm volatile("" :: "v"(ps1), "v"(ps2), "v"(ps3));
    PV(12); PV(13); PV(14);
    SCHED();
    if (late) WAITV(4); else WAITV(0);
    T2(56, );
    asm volatile("s_nop 0 ; both branches above cover V" : "+v"(vb[30]), "+v"(vb[31]));
    PV(15);
    TL(6);
    *(f32x4*)(lds + mw_) = oa;
    T2(54, );
    lds_barrier();
    T2(55, );
    if (wave == 0) {
      f32x4 m[8];
#pragma unroll
      for (int q = 0; q < 8; ++q) m[q] = *(const f32x4*)(lds + mw_ + q * 1024);
      SCHED();
      const f32x4 o = ((m[0] + m[1]) + (m[2] + m[3])) + ((m[4] + m[5]) + (m[6] + m[7]));
      st128(rs, oo, u32x4{sanf(o[0]), sanf(o[1]), sanf(o[2]), sanf(o[3])});
    }
    TL(7); SCHED();
  if (gs >= 0) {
    const int b = wave;
    BAR(); SCHED();
    u32x8 bw[9];
    u32x4 wdf[2][5];
    const int ga0 = L_W + ((lane >> 1) & 1) * 73728 + ((grp >> 1) * 16 + (grp & 1) * 4 + ((n >> 2) & 1) + (n & 8)) * 128 + ((wave ^ ((grp & 1) * 2 + ((n >> 1) & 4)) ^ ((lane >> 1) & 1)) << 4) + (lane & 1) * 8;
#define GUF(S0, S1) do { _Pragma("unroll") for (int s = S0; s < S1; ++s) { const int a_ = ga0 + (s >> 2) * 32768, o_ = (s & 3) * 8192; \
      const u32x2 r0 = trd(lds, a_ + o_), r1 = trd(lds, a_ + o_ + 256), r2 = trd(lds, a_ + o_ + 4096), r3 = trd(lds, a_ + o_ + 4352); \
      bw[s] = u32x8{r0[0], r0[1], r1[0], r1[1], r2[0], r2[1], r3[0], r3[1]}; } } while (0)
    {
      u32x4 rd[8];
      GU(2); GU(3);
      TL(23);
      BAR();
      const char* db = (const char*)(wd + (size_t)gs * 64 * D + wave * 64);
      const u32 dv = (n >> 3) * 36864 + grp * 9216 + (n & 7) * 16;
#pragma unroll
      for (int i = 0; i < 8; ++i) ald128nt(rd[i], db + i * 1152, dv);
      {
        const int r = lane >> 3;
        dma((const char*)(wd + (size_t)(gs * 64 + wave * 8) * D + 512), r * 1152 + (((lane & 7) ^ (r & 2) ^ ((wave & 1) * 4)) << 4), lds_base + L_B + wave * 1024);
      }
      TL(19);
      asm volatile("s_waitcnt vmcnt(9)\n\ts_barrier" ::: "memory");
      TL(22);
      GUF(0, 4);
      SCHED();
      GU(4); GU(5); GU(6); GU(7); GU(8);
      TL(20);
      asm volatile("s_waitcnt vmcnt(11)" : "+v"(rd[0]), "+v"(rd[1]), "+v"(rd[2]), "+v"(rd[3]), "+v"(rd[4]), "+v"(rd[5]), "+v"(rd[6]), "+v"(rd[7]) :: "memory");
#pragma unroll
      for (int j = 0; j < 4; ++j)
#pragma unroll
        for (int t = 0; t < 4; ++t) {
          const u32 a = (t & 1) ? phi(rd[2 * j][t >> 1], rd[2 * j + 1][t >> 1]) : plo(rd[2 * j][t >> 1], rd[2 * j + 1][t >> 1]);
          const u32 b_ = (t & 1) ? phi(rd[2 * j][2 + (t >> 1)], rd[2 * j + 1][2 + (t >> 1)]) : plo(rd[2 * j][2 + (t >> 1)], rd[2 * j + 1][2 + (t >> 1)]);
          wdf[0][t][j] = (u32)__builtin_amdgcn_update_dpp((int)a, (int)b_, 0x128, 0xf, 0xc, false);
          wdf[1][t][j] = (u32)__builtin_amdgcn_update_dpp((int)b_, (int)a, 0x128, 0xf, 0x3, false);
        }
#pragma unroll
      for (int t = 0; t < 4; ++t) asm volatile("" : "+v"(wdf[0][t]), "+v"(wdf[1][t]));
      TL(21);
    }
    asm volatile("s_waitcnt vmcnt(0) lgkmcnt(0)\n\ts_barrier" ::: "memory");
    {
      u32x4 hp[3];
      const int hb = W_H2 + b / 3 * 16384 + ((xcd >> 2) * 8192 ^ flip) + b % 3 * 2304, h0 = hb + lane * 16, h2o = hb + (128 + n) * 16;
      hp[0] = ld128(rs, h0); hp[1] = ld128(rs, h0 + 1024); hp[2] = ld128(rs, h2o);
      GUF(4, 9);
      const int q = n >> 2, p = lane & 3;
      const int a0 = L_B + (8 * grp + q) * 128 + ((((wave & 3) * 2 + (p >> 1)) ^ (q & 2) ^ ((grp & 1) * 4)) << 4) + (p & 1) * 8;
#pragma unroll
      for (int ks = 0; ks < 2; ++ks) {
        const u32x2 lo = trd(lds, a0 + ks * 4096), hi = trd(lds, a0 + ks * 4096 + 512);
        wdf[ks][4] = u32x4{lo[0], lo[1], hi[0], hi[1]};
      }
      TL(12);
#pragma unroll
      for (int s = 0; s < 9; ++s) asm volatile("" : "+v"(bw[s]));
      asm volatile("" : "+v"(wdf[0][4]), "+v"(wdf[1][4]));
      if (!HOK()) {
        int sp = 0;
        _Pragma("nounroll") do { nap(2); hp[0] = ld128(rs, h0); hp[1] = ld128(rs, h0 + 1024); hp[2] = ld128(rs, h2o); } while (!HOK() && ++sp < SPIN_CAP);
      }
      TL(13);
      *(u32x2*)(lds + ia) = u32x2{hp[0][0], hp[0][1]};
      *(u32x2*)(lds + ia + 4096) = u32x2{hp[1][0], hp[1][1]};
      if (lane < 16) *(u32x2*)(lds + ia + 8192) = u32x2{hp[2][0], hp[2][1]};
      lds_barrier();
      T2(47, );
      u32x4 af[9];
#pragma unroll
      for (int s = 0; s < 9; ++s) af[s] = *(const u32x4*)(lds + ra + s * 1024);
      SCHED();
      f32x4 d = {0.f, 0.f, 0.f, 0.f};
#pragma unroll
      for (int s = 0; s < 9; ++s) d = smf(af[s], bw[s], d, ix);
      T2(48, "+v"(d));
      int r[2];
#pragma unroll
      for (int j = 0; j < 2; ++j) {
        const auto t = __builtin_amdgcn_permlane32_swap(__float_as_uint(d[j]), __float_as_uint(d[j + 2]), false, false);
        r[j] = __float_as_int(rbf(__uint_as_float(t[0]) + __uint_as_float(t[1])));
      }
      const float g = __int_as_float(__builtin_amdgcn_update_dpp(r[0], r[1], 0x128, 0xf, 0xc, false)), u = __int_as_float(__builtin_amdgcn_update_dpp(r[1], r[0], 0x128, 0xf, 0x3, false));
      *(u16*)(lds + tw) = (u16)cvt2(rbf(silu(g)) * u, 0.f);
    }
    T2(49, );
    lds_barrier();
    TL(14);
    {
      u32x4 af[2];
#pragma unroll
      for (int ks = 0; ks < 2; ++ks) af[ks] = *(const u32x4*)(lds + tr_ + ks * 64);
      f32x4 d[5];
#pragma unroll
      for (int t = 0; t < 5; ++t) d[t] = mfma(wdf[1][t], af[1], mfma(wdf[0][t], af[0], f32x4{0.f, 0.f, 0.f, 0.f}));
      T2(50, "+v"(d[0]), "+v"(d[1]), "+v"(d[2]), "+v"(d[3]), "+v"(d[4]));
#define YS(t) sanf(__int_as_float(__builtin_amdgcn_update_dpp(__float_as_int(d[t][j]), __float_as_int(d[t][j + 2]), 0xe4, 0xf, 0xc, false)))
#pragma unroll
      for (int j = 0; j < 2; ++j)
        __builtin_amdgcn_raw_buffer_store_b128(u32x4{YS(0), YS(1), YS(2), YS(3)},
                                               rs, yo[0], j * (gs < 64 ? 32768 : 2048), 16);
      if (n < 8 && wave < 4)
        st128(rs, yo[1], u32x4{sanf(d[4][0]), sanf(d[4][1]), sanf(d[4][2]), sanf(d[4][3])});
    }
    TL(15);
    {
      float gbv = 0.f, x1v = 0.f, h2 = 0.f, rsel = 0.f;
      if (wave == 7) {
        u32x4 gq, xq, rq;
        _Pragma("nounroll") for (int sp = 0; sp < SPIN_CAP; ++sp) {
          gq = ld128(rs, fo[0]); xq = ld128(rs, fo[1]); rq = ld128(rs, fo[2]);
          if (__all((tag_x(gq, e0, e1) | tag_x(xq, e0, e1) | tag_x(rq, e0, e1)) == 0u)) break;
          nap(1);
        }
        const u32 gw = (lane & 2) ? gq[1] : gq[0], xw = (lane & 2) ? xq[1] : xq[0];
        gbv = (lane & 1) ? hi16(gw) : lo16(gw); x1v = (lane & 1) ? hi16(xw) : lo16(xw);
        h2 = b2f(*(const u16*)(lds + fo[4])); rsel = __uint_as_float(rq[0]);
        asm volatile("" : "+v"(gbv), "+v"(x1v), "+v"(h2), "+v"(rsel));
      }
      WAITV(0); T2(51, );
      u32x4 y[3] = {};
      const int gy = gs >= 36 ? gs - 36 : gs;
      _Pragma("nounroll") for (int sp = 0; sp < SPIN_CAP; ++sp) {
        y[0] = __builtin_amdgcn_raw_buffer_load_b128(rs, fo[3], gy * 32768, 16); y[1] = __builtin_amdgcn_raw_buffer_load_b128(rs, fo[3], gy * 32768 + 16384, 16);
        if (wave < 2) y[2] = __builtin_amdgcn_raw_buffer_load_b128(rs, fo[3], (72 + (gy >> 2)) * 16384 + (gy & 3) * 2048, 16);
        if (__all(mxu(mxu(mx4(y[0]), mx4(y[1])), mx4(y[2])) != 0xffffffffu)) break;
        nap(1);
      }
      TL(16);
      f32x4 s;
#pragma unroll
      for (int k = 0; k < 4; ++k) s[k] = xsum32(xsum16((__uint_as_float(y[0][k]) + __uint_as_float(y[1][k])) + __uint_as_float(y[2][k])));
      if (lane < 16) *(f32x4*)(lds + L_C + (wave * 16 + lane) * 16) = s;
      T2(52, );
      lds_barrier();
      T2(53, );
      if (wave == 7) {
        float t[8];
#pragma unroll
        for (int ww = 0; ww < 8; ++ww) t[ww] = *(const float*)(lds + fo[5] + ww * 256);
        SCHED();
        const float y2 = rbf(rbf(((t[0] + t[1]) + (t[2] + t[3])) + ((t[4] + t[5]) + (t[6] + t[7]))) * rsel);
        const float res = x1v + rbf(gbv * rbf(h2 + y2));
        __builtin_amdgcn_raw_buffer_store_b16((u16)(cvt2(res, 0.f) & 0xffffu), __builtin_amdgcn_make_buffer_rsrc(out, 0, 8 * D * 2, 0x00020000), fo[6], 0, 0);
      }
      TL(17);
    }
  }
  {
  int t3_ = threadIdx.x; asm volatile("" : "+v"(t3_));
  const int tid = t3_, lane = tid & 63, n = lane & 15, grp = lane >> 4;
  if (qs >= 0) {
    const u16* mat = (wave & 1) ? wg : wu;
    BAR();
    const int g0 = ((3 - (int)((uintptr_t)mat >> 7) - xcd * 9) & 7) + (wave >= 4 ? 8 : 0);
    if (g0 < 9) dma_keep(mat, (256 + (idx - 6) * 40 + ((wave >> 1) & 1) * 20 + min(lane, 19)) * 9216 + (xcd * 9 + g0) * 128, lds_base + L_W + wave * 256);
    const int l9 = min(lane, 44), rr = l9 / 9;
#pragma unroll
    for (int i = 0; i < 2; ++i) dma_keep(mat, (256 + ((i * 8 + idx - 6) * 4 + (wave >> 1)) * 5 + rr) * 9216 + (xcd * 9 + l9 - rr * 9) * 128, lds_base + L_W + wave * 256);
  }
  if (ml >= 0) {
    BAR();
    if (mw < 18) { ML(0); ML(1); ML(2); ML(3); }
    ML(4); ML(5); ML(6); ML(7); ML(8);
    TL(38);
    u32x4 af[9];
    f32x4 d = {0.f, 0.f, 0.f, 0.f};
    const int q = n >> 2, ch = (ml - 18) / 9, sl = ml - 18 - ch * 9;
    int a0 = L_W + ((grp >> 1) * 16 + (grp & 1) * 4 + (q & 1) + (q >> 1) * 8) * 128 + (((wave & 3) ^ ((grp & 1) + (q & 2))) << 5) + (lane & 3) * 8, ix = n < 8 ? 0x4444 : 0xeeee;
    int mo = (ch < 3 ? W_M + ((grp >> 1) * 2 + (n & 1)) * 16384 + ((grp & 1) * 8192 ^ flip) + ch * 2304 : W_GB + ((grp & 1) * 4 + (grp >> 1) * 2 + (n & 1)) * 2304) + (sl * 16 + wave * 4 + q) * 16;
#pragma unroll
    for (int s = 0; s < 9; ++s) af[s] = *(const u32x4*)(lds + L_IM2 + (grp * 16 + (n & 8) + ((n & 7) ^ (grp * 2 + (n >> 3)))) * 16 + s * 1024);
#define MLS(S0, S1) do { if (wave < 4) { u32x8 f_[S1 - S0]; _Pragma("unroll") for (int s = S0; s < S1; ++s) { const int a_ = a0 + (s >> 2) * 32768, o_ = (s & 3) * 8192; \
      const u32x2 r0 = trd(lds, a_ + o_), r1 = trd(lds, a_ + o_ + 256), r2 = trd(lds, a_ + o_ + 4096), r3 = trd(lds, a_ + o_ + 4352); \
      f_[s - S0] = u32x8{r0[0], r0[1], r1[0], r1[1], r2[0], r2[1], r3[0], r3[1]}; } SCHED(); \
      _Pragma("unroll") for (int s = S0; s < S1; ++s) d = smf(af[s], f_[s - S0], d, ix); } } while (0)
    asm volatile("s_waitcnt vmcnt(5)\n\ts_barrier" : "+v"(a0), "+v"(ix), "+v"(mo) :: "memory");
    MLS(0, 4);
    asm volatile("s_waitcnt vmcnt(2)\n\ts_barrier" : "+v"(d) :: "memory");
    MLS(4, 7);
    asm volatile("s_waitcnt vmcnt(0)\n\ts_barrier" : "+v"(d), "+v"(af[7]), "+v"(af[8]) :: "memory");
    TL(39);
    MLS(7, 9);
    if (wave < 4) {
      float r[2];
#pragma unroll
      for (int j = 0; j < 2; ++j) {
        const auto t = __builtin_amdgcn_permlane32_swap(__float_as_uint(d[j]), __float_as_uint(d[j + 2]), false, false);
        r[j] = __uint_as_float(t[0]) + __uint_as_float(t[1]);
      }
      const float o = dppf<0xB1>((n & 1) ? r[0] : r[1]);
      const u32 pk = (n & 1) ? cvt2(o, r[1]) : cvt2(r[0], o);
      const u32 p2 = (u32)__builtin_amdgcn_mov_dpp((int)pk, 0x4E, 0xf, 0xf, true);
      if (!(n & 2)) st128(rs, mo, u32x4{pk, p2, e0, e1});
    }
    if (ml < 45) TL(18); else TL(36);
  }

  if (lnb >= 0 && wave < 3) {
    const int b = lnb, g = tid < 144 ? tid : 143;
    const bool on = tid < 144;
    float* ex = (float*)(lds + L_TILE);
    const u32x2 xa = __builtin_amdgcn_raw_buffer_load_b64(__builtin_amdgcn_make_buffer_rsrc((void*)x, 0, 8 * D * 2, 0x00020000), (b * D + g * 4) * 2, 0, 0);
    const u32x2 wra = __builtin_amdgcn_raw_buffer_load_b64(__builtin_amdgcn_make_buffer_rsrc((void*)w_r, 0, D * 2, 0x00020000), g * 8, 0, 0);
    u32x4 mm[3], xp[8];
#define XPLD() do { _Pragma("unroll") for (int p = 0; p < 8; ++p) xp[p] = ld128(rs, W_XP + ((b & 3) * 3 + p / 3) * 16384 + ((b >> 2) * 8192 ^ flip) + p % 3 * 2304 + g * 16); } while (0)
    _Pragma("nounroll") for (int sp = 0; sp < SPIN_CAP; ++sp) {
#pragma unroll
      for (int k = 0; k < 3; ++k) mm[k] = ld128(rs, W_M + (b & 3) * 16384 + ((b >> 2) * 8192 ^ flip) + k * 2304 + g * 16);
      XPLD();
      if (__all((tag_x(mm[0], e0, e1) | tag_x(mm[1], e0, e1) | tag_x(mm[2], e0, e1)) == 0u)) break;
      nap(2);
    }
    TL(37);
    f32x2 xv[2], ga[2], sb[2], cb1[2], wr[2];
#pragma unroll
    for (int i = 0; i < 2; ++i) {
      xv[i] = up2(xa[i]); wr[i] = up2(wra[i]); ga[i] = up2(mm[0][i]); sb[i] = up2(mm[1][i]); cb1[i] = rbf2(f32x2{1.f, 1.f} + up2(mm[2][i]));
      asm volatile("" : "+v"(xv[i]), "+v"(ga[i]), "+v"(sb[i]), "+v"(cb1[i]), "+v"(wr[i]));
    }
    int ho = W_H2 + b / 3 * 16384 + ((b >> 2) * 8192 ^ flip ^ 8192) + b % 3 * 2304 + g * 16, xo = W_X1 + (b * 152 + g) * 16;
    int ho2 = ho ^ 8192;
    asm volatile("" : "+v"(ho), "+v"(ho2), "+v"(xo));
    _Pragma("nounroll") for (int sp = 0; sp < SPIN_CAP; ++sp) {
      if (__all(mxu(mxu(mxu(mx4(xp[0]), mx4(xp[1])), mxu(mx4(xp[2]), mx4(xp[3]))), mxu(mxu(mx4(xp[4]), mx4(xp[5])), mxu(mx4(xp[6]), mx4(xp[7])))) != 0xffffffffu)) break;
      nap(1);
      XPLD();
    }
    TL(10);
    f32x2 v[2];
#pragma unroll
    for (int i = 0; i < 2; ++i) {
      f32x2 pp[8];
#pragma unroll
      for (int p = 0; p < 8; ++p) pp[p] = f32x2{__uint_as_float(xp[p][2 * i]), __uint_as_float(xp[p][2 * i + 1])};
      v[i] = rbf2(xv[i] + rbf2(ga[i] * rbf2(((pp[0] + pp[1]) + (pp[2] + pp[3])) + ((pp[4] + pp[5]) + (pp[6] + pp[7])))));
    }
    const f32x2 s2 = v[0] + v[1];
    const float mw = wsum(on ? s2.x + s2.y : 0.f) * (wave < 2 ? 1.f / 256 : 1.f / 64);
    T2(44, );
    const f32x2 t0 = v[0] - f32x2{mw, mw}, t1 = v[1] - f32x2{mw, mw}, q2 = t0 * t0 + t1 * t1;
    const float m2 = wsum(on ? q2.x + q2.y : 0.f);
    if (lane == 0) *(f32x2*)(ex + wave * 2) = f32x2{mw, m2};
    T2(45, );
    lds_barrier();
    T2(46, );
    const f32x4 e01 = *(const f32x4*)ex; const f32x2 e2 = *(const f32x2*)(ex + 4);
    const float mu = (e01[0] + e01[2]) * (256.f / D) + e2.x * (64.f / D);
    const float d0 = e01[0] - mu, d1 = e01[2] - mu, d2 = e2.x - mu;
    const float var = rbf(((e01[1] + e01[3]) + e2.y + 256.f * (d0 * d0 + d1 * d1) + 64.f * d2 * d2) * (1.f / D));
    const float mean = rbf(mu), rden = frcp(rbf(__builtin_amdgcn_sqrtf(rbf(var + 1e-5f))));
    u32 hw[2];
#pragma unroll
    for (int i = 0; i < 2; ++i) {
      const f32x2 t = rbf2(rbf2(rbf2(v[i] - f32x2{mean, mean}) * f32x2{rden, rden}) * cb1[i]) + sb[i];
      hw[i] = cvt2(t.x, t.y);
    }
    if (on) {
      st128(rs, ho, u32x4{hw[0], hw[1], e0, e1});
      st128(rs, ho2, u32x4{hw[0], hw[1], e0, e1});
      st128(rs, xo, u32x4{cvt2(v[0].x, v[0].y), cvt2(v[1].x, v[1].y), e0, e1});
    }
    TL(11);
    const f32x2 r2 = up2(hw[0]) * wr[0] + up2(hw[1]) * wr[1];
    const float rw = wsum(on ? r2.x + r2.y : 0.f);
    if (lane == 0) ex[8 + wave] = rw;
    lds_barrier();
    if (tid < 8) {
      const float r = rbf((ex[8] + ex[9]) + ex[10]);
      st128(rs, W_X1 + (b * 152 + 144 + tid) * 16, u32x4{__float_as_uint(rbf(1.f / (1.f + __expf(-r)))), 0u, e0, e1});
    }
  }

  if (oj >= 0) {
    const int ob = W_O + ((op & 3) * 2 + (wave >> 2)) * 32768 + (lane >> 5) * 16384 + ((op >> 2) * 8192 ^ flip) + ((lane >> 3) & 3) * 2048 + (wave & 3) * 256 + (lane & 7) * 16;
    u32x4 a[4];
#pragma unroll
    for (int p = 0; p < 4; ++p) a[p] = ld128(rs, ob + (p >> 1) * 1024 + (p & 1) * 128);
    SCHED();
    u32x8 wf[8];
    if (wave < 4) {
      const int q = n >> 2, p = lane & 3;
      const int a0 = L_W + ((grp >> 1) * 16 + (grp & 1) * 4 + (q & 1) + (q >> 1) * 8) * 128 + ((wave ^ ((grp & 1) + (q & 2))) << 5) + p * 8;
#pragma unroll
      for (int s = 0; s < 8; ++s) {
        const u32x2 r0 = trd(lds, a0 + s * 8192), r1 = trd(lds, a0 + s * 8192 + 256), r2 = trd(lds, a0 + s * 8192 + 4096), r3 = trd(lds, a0 + s * 8192 + 4352);
        wf[s] = u32x8{r0[0], r0[1], r1[0], r1[1], r2[0], r2[1], r3[0], r3[1]};
      }
#pragma unroll
      for (int s = 0; s < 8; ++s) asm volatile("" : "+v"(wf[s]));
    }
    int rw = L_OI + wave * 1024 + (((lane >> 1) & 3) * 16 + (lane & 1) * 8 + ((lane >> 3) ^ (lane & 7))) * 16, rr = L_OI + (grp * 16 + (n & 8) + ((n & 7) ^ (grp * 2 + (n >> 3)))) * 16;
    int xo = W_XP + ((grp >> 1) * 6 + op / 3) * 16384 + ((grp & 1) * 8192 ^ flip) + op % 3 * 2304 + oj * 256 + wave * 64 + n * 4, ix = n < 8 ? 0x4444 : 0xeeee;
    asm volatile("" : "+v"(rw), "+v"(rr), "+v"(xo), "+v"(ix));
    _Pragma("nounroll") for (int sp = 0; sp < SPIN_CAP; ++sp) {
      const bool m0 = mxu(mx4(a[0]), mx4(a[1])) == 0xffffffffu, m1 = mxu(mx4(a[2]), mx4(a[3])) == 0xffffffffu;
      if (!__any(m0 || m1)) break;
      nap(2);
      if (m0) { a[0] = ld128(rs, ob); a[1] = ld128(rs, ob + 128); }
      if (m1) { a[2] = ld128(rs, ob + 1024); a[3] = ld128(rs, ob + 1152); }
    }
    TL(8);
#define OS(p, j) (__uint_as_float(a[p][j]) + __uint_as_float(a[(p) + 2][j]))
    *(u32x4*)(lds + rw) = u32x4{cvt2(OS(0, 0), OS(0, 1)), cvt2(OS(0, 2), OS(0, 3)), cvt2(OS(1, 0), OS(1, 1)), cvt2(OS(1, 2), OS(1, 3))};
    T2(41, );
    lds_barrier();
    T2(42, );
    if (wave < 4) {
      u32x4 af[8];
#pragma unroll
      for (int s = 0; s < 8; ++s) af[s] = *(const u32x4*)(lds + rr + s * 1024);
      SCHED();
      f32x4 d = {0.f, 0.f, 0.f, 0.f};
#pragma unroll
      for (int s = 0; s < 8; ++s) d = smf(af[s], wf[s], d, ix);
      T2(43, "+v"(d));
#pragma unroll
      for (int j = 0; j < 2; ++j) {
        const auto r = __builtin_amdgcn_permlane32_swap(__float_as_uint(d[j]), __float_as_uint(d[j + 2]), false, false);
        __builtin_amdgcn_raw_buffer_store_b32(sanf(__uint_as_float(r[0]) + __uint_as_float(r[1])), rs, xo, j * 49152, 16);
      }
    }
    TL(9);
  }
  TLOUT();
  }
  }
}

extern "C" void kernel_launch(void* const* d_in, const int* in_sizes, int n_in, void* d_out, int out_size, void* d_ws, size_t ws_size,
                              hipStream_t stream) {
  fused_kernel<<<NWG, NT, 0, stream>>>((const u16*)d_in[0], (const u16*)d_in[1], (const u16*)d_in[10], (const u16*)d_in[2], (const u16*)d_in[3], (const u16*)d_in[4],
                                       (const u16*)d_in[6], (const u16*)d_in[7], (const u16*)d_in[8], (const u16*)d_in[9],
                                       (const u16*)d_in[11], (unsigned char*)d_ws, (u16*)d_out);
}
```

```cpp
#include <hip/hip_runtime.h>
#include <hip/hip_bf16.h>
#include <cstdint>

typedef unsigned u32;
typedef unsigned short u16;
typedef u32 u32x4 __attribute__((ext_vector_type(4)));
typedef u32 u32x2 __attribute__((ext_vector_type(2)));
typedef float f32x4 __attribute__((ext_vector_type(4)));
typedef float f32x2 __attribute__((ext_vector_type(2)));
typedef short s16x4 __attribute__((ext_vector_type(4)));
typedef __bf16 bf16x8 __attribute__((ext_vector_type(8)));
typedef __bf16 bf16x2 __attribute__((ext_vector_type(2)));
typedef __bf16 bf16x16 __attribute__((ext_vector_type(16)));
typedef u32 u32x8 __attribute__((ext_vector_type(8)));
typedef __attribute__((address_space(3))) unsigned char lds_t;

constexpr int D = 576, SK = 8192, WIN = 2048, FF = 4608, NQ = 4096, NMOD = 6 * D;
constexpr int NWG = 256, NT = 512, NG = 72;
constexpr int SPIN_CAP = 1 << 15;

constexpr int W_O = 0;
constexpr int W_XP = W_O + 262144;
constexpr int W_Y = W_XP + 12 * 16384;
constexpr int W_MS = W_Y + 81 * 16384;
constexpr int W_M = W_MS;
constexpr int W_H2 = W_M + 4 * 16384;
constexpr int W_PR = W_H2 + 3 * 16384;
constexpr int W_GB = W_PR + 2 * 16384;
constexpr int W_FL = W_GB + 18432;
constexpr int W_H = W_FL + 2048;
constexpr int W_Q = W_H + 36864;
constexpr int W_ST = W_Q + 131072;
constexpr int W_X1 = W_ST + 32768;
constexpr int W_END = W_X1 + 8 * 152 * 16;
constexpr int NPZ = 174, PZ = (W_MS / 16 + NPZ - 1) / NPZ;
static_assert(PZ <= 2 * NT, "poison split");

constexpr int L_W = 0;
constexpr int L_IMG = 73728;
constexpr int L_TILE = L_IMG + 9216;
constexpr int L_IM2 = 86016;
constexpr int L_OI = 65536;
constexpr int L_B = 147456;
constexpr int L_C = 155648;
constexpr int LDS_BYTES = 163840;

#define SCHED() __builtin_amdgcn_sched_barrier(0)
#define BAR() asm volatile("s_barrier" ::: "memory")
#define TL(i) do {} while (0)
#define TLOUT() do {} while (0)
#define T2(i, ...) do {} while (0)
#define WAITV_(n) do { asm volatile("s_waitcnt vmcnt(" #n ")" ::: "memory"); SCHED(); } while (0)
#define WAITV(n) WAITV_(n)
#define W0 3
#define W1 5
#define W2 7
#define TH1 15
#define TH2 40
#define STR_(x) #x
#define STR(x) STR_(x)
#define WAITD() do { asm volatile("s_waitcnt vmcnt(" STR(W2) ")\n\ts_cmpk_gt_i32 %0, " STR(TH2) " ; throttle\n\ts_cbranch_scc1 4 ; throttle\n\ts_waitcnt vmcnt(" STR(W1) ") ; throttle\n\t" \
    "s_cmpk_gt_i32 %0, " STR(TH1) " ; throttle\n\ts_cbranch_scc1 1 ; throttle\n\ts_waitcnt vmcnt(" STR(W0) ") ; throttle" :: "s"(__builtin_amdgcn_readfirstlane(lag)) : "memory", "scc"); SCHED(); } while (0)
#define PR 114
#define R1 34
#define G1 10
#define OH 175
#define OF 99
#define OL 75

__device__ __forceinline__ float lo16(u32 u) { return __uint_as_float(u << 16); }
__device__ __forceinline__ float hi16(u32 u) { return __uint_as_float(u & 0xffff0000u); }
__device__ __forceinline__ float b2f(u16 u) { return __uint_as_float((u32)u << 16); }
__device__ __forceinline__ u32 cvt2(float a, float b) { return __builtin_bit_cast(u32, __builtin_convertvector(f32x2{a, b}, bf16x2)); }
__device__ __forceinline__ float rbf(float v) { return __uint_as_float(cvt2(0.f, v)); }
__device__ __forceinline__ u32 sanf(float f) { u32 v = __float_as_uint(f); return v < 0xfffffffeu ? v : 0xfffffffeu; }
__device__ __forceinline__ f32x2 up2(u32 u) { return f32x2{lo16(u), hi16(u)}; }
__device__ __forceinline__ f32x2 rbf2(f32x2 v) { return f32x2{rbf(v.x), rbf(v.y)}; }
__device__ __forceinline__ void lds_barrier() { asm volatile("s_waitcnt lgkmcnt(0)\n\ts_barrier" ::: "memory"); }
__device__ __forceinline__ f32x4 mfma(u32x4 a, u32x4 b, f32x4 c) {
  return __builtin_amdgcn_mfma_f32_16x16x32_bf16(__builtin_bit_cast(bf16x8, a), __builtin_bit_cast(bf16x8, b), c, 0, 0, 0);
}
__device__ __forceinline__ f32x4 smf(u32x4 a, u32x8 b, f32x4 c, int idx) {
  return __builtin_amdgcn_smfmac_f32_16x16x64_bf16(__builtin_bit_cast(bf16x8, a), __builtin_bit_cast(bf16x16, b), c, idx, 0, 0);
}
__device__ __forceinline__ u32 plo(u32 a, u32 b) { return __builtin_amdgcn_perm(b, a, 0x05040100u); }
__device__ __forceinline__ u32 phi(u32 a, u32 b) { return __builtin_amdgcn_perm(b, a, 0x07060302u); }

extern "C" __device__ __attribute__((const)) unsigned long long llvm_dispatch_id() __asm("llvm.amdgcn.dispatch.id");
typedef __amdgpu_buffer_rsrc_t rsrc_t;
__device__ __forceinline__ u32x4 ld128(rsrc_t rs, int off) { return __builtin_amdgcn_raw_buffer_load_b128(rs, off, 0, 16); }
__device__ __forceinline__ void st128(rsrc_t rs, int off, u32x4 v) { __builtin_amdgcn_raw_buffer_store_b128(v, rs, off, 0, 16); }
__device__ __forceinline__ void st64(rsrc_t rs, int off, u32x2 v) { __builtin_amdgcn_raw_buffer_store_b64(v, rs, off, 0, 16); }
__device__ __forceinline__ void dma(const void* gbase, u32 voff, unsigned lds_dst) {
  unsigned keep;
  asm volatile("s_mov_b32 %0, m0\n\ts_mov_b32 m0, %3\n\ts_nop 2\n\tglobal_load_lds_dwordx4 %1, %2 nt\n\ts_mov_b32 m0, %0"
               : "=&s"(keep) : "v"(voff), "s"(gbase), "s"(lds_dst) : "memory");
}
__device__ __forceinline__ void dma_keep(const void* gbase, u32 voff, unsigned lds_dst) {
  unsigned keep;
  asm volatile("s_mov_b32 %0, m0\n\ts_mov_b32 m0, %3\n\ts_nop 2\n\tglobal_load_lds_dword %1, %2\n\ts_mov_b32 m0, %0"
               : "=&s"(keep) : "v"(voff), "s"(gbase), "s"(lds_dst) : "memory");
}
__device__ __forceinline__ void dma_sc(const void* gbase, u32 voff, unsigned lds_dst) {
  unsigned keep;
  asm volatile("s_mov_b32 %0, m0\n\ts_mov_b32 m0, %3\n\ts_nop 2\n\tglobal_load_lds_dwordx4 %1, %2 sc1\n\ts_mov_b32 m0, %0"
               : "=&s"(keep) : "v"(voff), "s"(gbase), "s"(lds_dst) : "memory");
}
__device__ __forceinline__ void ald128(u32x4& d, const void* gbase, u32 voff) { unsigned long long t; asm volatile("s_mov_b64 %1, %3\n\tglobal_load_dwordx4 %0, %2, %1" : "=v"(d), "=&s"(t) : "v"(voff), "s"(gbase) : "memory"); }
__device__ __forceinline__ void ald64(u32x2& d, const void* gbase, u32 voff) { unsigned long long t; asm volatile("s_mov_b64 %1, %3\n\tglobal_load_dwordx2 %0, %2, %1" : "=v"(d), "=&s"(t) : "v"(voff), "s"(gbase) : "memory"); }
__device__ __forceinline__ void ald128nt(u32x4& d, const void* gbase, u32 voff) { unsigned long long t; asm volatile("s_mov_b64 %1, %3\n\tglobal_load_dwordx4 %0, %2, %1 nt" : "=v"(d), "=&s"(t) : "v"(voff), "s"(gbase) : "memory"); }
__device__ __forceinline__ void ald128sc(u32x4& d, const void* gbase, u32 voff) { unsigned long long t; asm volatile("s_mov_b64 %1, %3\n\tglobal_load_dwordx4 %0, %2, %1 sc1" : "=v"(d), "=&s"(t) : "v"(voff), "s"(gbase) : "memory"); }
__device__ __forceinline__ u32x2 trd(const unsigned char* lds, int off) {
  return __builtin_bit_cast(u32x2, __builtin_amdgcn_ds_read_tr16_b64_v4i16((__attribute__((address_space(3))) s16x4*)((lds_t*)lds + off)));
}
#define nap(n) do { __builtin_amdgcn_s_sleep(n); asm volatile("" ::: "memory"); } while (0)
__device__ __forceinline__ float frcp(float v) { return __builtin_amdgcn_rcpf(v); }
__device__ __forceinline__ float fdiv(float a, float d, float r) { const float q = a * r; return fmaf(fmaf(-q, d, a), r, q); }
__device__ __forceinline__ float silu(float v) { const float d = 1.f + __expf(-v); const float r = frcp(d); return d < 3.0e38f ? fdiv(v, d, r) : v * r; }
template <int CTRL> __device__ __forceinline__ float dppf(float x) {
  return __builtin_bit_cast(float, __builtin_amdgcn_mov_dpp(__builtin_bit_cast(int, x), CTRL, 0xf, 0xf, true));
}
__device__ __forceinline__ u32 mxu(u32 a, u32 b) { return a > b ? a : b; }
__device__ __forceinline__ u32 mx4(u32x4 v) { return mxu(mxu(v[0], v[1]), mxu(v[2], v[3])); }
__device__ __forceinline__ float rl(float v, int l) { return __builtin_bit_cast(float, __builtin_amdgcn_readlane(__builtin_bit_cast(int, v), l)); }
__device__ __forceinline__ float wsum(float v) {
  v += dppf<0xB1>(v); v += dppf<0x4E>(v); v += dppf<0x141>(v); v += dppf<0x140>(v);
  return (rl(v, 0) + rl(v, 16)) + (rl(v, 32) + rl(v, 48));
}
__device__ __forceinline__ float up32(float v) { const u32 u = __float_as_uint(v); return __uint_as_float(__builtin_amdgcn_permlane32_swap(u, u, false, false)[1]); }
__device__ __forceinline__ float xsum32(float v) { const u32 u = __float_as_uint(v); const auto r = __builtin_amdgcn_permlane32_swap(u, u, false, false); return __uint_as_float(r[0]) + __uint_as_float(r[1]); }
__device__ __forceinline__ float xsum16(float v) { const u32 u = __float_as_uint(v); const auto r = __builtin_amdgcn_permlane16_swap(u, u, false, false); return __uint_as_float(r[0]) + __uint_as_float(r[1]); }
__device__ __forceinline__ f32x4 half_sum(f32x4 d) {
  f32x4 r;
#pragma unroll
  for (int j = 0; j < 4; ++j) r[j] = d[j] + dppf<0x108>(up32(d[j]));
  return r;
}
__device__ __forceinline__ f32x2 half_sum2(f32x4 d) {
  f32x2 r;
#pragma unroll
  for (int j = 0; j < 2; ++j) {
    const auto s = __builtin_amdgcn_permlane32_swap(__float_as_uint(d[j]), __float_as_uint(d[j + 2]), false, false);
    r[j] = __uint_as_float(s[0]) + dppf<0x108>(__uint_as_float(s[1]));
  }
  return r;
}
__device__ __forceinline__ void unpack8(u32x4 u, float* f) {
#pragma unroll
  for (int j = 0; j < 4; ++j) { f[2 * j] = lo16(u[j]); f[2 * j + 1] = hi16(u[j]); }
}
__device__ __forceinline__ void ln_row(const float (&v)[16], bool has2, float& mean, float& den) {
  float s = 0.f;
#pragma unroll
  for (int i = 0; i < 8; ++i) s += v[i];
  if (has2) {
#pragma unroll
    for (int i = 8; i < 16; ++i) s += v[i];
  }
  s = wsum(s);
  const float mu = s * (1.f / D);
  mean = rbf(mu);
  float q = 0.f;
#pragma unroll
  for (int i = 0; i < 8; ++i) { const float t = v[i] - mu; q = fmaf(t, t, q); }
  if (has2) {
#pragma unroll
    for (int i = 8; i < 16; ++i) { const float t = v[i] - mu; q = fmaf(t, t, q); }
  }
  q = wsum(q);
  const float var = rbf(q * (1.f / D));
  den = rbf(sqrtf(rbf(var + 1e-5f)));
}
template <int J0 = 0, int J1 = 9>
__device__ __forceinline__ void slab_dma(const u16* base, int stride_b, unsigned lds_dst, int wave, int lane) {
  const u32 v0 = (lane >> 3) * stride_b + (((lane & 7) ^ ((wave & 1) * 4 + (lane >> 4))) << 4), v1 = v0 ^ 32u;
  const u32 v4 = wave >= 4 ? v1 : v0;
  const char* gb = (const char*)base + (size_t)wave * 8 * stride_b;
#pragma unroll
  for (int j = J0; j < J1; ++j) dma(gb + (size_t)j * 64 * stride_b, j < 4 ? v0 : j == 4 ? v4 : v1, lds_dst + (j * 8 + wave) * 1024);
}
__device__ __forceinline__ void slab_frags(const unsigned char* lds, int base, int wave, int lane, u32x4 (&wf)[9]) {
  const int g = lane >> 4, q = (lane & 15) >> 2, p = lane & 3, hf = p >> 1;
  const int a0 = base + (hf * 288 + 8 * g + q) * 128 + ((wave ^ ((g & 1) * 4 + (q >> 1)) ^ (hf * 2)) << 4) + (p & 1) * 8, a1 = (a0 ^ 32) + 512;
#pragma unroll
  for (int s = 0; s < 9; ++s) {
    const u32x2 lo = trd(lds, a0 + s * 4096), hi = trd(lds, a1 + s * 4096);
    wf[s] = u32x4{lo[0], lo[1], hi[0], hi[1]};
  }
}
__device__ __forceinline__ void img_frags(const unsigned char* lds, int img, int lane, u32x4 (&af)[9]) {
#pragma unroll
  for (int s = 0; s < 9; ++s) af[s] = *(const u32x4*)(lds + img + (s * 64 + lane) * 16);
}
__device__ __forceinline__ f32x4 gemv9(const u32x4 (&wf)[9], const u32x4 (&af)[9]) {
  f32x4 acc[3];
  SCHED();
#pragma unroll
  for (int s = 0; s < 9; ++s) acc[s % 3] = mfma(wf[s], af[s], s < 3 ? f32x4{0.f, 0.f, 0.f, 0.f} : acc[s % 3]);
  return (acc[0] + acc[1]) + acc[2];
}
__device__ __forceinline__ int img_off(int b, int k8) {
  const int hf = k8 >= 36 ? 1 : 0, kk = k8 - hf * 36;
  return ((kk >> 2) * 64 + (kk & 3) * 16 + hf * 8 + b) * 16;
}
__device__ __forceinline__ int im2_off(int b, int k8) { return (k8 * 8 + (b ^ (k8 & 7))) * 16; }
__device__ __forceinline__ u32 tag_x(u32x4 v, u32 e0, u32 e1) { return (v[2] ^ e0) | (v[3] ^ e1); }
__device__ __forceinline__ bool tag_ok(u32x4 v, u32 e0, u32 e1) { return tag_x(v, e0, e1) == 0u; }

constexpr int CODE_BYTES = 33300;

__global__ void __launch_bounds__(NT, 2) fused_kernel(const u16* __restrict__ x, const u16* __restrict__ kc, const u16* __restrict__ c, const u16* __restrict__ vc,
                                                      const u16* __restrict__ wq, const u16* __restrict__ wo, const u16* __restrict__ w_r,
                                                      const u16* __restrict__ wg, const u16* __restrict__ wu, const u16* __restrict__ wd,
                                                      const u16* __restrict__ w_mod, unsigned char* ws, u16* out) {
  __shared__ __attribute__((aligned(16))) unsigned char lds[LDS_BYTES];
  SCHED();
  const int w = blockIdx.x, xcd = w & 7, idx = w >> 3;
  const int tid = threadIdx.x, lane = tid & 63, wave = __builtin_amdgcn_readfirstlane(tid >> 6);
  const int n = lane & 15, grp = lane >> 4;
  const unsigned lds_base = (unsigned)(uintptr_t)(lds_t*)lds;
  {
    unsigned long long pc0; unsigned keep;
    asm volatile("s_getpc_b64 %0\n\ts_mov_b32 %1, m0\n\ts_mov_b32 m0, %3\n\ts_cmp_lt_i32 %4, 216\n\ts_cbranch_scc1 2 ; throttle\n\tglobal_load_lds_dword %2, %0\n\ts_mov_b32 m0, %1"
                 : "=&s"(pc0), "=&s"(keep) : "v"(min((w >> 3) * 8192 - 27 * 8192 + lane * 128, CODE_BYTES - 128)), "s"(lds_base + 107520), "s"(wave ? 0 : w) : "memory", "scc");
  }
  const int s_att = w & 1, kvh = (w >> 1) & 15, b_att = w >> 5;
  size_t pairbase = ((size_t)(w >> 1) * SK + (SK - WIN)) * 256;
  int wpart = (wave & 3) * 4096 + s_att * 16384 + (wave >> 2) * 262144;
  int ko = wpart + (n >> 1) * 256 + (n & 1) * 64 + grp * 16;
  int eo[4] = {tid * 16, 8192 + lane * 16, wave * 1152 + lane * 16, wave * 1152 + (lane & 7) * 16};
  asm volatile("" : "+s"(pairbase), "+s"(wpart), "+v"(ko), "+v"(eo[0]), "+v"(eo[1]), "+v"(eo[2]), "+v"(eo[3]));
  SCHED();
  const char* kp = (const char*)kc + pairbase;
#define KLD(u) do { unsigned long long t_; asm volatile("s_mov_b64 %1, %3\n\tglobal_load_dwordx4 %0, %2, %1 offset:%4 nt" : "=v"(kb[u]), "=&s"(t_) : "v"(ko), "s"(kp + ((u) >> 2) * 32768), "i"((((u) >> 1) & 1) * 2048 + ((u) & 1) * 128) : "memory"); } while (0)
  u32x4 kb[32], vb[32];
  asm volatile("s_cmp_gt_i32 %12, 17\n\ts_cbranch_scc1 9 ; throttle\n\tglobal_load_dwordx4 %0, %4, %9 ; throttle\n\tglobal_load_dwordx4 %1, %5, %9 ; throttle\n\t"
               "global_load_dwordx4 %2, %6, %10 ; throttle\n\tglobal_load_dwordx4 %3, %7, %10 offset:1024 ; throttle\n\ts_branch 8 ; throttle\n\t"
               "global_load_dwordx4 %0, %8, %11 nt\n\tglobal_load_dwordx4 %1, %8, %11 offset:128 nt\n\tglobal_load_dwordx4 %2, %8, %11 offset:2048 nt\n\tglobal_load_dwordx4 %3, %8, %11 offset:2176 nt"
               : "=&v"(kb[0]), "=&v"(kb[1]), "=&v"(kb[2]), "=&v"(kb[3]) : "v"(eo[0]), "v"(eo[1]), "v"(eo[2]), "v"(eo[3]), "v"(ko), "s"(c), "s"(x), "s"(kp), "s"(w) : "memory", "scc");
  TL(32);
  SCHED();
  asm volatile("" : "+s"(vc), "+s"(w_r), "+s"(wu), "+s"(wd), "+s"(out), "+s"(x), "+s"(w_mod), "+s"(wg), "+s"(wq), "+s"(wo), "+s"(c), "+s"(ws));
  const rsrc_t rs = __builtin_amdgcn_make_buffer_rsrc(ws, 0, W_END, 0x00020000);
  const unsigned long long epoch = (llvm_dispatch_id() + 1ull) * 0x9E3779B97F4A7C15ull;
  const u32 e0 = (u32)epoch, e1 = (u32)(epoch >> 32);

  const int mw = idx < 6 ? idx * 8 + xcd : -1;
  const int md = mw < 18 ? mw : -1;
  const int ml = mw >= 18 ? mw : mw >= 0 && mw < 6 ? 48 + mw : -1;
  const int lnb = mw >= 6 && mw < 14 ? xcd : -1;
  const int qs = idx >= 6 && idx < 14 ? xcd * 8 + idx - 6 : -1;
  const int oj = idx >= 14 && idx < 23 ? idx - 14 : -1, op = xcd;
  const int gs = idx >= 23 ? xcd * 9 + idx - 23 : -1;

  {
    const char* vp = (const char*)vc + pairbase;
    const int vo = wpart + lane * 16;
#define VLD(u) do { unsigned long long t_; asm volatile("s_mov_b64 %1, %3\n\tglobal_load_dwordx4 %0, %2, %1 offset:%4 nt" : "=v"(vb[u]), "=&s"(t_) : "v"(vo), "s"(vp + ((u) >> 2) * 32768), "i"(((u) & 3) * 1024) : "memory"); } while (0)
#define TIE8(a, o) asm volatile("" : "+v"(a[o]), "+v"(a[(o) + 1]), "+v"(a[(o) + 2]), "+v"(a[(o) + 3]), "+v"(a[(o) + 4]), "+v"(a[(o) + 5]), "+v"(a[(o) + 6]), "+v"(a[(o) + 7]))
    const u32 tp0 = (u32)__builtin_amdgcn_s_memrealtime() + (gs >= 0 ? OF : mw >= 18 ? OL : OH);
    int lag = gs >= 0 || mw >= 18 ? 0 : 1000;
#define PACE(i) do { const int t_ = ((i) * PR >> 2) + ((i) >= R1 ? G1 : 0); lag = (int)((u32)__builtin_amdgcn_s_memrealtime() - tp0) - t_; \
      if (lag < 0) { _Pragma("nounroll") do __builtin_amdgcn_s_sleep(2); while ((int)((u32)__builtin_amdgcn_s_memrealtime() - tp0) < t_); } SCHED(); } while (0)
#define REP4(M, u, W) do { M(u); W; M((u) + 1); W; M((u) + 2); W; M((u) + 3); W; } while (0)
    static_assert(W2 <= 7, "the hand-placed waits assume it");
    const int pz = mw >= 18 ? mw - 18 : idx >= 14 ? w - 82 : -1;
#define POISON() do { if (pz >= 0) { _Pragma("unroll") for (int i = 0; i < 2; ++i) { const int g_ = pz * PZ + i * NT + tid; \
      if (i * NT + tid < PZ && g_ < W_MS / 16) st128(rs, g_ * 16, u32x4{0xffffffffu, 0xffffffffu, 0xffffffffu, 0xffffffffu}); } } SCHED(); } while (0)
#define SILU_IMAGE(c0, c1, both) do { float f[8]; unpack8(c0, f); u32x4 o; \
        _Pragma("unroll") for (int j = 0; j < 4; ++j) o[j] = cvt2(silu(f[2 * j]), silu(f[2 * j + 1])); \
        const int b_ = tid / 72, ch_ = tid - b_ * 72; \
        if (both) *(u32x4*)(lds + L_IMG + img_off(b_, ch_)) = o; \
        *(u32x4*)(lds + L_IM2 + im2_off(b_, ch_)) = o; \
        if (wave == 0) { unpack8(c1, f); \
          _Pragma("unroll") for (int j = 0; j < 4; ++j) o[j] = cvt2(silu(f[2 * j]), silu(f[2 * j + 1])); \
          if (both) *(u32x4*)(lds + L_IMG + img_off(7, 8 + lane)) = o; \
          *(u32x4*)(lds + L_IM2 + im2_off(7, 8 + lane)) = o; } } while (0)
    if (md >= 0) {
      u32x2 xc;
      int ho = W_H + img_off(n & 7, (md >= 9 ? md - 9 : md) * 8 + wave) * 2 + (grp & 1) * 16;
      ald64(xc, x, (n & 7) * 1152 + ((md >= 9 ? md - 9 : 0) * 64 + wave * 8 + (grp & 1) * 4) * 2);
      TL(35);
      BAR();
      float* st = (float*)(lds + L_TILE + 1024);
      if (wave < 4) {
        slab_dma(w_mod + md * 64, NMOD * 2, lds_base + L_W, wave, lane);
        asm volatile("s_waitcnt vmcnt(9)" ::: "memory");
      } else asm volatile("s_waitcnt vmcnt(0)" ::: "memory");
      asm volatile("s_nop 0 ; both branches above cover V" : "+v"(kb[0]), "+v"(kb[1]), "+v"(kb[2]), "+v"(kb[3]), "+v"(xc) :: "memory");
      {
        SILU_IMAGE(kb[0], kb[1], true);
        float v[16]; unpack8(kb[2], v); unpack8(kb[3], v + 8);
        float mean, den; ln_row(v, lane < 8, mean, den);
        if (lane == 0) { st[wave] = mean; st[8 + wave] = den; }
      }
      if (wave >= 4) slab_dma(w_mod + md * 64, NMOD * 2, lds_base + L_W, wave, lane);
      lds_barrier();
      REP4(KLD, 0, );
      u32x4 wf[9], af[9];
      img_frags(lds, L_IMG, lane, af);
      const float mean = st[n & 7], rd = frcp(st[8 + (n & 7)]);
      float ln[4] = {rbf(rbf(lo16(xc[0]) - mean) * rd), rbf(rbf(hi16(xc[0]) - mean) * rd), rbf(rbf(lo16(xc[1]) - mean) * rd), rbf(rbf(hi16(xc[1]) - mean) * rd)};
#pragma unroll
      for (int s = 0; s < 9; ++s) asm volatile("" : "+v"(af[s]));
      asm volatile("" : "+v"(ln[0]), "+v"(ln[1]), "+v"(ln[2]), "+v"(ln[3]), "+v"(ho));
      WAITV(4); BAR(); SCHED();
      slab_frags(lds, L_W, wave, lane, wf);
      const f32x4 s = half_sum(gemv9(wf, af));
      f32x4 r = s;
      if (md >= 9) {
#pragma unroll
        for (int j = 0; j < 4; ++j) r[j] = ln[j] * rbf(1.f + rbf(s[j]));
      }
      if (n < 8 && grp < 2) st128(rs, ho + (md >= 9 ? 18432 : 0), u32x4{cvt2(r[0], r[1]), cvt2(r[2], r[3]), e0, e1});
      SCHED();
      TL(1);
      BAR();
      KLD(4); KLD(5); WAITV(W2);
      KLD(6); WAITV(W2); KLD(7); WAITV(W2); KLD(8); WAITV(W2); KLD(9); WAITV(W2); KLD(10); WAITV(W2);
      KLD(11); WAITV(W2); REP4(KLD, 12, WAITV(W2)); REP4(KLD, 16, WAITV(W2));
      REP4(KLD, 20, WAITV(W2)); KLD(24); WAITV(W2); KLD(25);
    } else {
      constexpr int L_LAND = 98304;
      const unsigned land = lds_base + L_LAND + wave * 6144;
      const unsigned char* lp = lds + L_LAND + wave * 6144 + lane * 16;
      const int sl_ = (3 - (int)((uintptr_t)wq >> 7)) & 7;
      const int s2_ = (63 - (int)((uintptr_t)wq >> 7)) & 63;
      const bool slowq = qs >= 0 && ((qs & 7) == sl_ || qs == s2_);
      if (gs >= 0 && wave == 3 && xcd == (s2_ >> 3)) dma_keep((const char*)wq + (size_t)(idx - 23) * 64 * 8192 + s2_ * 128, lane * 8192, land);
      if (gs >= 0 && wave == 0) dma_keep((const char*)wq + (size_t)(idx - 23) * 64 * 8192 + xcd * 1024 + sl_ * 128, lane * 8192, land);
      if (gs >= 0 && (wave == 1 || (wave == 2 && xcd < 2))) dma_keep((const char*)w_mod + (size_t)((idx - 23) * 64 + 32) * (NMOD * 2) + xcd * 128 + (wave - 1) * 2048, (lane & 31) * (NMOD * 2) + (wave == 1 ? (lane >> 5) * 1024 : 0), land);
      POISON();
      const u32 v0 = (lane >> 3) * 1152 + (((lane & 7) ^ ((lane >> 4) & 2)) << 4);
      const char* gb = (const char*)(wo + (size_t)(op * 512 + wave * 64) * D + oj * 64);
      if (ml >= 0) { dma_sc(c, tid * 16, land); dma_sc(c, (512 + lane) * 16, land + 1024); }
      if (oj < 0 && qs < 0) {
        const int i_ = (gs >= 0 ? idx - 19 : idx - 2) * 8 + wave, o_ = (i_ >> 6) * 16384 + (i_ & 63) * 128 + (((xcd >> 2) ^ wave) & 1) * 8192;
        unsigned long long t0, t1, t2; u32 d_;
        asm volatile("s_memtime %0\n\ts_waitcnt lgkmcnt(0)\n\ts_load_dword %3, %4, %5\n\ts_waitcnt lgkmcnt(0)\n\ts_memtime %1\n\ts_waitcnt lgkmcnt(0)\n\t"
                     "s_load_dword %3, %4, %6\n\ts_waitcnt lgkmcnt(0)\n\ts_memtime %2\n\ts_waitcnt lgkmcnt(0)"
                     : "=&s"(t0), "=&s"(t1), "=&s"(t2), "=&s"(d_) : "s"(ws + W_PR), "s"(o_), "s"(o_ ^ 8192) : "memory");
        const int a_ = (int)((u32)t1 - (u32)t0), b_ = (int)((u32)t2 - (u32)t1);
        if (lane == 0) *(int*)(lds + L_B + 192 + wave * 4) = (wave & 1) ? b_ - a_ : a_ - b_;
        TL(58);
      }
#define WK(j) do { if (oj >= 0) { if ((j) < 8) dma(gb + (j) * 8 * 1152, v0 ^ (((j) & 1) * 64), lds_base + L_W + (wave * 8 + (j)) * 1024); } \
                   else if (slowq) { if ((j) == 8) slab_dma(wq + qs * 64, NQ * 2, lds_base + L_W, wave, lane); } \
                   else if (qs >= 0 && (j) < 9) slab_dma<(j) < 9 ? (j) : 0, (j) < 9 ? (j) + 1 : 0>(wq + qs * 64, NQ * 2, lds_base + L_W, wave, lane); \
                   else if (qs < 0 && (j) >= 4) PACE(j); \
                   if ((j) >= 4) KLD(j); if ((j) == 4) TL(33); WAITD(); if ((j) == 5) TL(34); } while (0)
      WK(0); WK(1); WK(2); WK(3);
      WK(4); WK(5);
      WK(6); WK(7);
      if (oj < 0 && qs < 0) WAITV(4);
      TL(24);
      if (ml >= 0) { const u32x4 c0 = *(const u32x4*)lp, c1 = *(const u32x4*)(lp + 1024); SILU_IMAGE(c0, c1, false); }
      lds_barrier();
      if (pz >= 0 && tid == 448) {
        const u32x4 v0_ = *(const u32x4*)(lds + L_B + 192), v1_ = *(const u32x4*)(lds + L_B + 208);
        const int d_ = (int)(v0_[0] + v0_[1] + v0_[2] + v0_[3] + v1_[0] + v1_[1] + v1_[2] + v1_[3]);
        st64(rs, W_FL + pz * 8, u32x2{e0 ^ (oj >= 0 ? pz & 1 : d_ > 0), e1});
      }
      SCHED();
      WK(8); WK(9); WK(10); WK(11);
      TL(25);
      PACE(12); REP4(KLD, 12, WAITD());
      TL(26);
      const int hg0 = (wave * 144 + lane) * 16, hg2 = (wave * 144 + 128 + n) * 16;
#define HREQ() do { dma_sc(ws + W_H, hg0, land); dma_sc(ws + W_H + 18432, hg0, land + 1024); dma_sc(ws + W_H + 1024, hg0, land + 2048); \
                    dma_sc(ws + W_H + 18432 + 1024, hg0, land + 3072); dma_sc(ws + W_H, hg2, land + 4096); dma_sc(ws + W_H + 18432, hg2, land + 5120); } while (0)
#define HGET() do { _Pragma("unroll") for (int i = 0; i < 6; ++i) hq[i] = *(const u32x4*)(lp + i * 1024); } while (0)
#define HQOK() __all((tag_x(hq[0], e0, e1) | tag_x(hq[1], e0, e1) | tag_x(hq[2], e0, e1) | tag_x(hq[3], e0, e1) | tag_x(hq[4], e0, e1) | tag_x(hq[5], e0, e1)) == 0u)
#define HOK() __all((tag_x(hp[0], e0, e1) | tag_x(hp[1], e0, e1) | tag_x(hp[2], e0, e1)) == 0u)
      if (qs >= 0) HREQ();
      PACE(16); REP4(KLD, 16, WAITD());
      if (qs >= 0) WAITV(4);
      TL(27);
      PACE(20); KLD(20); KLD(21);
      if (qs >= 0) {
        u32x4 hq[6]; HGET();
        _Pragma("nounroll") for (int sp = 0; sp < SPIN_CAP && !HQOK(); ++sp) { nap(16); HREQ(); WAITV(0); HGET(); }
        TL(2);
#define HSUM(a, b) cvt2(lo16(a) + lo16(b), hi16(a) + hi16(b))
        *(u32x2*)(lds + L_IMG + hg0 / 2) = u32x2{HSUM(hq[0][0], hq[1][0]), HSUM(hq[0][1], hq[1][1])};
        *(u32x2*)(lds + L_IMG + 512 + hg0 / 2) = u32x2{HSUM(hq[2][0], hq[3][0]), HSUM(hq[2][1], hq[3][1])};
        if (lane < 16) *(u32x2*)(lds + L_IMG + hg2 / 2) = u32x2{HSUM(hq[4][0], hq[5][0]), HSUM(hq[4][1], hq[5][1])};
      } else WAITD();
      KLD(22); KLD(23);
      if (qs >= 0) {
        u32x4 wf[9], af[9];
        lds_barrier();
        slab_frags(lds, L_W, wave, lane, wf);
        img_frags(lds, L_IMG, lane, af);
        const f32x4 s = half_sum(gemv9(wf, af));
        if (n < 8 && grp < 2) *(u32x2*)(lds + L_TILE + n * 128 + wave * 16 + grp * 8) = u32x2{cvt2(s[0], s[1]), cvt2(s[2], s[3])};
      } else WAITD();
      PACE(24); KLD(24); KLD(25);
      if (qs >= 0) {
        lds_barrier();
        if (wave < 2) {
          const u32x2 t2 = *(const u32x2*)(lds + L_TILE + tid * 8);
          st128(rs, W_Q + ((tid >> 4) * 1024 + qs * 16 + n) * 16, u32x4{t2[0], t2[1], e0, e1});
        }
        TL(3);
      }
      SCHED();
    }
    const unsigned char* qg = ws + W_Q + (b_att * 1024 + kvh * 64) * 16;
    KLD(26); WAITD(); KLD(27); WAITD(); PACE(28); KLD(28); WAITD(); KLD(29); WAITD();
    PACE(30); KLD(30); WAITD(); KLD(31); WAITD();
    TL(28);
    if (gs >= 0 && wave >= 2 && wave < 6) {
      const u16* mat = (wave & 1) ? wg : wu;
      const int g0 = ((3 - (int)((uintptr_t)mat >> 7) - xcd * 9) & 7) + (wave >= 4 ? 8 : 0);
      if (g0 < 9) dma_keep(mat, ((idx - 23) * 29 + min(lane, 28)) * 9216 + (xcd * 9 + g0) * 128, lds_base + L_B + 4096 + wave * 256);
    }
    PACE(32); VLD(0); WAITD(); VLD(1); WAITD(); PACE(34); VLD(2); WAITD(); VLD(3); WAITD();
#define QLOOK() do { if (wave == 1) dma_sc(qg, lane * 16, lds_base + L_B + 1024); } while (0)
    QLOOK();
#define FLOOK(l) do { dma_sc(ws + W_FL, (l) * 16, lds_base + L_B + 2048); dma_sc(ws + W_FL, 1024 + min(l, NPZ / 2 - 65) * 16, lds_base + L_B + 3072); } while (0)
    if (wave == 2) FLOOK(lane);
    PACE(36); VLD(4); WAITD(); VLD(5); WAITD(); PACE(38); VLD(6); WAITD(); VLD(7); WAITD();
    QLOOK();
    TL(29);
    PACE(40); VLD(8); WAITD(); VLD(9); WAITD(); PACE(42); QLOOK(); VLD(10); WAITD(); VLD(11); WAITD();
    TIE8(kb, 0); TIE8(kb, 8); TIE8(kb, 16); TIE8(kb, 24);
    PACE(44); QLOOK(); VLD(12); VLD(13);
    u32x4 qf[2];
    {
      const u32x4* ql = (const u32x4*)(lds + L_B + 1024 + ((n >> 3) * 32 + (n & 1) * 8 + grp * 2) * 16);
      u32x4 g[4];
      _Pragma("nounroll") for (int sp = 0; sp < SPIN_CAP; ++sp) {
        g[0] = ql[0]; g[1] = ql[1]; g[2] = ql[16]; g[3] = ql[17];
        if (__all((tag_x(g[0], e0, e1) | tag_x(g[1], e0, e1) | tag_x(g[2], e0, e1) | tag_x(g[3], e0, e1)) == 0u)) break;
        if (wave == 1 && sp >= 6) { dma_sc(qg, lane * 16, lds_base + L_B + 1024); WAITV(0); } else nap(4);
      }
      asm volatile("" ::: "memory");
      g[0] = ql[0]; g[1] = ql[1]; g[2] = ql[16]; g[3] = ql[17];
      qf[0] = u32x4{g[0][0], g[0][1], g[1][0], g[1][1]};
      qf[1] = u32x4{g[2][0], g[2][1], g[3][0], g[3][1]};
    }
    TL(4);
    constexpr float SCALE = 0.088388347648318440f;
    f32x2 sc[16];
    float mx = -INFINITY;
#pragma unroll
    for (int T = 0; T < 16; ++T) {
      f32x4 acc = mfma(kb[T * 2], qf[0], f32x4{0.f, 0.f, 0.f, 0.f});
      acc = mfma(kb[T * 2 + 1], qf[1], acc);
      sc[T] = f32x2{acc[0] + dppf<0xB1>(acc[1]), acc[2] + dppf<0xB1>(acc[3])};
      if (T == 7 || T == 15) { SCHED(); if (T == 7) VLD(14); else VLD(15); SCHED(); }
    }
#pragma unroll
    for (int T = 0; T < 16; ++T)
#pragma unroll
      for (int j = 0; j < 2; ++j) { const float v = rbf(rbf(sc[T][j]) * SCALE); sc[T][j] = v; mx = fmaxf(mx, v); }
    SCHED(); VLD(16); SCHED();
    mx = fmaxf(mx, __shfl_xor(mx, 16));
    mx = fmaxf(mx, __shfl_xor(mx, 32));
    float ls = 0.f;
#pragma unroll
    for (int T = 0; T < 16; ++T)
#pragma unroll
      for (int j = 0; j < 2; ++j) { sc[T][j] = __expf(sc[T][j] - mx); ls += sc[T][j]; }
    SCHED(); VLD(17); SCHED();
    ls += __shfl_xor(ls, 16);
    ls += __shfl_xor(ls, 32);
    float* stat = (float*)(lds + L_B);
    if (lane == 0 || lane == 8) *(f32x2*)(stat + (wave * 2 + (lane >> 3)) * 2) = f32x2{mx, ls};
    SCHED(); VLD(18); VLD(19); SCHED();
    if (wave == 2) {
      int l2 = lane; asm volatile("" : "+v"(l2));
      u32x4 a, b;
      _Pragma("nounroll") for (int sp = 0; sp < SPIN_CAP; ++sp) {
        a = *(const u32x4*)(lds + L_B + 2048 + l2 * 16); b = *(const u32x4*)(lds + L_B + 3072 + l2 * 16);
        if (__all(((((a[0] ^ e0) | (a[2] ^ e0) | (b[0] ^ e0) | (b[2] ^ e0)) & ~1u) | (a[1] ^ e1) | (a[3] ^ e1) | (b[1] ^ e1) | (b[3] ^ e1)) == 0u)) break;
        nap(1); FLOOK(l2); WAITV(0);
      }
      const unsigned long long mk = (1ull << (NPZ / 2 - 64)) - 1;
      const int cnt = __popcll(__ballot(a[0] != e0)) + __popcll(__ballot(a[2] != e0)) + __popcll(__ballot(b[0] != e0) & mk) + __popcll(__ballot(b[2] != e0) & mk);
      if (l2 == 0) *(int*)(lds + L_B + 128) = cnt > NPZ / 2 ? 8192 : 0;
    }
    lds_barrier();
    const int flip = __builtin_amdgcn_readfirstlane(*(const int*)(lds + L_B + 128));
    float M = -INFINITY, L = 0.f;
    {
      f32x2 t[8];
#pragma unroll
      for (int q = 0; q < 8; ++q) { t[q] = *(const f32x2*)(stat + (q * 2 + (n >> 3)) * 2); M = fmaxf(M, t[q].x); }
#pragma unroll
      for (int q = 0; q < 8; ++q) L = fmaf(__expf(t[q].x - M), t[q].y, L);
    }
    if (tid >= 4 && tid < 12) st128(rs, W_ST + (w * 8 + lane - 4) * 16, u32x4{__float_as_uint(M), __float_as_uint(L), e0, e1});
    SCHED();
    TL(30);
    PACE(52); VLD(20); WAITD(); VLD(21); WAITD();
    const int po = W_ST + ((w ^ 1) * 8 + (n >> 3) * 4) * 16;
    int l1 = lane; asm volatile("" : "+v"(l1));
    u32 km[4] = {(l1 & 4) ? 0u : 0xffffu, (l1 & 4) ? 0xffffu : 0u, (l1 & 4) ? 0u : 0xffff0000u, (l1 & 4) ? 0xffff0000u : 0u};
    int px = (l1 & 3) * 0x5555;
    asm volatile("" : "+v"(km[0]), "+v"(km[1]), "+v"(km[2]), "+v"(km[3]), "+v"(px));
    PACE(54);
    VLD(22); WAITD(); VLD(23); WAITD();
    u32x4 ps, ps1, ps2, ps3;
    ald128sc(ps, ws, po);
    PACE(56);
    VLD(24); WAITD(); VLD(25); WAITD();
    TL(31);
    ald128sc(ps1, ws, po);
    PACE(58);
    VLD(26); WAITD(); VLD(27); WAITD();
    ald128sc(ps2, ws, po);
    VLD(28); VLD(29); WAITV(8);
    asm volatile("" : "+v"(ps));
    TIE8(vb, 0); TIE8(vb, 8); TIE8(vb, 16);
    ald128sc(ps3, ws, po);
    VLD(30); VLD(31);
    if (!__all(tag_ok(ps, e0, e1))) {
      WAITV(8);
      asm volatile("" : "+v"(ps1));
      ps = ps1;
      if (!__all(tag_ok(ps, e0, e1))) {
        WAITV(5);
        asm volatile("" : "+v"(ps2));
        ps = ps2;
        if (!__all(tag_ok(ps, e0, e1))) {
          WAITV(2);
          asm volatile("" : "+v"(ps3));
          ps = ps3;
          if (!__all(tag_ok(ps, e0, e1))) {
            int sp = 0;
            _Pragma("nounroll") do { nap(1); ps = ld128(rs, po); } while (!__all(tag_ok(ps, e0, e1)) && ++sp < SPIN_CAP);
          }
        }
      }
    }
    float fac;
    {
      const float M2 = __uint_as_float(ps[0]), L2 = __uint_as_float(ps[1]);
      const float Mg = fmaxf(M, M2);
      const float Lg = __expf(M - Mg) * L + __expf(M2 - Mg) * L2;
      fac = __expf(mx - Mg) / Lg;
    }
    TL(5);
    u32x4 pf[16];
    u32 pp[16];
#pragma unroll
    for (int T = 0; T < 16; ++T) pp[T] = cvt2(sc[T][0] * fac, sc[T][1] * fac);
    SCHED();
#pragma unroll
    for (int T = 0; T < 16; ++T) {
      const u32 p = (u32)__builtin_amdgcn_mov_dpp((int)pp[T], 0xa0, 0xf, 0xf, true);
      pf[T] = u32x4{p & km[0], p & km[1], p & km[2], p & km[3]};
    }
    f32x4 oa = {0.f, 0.f, 0.f, 0.f};
#define PV(T) oa = smf(pf[T], __builtin_shufflevector(vb[2 * (T)], vb[2 * (T) + 1], 0, 1, 2, 3, 4, 5, 6, 7), oa, px)
#pragma unroll
    for (int T = 0; T < 12; ++T) PV(T);
    SCHED();
    int t2_ = threadIdx.x; asm volatile("" : "+v"(t2_));
    const int tid = t2_, lane = tid & 63, n = lane & 15, grp = lane >> 4;
    int oo = W_O + (kvh & 7) * 32768 + (b_att >> 2) * 16384 + ((kvh >> 3) * 8192 ^ flip) + (b_att & 3) * 2048 + s_att * 1024 + (((grp >> 1) * 2 + (n >> 3)) * 16 + (grp & 1) * 8 + (n & 7)) * 16;
    int mw_ = L_C + tid * 16;
    asm volatile("" : "+v"(oo), "+v"(mw_), "+v"(pf[12]), "+v"(pf[13]), "+v"(pf[14]), "+v"(pf[15]));
    constexpr int L_H2 = L_W;
    constexpr int L_ACT = L_C + 4608;
    int ia, ra, ix, yo[2], fo[7], tw, tr_;
    const u32 gv = (lane >> 3) * (FF * 2) + (((lane & 7) ^ ((wave & 1) * 4 + ((lane >> 4) & 2))) << 4);
#define GU(j) do { const size_t o_ = ((size_t)((j) * 64 + wave * 8) * FF + gs * 64) * 2; dma((const char*)wg + o_, gv, lds_base + L_W + ((j) * 8 + wave) * 1024); \
                   dma((const char*)wu + o_, gv ^ 16u, lds_base + L_W + 73728 + ((j) * 8 + wave) * 1024); } while (0)
#define ML(j) dma((const char*)w_mod + ((size_t)((j) * 64 + wave * 8) * NMOD + ml * 64) * 2, (lane >> 3) * (NMOD * 2) + (((lane & 7) ^ ((wave & 1) * 4 + ((lane >> 4) & 2))) << 4), lds_base + L_W + ((j) * 8 + wave) * 1024)
    const bool late = gs >= 0 || mw >= 18;
    if (late) {
      BAR();
      if (gs >= 0) { GU(0); GU(1); } else { ML(0); ML(1); ML(2); ML(3); }
      {
    const int b = wave;
    ia = L_H2 + grp * 1024 + ((n >> 2) * 16 + (n & 2) * 4 + (b ^ (n >> 1))) * 16 + (n & 1) * 8;
    ra = L_H2 + (grp * 16 + (n & 8) + ((n & 7) ^ (grp * 2 + (n >> 3)))) * 16; ix = n < 8 ? 0x4444 : 0xeeee;
    tw = L_ACT + ((grp & 1) * 4 + (grp >> 1) * 2 + (n >> 3)) * 144 + wave * 16 + (n & 7) * 2; tr_ = L_ACT + (n & 7) * 144 + grp * 16;
    {
      const int f0 = wave * 8 + (grp & 1) * 4 + (n >> 3) * 2, h0 = f0 >= 36, g0 = f0 - h0 * 36, g1 = 28 + wave * 2 + (grp >> 1);
      yo[0] = W_Y + (gs < 64 ? g0 * 32768 + (gs >> 5) * 16384 : (72 + (g0 >> 2)) * 16384 + (g0 & 3) * 2048) + (h0 * 8192 ^ flip) + (gs & 31) * 256 + ((grp >> 1) * 8 + (n & 7)) * 16;
      yo[1] = W_Y + (gs < 64 ? g1 * 32768 + (gs >> 5) * 16384 : (72 + (g1 >> 2)) * 16384 + (g1 & 3) * 2048) + (8192 ^ flip) + (gs & 31) * 256 + ((grp & 1) * 8 + n) * 16;
    }
    {
      const int b_ = lane >> 3, cc = lane & 7, col = gs * 8 + cc;
      fo[0] = W_GB + (b_ * 144 + gs * 2 + (cc >> 2)) * 16;
      fo[1] = W_X1 + (b_ * 152 + gs * 2 + (cc >> 2)) * 16; fo[2] = W_X1 + (b_ * 152 + 144) * 16;
      fo[3] = W_Y + ((xcd >> 2) * 8192 ^ flip) + tid * 16;
      fo[4] = L_H2 + ((gs >> 1) * 16 + (gs & 1) * 8 + (b_ ^ (gs & 7))) * 16 + cc * 2;
      fo[5] = L_C + (((cc >> 2) * 8 + b_) * 4 + (cc & 3)) * 4;
      fo[6] = (b_ * D + col) * 2;
    }
    asm volatile("" : "+v"(ia), "+v"(ra), "+v"(ix), "+v"(tw), "+v"(tr_), "+v"(yo[0]), "+v"(yo[1]),
                 "+v"(fo[0]), "+v"(fo[1]), "+v"(fo[2]), "+v"(fo[3]), "+v"(fo[4]), "+v"(fo[5]), "+v"(fo[6]));
      }
      WAITV(6);
    } else WAITV(2);
    asm volatile("s_nop 0 ; both branches above cover V up to the last two" : "+v"(vb[24]), "+v"(vb[25]), "+v"(vb[26]), "+v"(vb[27]), "+v"(vb[28]), "+v"(vb[29]));
    asm volatile("" :: "v"(ps1), "v"(ps2), "v"(ps3));
    PV(12); PV(13); PV(14);
    SCHED();
    if (late) WAITV(4); else WAITV(0);
    T2(56, );
    asm volatile("s_nop 0 ; both branches above cover V" : "+v"(vb[30]), "+v"(vb[31]));
    PV(15);
    TL(6);
    *(f32x4*)(lds + mw_) = oa;
    T2(54, );
    lds_barrier();
    T2(55, );
    if (wave == 0) {
      f32x4 m[8];
#pragma unroll
      for (int q = 0; q < 8; ++q) m[q] = *(const f32x4*)(lds + mw_ + q * 1024);
      SCHED();
      const f32x4 o = ((m[0] + m[1]) + (m[2] + m[3])) + ((m[4] + m[5]) + (m[6] + m[7]));
      st128(rs, oo, u32x4{sanf(o[0]), sanf(o[1]), sanf(o[2]), sanf(o[3])});
    }
    TL(7); SCHED();
  if (gs >= 0) {
    const int b = wave;
    BAR(); SCHED();
    u32x8 bw[9];
    u32x4 wdf[2][5];
    const int ga0 = L_W + ((lane >> 1) & 1) * 73728 + ((grp >> 1) * 16 + (grp & 1) * 4 + ((n >> 2) & 1) + (n & 8)) * 128 + ((wave ^ ((grp & 1) * 2 + ((n >> 1) & 4)) ^ ((lane >> 1) & 1)) << 4) + (lane & 1) * 8;
#define GUF(S0, S1) do { _Pragma("unroll") for (int s = S0; s < S1; ++s) { const int a_ = ga0 + (s >> 2) * 32768, o_ = (s & 3) * 8192; \
      const u32x2 r0 = trd(lds, a_ + o_), r1 = trd(lds, a_ + o_ + 256), r2 = trd(lds, a_ + o_ + 4096), r3 = trd(lds, a_ + o_ + 4352); \
      bw[s] = u32x8{r0[0], r0[1], r1[0], r1[1], r2[0], r2[1], r3[0], r3[1]}; } } while (0)
    {
      u32x4 rd[8];
      GU(2); GU(3);
      TL(23);
      BAR();
      const char* db = (const char*)(wd + (size_t)gs * 64 * D + wave * 64);
      const u32 dv = (n >> 3) * 36864 + grp * 9216 + (n & 7) * 16;
#pragma unroll
      for (int i = 0; i < 8; ++i) ald128nt(rd[i], db + i * 1152, dv);
      {
        const int r = lane >> 3;
        dma((const char*)(wd + (size_t)(gs * 64 + wave * 8) * D + 512), r * 1152 + (((lane & 7) ^ (r & 2) ^ ((wave & 1) * 4)) << 4), lds_base + L_B + wave * 1024);
      }
      TL(19);
      asm volatile("s_waitcnt vmcnt(9)\n\ts_barrier" ::: "memory");
      TL(22);
      GUF(0, 4);
      SCHED();
      GU(4); GU(5); GU(6); GU(7); GU(8);
      TL(20);
      asm volatile("s_waitcnt vmcnt(11)" : "+v"(rd[0]), "+v"(rd[1]), "+v"(rd[2]), "+v"(rd[3]), "+v"(rd[4]), "+v"(rd[5]), "+v"(rd[6]), "+v"(rd[7]) :: "memory");
#pragma unroll
      for (int j = 0; j < 4; ++j)
#pragma unroll
        for (int t = 0; t < 4; ++t) {
          const u32 a = (t & 1) ? phi(rd[2 * j][t >> 1], rd[2 * j + 1][t >> 1]) : plo(rd[2 * j][t >> 1], rd[2 * j + 1][t >> 1]);
          const u32 b_ = (t & 1) ? phi(rd[2 * j][2 + (t >> 1)], rd[2 * j + 1][2 + (t >> 1)]) : plo(rd[2 * j][2 + (t >> 1)], rd[2 * j + 1][2 + (t >> 1)]);
          wdf[0][t][j] = (u32)__builtin_amdgcn_update_dpp((int)a, (int)b_, 0x128, 0xf, 0xc, false);
          wdf[1][t][j] = (u32)__builtin_amdgcn_update_dpp((int)b_, (int)a, 0x128, 0xf, 0x3, false);
        }
#pragma unroll
      for (int t = 0; t < 4; ++t) asm volatile("" : "+v"(wdf[0][t]), "+v"(wdf[1][t]));
      TL(21);
    }
    asm volatile("s_waitcnt vmcnt(0) lgkmcnt(0)\n\ts_barrier" ::: "memory");
    {
      u32x4 hp[3];
      const int hb = W_H2 + b / 3 * 16384 + ((xcd >> 2) * 8192 ^ flip) + b % 3 * 2304, h0 = hb + lane * 16, h2o = hb + (128 + n) * 16;
      hp[0] = ld128(rs, h0); hp[1] = ld128(rs, h0 + 1024); hp[2] = ld128(rs, h2o);
      GUF(4, 9);
      const int q = n >> 2, p = lane & 3;
      const int a0 = L_B + (8 * grp + q) * 128 + ((((wave & 3) * 2 + (p >> 1)) ^ (q & 2) ^ ((grp & 1) * 4)) << 4) + (p & 1) * 8;
#pragma unroll
      for (int ks = 0; ks < 2; ++ks) {
        const u32x2 lo = trd(lds, a0 + ks * 4096), hi = trd(lds, a0 + ks * 4096 + 512);
        wdf[ks][4] = u32x4{lo[0], lo[1], hi[0], hi[1]};
      }
      TL(12);
#pragma unroll
      for (int s = 0; s < 9; ++s) asm volatile("" : "+v"(bw[s]));
      asm volatile("" : "+v"(wdf[0][4]), "+v"(wdf[1][4]));
      if (!HOK()) {
        int sp = 0;
        _Pragma("nounroll") do { nap(2); hp[0] = ld128(rs, h0); hp[1] = ld128(rs, h0 + 1024); hp[2] = ld128(rs, h2o); } while (!HOK() && ++sp < SPIN_CAP);
      }
      TL(13);
      *(u32x2*)(lds + ia) = u32x2{hp[0][0], hp[0][1]};
      *(u32x2*)(lds + ia + 4096) = u32x2{hp[1][0], hp[1][1]};
      if (lane < 16) *(u32x2*)(lds + ia + 8192) = u32x2{hp[2][0], hp[2][1]};
      lds_barrier();
      T2(47, );
      u32x4 af[9];
#pragma unroll
      for (int s = 0; s < 9; ++s) af[s] = *(const u32x4*)(lds + ra + s * 1024);
      SCHED();
      f32x4 d = {0.f, 0.f, 0.f, 0.f};
#pragma unroll
      for (int s = 0; s < 9; ++s) d = smf(af[s], bw[s], d, ix);
      T2(48, "+v"(d));
      int r[2];
#pragma unroll
      for (int j = 0; j < 2; ++j) {
        const auto t = __builtin_amdgcn_permlane32_swap(__float_as_uint(d[j]), __float_as_uint(d[j + 2]), false, false);
        r[j] = __float_as_int(rbf(__uint_as_float(t[0]) + __uint_as_float(t[1])));
      }
      const float g = __int_as_float(__builtin_amdgcn_update_dpp(r[0], r[1], 0x128, 0xf, 0xc, false)), u = __int_as_float(__builtin_amdgcn_update_dpp(r[1], r[0], 0x128, 0xf, 0x3, false));
      *(u16*)(lds + tw) = (u16)cvt2(rbf(silu(g)) * u, 0.f);
    }
    T2(49, );
    lds_barrier();
    TL(14);
    {
      u32x4 af[2];
#pragma unroll
      for (int ks = 0; ks < 2; ++ks) af[ks] = *(const u32x4*)(lds + tr_ + ks * 64);
      f32x4 d[5];
#pragma unroll
      for (int t = 0; t < 5; ++t) d[t] = mfma(wdf[1][t], af[1], mfma(wdf[0][t], af[0], f32x4{0.f, 0.f, 0.f, 0.f}));
      T2(50, "+v"(d[0]), "+v"(d[1]), "+v"(d[2]), "+v"(d[3]), "+v"(d[4]));
#define YS(t) sanf(__int_as_float(__builtin_amdgcn_update_dpp(__float_as_int(d[t][j]), __float_as_int(d[t][j + 2]), 0xe4, 0xf, 0xc, false)))
#pragma unroll
      for (int j = 0; j < 2; ++j)
        __builtin_amdgcn_raw_buffer_store_b128(u32x4{YS(0), YS(1), YS(2), YS(3)},
                                               rs, yo[0], j * (gs < 64 ? 32768 : 2048), 16);
      if (n < 8 && wave < 4)
        st128(rs, yo[1], u32x4{sanf(d[4][0]), sanf(d[4][1]), sanf(d[4][2]), sanf(d[4][3])});
    }
    TL(15);
    {
      float gbv = 0.f, x1v = 0.f, h2 = 0.f, rsel = 0.f;
      if (wave == 7) {
        u32x4 gq, xq, rq;
        _Pragma("nounroll") for (int sp = 0; sp < SPIN_CAP; ++sp) {
          gq = ld128(rs, fo[0]); xq = ld128(rs, fo[1]); rq = ld128(rs, fo[2]);
          if (__all((tag_x(gq, e0, e1) | tag_x(xq, e0, e1) | tag_x(rq, e0, e1)) == 0u)) break;
          nap(1);
        }
        const u32 gw = (lane & 2) ? gq[1] : gq[0], xw = (lane & 2) ? xq[1] : xq[0];
        gbv = (lane & 1) ? hi16(gw) : lo16(gw); x1v = (lane & 1) ? hi16(xw) : lo16(xw);
        h2 = b2f(*(const u16*)(lds + fo[4])); rsel = __uint_as_float(rq[0]);
        asm volatile("" : "+v"(gbv), "+v"(x1v), "+v"(h2), "+v"(rsel));
      }
      WAITV(0); T2(51, );
      u32x4 y[3] = {};
      const int gy = gs >= 36 ? gs - 36 : gs;
      _Pragma("nounroll") for (int sp = 0; sp < SPIN_CAP; ++sp) {
        y[0] = __builtin_amdgcn_raw_buffer_load_b128(rs, fo[3], gy * 32768, 16); y[1] = __builtin_amdgcn_raw_buffer_load_b128(rs, fo[3], gy * 32768 + 16384, 16);
        if (wave < 2) y[2] = __builtin_amdgcn_raw_buffer_load_b128(rs, fo[3], (72 + (gy >> 2)) * 16384 + (gy & 3) * 2048, 16);
        if (__all(mxu(mxu(mx4(y[0]), mx4(y[1])), mx4(y[2])) != 0xffffffffu)) break;
        nap(1);
      }
      TL(16);
      f32x4 s;
#pragma unroll
      for (int k = 0; k < 4; ++k) s[k] = xsum32(xsum16((__uint_as_float(y[0][k]) + __uint_as_float(y[1][k])) + __uint_as_float(y[2][k])));
      if (lane < 16) *(f32x4*)(lds + L_C + (wave * 16 + lane) * 16) = s;
      T2(52, );
      lds_barrier();
      T2(53, );
      if (wave == 7) {
        float t[8];
#pragma unroll
        for (int ww = 0; ww < 8; ++ww) t[ww] = *(const float*)(lds + fo[5] + ww * 256);
        SCHED();
        const float y2 = rbf(rbf(((t[0] + t[1]) + (t[2] + t[3])) + ((t[4] + t[5]) + (t[6] + t[7]))) * rsel);
        const float res = x1v + rbf(gbv * rbf(h2 + y2));
        __builtin_amdgcn_raw_buffer_store_b16((u16)(cvt2(res, 0.f) & 0xffffu), __builtin_amdgcn_make_buffer_rsrc(out, 0, 8 * D * 2, 0x00020000), fo[6], 0, 0);
      }
      TL(17);
    }
  }
  {
  int t3_ = threadIdx.x; asm volatile("" : "+v"(t3_));
  const int tid = t3_, lane = tid & 63, n = lane & 15, grp = lane >> 4;
  if (qs >= 0) {
    const u16* mat = (wave & 1) ? wg : wu;
    BAR();
    const int g0 = ((3 - (int)((uintptr_t)mat >> 7) - xcd * 9) & 7) + (wave >= 4 ? 8 : 0);
    if (g0 < 9) dma_keep(mat, (256 + (idx - 6) * 40 + ((wave >> 1) & 1) * 20 + min(lane, 19)) * 9216 + (xcd * 9 + g0) * 128, lds_base + L_W + wave * 256);
    const int l9 = min(lane, 44), rr = l9 / 9;
#pragma unroll
    for (int i = 0; i < 2; ++i) dma_keep(mat, (256 + ((i * 8 + idx - 6) * 4 + (wave >> 1)) * 5 + rr) * 9216 + (xcd * 9 + l9 - rr * 9) * 128, lds_base + L_W + wave * 256);
  }
  if (ml >= 0) {
    BAR();
    if (mw < 18) { ML(0); ML(1); ML(2); ML(3); }
    ML(4); ML(5); ML(6); ML(7); ML(8);
    TL(38);
    u32x4 af[9];
    f32x4 d = {0.f, 0.f, 0.f, 0.f};
    const int q = n >> 2, ch = (ml - 18) / 9, sl = ml - 18 - ch * 9;
    int a0 = L_W + ((grp >> 1) * 16 + (grp & 1) * 4 + (q & 1) + (q >> 1) * 8) * 128 + (((wave & 3) ^ ((grp & 1) + (q & 2))) << 5) + (lane & 3) * 8, ix = n < 8 ? 0x4444 : 0xeeee;
    int mo = (ch < 3 ? W_M + ((grp >> 1) * 2 + (n & 1)) * 16384 + ((grp & 1) * 8192 ^ flip) + ch * 2304 : W_GB + ((grp & 1) * 4 + (grp >> 1) * 2 + (n & 1)) * 2304) + (sl * 16 + wave * 4 + q) * 16;
#pragma unroll
    for (int s = 0; s < 9; ++s) af[s] = *(const u32x4*)(lds + L_IM2 + (grp * 16 + (n & 8) + ((n & 7) ^ (grp * 2 + (n >> 3)))) * 16 + s * 1024);
#define MLS(S0, S1) do { if (wave < 4) { u32x8 f_[S1 - S0]; _Pragma("unroll") for (int s = S0; s < S1; ++s) { const int a_ = a0 + (s >> 2) * 32768, o_ = (s & 3) * 8192; \
      const u32x2 r0 = trd(lds, a_ + o_), r1 = trd(lds, a_ + o_ + 256), r2 = trd(lds, a_ + o_ + 4096), r3 = trd(lds, a_ + o_ + 4352); \
      f_[s - S0] = u32x8{r0[0], r0[1], r1[0], r1[1], r2[0], r2[1], r3[0], r3[1]}; } SCHED(); \
      _Pragma("unroll") for (int s = S0; s < S1; ++s) d = smf(af[s], f_[s - S0], d, ix); } } while (0)
    asm volatile("s_waitcnt vmcnt(5)\n\ts_barrier" : "+v"(a0), "+v"(ix), "+v"(mo) :: "memory");
    MLS(0, 4);
    asm volatile("s_waitcnt vmcnt(2)\n\ts_barrier" : "+v"(d) :: "memory");
    MLS(4, 7);
    asm volatile("s_waitcnt vmcnt(0)\n\ts_barrier" : "+v"(d), "+v"(af[7]), "+v"(af[8]) :: "memory");
    TL(39);
    MLS(7, 9);
    if (wave < 4) {
      float r[2];
#pragma unroll
      for (int j = 0; j < 2; ++j) {
        const auto t = __builtin_amdgcn_permlane32_swap(__float_as_uint(d[j]), __float_as_uint(d[j + 2]), false, false);
        r[j] = __uint_as_float(t[0]) + __uint_as_float(t[1]);
      }
      const float o = dppf<0xB1>((n & 1) ? r[0] : r[1]);
      const u32 pk = (n & 1) ? cvt2(o, r[1]) : cvt2(r[0], o);
      const u32 p2 = (u32)__builtin_amdgcn_mov_dpp((int)pk, 0x4E, 0xf, 0xf, true);
      if (!(n & 2)) st128(rs, mo, u32x4{pk, p2, e0, e1});
    }
    if (ml < 45) TL(18); else TL(36);
  }

  if (lnb >= 0 && wave < 3) {
    const int b = lnb, g = tid < 144 ? tid : 143;
    const bool on = tid < 144;
    float* ex = (float*)(lds + L_TILE);
    const u32x2 xa = __builtin_amdgcn_raw_buffer_load_b64(__builtin_amdgcn_make_buffer_rsrc((void*)x, 0, 8 * D * 2, 0x00020000), (b * D + g * 4) * 2, 0, 0);
    const u32x2 wra = __builtin_amdgcn_raw_buffer_load_b64(__builtin_amdgcn_make_buffer_rsrc((void*)w_r, 0, D * 2, 0x00020000), g * 8, 0, 0);
    u32x4 mm[3], xp[8];
#define XPLD() do { _Pragma("unroll") for (int p = 0; p < 8; ++p) xp[p] = ld128(rs, W_XP + ((b & 3) * 3 + p / 3) * 16384 + ((b >> 2) * 8192 ^ flip) + p % 3 * 2304 + g * 16); } while (0)
    _Pragma("nounroll") for (int sp = 0; sp < SPIN_CAP; ++sp) {
#pragma unroll
      for (int k = 0; k < 3; ++k) mm[k] = ld128(rs, W_M + (b & 3) * 16384 + ((b >> 2) * 8192 ^ flip) + k * 2304 + g * 16);
      XPLD();
      if (__all((tag_x(mm[0], e0, e1) | tag_x(mm[1], e0, e1) | tag_x(mm[2], e0, e1)) == 0u)) break;
      nap(2);
    }
    TL(37);
    f32x2 xv[2], ga[2], sb[2], cb1[2], wr[2];
#pragma unroll
    for (int i = 0; i < 2; ++i) {
      xv[i] = up2(xa[i]); wr[i] = up2(wra[i]); ga[i] = up2(mm[0][i]); sb[i] = up2(mm[1][i]); cb1[i] = rbf2(f32x2{1.f, 1.f} + up2(mm[2][i]));
      asm volatile("" : "+v"(xv[i]), "+v"(ga[i]), "+v"(sb[i]), "+v"(cb1[i]), "+v"(wr[i]));
    }
    int ho = W_H2 + b / 3 * 16384 + ((b >> 2) * 8192 ^ flip ^ 8192) + b % 3 * 2304 + g * 16, xo = W_X1 + (b * 152 + g) * 16;
    int ho2 = ho ^ 8192;
    asm volatile("" : "+v"(ho), "+v"(ho2), "+v"(xo));
    _Pragma("nounroll") for (int sp = 0; sp < SPIN_CAP; ++sp) {
      if (__all(mxu(mxu(mxu(mx4(xp[0]), mx4(xp[1])), mxu(mx4(xp[2]), mx4(xp[3]))), mxu(mxu(mx4(xp[4]), mx4(xp[5])), mxu(mx4(xp[6]), mx4(xp[7])))) != 0xffffffffu)) break;
      nap(1);
      XPLD();
    }
    TL(10);
    f32x2 v[2];
#pragma unroll
    for (int i = 0; i < 2; ++i) {
      f32x2 pp[8];
#pragma unroll
      for (int p = 0; p < 8; ++p) pp[p] = f32x2{__uint_as_float(xp[p][2 * i]), __uint_as_float(xp[p][2 * i + 1])};
      v[i] = rbf2(xv[i] + rbf2(ga[i] * rbf2(((pp[0] + pp[1]) + (pp[2] + pp[3])) + ((pp[4] + pp[5]) + (pp[6] + pp[7])))));
    }
    const f32x2 s2 = v[0] + v[1];
    const float mw = wsum(on ? s2.x + s2.y : 0.f) * (wave < 2 ? 1.f / 256 : 1.f / 64);
    T2(44, );
    const f32x2 t0 = v[0] - f32x2{mw, mw}, t1 = v[1] - f32x2{mw, mw}, q2 = t0 * t0 + t1 * t1;
    const float m2 = wsum(on ? q2.x + q2.y : 0.f);
    if (lane == 0) *(f32x2*)(ex + wave * 2) = f32x2{mw, m2};
    T2(45, );
    lds_barrier();
    T2(46, );
    const f32x4 e01 = *(const f32x4*)ex; const f32x2 e2 = *(const f32x2*)(ex + 4);
    const float mu = (e01[0] + e01[2]) * (256.f / D) + e2.x * (64.f / D);
    const float d0 = e01[0] - mu, d1 = e01[2] - mu, d2 = e2.x - mu;
    const float var = rbf(((e01[1] + e01[3]) + e2.y + 256.f * (d0 * d0 + d1 * d1) + 64.f * d2 * d2) * (1.f / D));
    const float mean = rbf(mu), rden = frcp(rbf(__builtin_amdgcn_sqrtf(rbf(var + 1e-5f))));
    u32 hw[2];
#pragma unroll
    for (int i = 0; i < 2; ++i) {
      const f32x2 t = rbf2(rbf2(rbf2(v[i] - f32x2{mean, mean}) * f32x2{rden, rden}) * cb1[i]) + sb[i];
      hw[i] = cvt2(t.x, t.y);
    }
    if (on) {
      st128(rs, ho, u32x4{hw[0], hw[1], e0, e1});
      st128(rs, ho2, u32x4{hw[0], hw[1], e0, e1});
      st128(rs, xo, u32x4{cvt2(v[0].x, v[0].y), cvt2(v[1].x, v[1].y), e0, e1});
    }
    TL(11);
    const f32x2 r2 = up2(hw[0]) * wr[0] + up2(hw[1]) * wr[1];
    const float rw = wsum(on ? r2.x + r2.y : 0.f);
    if (lane == 0) ex[8 + wave] = rw;
    lds_barrier();
    if (tid < 8) {
      const float r = rbf((ex[8] + ex[9]) + ex[10]);
      st128(rs, W_X1 + (b * 152 + 144 + tid) * 16, u32x4{__float_as_uint(rbf(1.f / (1.f + __expf(-r)))), 0u, e0, e1});
    }
  }

  if (oj >= 0) {
    const int ob = W_O + ((op & 3) * 2 + (wave >> 2)) * 32768 + (lane >> 5) * 16384 + ((op >> 2) * 8192 ^ flip) + ((lane >> 3) & 3) * 2048 + (wave & 3) * 256 + (lane & 7) * 16;
    u32x4 a[4];
#pragma unroll
    for (int p = 0; p < 4; ++p) a[p] = ld128(rs, ob + (p >> 1) * 1024 + (p & 1) * 128);
    SCHED();
    u32x8 wf[8];
    if (wave < 4) {
      const int q = n >> 2, p = lane & 3;
      const int a0 = L_W + ((grp >> 1) * 16 + (grp & 1) * 4 + (q & 1) + (q >> 1) * 8) * 128 + ((wave ^ ((grp & 1) + (q & 2))) << 5) + p * 8;
#pragma unroll
      for (int s = 0; s < 8; ++s) {
        const u32x2 r0 = trd(lds, a0 + s * 8192), r1 = trd(lds, a0 + s * 8192 + 256), r2 = trd(lds, a0 + s * 8192 + 4096), r3 = trd(lds, a0 + s * 8192 + 4352);
        wf[s] = u32x8{r0[0], r0[1], r1[0], r1[1], r2[0], r2[1], r3[0], r3[1]};
      }
#pragma unroll
      for (int s = 0; s < 8; ++s) asm volatile("" : "+v"(wf[s]));
    }
    int rw = L_OI + wave * 1024 + (((lane >> 1) & 3) * 16 + (lane & 1) * 8 + ((lane >> 3) ^ (lane & 7))) * 16, rr = L_OI + (grp * 16 + (n & 8) + ((n & 7) ^ (grp * 2 + (n >> 3)))) * 16;
    int xo = W_XP + ((grp >> 1) * 6 + op / 3) * 16384 + ((grp & 1) * 8192 ^ flip) + op % 3 * 2304 + oj * 256 + wave * 64 + n * 4, ix = n < 8 ? 0x4444 : 0xeeee;
    asm volatile("" : "+v"(rw), "+v"(rr), "+v"(xo), "+v"(ix));
    _Pragma("nounroll") for (int sp = 0; sp < SPIN_CAP; ++sp) {
      const bool m0 = mxu(mx4(a[0]), mx4(a[1])) == 0xffffffffu, m1 = mxu(mx4(a[2]), mx4(a[3])) == 0xffffffffu;
      if (!__any(m0 || m1)) break;
      nap(2);
      if (m0) { a[0] = ld128(rs, ob); a[1] = ld128(rs, ob + 128); }
      if (m1) { a[2] = ld128(rs, ob + 1024); a[3] = ld128(rs, ob + 1152); }
    }
    TL(8);
#define OS(p, j) (__uint_as_float(a[p][j]) + __uint_as_float(a[(p) + 2][j]))
    *(u32x4*)(lds + rw) = u32x4{cvt2(OS(0, 0), OS(0, 1)), cvt2(OS(0, 2), OS(0, 3)), cvt2(OS(1, 0), OS(1, 1)), cvt2(OS(1, 2), OS(1, 3))};
    T2(41, );
    lds_barrier();
    T2(42, );
    if (wave < 4) {
      u32x4 af[8];
#pragma unroll
      for (int s = 0; s < 8; ++s) af[s] = *(const u32x4*)(lds + rr + s * 1024);
      SCHED();
      f32x4 d = {0.f, 0.f, 0.f, 0.f};
#pragma unroll
      for (int s = 0; s < 8; ++s) d = smf(af[s], wf[s], d, ix);
      T2(43, "+v"(d));
#pragma unroll
      for (int j = 0; j < 2; ++j) {
        const auto r = __builtin_amdgcn_permlane32_swap(__float_as_uint(d[j]), __float_as_uint(d[j + 2]), false, false);
        __builtin_amdgcn_raw_buffer_store_b32(sanf(__uint_as_float(r[0]) + __uint_as_float(r[1])), rs, xo, j * 49152, 16);
      }
    }
    TL(9);
  }
  TLOUT();
  }
  }
}

extern "C" void kernel_launch(void* const* d_in, const int* in_sizes, int n_in, void* d_out, int out_size, void* d_ws, size_t ws_size,
                              hipStream_t stream) {
  fused_kernel<<<NWG, NT, 0, stream>>>((const u16*)d_in[0], (const u16*)d_in[1], (const u16*)d_in[10], (const u16*)d_in[2], (const u16*)d_in[3], (const u16*)d_in[4],
                                       (const u16*)d_in[6], (const u16*)d_in[7], (const u16*)d_in[8], (const u16*)d_in[9],
                                       (const u16*)d_in[11], (unsigned char*)d_ws, (u16*)d_out);
}
```

```cpp
#include <hip/hip_runtime.h>
#include <hip/hip_bf16.h>
#include <cstdint>

typedef unsigned u32;
typedef unsigned short u16;
typedef u32 u32x4 __attribute__((ext_vector_type(4)));
typedef u32 u32x2 __attribute__((ext_vector_type(2)));
typedef float f32x4 __attribute__((ext_vector_type(4)));
typedef float f32x2 __attribute__((ext_vector_type(2)));
typedef short s16x4 __attribute__((ext_vector_type(4)));
typedef __bf16 bf16x8 __attribute__((ext_vector_type(8)));
typedef __bf16 bf16x2 __attribute__((ext_vector_type(2)));
typedef __bf16 bf16x16 __attribute__((ext_vector_type(16)));
typedef u32 u32x8 __attribute__((ext_vector_type(8)));
typedef __attribute__((address_space(3))) unsigned char lds_t;

constexpr int D = 576, SK = 8192, WIN = 2048, FF = 4608, NQ = 4096, NMOD = 6 * D;
constexpr int NWG = 256, NT = 512, NG = 72;
constexpr int SPIN_CAP = 1 << 15;

constexpr int W_O = 0;
constexpr int W_XP = W_O + 262144;
constexpr int W_Y = W_XP + 12 * 16384;
constexpr int W_MS = W_Y + 81 * 16384;
constexpr int W_M = W_MS;
constexpr int W_H2 = W_M + 4 * 16384;
constexpr int W_PR = W_H2 + 3 * 16384;
constexpr int W_GB = W_PR + 2 * 16384;
constexpr int W_FL = W_GB + 18432;
constexpr int W_H = W_FL + 2048;
constexpr int W_Q = W_H + 36864;
constexpr int W_ST = W_Q + 131072;
constexpr int W_X1 = W_ST + 32768;
constexpr int W_END = W_X1 + 8 * 152 * 16;
constexpr int NPZ = 174, PZ = (W_MS / 16 + NPZ - 1) / NPZ;
static_assert(PZ <= 2 * NT, "poison split");

constexpr int L_W = 0;
constexpr int L_IMG = 73728;
constexpr int L_TILE = L_IMG + 9216;
constexpr int L_IM2 = 86016;
constexpr int L_OI = 65536;
constexpr int L_B = 147456;
constexpr int L_C = 155648;
constexpr int LDS_BYTES = 163840;

#define SCHED() __builtin_amdgcn_sched_barrier(0)
#define BAR() asm volatile("s_barrier" ::: "memory")
#define TL(i) do {} while (0)
#define TLOUT() do {} while (0)
#define T2(i, ...) do {} while (0)
#define WAITV_(n) do { asm volatile("s_waitcnt vmcnt(" #n ")" ::: "memory"); SCHED(); } while (0)
#define WAITV(n) WAITV_(n)
#define W0 3
#define W1 5
#define W2 7
#define TH1 15
#define TH2 40
#define STR_(x) #x
#define STR(x) STR_(x)
#define WAITD() do { asm volatile("s_waitcnt vmcnt(" STR(W2) ")\n\ts_cmpk_gt_i32 %0, " STR(TH2) " ; throttle\n\ts_cbranch_scc1 4 ; throttle\n\ts_waitcnt vmcnt(" STR(W1) ") ; throttle\n\t" \
    "s_cmpk_gt_i32 %0, " STR(TH1) " ; throttle\n\ts_cbranch_scc1 1 ; throttle\n\ts_waitcnt vmcnt(" STR(W0) ") ; throttle" :: "s"(__builtin_amdgcn_readfirstlane(lag)) : "memory", "scc"); SCHED(); } while (0)
#define PR 114
#define R1 34
#define G1 10
#define OH 175
#define OF 99
#define OL 75

__device__ __forceinline__ float lo16(u32 u) { return __uint_as_float(u << 16); }
__device__ __forceinline__ float hi16(u32 u) { return __uint_as_float(u & 0xffff0000u); }
__device__ __forceinline__ float b2f(u16 u) { return __uint_as_float((u32)u << 16); }
__device__ __forceinline__ u32 cvt2(float a, float b) { return __builtin_bit_cast(u32, __builtin_convertvector(f32x2{a, b}, bf16x2)); }
__device__ __forceinline__ float rbf(float v) { return __uint_as_float(cvt2(0.f, v)); }
__device__ __forceinline__ u32 sanf(float f) { u32 v = __float_as_uint(f); return v < 0xfffffffeu ? v : 0xfffffffeu; }
__device__ __forceinline__ f32x2 up2(u32 u) { return f32x2{lo16(u), hi16(u)}; }
__device__ __forceinline__ f32x2 rbf2(f32x2 v) { return f32x2{rbf(v.x), rbf(v.y)}; }
__device__ __forceinline__ void lds_barrier() { asm volatile("s_waitcnt lgkmcnt(0)\n\ts_barrier" ::: "memory"); }
__device__ __forceinline__ f32x4 mfma(u32x4 a, u32x4 b, f32x4 c) {
  return __builtin_amdgcn_mfma_f32_16x16x32_bf16(__builtin_bit_cast(bf16x8, a), __builtin_bit_cast(bf16x8, b), c, 0, 0, 0);
}
__device__ __forceinline__ f32x4 smf(u32x4 a, u32x8 b, f32x4 c, int idx) {
  return __builtin_amdgcn_smfmac_f32_16x16x64_bf16(__builtin_bit_cast(bf16x8, a), __builtin_bit_cast(bf16x16, b), c, idx, 0, 0);
}
__device__ __forceinline__ u32 plo(u32 a, u32 b) { return __builtin_amdgcn_perm(b, a, 0x05040100u); }
__device__ __forceinline__ u32 phi(u32 a, u32 b) { return __builtin_amdgcn_perm(b, a, 0x07060302u); }

extern "C" __device__ __attribute__((const)) unsigned long long llvm_dispatch_id() __asm("llvm.amdgcn.dispatch.id");
typedef __amdgpu_buffer_rsrc_t rsrc_t;
__device__ __forceinline__ u32x4 ld128(rsrc_t rs, int off) { return __builtin_amdgcn_raw_buffer_load_b128(rs, off, 0, 16); }
__device__ __forceinline__ void st128(rsrc_t rs, int off, u32x4 v) { __builtin_amdgcn_raw_buffer_store_b128(v, rs, off, 0, 16); }
__device__ __forceinline__ void st64(rsrc_t rs, int off, u32x2 v) { __builtin_amdgcn_raw_buffer_store_b64(v, rs, off, 0, 16); }
__device__ __forceinline__ void dma(const void* gbase, u32 voff, unsigned lds_dst) {
  unsigned keep;
  asm volatile("s_mov_b32 %0, m0\n\ts_mov_b32 m0, %3\n\ts_nop 2\n\tglobal_load_lds_dwordx4 %1, %2 nt\n\ts_mov_b32 m0, %0"
               : "=&s"(keep) : "v"(voff), "s"(gbase), "s"(lds_dst) : "memory");
}
__device__ __forceinline__ void dma_keep(const void* gbase, u32 voff, unsigned lds_dst) {
  unsigned keep;
  asm volatile("s_mov_b32 %0, m0\n\ts_mov_b32 m0, %3\n\ts_nop 2\n\tglobal_load_lds_dword %1, %2\n\ts_mov_b32 m0, %0"
               : "=&s"(keep) : "v"(voff), "s"(gbase), "s"(lds_dst) : "memory");
}
__device__ __forceinline__ void dma_sc(const void* gbase, u32 voff, unsigned lds_dst) {
  unsigned keep;
  asm volatile("s_mov_b32 %0, m0\n\ts_mov_b32 m0, %3\n\ts_nop 2\n\tglobal_load_lds_dwordx4 %1, %2 sc1\n\ts_mov_b32 m0, %0"
               : "=&s"(keep) : "v"(voff), "s"(gbase), "s"(lds_dst) : "memory");
}
__device__ __forceinline__ void ald128(u32x4& d, const void* gbase, u32 voff) { unsigned long long t; asm volatile("s_mov_b64 %1, %3\n\tglobal_load_dwordx4 %0, %2, %1" : "=v"(d), "=&s"(t) : "v"(voff), "s"(gbase) : "memory"); }
__device__ __forceinline__ void ald64(u32x2& d, const void* gbase, u32 voff) { unsigned long long t; asm volatile("s_mov_b64 %1, %3\n\tglobal_load_dwordx2 %0, %2, %1" : "=v"(d), "=&s"(t) : "v"(voff), "s"(gbase) : "memory"); }
__device__ __forceinline__ void ald128nt(u32x4& d, const void* gbase, u32 voff) { unsigned long long t; asm volatile("s_mov_b64 %1, %3\n\tglobal_load_dwordx4 %0, %2, %1 nt" : "=v"(d), "=&s"(t) : "v"(voff), "s"(gbase) : "memory"); }
__device__ __forceinline__ void ald128sc(u32x4& d, const void* gbase, u32 voff) { unsigned long long t; asm volatile("s_mov_b64 %1, %3\n\tglobal_load_dwordx4 %0, %2, %1 sc1" : "=v"(d), "=&s"(t) : "v"(voff), "s"(gbase) : "memory"); }
__device__ __forceinline__ u32x2 trd(const unsigned char* lds, int off) {
  return __builtin_bit_cast(u32x2, __builtin_amdgcn_ds_read_tr16_b64_v4i16((__attribute__((address_space(3))) s16x4*)((lds_t*)lds + off)));
}
#define nap(n) do { __builtin_amdgcn_s_sleep(n); asm volatile("" ::: "memory"); } while (0)
__device__ __forceinline__ float frcp(float v) { return __builtin_amdgcn_rcpf(v); }
__device__ __forceinline__ float fdiv(float a, float d, float r) { const float q = a * r; return fmaf(fmaf(-q, d, a), r, q); }
__device__ __forceinline__ float silu(float v) { const float d = 1.f + __expf(-v); const float r = frcp(d); return d < 3.0e38f ? fdiv(v, d, r) : v * r; }
template <int CTRL> __device__ __forceinline__ float dppf(float x) {
  return __builtin_bit_cast(float, __builtin_amdgcn_mov_dpp(__builtin_bit_cast(int, x), CTRL, 0xf, 0xf, true));
}
__device__ __forceinline__ u32 mxu(u32 a, u32 b) { return a > b ? a : b; }
__device__ __forceinline__ u32 mx4(u32x4 v) { return mxu(mxu(v[0], v[1]), mxu(v[2], v[3])); }
__device__ __forceinline__ float rl(float v, int l) { return __builtin_bit_cast(float, __builtin_amdgcn_readlane(__builtin_bit_cast(int, v), l)); }
__device__ __forceinline__ float wsum(float v) {
  v += dppf<0xB1>(v); v += dppf<0x4E>(v); v += dppf<0x141>(v); v += dppf<0x140>(v);
  return (rl(v, 0) + rl(v, 16)) + (rl(v, 32) + rl(v, 48));
}
__device__ __forceinline__ float up32(float v) { const u32 u = __float_as_uint(v); return __uint_as_float(__builtin_amdgcn_permlane32_swap(u, u, false, false)[1]); }
__device__ __forceinline__ float addf(u32 a, u32 b) { float r; asm("v_add_f32 %0, %1, %2" : "=v"(r) : "v"(a), "v"(b)); return r; }
__device__ __forceinline__ float fold32(float a, float b) { const auto r = __builtin_amdgcn_permlane32_swap(__float_as_uint(a), __float_as_uint(b), false, false); return addf(r[0], r[1]); }
__device__ __forceinline__ float fold16(float a, float b) { const auto r = __builtin_amdgcn_permlane16_swap(__float_as_uint(a), __float_as_uint(b), false, false); return addf(r[0], r[1]); }
__device__ __forceinline__ float xsum32(float v) { const u32 u = __float_as_uint(v); const auto r = __builtin_amdgcn_permlane32_swap(u, u, false, false); return __uint_as_float(r[0]) + __uint_as_float(r[1]); }
__device__ __forceinline__ float xsum16(float v) { const u32 u = __float_as_uint(v); const auto r = __builtin_amdgcn_permlane16_swap(u, u, false, false); return __uint_as_float(r[0]) + __uint_as_float(r[1]); }
__device__ __forceinline__ f32x4 half_sum(f32x4 d) {
  f32x4 r;
#pragma unroll
  for (int j = 0; j < 4; ++j) r[j] = d[j] + dppf<0x108>(up32(d[j]));
  return r;
}
__device__ __forceinline__ f32x2 half_sum2(f32x4 d) {
  f32x2 r;
#pragma unroll
  for (int j = 0; j < 2; ++j) {
    const auto s = __builtin_amdgcn_permlane32_swap(__float_as_uint(d[j]), __float_as_uint(d[j + 2]), false, false);
    r[j] = __uint_as_float(s[0]) + dppf<0x108>(__uint_as_float(s[1]));
  }
  return r;
}
__device__ __forceinline__ void unpack8(u32x4 u, float* f) {
#pragma unroll
  for (int j = 0; j < 4; ++j) { f[2 * j] = lo16(u[j]); f[2 * j + 1] = hi16(u[j]); }
}
__device__ __forceinline__ void ln_row(const float (&v)[16], bool has2, float& mean, float& den) {
  float s = 0.f;
#pragma unroll
  for (int i = 0; i < 8; ++i) s += v[i];
  if (has2) {
#pragma unroll
    for (int i = 8; i < 16; ++i) s += v[i];
  }
  s = wsum(s);
  const float mu = s * (1.f / D);
  mean = rbf(mu);
  float q = 0.f;
#pragma unroll
  for (int i = 0; i < 8; ++i) { const float t = v[i] - mu; q = fmaf(t, t, q); }
  if (has2) {
#pragma unroll
    for (int i = 8; i < 16; ++i) { const float t = v[i] - mu; q = fmaf(t, t, q); }
  }
  q = wsum(q);
  const float var = rbf(q * (1.f / D));
  den = rbf(sqrtf(rbf(var + 1e-5f)));
}
template <int J0 = 0, int J1 = 9>
__device__ __forceinline__ void slab_dma(const u16* base, int stride_b, unsigned lds_dst, int wave, int lane) {
  const u32 v0 = (lane >> 3) * stride_b + (((lane & 7) ^ ((wave & 1) * 4 + (lane >> 4))) << 4), v1 = v0 ^ 32u;
  const u32 v4 = wave >= 4 ? v1 : v0;
  const char* gb = (const char*)base + (size_t)wave * 8 * stride_b;
#pragma unroll
  for (int j = J0; j < J1; ++j) dma(gb + (size_t)j * 64 * stride_b, j < 4 ? v0 : j == 4 ? v4 : v1, lds_dst + (j * 8 + wave) * 1024);
}
__device__ __forceinline__ void slab_frags(const unsigned char* lds, int base, int wave, int lane, u32x4 (&wf)[9]) {
  const int g = lane >> 4, q = (lane & 15) >> 2, p = lane & 3, hf = p >> 1;
  const int a0 = base + (hf * 288 + 8 * g + q) * 128 + ((wave ^ ((g & 1) * 4 + (q >> 1)) ^ (hf * 2)) << 4) + (p & 1) * 8, a1 = (a0 ^ 32) + 512;
#pragma unroll
  for (int s = 0; s < 9; ++s) {
    const u32x2 lo = trd(lds, a0 + s * 4096), hi = trd(lds, a1 + s * 4096);
    wf[s] = u32x4{lo[0], lo[1], hi[0], hi[1]};
  }
}
__device__ __forceinline__ void img_frags(const unsigned char* lds, int img, int lane, u32x4 (&af)[9]) {
#pragma unroll
  for (int s = 0; s < 9; ++s) af[s] = *(const u32x4*)(lds + img + (s * 64 + lane) * 16);
}
__device__ __forceinline__ f32x4 gemv9(const u32x4 (&wf)[9], const u32x4 (&af)[9]) {
  f32x4 acc[3];
  SCHED();
#pragma unroll
  for (int s = 0; s < 9; ++s) acc[s % 3] = mfma(wf[s], af[s], s < 3 ? f32x4{0.f, 0.f, 0.f, 0.f} : acc[s % 3]);
  return (acc[0] + acc[1]) + acc[2];
}
__device__ __forceinline__ int img_off(int b, int k8) {
  const int hf = k8 >= 36 ? 1 : 0, kk = k8 - hf * 36;
  return ((kk >> 2) * 64 + (kk & 3) * 16 + hf * 8 + b) * 16;
}
__device__ __forceinline__ int im2_off(int b, int k8) { return (k8 * 8 + (b ^ (k8 & 7))) * 16; }
__device__ __forceinline__ u32 tag_x(u32x4 v, u32 e0, u32 e1) { return (v[2] ^ e0) | (v[3] ^ e1); }
__device__ __forceinline__ bool tag_ok(u32x4 v, u32 e0, u32 e1) { return tag_x(v, e0, e1) == 0u; }

__global__ void __launch_bounds__(NT, 2) fused_kernel(const u16* __restrict__ x, const u16* __restrict__ kc, const u16* __restrict__ c, const u16* __restrict__ vc,
                                                      const u16* __restrict__ wq, const u16* __restrict__ wo, const u16* __restrict__ w_r,
                                                      const u16* __restrict__ wg, const u16* __restrict__ wu, const u16* __restrict__ wd,
                                                      const u16* __restrict__ w_mod, unsigned char* ws, u16* out) {
  __shared__ __attribute__((aligned(16))) unsigned char lds[LDS_BYTES];
  SCHED();
  const int w = blockIdx.x, xcd = w & 7, idx = w >> 3;
  const int tid = threadIdx.x, lane = tid & 63, wave = __builtin_amdgcn_readfirstlane(tid >> 6);
  const int n = lane & 15, grp = lane >> 4;
  const unsigned lds_base = (unsigned)(uintptr_t)(lds_t*)lds;
  {
    unsigned long long pc0; unsigned keep; int co = (w >> 3) * 8192 - 27 * 8192 + lane * 128;
    asm volatile("s_getpc_b64 %0\n1:\tv_min_i32 %2, .Lcode_end - 1b, %2\n\ts_mov_b32 %1, m0\n\ts_mov_b32 m0, %3\n\ts_cmp_lt_i32 %4, 216\n\ts_cbranch_scc1 2 ; throttle\n\tglobal_load_lds_dword %2, %0\n\ts_mov_b32 m0, %1"
                 : "=&s"(pc0), "=&s"(keep), "+v"(co) : "s"(lds_base + 107520), "s"(wave ? 0 : w) : "memory", "scc");
  }
  const int s_att = w & 1, kvh = (w >> 1) & 15, b_att = w >> 5;
  size_t pairbase = ((size_t)(w >> 1) * SK + (SK - WIN)) * 256;
  int wpart = (wave & 3) * 4096 + s_att * 16384 + (wave >> 2) * 262144;
  int ko = wpart + (n >> 1) * 256 + (n & 1) * 64 + grp * 16;
  int eo[4] = {tid * 16, 8192 + lane * 16, wave * 1152 + lane * 16, wave * 1152 + (lane & 7) * 16};
  asm volatile("" : "+s"(pairbase), "+s"(wpart), "+v"(ko), "+v"(eo[0]), "+v"(eo[1]), "+v"(eo[2]), "+v"(eo[3]));
  SCHED();
  const char* kp = (const char*)kc + pairbase;
#define KLD(u) do { unsigned long long t_; asm volatile("s_mov_b64 %1, %3\n\tglobal_load_dwordx4 %0, %2, %1 offset:%4 nt" : "=v"(kb[u]), "=&s"(t_) : "v"(ko), "s"(kp + ((u) >> 2) * 32768), "i"((((u) >> 1) & 1) * 2048 + ((u) & 1) * 128) : "memory"); } while (0)
  u32x4 kb[32], vb[32];
  asm volatile("s_cmp_gt_i32 %12, 17\n\ts_cbranch_scc1 9 ; throttle\n\tglobal_load_dwordx4 %0, %4, %9 ; throttle\n\tglobal_load_dwordx4 %1, %5, %9 ; throttle\n\t"
               "global_load_dwordx4 %2, %6, %10 ; throttle\n\tglobal_load_dwordx4 %3, %7, %10 offset:1024 ; throttle\n\ts_branch 8 ; throttle\n\t"
               "global_load_dwordx4 %0, %8, %11 nt\n\tglobal_load_dwordx4 %1, %8, %11 offset:128 nt\n\tglobal_load_dwordx4 %2, %8, %11 offset:2048 nt\n\tglobal_load_dwordx4 %3, %8, %11 offset:2176 nt"
               : "=&v"(kb[0]), "=&v"(kb[1]), "=&v"(kb[2]), "=&v"(kb[3]) : "v"(eo[0]), "v"(eo[1]), "v"(eo[2]), "v"(eo[3]), "v"(ko), "s"(c), "s"(x), "s"(kp), "s"(w) : "memory", "scc");
  TL(32);
  SCHED();
  asm volatile("" : "+s"(vc), "+s"(w_r), "+s"(wu), "+s"(wd), "+s"(out), "+s"(x), "+s"(w_mod), "+s"(wg), "+s"(wq), "+s"(wo), "+s"(c), "+s"(ws));
  const rsrc_t rs = __builtin_amdgcn_make_buffer_rsrc(ws, 0, W_END, 0x00020000);
  const unsigned long long epoch = (llvm_dispatch_id() + 1ull) * 0x9E3779B97F4A7C15ull;
  const u32 e0 = (u32)epoch, e1 = (u32)(epoch >> 32);

  const int mw = idx < 6 ? idx * 8 + xcd : -1;
  const int md = mw < 18 ? mw : -1;
  const int ml = mw >= 18 ? mw : mw >= 0 && mw < 6 ? 48 + mw : -1;
  const int lnb = mw >= 6 && mw < 14 ? xcd : -1;
  const int qs = idx >= 6 && idx < 14 ? xcd * 8 + idx - 6 : -1;
  const int oj = idx >= 14 && idx < 23 ? idx - 14 : -1, op = xcd;
  const int gs = idx >= 23 ? xcd * 9 + idx - 23 : -1;

  {
    const char* vp = (const char*)vc + pairbase;
    const int vo = wpart + lane * 16;
#define VLD(u) do { unsigned long long t_; asm volatile("s_mov_b64 %1, %3\n\tglobal_load_dwordx4 %0, %2, %1 offset:%4 nt" : "=v"(vb[u]), "=&s"(t_) : "v"(vo), "s"(vp + ((u) >> 2) * 32768), "i"(((u) & 3) * 1024) : "memory"); } while (0)
#define TIE8(a, o) asm volatile("" : "+v"(a[o]), "+v"(a[(o) + 1]), "+v"(a[(o) + 2]), "+v"(a[(o) + 3]), "+v"(a[(o) + 4]), "+v"(a[(o) + 5]), "+v"(a[(o) + 6]), "+v"(a[(o) + 7]))
    const u32 tp0 = (u32)__builtin_amdgcn_s_memrealtime() + (gs >= 0 ? OF : mw >= 18 ? OL : OH);
    int lag = gs >= 0 || mw >= 18 ? 0 : 1000;
#define PACE(i) do { const int t_ = ((i) * PR >> 2) + ((i) >= R1 ? G1 : 0); lag = (int)((u32)__builtin_amdgcn_s_memrealtime() - tp0) - t_; \
      if (lag < 0) { _Pragma("nounroll") do __builtin_amdgcn_s_sleep(2); while ((int)((u32)__builtin_amdgcn_s_memrealtime() - tp0) < t_); } SCHED(); } while (0)
#define REP4(M, u, W) do { M(u); W; M((u) + 1); W; M((u) + 2); W; M((u) + 3); W; } while (0)
    static_assert(W2 <= 7, "the hand-placed waits assume it");
    const int pz = mw >= 18 ? mw - 18 : idx >= 14 ? w - 82 : -1;
#define POISON() do { if (pz >= 0) { _Pragma("unroll") for (int i = 0; i < 2; ++i) { const int g_ = pz * PZ + i * NT + tid; \
      if (i * NT + tid < PZ && g_ < W_MS / 16) st128(rs, g_ * 16, u32x4{0xffffffffu, 0xffffffffu, 0xffffffffu, 0xffffffffu}); } } SCHED(); } while (0)
#define SILU_IMAGE(c0, c1, both) do { float f[8]; unpack8(c0, f); u32x4 o; \
        _Pragma("unroll") for (int j = 0; j < 4; ++j) o[j] = cvt2(silu(f[2 * j]), silu(f[2 * j + 1])); \
        const int b_ = tid / 72, ch_ = tid - b_ * 72; \
        if (both) *(u32x4*)(lds + L_IMG + img_off(b_, ch_)) = o; \
        *(u32x4*)(lds + L_IM2 + im2_off(b_, ch_)) = o; \
        if (wave == 0) { unpack8(c1, f); \
          _Pragma("unroll") for (int j = 0; j < 4; ++j) o[j] = cvt2(silu(f[2 * j]), silu(f[2 * j + 1])); \
          if (both) *(u32x4*)(lds + L_IMG + img_off(7, 8 + lane)) = o; \
          *(u32x4*)(lds + L_IM2 + im2_off(7, 8 + lane)) = o; } } while (0)
    if (md >= 0) {
      u32x2 xc;
      int ho = W_H + img_off(n & 7, (md >= 9 ? md - 9 : md) * 8 + wave) * 2 + (grp & 1) * 16;
      ald64(xc, x, (n & 7) * 1152 + ((md >= 9 ? md - 9 : 0) * 64 + wave * 8 + (grp & 1) * 4) * 2);
      TL(35);
      BAR();
      float* st = (float*)(lds + L_TILE + 1024);
      if (wave < 4) {
        slab_dma(w_mod + md * 64, NMOD * 2, lds_base + L_W, wave, lane);
        asm volatile("s_waitcnt vmcnt(9)" ::: "memory");
      } else asm volatile("s_waitcnt vmcnt(0)" ::: "memory");
      asm volatile("s_nop 0 ; both branches above cover V" : "+v"(kb[0]), "+v"(kb[1]), "+v"(kb[2]), "+v"(kb[3]), "+v"(xc) :: "memory");
      {
        SILU_IMAGE(kb[0], kb[1], true);
        float v[16]; unpack8(kb[2], v); unpack8(kb[3], v + 8);
        float mean, den; ln_row(v, lane < 8, mean, den);
        if (lane == 0) { st[wave] = mean; st[8 + wave] = den; }
      }
      if (wave >= 4) slab_dma(w_mod + md * 64, NMOD * 2, lds_base + L_W, wave, lane);
      lds_barrier();
      REP4(KLD, 0, );
      u32x4 wf[9], af[9];
      img_frags(lds, L_IMG, lane, af);
      const float mean = st[n & 7], rd = frcp(st[8 + (n & 7)]);
      float ln[4] = {rbf(rbf(lo16(xc[0]) - mean) * rd), rbf(rbf(hi16(xc[0]) - mean) * rd), rbf(rbf(lo16(xc[1]) - mean) * rd), rbf(rbf(hi16(xc[1]) - mean) * rd)};
#pragma unroll
      for (int s = 0; s < 9; ++s) asm volatile("" : "+v"(af[s]));
      asm volatile("" : "+v"(ln[0]), "+v"(ln[1]), "+v"(ln[2]), "+v"(ln[3]), "+v"(ho));
      WAITV(4); BAR(); SCHED();
      slab_frags(lds, L_W, wave, lane, wf);
      const f32x4 s = half_sum(gemv9(wf, af));
      f32x4 r = s;
      if (md >= 9) {
#pragma unroll
        for (int j = 0; j < 4; ++j) r[j] = ln[j] * rbf(1.f + rbf(s[j]));
      }
      if (n < 8 && grp < 2) st128(rs, ho + (md >= 9 ? 18432 : 0), u32x4{cvt2(r[0], r[1]), cvt2(r[2], r[3]), e0, e1});
      SCHED();
      TL(1);
      BAR();
      KLD(4); KLD(5); WAITV(W2);
      KLD(6); WAITV(W2); KLD(7); WAITV(W2); KLD(8); WAITV(W2); KLD(9); WAITV(W2); KLD(10); WAITV(W2);
      KLD(11); WAITV(W2); REP4(KLD, 12, WAITV(W2)); REP4(KLD, 16, WAITV(W2));
      REP4(KLD, 20, WAITV(W2)); KLD(24); WAITV(W2); KLD(25);
    } else {
      constexpr int L_LAND = 98304;
      const unsigned land = lds_base + L_LAND + wave * 6144;
      const unsigned char* lp = lds + L_LAND + wave * 6144 + lane * 16;
      const int sl_ = (3 - (int)((uintptr_t)wq >> 7)) & 7;
      const int s2_ = (63 - (int)((uintptr_t)wq >> 7)) & 63;
      const bool slowq = qs >= 0 && ((qs & 7) == sl_ || qs == s2_);
      if (gs >= 0 && wave == 3 && xcd == (s2_ >> 3)) dma_keep((const char*)wq + (size_t)(idx - 23) * 64 * 8192 + s2_ * 128, lane * 8192, land);
      if (gs >= 0 && wave == 0) dma_keep((const char*)wq + (size_t)(idx - 23) * 64 * 8192 + xcd * 1024 + sl_ * 128, lane * 8192, land);
      if (gs >= 0 && (wave == 1 || (wave == 2 && xcd < 2))) dma_keep((const char*)w_mod + (size_t)((idx - 23) * 64 + 32) * (NMOD * 2) + xcd * 128 + (wave - 1) * 2048, (lane & 31) * (NMOD * 2) + (wave == 1 ? (lane >> 5) * 1024 : 0), land);
      POISON();
      const u32 v0 = (lane >> 3) * 1152 + (((lane & 7) ^ ((lane >> 4) & 2)) << 4);
      const char* gb = (const char*)(wo + (size_t)(op * 512 + wave * 64) * D + oj * 64);
      if (ml >= 0) { dma_sc(c, tid * 16, land); dma_sc(c, (512 + lane) * 16, land + 1024); }
      if (oj < 0 && qs < 0) {
        const int i_ = (gs >= 0 ? idx - 19 : idx - 2) * 8 + wave, o_ = (i_ >> 6) * 16384 + (i_ & 63) * 128 + (((xcd >> 2) ^ wave) & 1) * 8192;
        unsigned long long t0, t1, t2; u32 d_;
        asm volatile("s_memtime %0\n\ts_waitcnt lgkmcnt(0)\n\ts_load_dword %3, %4, %5\n\ts_waitcnt lgkmcnt(0)\n\ts_memtime %1\n\ts_waitcnt lgkmcnt(0)\n\t"
                     "s_load_dword %3, %4, %6\n\ts_waitcnt lgkmcnt(0)\n\ts_memtime %2\n\ts_waitcnt lgkmcnt(0)"
                     : "=&s"(t0), "=&s"(t1), "=&s"(t2), "=&s"(d_) : "s"(ws + W_PR), "s"(o_), "s"(o_ ^ 8192) : "memory");
        const int a_ = (int)((u32)t1 - (u32)t0), b_ = (int)((u32)t2 - (u32)t1);
        if (lane == 0) *(int*)(lds + L_B + 192 + wave * 4) = (wave & 1) ? b_ - a_ : a_ - b_;
        TL(58);
      }
#define WK(j) do { if (oj >= 0) { if ((j) < 8) dma(gb + (j) * 8 * 1152, v0 ^ (((j) & 1) * 64), lds_base + L_W + (wave * 8 + (j)) * 1024); } \
                   else if (slowq) { if ((j) == 8) slab_dma(wq + qs * 64, NQ * 2, lds_base + L_W, wave, lane); } \
                   else if (qs >= 0 && (j) < 9) slab_dma<(j) < 9 ? (j) : 0, (j) < 9 ? (j) + 1 : 0>(wq + qs * 64, NQ * 2, lds_base + L_W, wave, lane); \
                   else if (qs < 0 && (j) >= 4) PACE(j); \
                   if ((j) >= 4) KLD(j); if ((j) == 4) TL(33); WAITD(); if ((j) == 5) TL(34); } while (0)
      WK(0); WK(1); WK(2); WK(3);
      WK(4); WK(5);
      WK(6); WK(7);
      if (oj < 0 && qs < 0) WAITV(4);
      TL(24);
      if (ml >= 0) { const u32x4 c0 = *(const u32x4*)lp, c1 = *(const u32x4*)(lp + 1024); SILU_IMAGE(c0, c1, false); }
      lds_barrier();
      if (pz >= 0 && tid == 448) {
        const u32x4 v0_ = *(const u32x4*)(lds + L_B + 192), v1_ = *(const u32x4*)(lds + L_B + 208);
        const int d_ = (int)(v0_[0] + v0_[1] + v0_[2] + v0_[3] + v1_[0] + v1_[1] + v1_[2] + v1_[3]);
        st64(rs, W_FL + pz * 8, u32x2{e0 ^ (oj >= 0 ? pz & 1 : d_ > 0), e1});
      }
      SCHED();
      WK(8); WK(9); WK(10); WK(11);
      TL(25);
      PACE(12); REP4(KLD, 12, WAITD());
      TL(26);
      const int hg0 = (wave * 144 + lane) * 16, hg2 = (wave * 144 + 128 + n) * 16;
#define HREQ() do { dma_sc(ws + W_H, hg0, land); dma_sc(ws + W_H + 18432, hg0, land + 1024); dma_sc(ws + W_H + 1024, hg0, land + 2048); \
                    dma_sc(ws + W_H + 18432 + 1024, hg0, land + 3072); dma_sc(ws + W_H, hg2, land + 4096); dma_sc(ws + W_H + 18432, hg2, land + 5120); } while (0)
#define HGET() do { _Pragma("unroll") for (int i = 0; i < 6; ++i) hq[i] = *(const u32x4*)(lp + i * 1024); } while (0)
#define HQOK() __all((tag_x(hq[0], e0, e1) | tag_x(hq[1], e0, e1) | tag_x(hq[2], e0, e1) | tag_x(hq[3], e0, e1) | tag_x(hq[4], e0, e1) | tag_x(hq[5], e0, e1)) == 0u)
#define HOK() __all((tag_x(hp[0], e0, e1) | tag_x(hp[1], e0, e1) | tag_x(hp[2], e0, e1)) == 0u)
      if (qs >= 0) HREQ();
      PACE(16); REP4(KLD, 16, WAITD());
      if (qs >= 0) WAITV(4);
      TL(27);
      PACE(20); KLD(20); KLD(21);
      if (qs >= 0) {
        u32x4 hq[6]; HGET();
        _Pragma("nounroll") for (int sp = 0; sp < SPIN_CAP && !HQOK(); ++sp) { nap(16); HREQ(); WAITV(0); HGET(); }
        TL(2);
#define HSUM(a, b) cvt2(lo16(a) + lo16(b), hi16(a) + hi16(b))
        *(u32x2*)(lds + L_IMG + hg0 / 2) = u32x2{HSUM(hq[0][0], hq[1][0]), HSUM(hq[0][1], hq[1][1])};
        *(u32x2*)(lds + L_IMG + 512 + hg0 / 2) = u32x2{HSUM(hq[2][0], hq[3][0]), HSUM(hq[2][1], hq[3][1])};
        if (lane < 16) *(u32x2*)(lds + L_IMG + hg2 / 2) = u32x2{HSUM(hq[4][0], hq[5][0]), HSUM(hq[4][1], hq[5][1])};
      } else WAITD();
      KLD(22); KLD(23);
      if (qs >= 0) {
        u32x4 wf[9], af[9];
        lds_barrier();
        slab_frags(lds, L_W, wave, lane, wf);
        img_frags(lds, L_IMG, lane, af);
        const f32x4 s = half_sum(gemv9(wf, af));
        if (n < 8 && grp < 2) *(u32x2*)(lds + L_TILE + n * 128 + wave * 16 + grp * 8) = u32x2{cvt2(s[0], s[1]), cvt2(s[2], s[3])};
      } else WAITD();
      PACE(24); KLD(24); KLD(25);
      if (qs >= 0) {
        lds_barrier();
        if (wave < 2) {
          const u32x2 t2 = *(const u32x2*)(lds + L_TILE + tid * 8);
          st128(rs, W_Q + ((tid >> 4) * 1024 + qs * 16 + n) * 16, u32x4{t2[0], t2[1], e0, e1});
        }
        TL(3);
      }
      SCHED();
    }
    const unsigned char* qg = ws + W_Q + (b_att * 1024 + kvh * 64) * 16;
    KLD(26); WAITD(); KLD(27); WAITD(); PACE(28); KLD(28); WAITD(); KLD(29); WAITD();
    PACE(30); KLD(30); WAITD(); KLD(31); WAITD();
    TL(28);
    if (gs >= 0 && wave >= 2 && wave < 6) {
      const u16* mat = (wave & 1) ? wg : wu;
      const int g0 = ((3 - (int)((uintptr_t)mat >> 7) - xcd * 9) & 7) + (wave >= 4 ? 8 : 0);
      if (g0 < 9) dma_keep(mat, ((idx - 23) * 29 + min(lane, 28)) * 9216 + (xcd * 9 + g0) * 128, lds_base + L_B + 4096 + wave * 256);
    }
    PACE(32); VLD(0); WAITD(); VLD(1); WAITD(); PACE(34); VLD(2); WAITD(); VLD(3); WAITD();
#define QLOOK() do { if (wave == 1) dma_sc(qg, lane * 16, lds_base + L_B + 1024); } while (0)
    QLOOK();
#define FLOOK(l) do { dma_sc(ws + W_FL, (l) * 16, lds_base + L_B + 2048); dma_sc(ws + W_FL, 1024 + min(l, NPZ / 2 - 65) * 16, lds_base + L_B + 3072); } while (0)
    if (wave == 2) FLOOK(lane);
    PACE(36); VLD(4); WAITD(); VLD(5); WAITD(); PACE(38); VLD(6); WAITD(); VLD(7); WAITD();
    QLOOK();
    TL(29);
    PACE(40); VLD(8); WAITD(); VLD(9); WAITD(); PACE(42); QLOOK(); VLD(10); WAITD(); VLD(11); WAITD();
    TIE8(kb, 0); TIE8(kb, 8); TIE8(kb, 16); TIE8(kb, 24);
    PACE(44); QLOOK(); VLD(12); VLD(13);
    u32x4 qf[2];
    {
      const u32x4* ql = (const u32x4*)(lds + L_B + 1024 + ((n >> 3) * 32 + (n & 1) * 8 + grp * 2) * 16);
      u32x4 g[4];
      _Pragma("nounroll") for (int sp = 0; sp < SPIN_CAP; ++sp) {
        g[0] = ql[0]; g[1] = ql[1]; g[2] = ql[16]; g[3] = ql[17];
        if (__all((tag_x(g[0], e0, e1) | tag_x(g[1], e0, e1) | tag_x(g[2], e0, e1) | tag_x(g[3], e0, e1)) == 0u)) break;
        if (wave == 1 && sp >= 6) { dma_sc(qg, lane * 16, lds_base + L_B + 1024); WAITV(0); } else nap(4);
      }
      asm volatile("" ::: "memory");
      g[0] = ql[0]; g[1] = ql[1]; g[2] = ql[16]; g[3] = ql[17];
      qf[0] = u32x4{g[0][0], g[0][1], g[1][0], g[1][1]};
      qf[1] = u32x4{g[2][0], g[2][1], g[3][0], g[3][1]};
    }
    TL(4);
    constexpr float SCALE = 0.088388347648318440f;
    f32x2 sc[16];
    float mx = -INFINITY;
#pragma unroll
    for (int T = 0; T < 16; ++T) {
      f32x4 acc = mfma(kb[T * 2], qf[0], f32x4{0.f, 0.f, 0.f, 0.f});
      acc = mfma(kb[T * 2 + 1], qf[1], acc);
      sc[T] = f32x2{acc[0] + dppf<0xB1>(acc[1]), acc[2] + dppf<0xB1>(acc[3])};
      if (T == 7 || T == 15) { SCHED(); if (T == 7) VLD(14); else VLD(15); SCHED(); }
    }
#pragma unroll
    for (int T = 0; T < 16; ++T)
#pragma unroll
      for (int j = 0; j < 2; ++j) { const float v = rbf(rbf(sc[T][j]) * SCALE); sc[T][j] = v; mx = fmaxf(mx, v); }
    SCHED(); VLD(16); SCHED();
    mx = fmaxf(mx, __shfl_xor(mx, 16));
    mx = fmaxf(mx, __shfl_xor(mx, 32));
    float ls = 0.f;
#pragma unroll
    for (int T = 0; T < 16; ++T)
#pragma unroll
      for (int j = 0; j < 2; ++j) { sc[T][j] = __expf(sc[T][j] - mx); ls += sc[T][j]; }
    SCHED(); VLD(17); SCHED();
    ls += __shfl_xor(ls, 16);
    ls += __shfl_xor(ls, 32);
    float* stat = (float*)(lds + L_B);
    if (lane == 0 || lane == 8) *(f32x2*)(stat + (wave * 2 + (lane >> 3)) * 2) = f32x2{mx, ls};
    SCHED(); VLD(18); VLD(19); SCHED();
    if (wave == 2) {
      int l2 = lane; asm volatile("" : "+v"(l2));
      u32x4 a, b;
      _Pragma("nounroll") for (int sp = 0; sp < SPIN_CAP; ++sp) {
        a = *(const u32x4*)(lds + L_B + 2048 + l2 * 16); b = *(const u32x4*)(lds + L_B + 3072 + l2 * 16);
        if (__all(((((a[0] ^ e0) | (a[2] ^ e0) | (b[0] ^ e0) | (b[2] ^ e0)) & ~1u) | (a[1] ^ e1) | (a[3] ^ e1) | (b[1] ^ e1) | (b[3] ^ e1)) == 0u)) break;
        nap(1); FLOOK(l2); WAITV(0);
      }
      const unsigned long long mk = (1ull << (NPZ / 2 - 64)) - 1;
      const int cnt = __popcll(__ballot(a[0] != e0)) + __popcll(__ballot(a[2] != e0)) + __popcll(__ballot(b[0] != e0) & mk) + __popcll(__ballot(b[2] != e0) & mk);
      if (l2 == 0) *(int*)(lds + L_B + 128) = cnt > NPZ / 2 ? 8192 : 0;
    }
    lds_barrier();
    const int flip = __builtin_amdgcn_readfirstlane(*(const int*)(lds + L_B + 128));
    float M = -INFINITY, L = 0.f;
    {
      f32x2 t[8];
#pragma unroll
      for (int q = 0; q < 8; ++q) { t[q] = *(const f32x2*)(stat + (q * 2 + (n >> 3)) * 2); M = fmaxf(M, t[q].x); }
#pragma unroll
      for (int q = 0; q < 8; ++q) L = fmaf(__expf(t[q].x - M), t[q].y, L);
    }
    if (tid >= 4 && tid < 12) st128(rs, W_ST + (w * 8 + lane - 4) * 16, u32x4{__float_as_uint(M), __float_as_uint(L), e0, e1});
    SCHED();
    TL(30);
    PACE(52); VLD(20); WAITD(); VLD(21); WAITD();
    const int po = W_ST + ((w ^ 1) * 8 + (n >> 3) * 4) * 16;
    int l1 = lane; asm volatile("" : "+v"(l1));
    u32 km[4] = {(l1 & 4) ? 0u : 0xffffu, (l1 & 4) ? 0xffffu : 0u, (l1 & 4) ? 0u : 0xffff0000u, (l1 & 4) ? 0xffff0000u : 0u};
    int px = (l1 & 3) * 0x5555;
    asm volatile("" : "+v"(km[0]), "+v"(km[1]), "+v"(km[2]), "+v"(km[3]), "+v"(px));
    PACE(54);
    VLD(22); WAITD(); VLD(23); WAITD();
    u32x4 ps, ps1, ps2, ps3;
    ald128sc(ps, ws, po);
    PACE(56);
    VLD(24); WAITD(); VLD(25); WAITD();
    TL(31);
    ald128sc(ps1, ws, po);
    PACE(58);
    VLD(26); WAITD(); VLD(27); WAITD();
    ald128sc(ps2, ws, po);
    VLD(28); VLD(29); WAITV(8);
    asm volatile("" : "+v"(ps));
    TIE8(vb, 0); TIE8(vb, 8); TIE8(vb, 16);
    ald128sc(ps3, ws, po);
    VLD(30); VLD(31);
    if (!__all(tag_ok(ps, e0, e1))) {
      WAITV(8);
      asm volatile("" : "+v"(ps1));
      ps = ps1;
      if (!__all(tag_ok(ps, e0, e1))) {
        WAITV(5);
        asm volatile("" : "+v"(ps2));
        ps = ps2;
        if (!__all(tag_ok(ps, e0, e1))) {
          WAITV(2);
          asm volatile("" : "+v"(ps3));
          ps = ps3;
          if (!__all(tag_ok(ps, e0, e1))) {
            int sp = 0;
            _Pragma("nounroll") do { nap(1); ps = ld128(rs, po); } while (!__all(tag_ok(ps, e0, e1)) && ++sp < SPIN_CAP);
          }
        }
      }
    }
    float fac;
    {
      const float M2 = __uint_as_float(ps[0]), L2 = __uint_as_float(ps[1]);
      const float Mg = fmaxf(M, M2);
      const float Lg = __expf(M - Mg) * L + __expf(M2 - Mg) * L2;
      fac = __expf(mx - Mg) / Lg;
    }
    TL(5);
    u32x4 pf[16];
    u32 pp[16];
#pragma unroll
    for (int T = 0; T < 16; ++T) pp[T] = cvt2(sc[T][0] * fac, sc[T][1] * fac);
    SCHED();
#pragma unroll
    for (int T = 0; T < 16; ++T) {
      const u32 p = (u32)__builtin_amdgcn_mov_dpp((int)pp[T], 0xa0, 0xf, 0xf, true);
      pf[T] = u32x4{p & km[0], p & km[1], p & km[2], p & km[3]};
    }
    f32x4 oa = {0.f, 0.f, 0.f, 0.f};
#define PV(T) oa = smf(pf[T], __builtin_shufflevector(vb[2 * (T)], vb[2 * (T) + 1], 0, 1, 2, 3, 4, 5, 6, 7), oa, px)
#pragma unroll
    for (int T = 0; T < 12; ++T) PV(T);
    SCHED();
    int t2_ = threadIdx.x; asm volatile("" : "+v"(t2_));
    const int tid = t2_, lane = tid & 63, n = lane & 15, grp = lane >> 4;
    int oo = W_O + (kvh & 7) * 32768 + (b_att >> 2) * 16384 + ((kvh >> 3) * 8192 ^ flip) + (b_att & 3) * 2048 + s_att * 1024 + (((grp >> 1) * 2 + (n >> 3)) * 16 + (grp & 1) * 8 + (n & 7)) * 16;
    int mw_ = L_C + tid * 16;
    asm volatile("" : "+v"(oo), "+v"(mw_), "+v"(pf[12]), "+v"(pf[13]), "+v"(pf[14]), "+v"(pf[15]));
    constexpr int L_H2 = L_W;
    constexpr int L_ACT = L_C + 4608;
    int ia, ra, ix, yo[2], fo[7], tw, tr_, tr4;
    const u32 gv = (lane >> 3) * (FF * 2) + (((lane & 7) ^ ((wave & 1) * 4 + ((lane >> 4) & 2))) << 4);
#define GU(j) do { const size_t o_ = ((size_t)((j) * 64 + wave * 8) * FF + gs * 64) * 2; dma((const char*)wg + o_, gv, lds_base + L_W + ((j) * 8 + wave) * 1024); \
                   dma((const char*)wu + o_, gv ^ 16u, lds_base + L_W + 73728 + ((j) * 8 + wave) * 1024); } while (0)
#define ML(j) dma((const char*)w_mod + ((size_t)((j) * 64 + wave * 8) * NMOD + ml * 64) * 2, (lane >> 3) * (NMOD * 2) + (((lane & 7) ^ ((wave & 1) * 4 + ((lane >> 4) & 2))) << 4), lds_base + L_W + ((j) * 8 + wave) * 1024)
    const bool late = gs >= 0 || mw >= 18;
    if (late) {
      BAR();
      if (gs >= 0) { GU(0); GU(1); } else { ML(0); ML(1); ML(2); ML(3); }
      {
    const int b = wave;
    ia = L_H2 + grp * 1024 + ((n >> 2) * 16 + (n & 2) * 4 + (b ^ (n >> 1))) * 16 + (n & 1) * 8;
    ra = L_H2 + (grp * 16 + (n & 8) + ((n & 7) ^ (grp * 2 + (n >> 3)))) * 16; ix = n < 8 ? 0x4444 : 0xeeee;
    tw = L_ACT + ((grp & 1) * 4 + (grp >> 1) * 2 + (n >> 3)) * 144 + (wave >> 1) * 32 + (n & 2) * 8 + (wave & 1) * 8 + (n & 4) + (n & 1) * 2; tr_ = L_ACT + (n & 7) * 144 + grp * 32 + (n & 8) * 2;
    tr4 = L_ACT + ((n & 7) ^ (wave >> 2)) * 144 + grp * 32 + (n & 8) * 2;
    {
      const int g0 = wave * 4 + (grp & 2), g1 = 32 + (grp & 2) + (wave >> 2), yb = W_Y + ((grp & 1) * 8192 ^ flip) + (gs & 31) * 256;
      yo[0] = yb + (gs < 64 ? g0 * 32768 + (gs >> 5) * 16384 : (72 + (g0 >> 2)) * 16384 + (g0 & 3) * 2048) + n * 16;
      yo[1] = yb + (gs < 64 ? g1 * 32768 + (gs >> 5) * 16384 : (72 + (g1 >> 2)) * 16384 + (g1 & 3) * 2048) + (wave & 3) * 64 + n * 4;
    }
    {
      const int st = idx - 23;
      fo[0] = W_GB + (xcd * 144 + st * 16 + (lane >> 2)) * 16;
      fo[1] = W_X1 + (xcd * 152 + st * 16 + (lane >> 2)) * 16; fo[2] = W_X1 + (xcd * 152 + 144) * 16;
      fo[3] = W_Y + ((xcd >> 2) * 8192 ^ flip) + tid * 16;
      fo[4] = L_H2 + (st * 64 + (lane & 56) + (xcd ^ (lane >> 3))) * 16 + (lane & 7) * 2;
      fo[5] = L_C + (lane & 3) * 64 + (st < 8 ? (lane >> 3) + (lane & 4) * 2 : lane >> 2) * 4;
      fo[6] = (xcd * D + st * 64 + lane) * 2;
    }
    asm volatile("" : "+v"(ia), "+v"(ra), "+v"(ix), "+v"(tw), "+v"(tr_), "+v"(tr4), "+v"(yo[0]), "+v"(yo[1]),
                 "+v"(fo[0]), "+v"(fo[1]), "+v"(fo[2]), "+v"(fo[3]), "+v"(fo[4]), "+v"(fo[5]), "+v"(fo[6]));
      }
      WAITV(6);
    } else WAITV(2);
    asm volatile("s_nop 0 ; both branches above cover V up to the last two" : "+v"(vb[24]), "+v"(vb[25]), "+v"(vb[26]), "+v"(vb[27]), "+v"(vb[28]), "+v"(vb[29]));
    asm volatile("" :: "v"(ps1), "v"(ps2), "v"(ps3));
    PV(12); PV(13); PV(14);
    SCHED();
    if (late) WAITV(4); else WAITV(0);
    T2(56, );
    asm volatile("s_nop 0 ; both branches above cover V" : "+v"(vb[30]), "+v"(vb[31]));
    PV(15);
    TL(6);
    *(f32x4*)(lds + mw_) = oa;
    T2(54, );
    lds_barrier();
    T2(55, );
    if (wave == 0) {
      f32x4 m[8];
#pragma unroll
      for (int q = 0; q < 8; ++q) m[q] = *(const f32x4*)(lds + mw_ + q * 1024);
      SCHED();
      const f32x4 o = ((m[0] + m[1]) + (m[2] + m[3])) + ((m[4] + m[5]) + (m[6] + m[7]));
      st128(rs, oo, u32x4{sanf(o[0]), sanf(o[1]), sanf(o[2]), sanf(o[3])});
    }
    TL(7); SCHED();
  if (gs >= 0) {
    const int b = wave;
    BAR(); SCHED();
    u32x8 bw[9];
    u32x8 wd8[5];
    u32x4 gq, xq, rq;
    const int ga0 = L_W + ((lane >> 1) & 1) * 73728 + ((grp >> 1) * 16 + (grp & 1) * 4 + ((n >> 2) & 1) + (n & 8)) * 128 + ((wave ^ ((grp & 1) * 2 + ((n >> 1) & 4)) ^ ((lane >> 1) & 1)) << 4) + (lane & 1) * 8;
#define GUF(S0, S1) do { _Pragma("unroll") for (int s = S0; s < S1; ++s) { const int a_ = ga0 + (s >> 2) * 32768, o_ = (s & 3) * 8192; \
      const u32x2 r0 = trd(lds, a_ + o_), r1 = trd(lds, a_ + o_ + 256), r2 = trd(lds, a_ + o_ + 4096), r3 = trd(lds, a_ + o_ + 4352); \
      bw[s] = u32x8{r0[0], r0[1], r1[0], r1[1], r2[0], r2[1], r3[0], r3[1]}; } } while (0)
    {
      u32x4 rd[8];
      GU(2); GU(3);
      TL(23);
      BAR();
      const char* db = (const char*)(wd + (size_t)gs * 64 * D + wave * 64);
      const u32 dv = (n >> 3) * 36864 + grp * 9216 + (n & 7) * 16;
#pragma unroll
      for (int i = 0; i < 8; ++i) ald128nt(rd[i], db + i * 1152, dv);
      {
        const int r = lane >> 3;
        dma((const char*)(wd + (size_t)(gs * 64 + wave * 8) * D + 512), r * 1152 + (((lane & 7) ^ (r & 2) ^ ((wave & 1) * 4)) << 4), lds_base + L_B + wave * 1024);
      }
      TL(19);
      asm volatile("s_waitcnt vmcnt(9)\n\ts_barrier" ::: "memory");
      TL(22);
      GUF(0, 4);
      SCHED();
      GU(4); GU(5); GU(6); GU(7); GU(8);
      TL(20);
      asm volatile("s_waitcnt vmcnt(11)" : "+v"(rd[0]), "+v"(rd[1]), "+v"(rd[2]), "+v"(rd[3]), "+v"(rd[4]), "+v"(rd[5]), "+v"(rd[6]), "+v"(rd[7]) :: "memory");
#pragma unroll
      for (int j = 0; j < 4; ++j)
#pragma unroll
        for (int t = 0; t < 4; ++t) {
          const u32 a = (t & 1) ? phi(rd[2 * j][t >> 1], rd[2 * j + 1][t >> 1]) : plo(rd[2 * j][t >> 1], rd[2 * j + 1][t >> 1]);
          const u32 b_ = (t & 1) ? phi(rd[2 * j][2 + (t >> 1)], rd[2 * j + 1][2 + (t >> 1)]) : plo(rd[2 * j][2 + (t >> 1)], rd[2 * j + 1][2 + (t >> 1)]);
          wd8[t][j] = (u32)__builtin_amdgcn_update_dpp((int)a, (int)b_, 0x128, 0xf, 0xc, false);
          wd8[t][4 + j] = (u32)__builtin_amdgcn_update_dpp((int)b_, (int)a, 0x128, 0xf, 0x3, false);
        }
#pragma unroll
      for (int t = 0; t < 4; ++t) asm volatile("" : "+v"(wd8[t]));
      TL(21);
    }
    f32x4 dd[5] = {};
    int ys = gs < 64 ? 32768 : 2048;
    asm volatile("" : "+{v[176:179]}"(dd[0]), "+{v[180:183]}"(dd[1]), "+{v[184:187]}"(dd[2]), "+{v[188:191]}"(dd[3]), "+{v[192:195]}"(dd[4]), "+s"(ys));
    f32x4 d = {0.f, 0.f, 0.f, 0.f};
    int h0 = W_H2 + b / 3 * 16384 + ((xcd >> 2) * 8192 ^ flip) + b % 3 * 2304, h2o = h0 + (128 + n) * 16;
    h0 += lane * 16;
    int a0 = L_B + (8 * grp + (n >> 2)) * 128 + ((((wave & 3) * 2 + ((lane >> 1) & 1)) ^ ((n >> 2) & 2) ^ ((grp & 1) * 4)) << 4) + (lane & 1) * 8;
    asm volatile("" : "+v"(d), "+v"(h0), "+v"(h2o), "+v"(a0));
#define WD4() do { const u32x2 r0 = trd(lds, a0), r1 = trd(lds, a0 + 512), r2 = trd(lds, a0 + 4096), r3 = trd(lds, a0 + 4608); \
      wd8[4] = u32x8{r0[0], r0[1], r1[0], r1[1], r2[0], r2[1], r3[0], r3[1]}; } while (0)
    {
      u32x4 hp[3];
      asm volatile("s_waitcnt vmcnt(0) lgkmcnt(0)\n\ts_barrier" ::: "memory");
      hp[0] = ld128(rs, h0); hp[1] = ld128(rs, h0 + 1024); hp[2] = ld128(rs, h2o);
      GUF(4, 9); WD4();
      TL(12);
#pragma unroll
      for (int s = 0; s < 9; ++s) asm volatile("" : "+v"(bw[s]));
      asm volatile("" : "+v"(wd8[4]));
      if (!HOK()) {
        int sp = 0;
        _Pragma("nounroll") do { nap(2); hp[0] = ld128(rs, h0); hp[1] = ld128(rs, h0 + 1024); hp[2] = ld128(rs, h2o); } while (!HOK() && ++sp < SPIN_CAP);
      }
      TL(13);
      *(u32x2*)(lds + ia) = u32x2{hp[0][0], hp[0][1]};
      *(u32x2*)(lds + ia + 4096) = u32x2{hp[1][0], hp[1][1]};
      if (lane < 16) *(u32x2*)(lds + ia + 8192) = u32x2{hp[2][0], hp[2][1]};
      lds_barrier();
      T2(47, );
      u32x4 af[9];
#pragma unroll
      for (int s = 0; s < 9; ++s) af[s] = *(const u32x4*)(lds + ra + s * 1024);
      SCHED();
      if (wave == 7) { ald128sc(gq, ws, fo[0]); ald128sc(xq, ws, fo[1]); ald128sc(rq, ws, fo[2]); }
#pragma unroll
      for (int s = 0; s < 9; ++s) d = smf(af[s], bw[s], d, ix);
      T2(48, "+v"(d));
      int r[2];
#pragma unroll
      for (int j = 0; j < 2; ++j) {
        const auto t = __builtin_amdgcn_permlane32_swap(__float_as_uint(d[j]), __float_as_uint(d[j + 2]), false, false);
        r[j] = __float_as_int(rbf(__uint_as_float(t[0]) + __uint_as_float(t[1])));
      }
      const float g = __int_as_float(__builtin_amdgcn_update_dpp(r[0], r[1], 0x128, 0xf, 0xc, false)), u = __int_as_float(__builtin_amdgcn_update_dpp(r[1], r[0], 0x128, 0xf, 0x3, false));
      *(u16*)(lds + tw) = (u16)cvt2(rbf(silu(g)) * u, 0.f);
    }
    T2(49, );
    lds_barrier();
    TL(14);
    {
      const u32x4 af = *(const u32x4*)(lds + tr_), a4 = *(const u32x4*)(lds + tr4);
      asm volatile(
        "v_smfmac_f32_16x16x64_bf16 v[176:179], %5, %7, %12\nv_smfmac_f32_16x16x64_bf16 v[180:183], %5, %8, %12\nv_smfmac_f32_16x16x64_bf16 v[184:187], %5, %9, %12\n"
        "v_smfmac_f32_16x16x64_bf16 v[188:191], %5, %10, %12\nv_smfmac_f32_16x16x64_bf16 v[192:195], %6, %11, %12\ns_nop 3\nv_permlane32_swap_b32 v176, v178\n"
        "v_permlane32_swap_b32 v180, v182\nv_permlane32_swap_b32 v184, v186\nv_permlane32_swap_b32 v188, v190\nv_add_f32 v196, v176, v178\nv_add_f32 v197, v180, v182\n"
        "v_add_f32 v198, v184, v186\nv_add_f32 v199, v188, v190\nbuffer_store_dwordx4 v[196:199], %13, %15, 0 offen sc1\nv_permlane32_swap_b32 v177, v179\n"
        "v_permlane32_swap_b32 v181, v183\nv_permlane32_swap_b32 v185, v187\nv_permlane32_swap_b32 v189, v191\nv_add_f32 v200, v177, v179\nv_add_f32 v201, v181, v183\n"
        "v_add_f32 v202, v185, v187\nv_add_f32 v203, v189, v191\nbuffer_store_dwordx4 v[200:203], %13, %15, %16 offen sc1\nv_permlane32_swap_b32 v192, v194\nv_add_f32 v193, v192, v194\n"
        "buffer_store_dword v193, %14, %15, 0 offen sc1\n"
        : "+{v[176:179]}"(dd[0]), "+{v[180:183]}"(dd[1]), "+{v[184:187]}"(dd[2]), "+{v[188:191]}"(dd[3]), "+{v[192:195]}"(dd[4])
        : "v"(af), "v"(a4), "v"(wd8[0]), "v"(wd8[1]), "v"(wd8[2]), "v"(wd8[3]), "v"(wd8[4]), "v"(ix), "v"(yo[0]), "v"(yo[1]), "s"(rs), "s"(ys)
        : "memory", "v196", "v197", "v198", "v199", "v200", "v201", "v202", "v203");
    }
    TL(15);
    {
      float gbv = 0.f, x1v = 0.f, h2 = 0.f, rsel = 0.f;
      if (wave == 7) {
        asm volatile("s_waitcnt vmcnt(3)" : "+v"(gq), "+v"(xq), "+v"(rq) :: "memory");
        if (!__all((tag_x(gq, e0, e1) | tag_x(xq, e0, e1) | tag_x(rq, e0, e1)) == 0u)) {
          int sp = 0;
          _Pragma("nounroll") do { nap(1); gq = ld128(rs, fo[0]); xq = ld128(rs, fo[1]); rq = ld128(rs, fo[2]); } while (!__all((tag_x(gq, e0, e1) | tag_x(xq, e0, e1) | tag_x(rq, e0, e1)) == 0u) && ++sp < SPIN_CAP);
        }
        const u32 gw = (lane & 2) ? gq[1] : gq[0], xw = (lane & 2) ? xq[1] : xq[0];
        gbv = (lane & 1) ? hi16(gw) : lo16(gw); x1v = (lane & 1) ? hi16(xw) : lo16(xw);
        h2 = b2f(*(const u16*)(lds + fo[4])); rsel = __uint_as_float(rq[0]);
        asm volatile("" : "+v"(gbv), "+v"(x1v), "+v"(h2), "+v"(rsel));
      } else nap(5);
      mw_ = L_C + tid * 4; asm volatile("" : "+v"(mw_));
      T2(51, );
      const int gy = (idx - 23) * 4 + (xcd & 3);
      f32x4 y[3]; u32 cnt;
      asm volatile(
        "v_mov_b64 v[48:49], 0\nv_mov_b64 v[50:51], 0\ns_mov_b64 s[64:65], -1\ns_mov_b64 s[66:67], -1\ns_mov_b32 s68, %9\ns_mov_b32 s69, %9\ns_mov_b32 %3, 0x8000\n1:\n"
        "s_mov_b64 exec, s[64:65]\nbuffer_load_dwordx4 v[40:43], %4, %5, %6 offen sc1\ns_mov_b64 exec, s[66:67]\nbuffer_load_dwordx4 v[44:47], %4, %5, %7 offen sc1\n"
        "s_mov_b64 exec, s[68:69]\nbuffer_load_dwordx4 v[48:51], %4, %5, %8 offen sc1\ns_mov_b64 exec, -1\ns_sub_u32 %3, %3, 1\ns_waitcnt vmcnt(0)\n"
        "s_cbranch_scc1 2f ; throttle\nv_max_u32 v52, v40, v41\nv_max_u32 v53, v44, v45\nv_max_u32 v54, v48, v49\nv_max3_u32 v52, v52, v42, v43\n"
        "v_max3_u32 v53, v53, v46, v47\nv_max3_u32 v54, v54, v50, v51\nv_cmp_eq_u32 s[64:65], -1, v52\nv_cmp_eq_u32 s[66:67], -1, v53\nv_cmp_eq_u32 s[68:69], -1, v54\n"
        "s_or_b64 vcc, s[64:65], s[66:67]\ns_or_b64 vcc, vcc, s[68:69]\ns_cbranch_scc1 1b ; throttle\n2:\n"
        : "=&{v[40:43]}"(y[0]), "=&{v[44:47]}"(y[1]), "=&{v[48:51]}"(y[2]), "=&s"(cnt)
        : "v"(fo[3]), "s"(rs), "s"(gy * 32768), "s"(gy * 32768 + 16384), "s"((72 + (gy >> 2)) * 16384 + (gy & 3) * 2048), "s"((wave - 2) >> 31)
        : "memory", "vcc", "scc", "s64", "s65", "s66", "s67", "s68", "s69", "v52", "v53", "v54");
      TL(16);
      const f32x4 s = (y[0] + y[1]) + y[2];
      *(float*)(lds + mw_) = fold16(fold32(s[0], s[2]), fold32(s[1], s[3]));
      T2(52, );
      lds_barrier();
      T2(53, );
      if (wave == 7) {
        f32x2 t[4];
#pragma unroll
        for (int ww = 0; ww < 4; ++ww) t[ww] = f32x2{*(const float*)(lds + fo[5] + ww * 512), *(const float*)(lds + fo[5] + ww * 512 + 256)};
        SCHED();
        const f32x2 t2 = (t[0] + t[1]) + (t[2] + t[3]);
        const float y2 = rbf(rbf(t2.x + t2.y) * rsel);
        const float res = x1v + rbf(gbv * rbf(h2 + y2));
        __builtin_amdgcn_raw_buffer_store_b16((u16)(cvt2(res, 0.f) & 0xffffu), __builtin_amdgcn_make_buffer_rsrc(out, 0, 8 * D * 2, 0x00020000), fo[6], 0, 0);
      }
      TL(17);
      TLOUT();
      asm volatile(".Lcode_end:");
      __builtin_amdgcn_endpgm();
    }
  }
  {
  int t3_ = threadIdx.x; asm volatile("" : "+v"(t3_));
  const int tid = t3_, lane = tid & 63, n = lane & 15, grp = lane >> 4;
  if (qs >= 0) {
    const u16* mat = (wave & 1) ? wg : wu;
    BAR();
    const int g0 = ((3 - (int)((uintptr_t)mat >> 7) - xcd * 9) & 7) + (wave >= 4 ? 8 : 0);
    if (g0 < 9) dma_keep(mat, (256 + (idx - 6) * 40 + ((wave >> 1) & 1) * 20 + min(lane, 19)) * 9216 + (xcd * 9 + g0) * 128, lds_base + L_W + wave * 256);
    const int l9 = min(lane, 44), rr = l9 / 9;
#pragma unroll
    for (int i = 0; i < 2; ++i) dma_keep(mat, (256 + ((i * 8 + idx - 6) * 4 + (wave >> 1)) * 5 + rr) * 9216 + (xcd * 9 + l9 - rr * 9) * 128, lds_base + L_W + wave * 256);
  }
  if (ml >= 0) {
    BAR();
    if (mw < 18) { ML(0); ML(1); ML(2); ML(3); }
    ML(4); ML(5); ML(6); ML(7); ML(8);
    TL(38);
    u32x4 af[9];
    f32x4 d = {0.f, 0.f, 0.f, 0.f};
    const int q = n >> 2, ch = (ml - 18) / 9, sl = ml - 18 - ch * 9;
    int a0 = L_W + ((grp >> 1) * 16 + (grp & 1) * 4 + (q & 1) + (q >> 1) * 8) * 128 + (((wave & 3) ^ ((grp & 1) + (q & 2))) << 5) + (lane & 3) * 8, ix = n < 8 ? 0x4444 : 0xeeee;
    int mo = (ch < 3 ? W_M + ((grp >> 1) * 2 + (n & 1)) * 16384 + ((grp & 1) * 8192 ^ flip) + ch * 2304 : W_GB + ((grp & 1) * 4 + (grp >> 1) * 2 + (n & 1)) * 2304) + (sl * 16 + wave * 4 + q) * 16;
#pragma unroll
    for (int s = 0; s < 9; ++s) af[s] = *(const u32x4*)(lds + L_IM2 + (grp * 16 + (n & 8) + ((n & 7) ^ (grp * 2 + (n >> 3)))) * 16 + s * 1024);
#define MLS(S0, S1) do { if (wave < 4) { u32x8 f_[S1 - S0]; _Pragma("unroll") for (int s = S0; s < S1; ++s) { const int a_ = a0 + (s >> 2) * 32768, o_ = (s & 3) * 8192; \
      const u32x2 r0 = trd(lds, a_ + o_), r1 = trd(lds, a_ + o_ + 256), r2 = trd(lds, a_ + o_ + 4096), r3 = trd(lds, a_ + o_ + 4352); \
      f_[s - S0] = u32x8{r0[0], r0[1], r1[0], r1[1], r2[0], r2[1], r3[0], r3[1]}; } SCHED(); \
      _Pragma("unroll") for (int s = S0; s < S1; ++s) d = smf(af[s], f_[s - S0], d, ix); } } while (0)
    asm volatile("s_waitcnt vmcnt(5)\n\ts_barrier" : "+v"(a0), "+v"(ix), "+v"(mo) :: "memory");
    MLS(0, 4);
    asm volatile("s_waitcnt vmcnt(2)\n\ts_barrier" : "+v"(d) :: "memory");
    MLS(4, 7);
    asm volatile("s_waitcnt vmcnt(0)\n\ts_barrier" : "+v"(d), "+v"(af[7]), "+v"(af[8]) :: "memory");
    TL(39);
    MLS(7, 9);
    if (wave < 4) {
      float r[2];
#pragma unroll
      for (int j = 0; j < 2; ++j) {
        const auto t = __builtin_amdgcn_permlane32_swap(__float_as_uint(d[j]), __float_as_uint(d[j + 2]), false, false);
        r[j] = __uint_as_float(t[0]) + __uint_as_float(t[1]);
      }
      const float o = dppf<0xB1>((n & 1) ? r[0] : r[1]);
      const u32 pk = (n & 1) ? cvt2(o, r[1]) : cvt2(r[0], o);
      const u32 p2 = (u32)__builtin_amdgcn_mov_dpp((int)pk, 0x4E, 0xf, 0xf, true);
      if (!(n & 2)) st128(rs, mo, u32x4{pk, p2, e0, e1});
    }
    if (ml < 45) TL(18); else TL(36);
  }

  if (lnb >= 0 && wave < 3) {
    const int b = lnb, g = tid < 144 ? tid : 143;
    const bool on = tid < 144;
    float* ex = (float*)(lds + L_TILE);
    const u32x2 xa = __builtin_amdgcn_raw_buffer_load_b64(__builtin_amdgcn_make_buffer_rsrc((void*)x, 0, 8 * D * 2, 0x00020000), (b * D + g * 4) * 2, 0, 0);
    const u32x2 wra = __builtin_amdgcn_raw_buffer_load_b64(__builtin_amdgcn_make_buffer_rsrc((void*)w_r, 0, D * 2, 0x00020000), g * 8, 0, 0);
    u32x4 mm[3], xp[8];
#define XPLD() do { _Pragma("unroll") for (int p = 0; p < 8; ++p) xp[p] = ld128(rs, W_XP + ((b & 3) * 3 + p / 3) * 16384 + ((b >> 2) * 8192 ^ flip) + p % 3 * 2304 + g * 16); } while (0)
    _Pragma("nounroll") for (int sp = 0; sp < SPIN_CAP; ++sp) {
#pragma unroll
      for (int k = 0; k < 3; ++k) mm[k] = ld128(rs, W_M + (b & 3) * 16384 + ((b >> 2) * 8192 ^ flip) + k * 2304 + g * 16);
      XPLD();
      if (__all((tag_x(mm[0], e0, e1) | tag_x(mm[1], e0, e1) | tag_x(mm[2], e0, e1)) == 0u)) break;
      nap(2);
    }
    TL(37);
    f32x2 xv[2], ga[2], sb[2], cb1[2], wr[2];
#pragma unroll
    for (int i = 0; i < 2; ++i) {
      xv[i] = up2(xa[i]); wr[i] = up2(wra[i]); ga[i] = up2(mm[0][i]); sb[i] = up2(mm[1][i]); cb1[i] = rbf2(f32x2{1.f, 1.f} + up2(mm[2][i]));
      asm volatile("" : "+v"(xv[i]), "+v"(ga[i]), "+v"(sb[i]), "+v"(cb1[i]), "+v"(wr[i]));
    }
    int ho = W_H2 + b / 3 * 16384 + ((b >> 2) * 8192 ^ flip ^ 8192) + b % 3 * 2304 + g * 16, xo = W_X1 + (b * 152 + g) * 16;
    int ho2 = ho ^ 8192;
    asm volatile("" : "+v"(ho), "+v"(ho2), "+v"(xo));
    _Pragma("nounroll") for (int sp = 0; sp < SPIN_CAP; ++sp) {
      if (__all(mxu(mxu(mxu(mx4(xp[0]), mx4(xp[1])), mxu(mx4(xp[2]), mx4(xp[3]))), mxu(mxu(mx4(xp[4]), mx4(xp[5])), mxu(mx4(xp[6]), mx4(xp[7])))) != 0xffffffffu)) break;
      nap(1);
      XPLD();
    }
    TL(10);
    f32x2 v[2];
#pragma unroll
    for (int i = 0; i < 2; ++i) {
      f32x2 pp[8];
#pragma unroll
      for (int p = 0; p < 8; ++p) pp[p] = f32x2{__uint_as_float(xp[p][2 * i]), __uint_as_float(xp[p][2 * i + 1])};
      v[i] = rbf2(xv[i] + rbf2(ga[i] * rbf2(((pp[0] + pp[1]) + (pp[2] + pp[3])) + ((pp[4] + pp[5]) + (pp[6] + pp[7])))));
    }
    const f32x2 s2 = v[0] + v[1];
    const float mw = wsum(on ? s2.x + s2.y : 0.f) * (wave < 2 ? 1.f / 256 : 1.f / 64);
    T2(44, );
    const f32x2 t0 = v[0] - f32x2{mw, mw}, t1 = v[1] - f32x2{mw, mw}, q2 = t0 * t0 + t1 * t1;
    const float m2 = wsum(on ? q2.x + q2.y : 0.f);
    if (lane == 0) *(f32x2*)(ex + wave * 2) = f32x2{mw, m2};
    T2(45, );
    lds_barrier();
    T2(46, );
    const f32x4 e01 = *(const f32x4*)ex; const f32x2 e2 = *(const f32x2*)(ex + 4);
    const float mu = (e01[0] + e01[2]) * (256.f / D) + e2.x * (64.f / D);
    const float d0 = e01[0] - mu, d1 = e01[2] - mu, d2 = e2.x - mu;
    const float var = rbf(((e01[1] + e01[3]) + e2.y + 256.f * (d0 * d0 + d1 * d1) + 64.f * d2 * d2) * (1.f / D));
    const float mean = rbf(mu), rden = frcp(rbf(__builtin_amdgcn_sqrtf(rbf(var + 1e-5f))));
    u32 hw[2];
#pragma unroll
    for (int i = 0; i < 2; ++i) {
      const f32x2 t = rbf2(rbf2(rbf2(v[i] - f32x2{mean, mean}) * f32x2{rden, rden}) * cb1[i]) + sb[i];
      hw[i] = cvt2(t.x, t.y);
    }
    if (on) {
      st128(rs, ho, u32x4{hw[0], hw[1], e0, e1});
      st128(rs, ho2, u32x4{hw[0], hw[1], e0, e1});
      st128(rs, xo, u32x4{cvt2(v[0].x, v[0].y), cvt2(v[1].x, v[1].y), e0, e1});
    }
    TL(11);
    const f32x2 r2 = up2(hw[0]) * wr[0] + up2(hw[1]) * wr[1];
    const float rw = wsum(on ? r2.x + r2.y : 0.f);
    if (lane == 0) ex[8 + wave] = rw;
    lds_barrier();
    if (tid < 8) {
      const float r = rbf((ex[8] + ex[9]) + ex[10]);
      st128(rs, W_X1 + (b * 152 + 144 + tid) * 16, u32x4{__float_as_uint(rbf(1.f / (1.f + __expf(-r)))), 0u, e0, e1});
    }
  }

  if (oj >= 0) {
    const int ob = W_O + ((op & 3) * 2 + (wave >> 2)) * 32768 + (lane >> 5) * 16384 + ((op >> 2) * 8192 ^ flip) + ((lane >> 3) & 3) * 2048 + (wave & 3) * 256 + (lane & 7) * 16;
    u32x4 a[4];
#pragma unroll
    for (int p = 0; p < 4; ++p) a[p] = ld128(rs, ob + (p >> 1) * 1024 + (p & 1) * 128);
    SCHED();
    u32x8 wf[8];
    if (wave < 4) {
      const int q = n >> 2, p = lane & 3;
      const int a0 = L_W + ((grp >> 1) * 16 + (grp & 1) * 4 + (q & 1) + (q >> 1) * 8) * 128 + ((wave ^ ((grp & 1) + (q & 2))) << 5) + p * 8;
#pragma unroll
      for (int s = 0; s < 8; ++s) {
        const u32x2 r0 = trd(lds, a0 + s * 8192), r1 = trd(lds, a0 + s * 8192 + 256), r2 = trd(lds, a0 + s * 8192 + 4096), r3 = trd(lds, a0 + s * 8192 + 4352);
        wf[s] = u32x8{r0[0], r0[1], r1[0], r1[1], r2[0], r2[1], r3[0], r3[1]};
      }
#pragma unroll
      for (int s = 0; s < 8; ++s) asm volatile("" : "+v"(wf[s]));
    }
    int rw = L_OI + wave * 1024 + (((lane >> 1) & 3) * 16 + (lane & 1) * 8 + ((lane >> 3) ^ (lane & 7))) * 16, rr = L_OI + (grp * 16 + (n & 8) + ((n & 7) ^ (grp * 2 + (n >> 3)))) * 16;
    int xo = W_XP + ((grp >> 1) * 6 + op / 3) * 16384 + ((grp & 1) * 8192 ^ flip) + op % 3 * 2304 + oj * 256 + wave * 64 + n * 4, ix = n < 8 ? 0x4444 : 0xeeee;
    asm volatile("" : "+v"(rw), "+v"(rr), "+v"(xo), "+v"(ix));
    _Pragma("nounroll") for (int sp = 0; sp < SPIN_CAP; ++sp) {
      const bool m0 = mxu(mx4(a[0]), mx4(a[1])) == 0xffffffffu, m1 = mxu(mx4(a[2]), mx4(a[3])) == 0xffffffffu;
      if (!__any(m0 || m1)) break;
      nap(2);
      if (m0) { a[0] = ld128(rs, ob); a[1] = ld128(rs, ob + 128); }
      if (m1) { a[2] = ld128(rs, ob + 1024); a[3] = ld128(rs, ob + 1152); }
    }
    TL(8);
#define OS(p, j) (__uint_as_float(a[p][j]) + __uint_as_float(a[(p) + 2][j]))
    *(u32x4*)(lds + rw) = u32x4{cvt2(OS(0, 0), OS(0, 1)), cvt2(OS(0, 2), OS(0, 3)), cvt2(OS(1, 0), OS(1, 1)), cvt2(OS(1, 2), OS(1, 3))};
    T2(41, );
    lds_barrier();
    T2(42, );
    if (wave < 4) {
      u32x4 af[8];
#pragma unroll
      for (int s = 0; s < 8; ++s) af[s] = *(const u32x4*)(lds + rr + s * 1024);
      SCHED();
      f32x4 d = {0.f, 0.f, 0.f, 0.f};
#pragma unroll
      for (int s = 0; s < 8; ++s) d = smf(af[s], wf[s], d, ix);
      T2(43, "+v"(d));
#pragma unroll
      for (int j = 0; j < 2; ++j) {
        const auto r = __builtin_amdgcn_permlane32_swap(__float_as_uint(d[j]), __float_as_uint(d[j + 2]), false, false);
        __builtin_amdgcn_raw_buffer_store_b32(sanf(__uint_as_float(r[0]) + __uint_as_float(r[1])), rs, xo, j * 49152, 16);
      }
    }
    TL(9);
  }
  TLOUT();
  }
  }
}

extern "C" void kernel_launch(void* const* d_in, const int* in_sizes, int n_in, void* d_out, int out_size, void* d_ws, size_t ws_size,
                              hipStream_t stream) {
  fused_kernel<<<NWG, NT, 0, stream>>>((const u16*)d_in[0], (const u16*)d_in[1], (const u16*)d_in[10], (const u16*)d_in[2], (const u16*)d_in[3], (const u16*)d_in[4],
                                       (const u16*)d_in[6], (const u16*)d_in[7], (const u16*)d_in[8], (const u16*)d_in[9],
                                       (const u16*)d_in[11], (unsigned char*)d_ws, (u16*)d_out);
}
```

```cpp
#include <hip/hip_runtime.h>
#include <hip/hip_bf16.h>
#include <cstdint>

typedef unsigned u32;
typedef unsigned short u16;
typedef u32 u32x4 __attribute__((ext_vector_type(4)));
typedef u32 u32x2 __attribute__((ext_vector_type(2)));
typedef float f32x4 __attribute__((ext_vector_type(4)));
typedef float f32x2 __attribute__((ext_vector_type(2)));
typedef short s16x4 __attribute__((ext_vector_type(4)));
typedef __bf16 bf16x8 __attribute__((ext_vector_type(8)));
typedef __bf16 bf16x2 __attribute__((ext_vector_type(2)));
typedef __bf16 bf16x16 __attribute__((ext_vector_type(16)));
typedef u32 u32x8 __attribute__((ext_vector_type(8)));
typedef __attribute__((address_space(3))) unsigned char lds_t;

constexpr int D = 576, SK = 8192, WIN = 2048, FF = 4608, NQ = 4096, NMOD = 6 * D;
constexpr int NWG = 256, NT = 512;
constexpr int SPIN_CAP = 1 << 15;

constexpr int W_O = 0;
constexpr int W_XP = W_O + 262144;
constexpr int W_Y = W_XP + 12 * 16384;
constexpr int W_H2 = W_Y + 81 * 16384;
constexpr int W_MS = W_H2 + 2 * 16384;
constexpr int W_M = W_MS;
constexpr int W_PR = W_M + 4 * 16384;
constexpr int W_GB = W_PR + 2 * 16384;
constexpr int W_FL = W_GB + 18432;
constexpr int W_H = W_FL + 2048;
constexpr int W_Q = W_H + 36864;
constexpr int W_ST = W_Q + 131072;
constexpr int W_X1 = W_ST + 32768;
constexpr int W_END = W_X1 + 8 * 152 * 16;
constexpr int NPZ = 174, PZ = (W_MS / 16 + NPZ - 1) / NPZ;
static_assert(PZ <= 2 * NT, "poison split");

constexpr int L_W = 0;
constexpr int L_IMG = 73728;
constexpr int L_TILE = L_IMG + 9216;
constexpr int L_IM2 = 86016;
constexpr int L_OI = 65536;
constexpr int L_B = 147456;
constexpr int L_C = 155648;
constexpr int LDS_BYTES = 163840;

#define SCHED() __builtin_amdgcn_sched_barrier(0)
#define BAR() asm volatile("s_barrier" ::: "memory")
#define TL(i) do {} while (0)
#define ATL(i)
#define TLOUT() do {} while (0)
#define T2(i, ...) do {} while (0)
#define WAITV_(n) do { asm volatile("s_waitcnt vmcnt(" #n ")" ::: "memory"); SCHED(); } while (0)
#define WAITV(n) WAITV_(n)
#define W0 3
#define W1 5
#define W2 7
#define TH1 15
#define TH2 40
#define STR_(x) #x
#define STR(x) STR_(x)
#define WAITD() do { asm volatile("s_waitcnt vmcnt(" STR(W2) ")\n\ts_cmpk_gt_i32 %0, " STR(TH2) " ; throttle\n\ts_cbranch_scc1 4 ; throttle\n\ts_waitcnt vmcnt(" STR(W1) ") ; throttle\n\t" \
    "s_cmpk_gt_i32 %0, " STR(TH1) " ; throttle\n\ts_cbranch_scc1 1 ; throttle\n\ts_waitcnt vmcnt(" STR(W0) ") ; throttle" :: "s"(__builtin_amdgcn_readfirstlane(lag)) : "memory", "scc"); SCHED(); } while (0)
#define PR 114
#define R1 34
#define G1 10
#define OH 175
#define OF 99
#define OL 75

__device__ __forceinline__ float lo16(u32 u) { return __uint_as_float(u << 16); }
__device__ __forceinline__ float hi16(u32 u) { return __uint_as_float(u & 0xffff0000u); }
__device__ __forceinline__ float b2f(u16 u) { return __uint_as_float((u32)u << 16); }
__device__ __forceinline__ u32 cvt2(float a, float b) { return __builtin_bit_cast(u32, __builtin_convertvector(f32x2{a, b}, bf16x2)); }
__device__ __forceinline__ float rbf(float v) { return __uint_as_float(cvt2(0.f, v)); }
__device__ __forceinline__ f32x2 up2(u32 u) { return f32x2{lo16(u), hi16(u)}; }
__device__ __forceinline__ void lds_barrier() { asm volatile("s_waitcnt lgkmcnt(0)\n\ts_barrier" ::: "memory"); }
__device__ __forceinline__ f32x4 mfma(u32x4 a, u32x4 b, f32x4 c) {
  return __builtin_amdgcn_mfma_f32_16x16x32_bf16(__builtin_bit_cast(bf16x8, a), __builtin_bit_cast(bf16x8, b), c, 0, 0, 0);
}
__device__ __forceinline__ f32x4 smf(u32x4 a, u32x8 b, f32x4 c, int idx) {
  return __builtin_amdgcn_smfmac_f32_16x16x64_bf16(__builtin_bit_cast(bf16x8, a), __builtin_bit_cast(bf16x16, b), c, idx, 0, 0);
}
__device__ __forceinline__ u32 plo(u32 a, u32 b) { return __builtin_amdgcn_perm(b, a, 0x05040100u); }
__device__ __forceinline__ u32 phi(u32 a, u32 b) { return __builtin_amdgcn_perm(b, a, 0x07060302u); }

extern "C" __device__ __attribute__((const)) unsigned long long llvm_dispatch_id() __asm("llvm.amdgcn.dispatch.id");
typedef __amdgpu_buffer_rsrc_t rsrc_t;
__device__ __forceinline__ u32x4 ld128(rsrc_t rs, int off) { return __builtin_amdgcn_raw_buffer_load_b128(rs, off, 0, 16); }
__device__ __forceinline__ void st128(rsrc_t rs, int off, u32x4 v) { __builtin_amdgcn_raw_buffer_store_b128(v, rs, off, 0, 16); }
__device__ __forceinline__ void st64(rsrc_t rs, int off, u32x2 v) { __builtin_amdgcn_raw_buffer_store_b64(v, rs, off, 0, 16); }
__device__ __forceinline__ void dma(const void* gbase, u32 voff, unsigned lds_dst) {
  unsigned keep;
  asm volatile("s_mov_b32 %0, m0\n\ts_mov_b32 m0, %3\n\ts_nop 2\n\tglobal_load_lds_dwordx4 %1, %2 nt\n\ts_mov_b32 m0, %0"
               : "=&s"(keep) : "v"(voff), "s"(gbase), "s"(lds_dst) : "memory");
}
__device__ __forceinline__ void dma_keep(const void* gbase, u32 voff, unsigned lds_dst) {
  unsigned keep;
  asm volatile("s_mov_b32 %0, m0\n\ts_mov_b32 m0, %3\n\ts_nop 2\n\tglobal_load_lds_dword %1, %2\n\ts_mov_b32 m0, %0"
               : "=&s"(keep) : "v"(voff), "s"(gbase), "s"(lds_dst) : "memory");
}
__device__ __forceinline__ void dma_sc(const void* gbase, u32 voff, unsigned lds_dst) {
  unsigned keep;
  asm volatile("s_mov_b32 %0, m0\n\ts_mov_b32 m0, %3\n\ts_nop 2\n\tglobal_load_lds_dwordx4 %1, %2 sc1\n\ts_mov_b32 m0, %0"
               : "=&s"(keep) : "v"(voff), "s"(gbase), "s"(lds_dst) : "memory");
}
__device__ __forceinline__ void ald64(u32x2& d, const void* gbase, u32 voff) { unsigned long long t; asm volatile("s_mov_b64 %1, %3\n\tglobal_load_dwordx2 %0, %2, %1" : "=v"(d), "=&s"(t) : "v"(voff), "s"(gbase) : "memory"); }
__device__ __forceinline__ void ald128nt(u32x4& d, const void* gbase, u32 voff) { unsigned long long t; asm volatile("s_mov_b64 %1, %3\n\tglobal_load_dwordx4 %0, %2, %1 nt" : "=v"(d), "=&s"(t) : "v"(voff), "s"(gbase) : "memory"); }
__device__ __forceinline__ void ald128sc(u32x4& d, const void* gbase, u32 voff) { unsigned long long t; asm volatile("s_mov_b64 %1, %3\n\tglobal_load_dwordx4 %0, %2, %1 sc1" : "=v"(d), "=&s"(t) : "v"(voff), "s"(gbase) : "memory"); }
__device__ __forceinline__ u32x2 trd(const unsigned char* lds, int off) {
  return __builtin_bit_cast(u32x2, __builtin_amdgcn_ds_read_tr16_b64_v4i16((__attribute__((address_space(3))) s16x4*)((lds_t*)lds + off)));
}
#define nap(n) do { __builtin_amdgcn_s_sleep(n); asm volatile("" ::: "memory"); } while (0)
__device__ __forceinline__ float frcp(float v) { return __builtin_amdgcn_rcpf(v); }
__device__ __forceinline__ float fdiv(float a, float d, float r) { const float q = a * r; return fmaf(fmaf(-q, d, a), r, q); }
__device__ __forceinline__ float silu(float v) { const float d = 1.f + __expf(-v); const float r = frcp(d); return d < 3.0e38f ? fdiv(v, d, r) : v * r; }
template <int CTRL> __device__ __forceinline__ float dppf(float x) {
  return __builtin_bit_cast(float, __builtin_amdgcn_mov_dpp(__builtin_bit_cast(int, x), CTRL, 0xf, 0xf, true));
}
__device__ __forceinline__ float rl(float v, int l) { return __builtin_bit_cast(float, __builtin_amdgcn_readlane(__builtin_bit_cast(int, v), l)); }
__device__ __forceinline__ float wsum(float v) {
  v += dppf<0xB1>(v); v += dppf<0x4E>(v); v += dppf<0x141>(v); v += dppf<0x140>(v);
  return (rl(v, 0) + rl(v, 16)) + (rl(v, 32) + rl(v, 48));
}
__device__ __forceinline__ float up32(float v) { const u32 u = __float_as_uint(v); return __uint_as_float(__builtin_amdgcn_permlane32_swap(u, u, false, false)[1]); }
__device__ __forceinline__ f32x4 half_sum(f32x4 d) {
  f32x4 r;
#pragma unroll
  for (int j = 0; j < 4; ++j) r[j] = d[j] + dppf<0x108>(up32(d[j]));
  return r;
}
__device__ __forceinline__ void unpack8(u32x4 u, float* f) {
#pragma unroll
  for (int j = 0; j < 4; ++j) { f[2 * j] = lo16(u[j]); f[2 * j + 1] = hi16(u[j]); }
}
__device__ __forceinline__ void ln_row(const float (&v)[16], bool has2, float& mean, float& den) {
  float s = 0.f;
#pragma unroll
  for (int i = 0; i < 8; ++i) s += v[i];
  if (has2) {
#pragma unroll
    for (int i = 8; i < 16; ++i) s += v[i];
  }
  s = wsum(s);
  const float mu = s * (1.f / D);
  mean = rbf(mu);
  float q = 0.f;
#pragma unroll
  for (int i = 0; i < 8; ++i) { const float t = v[i] - mu; q = fmaf(t, t, q); }
  if (has2) {
#pragma unroll
    for (int i = 8; i < 16; ++i) { const float t = v[i] - mu; q = fmaf(t, t, q); }
  }
  q = wsum(q);
  const float var = rbf(q * (1.f / D));
  den = rbf(sqrtf(rbf(var + 1e-5f)));
}
template <int J0 = 0, int J1 = 9>
__device__ __forceinline__ void slab_dma(const u16* base, int stride_b, unsigned lds_dst, int wave, int lane) {
  const u32 v0 = (lane >> 3) * stride_b + (((lane & 7) ^ ((wave & 1) * 4 + (lane >> 4))) << 4), v1 = v0 ^ 32u;
  const u32 v4 = wave >= 4 ? v1 : v0;
  const char* gb = (const char*)base + (size_t)wave * 8 * stride_b;
#pragma unroll
  for (int j = J0; j < J1; ++j) dma(gb + (size_t)j * 64 * stride_b, j < 4 ? v0 : j == 4 ? v4 : v1, lds_dst + (j * 8 + wave) * 1024);
}
__device__ __forceinline__ void slab_frags(const unsigned char* lds, int base, int wave, int lane, u32x4 (&wf)[9]) {
  const int g = lane >> 4, q = (lane & 15) >> 2, p = lane & 3, hf = p >> 1;
  const int a0 = base + (hf * 288 + 8 * g + q) * 128 + ((wave ^ ((g & 1) * 4 + (q >> 1)) ^ (hf * 2)) << 4) + (p & 1) * 8, a1 = (a0 ^ 32) + 512;
#pragma unroll
  for (int s = 0; s < 9; ++s) {
    const u32x2 lo = trd(lds, a0 + s * 4096), hi = trd(lds, a1 + s * 4096);
    wf[s] = u32x4{lo[0], lo[1], hi[0], hi[1]};
  }
}
__device__ __forceinline__ void img_frags(const unsigned char* lds, int img, int lane, u32x4 (&af)[9]) {
#pragma unroll
  for (int s = 0; s < 9; ++s) af[s] = *(const u32x4*)(lds + img + (s * 64 + lane) * 16);
}
__device__ __forceinline__ f32x4 gemv9(const u32x4 (&wf)[9], const u32x4 (&af)[9]) {
  f32x4 acc[3];
  SCHED();
#pragma unroll
  for (int s = 0; s < 9; ++s) acc[s % 3] = mfma(wf[s], af[s], s < 3 ? f32x4{0.f, 0.f, 0.f, 0.f} : acc[s % 3]);
  return (acc[0] + acc[1]) + acc[2];
}
__device__ __forceinline__ int img_off(int b, int k8) {
  const int hf = k8 >= 36 ? 1 : 0, kk = k8 - hf * 36;
  return ((kk >> 2) * 64 + (kk & 3) * 16 + hf * 8 + b) * 16;
}
__device__ __forceinline__ int im2_off(int b, int k8) { return (k8 * 8 + (b ^ (k8 & 7))) * 16; }
__device__ __forceinline__ u32 tag_x(u32x4 v, u32 e0, u32 e1) { return (v[2] ^ e0) | (v[3] ^ e1); }
__device__ __forceinline__ bool tag_ok(u32x4 v, u32 e0, u32 e1) { return tag_x(v, e0, e1) == 0u; }

__global__ void __launch_bounds__(NT, 2) fused_kernel(const u16* __restrict__ x, const u16* __restrict__ kc, const u16* __restrict__ c, const u16* __restrict__ vc,
                                                      const u16* __restrict__ wq, const u16* __restrict__ wo, const u16* __restrict__ w_r,
                                                      const u16* __restrict__ wg, const u16* __restrict__ wu, const u16* __restrict__ wd,
                                                      const u16* __restrict__ w_mod, unsigned char* ws, u16* out) {
  __shared__ __attribute__((aligned(16))) unsigned char lds[LDS_BYTES];
  SCHED();
  const int w = blockIdx.x, xcd = w & 7, idx = w >> 3;
  const int tid = threadIdx.x, lane = tid & 63, wave = __builtin_amdgcn_readfirstlane(tid >> 6);
  const int n = lane & 15, grp = lane >> 4;
  const unsigned lds_base = (unsigned)(uintptr_t)(lds_t*)lds;
  {
    unsigned long long pc0; unsigned keep; int co = (w >> 3) * 8192 - 27 * 8192 + lane * 128;
    asm volatile("s_getpc_b64 %0\n1:\tv_min_i32 %2, .Lcode_end - 1b, %2\n\ts_mov_b32 %1, m0\n\ts_mov_b32 m0, %3\n\ts_cmp_lt_i32 %4, 216\n\ts_cbranch_scc1 2 ; throttle\n\tglobal_load_lds_dword %2, %0\n\ts_mov_b32 m0, %1"
                 : "=&s"(pc0), "=&s"(keep), "+v"(co) : "s"(lds_base + 107520), "s"(wave ? 0 : w) : "memory", "scc");
  }
  const int s_att = w & 1, kvh = (w & 4) * 2 + ((w >> 2) & 6) + ((w >> 1) & 1), b_att = w >> 5;
  size_t pairbase = ((size_t)(b_att * 16 + kvh) * SK + (SK - WIN)) * 256;
  int wpart = (wave & 3) * 4096 + s_att * 16384 + (wave >> 2) * 262144;
  int ko = wpart + (n >> 1) * 256 + (n & 1) * 64 + grp * 16;
  int eo[4] = {tid * 16, 8192 + lane * 16, wave * 1152 + lane * 16, wave * 1152 + (lane & 7) * 16};
  asm volatile("" : "+s"(pairbase), "+s"(wpart), "+v"(ko), "+v"(eo[0]), "+v"(eo[1]), "+v"(eo[2]), "+v"(eo[3]));
  SCHED();
  int tlv_ = 0;
  const char* kp = (const char*)kc + pairbase;
#define KLD(u) do { unsigned long long t_; asm volatile("s_mov_b64 %1, %3\n\tglobal_load_dwordx4 %0, %2, %1 offset:%4 nt" : "=v"(kb[u]), "=&s"(t_) : "v"(ko), "s"(kp + ((u) >> 2) * 32768), "i"((((u) >> 1) & 1) * 2048 + ((u) & 1) * 128) : "memory"); } while (0)
  u32x4 kb[32], vb[32];
  asm volatile("s_cmp_gt_i32 %12, 17\n\ts_cbranch_scc1 9 ; throttle\n\tglobal_load_dwordx4 %0, %4, %9 ; throttle\n\tglobal_load_dwordx4 %1, %5, %9 ; throttle\n\t"
               "global_load_dwordx4 %2, %6, %10 ; throttle\n\tglobal_load_dwordx4 %3, %7, %10 offset:1024 ; throttle\n\ts_branch 8 ; throttle\n\t"
               "global_load_dwordx4 %0, %8, %11 nt\n\tglobal_load_dwordx4 %1, %8, %11 offset:128 nt\n\tglobal_load_dwordx4 %2, %8, %11 offset:2048 nt\n\tglobal_load_dwordx4 %3, %8, %11 offset:2176 nt"
               : "=&v"(kb[0]), "=&v"(kb[1]), "=&v"(kb[2]), "=&v"(kb[3]) : "v"(eo[0]), "v"(eo[1]), "v"(eo[2]), "v"(eo[3]), "v"(ko), "s"(c), "s"(x), "s"(kp), "s"(w) : "memory", "scc");
  TL(32);
  SCHED();
  asm volatile("" : "+s"(vc), "+s"(w_r), "+s"(wu), "+s"(wd), "+s"(out), "+s"(x), "+s"(w_mod), "+s"(wg), "+s"(wq), "+s"(wo), "+s"(c), "+s"(ws));
  const rsrc_t rs = __builtin_amdgcn_make_buffer_rsrc(ws, 0, W_END, 0x00020000);
  const unsigned long long epoch = (llvm_dispatch_id() + 1ull) * 0x9E3779B97F4A7C15ull;
  const u32 e0 = (u32)epoch, e1 = (u32)(epoch >> 32);

  const int mw = idx < 6 ? idx * 8 + xcd : -1;
  const int md = mw < 18 ? mw : -1;
  const int ml = mw >= 18 ? mw : mw >= 0 && mw < 6 ? 48 + mw : -1;
  const int lnb = mw >= 6 && mw < 14 ? xcd : -1;
  const int qs = idx >= 6 && idx < 14 ? xcd * 8 + idx - 6 : -1;
  const int oj = idx >= 14 && idx < 23 ? idx - 14 : -1, op = xcd;
  const int gs = idx >= 23 ? xcd * 9 + idx - 23 : -1;

  {
    const char* vp = (const char*)vc + pairbase;
    const int vo = wpart + lane * 16;
#define VLD(u) do { unsigned long long t_; asm volatile("s_mov_b64 %1, %3\n\tglobal_load_dwordx4 %0, %2, %1 offset:%4 nt" : "=v"(vb[u]), "=&s"(t_) : "v"(vo), "s"(vp + ((u) >> 2) * 32768), "i"(((u) & 3) * 1024) : "memory"); } while (0)
#define TIE8(a, o) asm volatile("" : "+v"(a[o]), "+v"(a[(o) + 1]), "+v"(a[(o) + 2]), "+v"(a[(o) + 3]), "+v"(a[(o) + 4]), "+v"(a[(o) + 5]), "+v"(a[(o) + 6]), "+v"(a[(o) + 7]))
    const u32 tp0 = (u32)__builtin_amdgcn_s_memrealtime() + (gs >= 0 ? OF : mw >= 18 ? OL : OH);
    int lag = gs >= 0 || mw >= 18 ? 0 : 1000;
#define PACE(i) do { const int t_ = ((i) * PR >> 2) + ((i) >= R1 ? G1 : 0); lag = (int)((u32)__builtin_amdgcn_s_memrealtime() - tp0) - t_; \
      if (lag < 0) { _Pragma("nounroll") do __builtin_amdgcn_s_sleep(2); while ((int)((u32)__builtin_amdgcn_s_memrealtime() - tp0) < t_); } SCHED(); } while (0)
#define REP4(M, u, W) do { M(u); W; M((u) + 1); W; M((u) + 2); W; M((u) + 3); W; } while (0)
    static_assert(W2 <= 7, "the hand-placed waits assume it");
    const int pz = mw >= 18 ? mw - 18 : idx >= 14 ? w - 82 : -1;
#define POISON() do { if (pz >= 0) { _Pragma("unroll") for (int i = 0; i < 2; ++i) { const int g_ = pz * PZ + i * NT + tid; \
      if (i * NT + tid < PZ && g_ < W_MS / 16) st128(rs, g_ * 16, u32x4{0xffffffffu, 0xffffffffu, 0xffffffffu, 0xffffffffu}); } } SCHED(); } while (0)
#define SILU_IMAGE(c0, c1, both) do { float f[8]; unpack8(c0, f); u32x4 o; \
        _Pragma("unroll") for (int j = 0; j < 4; ++j) o[j] = cvt2(silu(f[2 * j]), silu(f[2 * j + 1])); \
        const int b_ = tid / 72, ch_ = tid - b_ * 72; \
        if (both) *(u32x4*)(lds + L_IMG + img_off(b_, ch_)) = o; \
        *(u32x4*)(lds + L_IM2 + im2_off(b_, ch_)) = o; \
        if (wave == 0) { unpack8(c1, f); \
          _Pragma("unroll") for (int j = 0; j < 4; ++j) o[j] = cvt2(silu(f[2 * j]), silu(f[2 * j + 1])); \
          if (both) *(u32x4*)(lds + L_IMG + img_off(7, 8 + lane)) = o; \
          *(u32x4*)(lds + L_IM2 + im2_off(7, 8 + lane)) = o; } } while (0)
    if (md >= 0) {
      u32x2 xc;
      int ho = W_H + img_off(n & 7, (md >= 9 ? md - 9 : md) * 8 + wave) * 2 + (grp & 1) * 16;
      ald64(xc, x, (n & 7) * 1152 + ((md >= 9 ? md - 9 : 0) * 64 + wave * 8 + (grp & 1) * 4) * 2);
      TL(35);
      BAR();
      float* st = (float*)(lds + L_TILE + 1024);
      if (wave < 4) {
        slab_dma(w_mod + md * 64, NMOD * 2, lds_base + L_W, wave, lane);
        asm volatile("s_waitcnt vmcnt(9)" ::: "memory");
      } else asm volatile("s_waitcnt vmcnt(0)" ::: "memory");
      asm volatile("s_nop 0 ; both branches above cover V" : "+v"(kb[0]), "+v"(kb[1]), "+v"(kb[2]), "+v"(kb[3]), "+v"(xc) :: "memory");
      {
        SILU_IMAGE(kb[0], kb[1], true);
        float v[16]; unpack8(kb[2], v); unpack8(kb[3], v + 8);
        float mean, den; ln_row(v, lane < 8, mean, den);
        if (lane == 0) { st[wave] = mean; st[8 + wave] = den; }
      }
      if (wave >= 4) slab_dma(w_mod + md * 64, NMOD * 2, lds_base + L_W, wave, lane);
      lds_barrier();
      REP4(KLD, 0, );
      u32x4 wf[9], af[9];
      img_frags(lds, L_IMG, lane, af);
      const float mean = st[n & 7], rd = frcp(st[8 + (n & 7)]);
      float ln[4] = {rbf(rbf(lo16(xc[0]) - mean) * rd), rbf(rbf(hi16(xc[0]) - mean) * rd), rbf(rbf(lo16(xc[1]) - mean) * rd), rbf(rbf(hi16(xc[1]) - mean) * rd)};
#pragma unroll
      for (int s = 0; s < 9; ++s) asm volatile("" : "+v"(af[s]));
      asm volatile("" : "+v"(ln[0]), "+v"(ln[1]), "+v"(ln[2]), "+v"(ln[3]), "+v"(ho));
      WAITV(4); BAR(); SCHED();
      slab_frags(lds, L_W, wave, lane, wf);
      const f32x4 s = half_sum(gemv9(wf, af));
      f32x4 r = s;
      if (md >= 9) {
#pragma unroll
        for (int j = 0; j < 4; ++j) r[j] = ln[j] * rbf(1.f + rbf(s[j]));
      }
      if (n < 8 && grp < 2) st128(rs, ho + (md >= 9 ? 18432 : 0), u32x4{cvt2(r[0], r[1]), cvt2(r[2], r[3]), e0, e1});
      SCHED();
      TL(1);
      BAR();
      KLD(4); KLD(5); WAITV(W2);
      KLD(6); WAITV(W2); KLD(7); WAITV(W2); KLD(8); WAITV(W2); KLD(9); WAITV(W2); KLD(10); WAITV(W2);
      KLD(11); WAITV(W2); REP4(KLD, 12, WAITV(W2)); REP4(KLD, 16, WAITV(W2));
      REP4(KLD, 20, WAITV(W2)); KLD(24); WAITV(W2); KLD(25);
    } else {
      constexpr int L_LAND = 98304;
      const unsigned land = lds_base + L_LAND + wave * 6144;
      const unsigned char* lp = lds + L_LAND + wave * 6144 + lane * 16;
      const int sl_ = (3 - (int)((uintptr_t)wq >> 7)) & 7;
      const int s2_ = (63 - (int)((uintptr_t)wq >> 7)) & 63;
      const bool slowq = qs >= 0 && ((qs & 7) == sl_ || qs == s2_);
      if (gs >= 0 && wave == 3 && xcd == (s2_ >> 3)) dma_keep((const char*)wq + (size_t)(idx - 23) * 64 * 8192 + s2_ * 128, lane * 8192, land);
      if (gs >= 0 && wave == 0) dma_keep((const char*)wq + (size_t)(idx - 23) * 64 * 8192 + xcd * 1024 + sl_ * 128, lane * 8192, land);
      if (gs >= 0 && (wave == 1 || (wave == 2 && xcd < 2))) dma_keep((const char*)w_mod + (size_t)((idx - 23) * 64 + 32) * (NMOD * 2) + xcd * 128 + (wave - 1) * 2048, (lane & 31) * (NMOD * 2) + (wave == 1 ? (lane >> 5) * 1024 : 0), land);
      POISON();
      const u32 v0 = (lane >> 3) * 1152 + (((lane & 7) ^ ((lane >> 4) & 2)) << 4);
      const char* gb = (const char*)(wo + (size_t)(op * 512 + wave * 64) * D + oj * 64);
      if (ml >= 0) { dma_sc(c, tid * 16, land); dma_sc(c, (512 + lane) * 16, land + 1024); }
      if (oj < 0 && qs < 0) {
        const int i_ = (gs >= 0 ? idx - 19 : idx - 2) * 8 + wave, o_ = (i_ >> 6) * 16384 + (i_ & 63) * 128 + (((xcd >> 2) ^ wave) & 1) * 8192;
        unsigned long long t0, t1, t2; u32 d_;
        asm volatile("s_memtime %0\n\ts_waitcnt lgkmcnt(0)\n\ts_load_dword %3, %4, %5\n\ts_waitcnt lgkmcnt(0)\n\ts_memtime %1\n\ts_waitcnt lgkmcnt(0)\n\t"
                     "s_load_dword %3, %4, %6\n\ts_waitcnt lgkmcnt(0)\n\ts_memtime %2\n\ts_waitcnt lgkmcnt(0)"
                     : "=&s"(t0), "=&s"(t1), "=&s"(t2), "=&s"(d_) : "s"(ws + W_PR), "s"(o_), "s"(o_ ^ 8192) : "memory");
        const int a_ = (int)((u32)t1 - (u32)t0), b_ = (int)((u32)t2 - (u32)t1);
        if (lane == 0) *(int*)(lds + L_B + 192 + wave * 4) = (wave & 1) ? b_ - a_ : a_ - b_;
        TL(58);
      }
#define WK(j) do { if (oj >= 0) { if ((j) < 8) dma(gb + (j) * 8 * 1152, v0 ^ (((j) & 1) * 64), lds_base + L_W + (wave * 8 + (j)) * 1024); } \
                   else if (slowq) { if ((j) == 8) slab_dma(wq + qs * 64, NQ * 2, lds_base + L_W, wave, lane); } \
                   else if (qs >= 0 && (j) < 9) slab_dma<(j) < 9 ? (j) : 0, (j) < 9 ? (j) + 1 : 0>(wq + qs * 64, NQ * 2, lds_base + L_W, wave, lane); \
                   else if (qs < 0 && (j) >= 4) PACE(j); \
                   if ((j) >= 4) KLD(j); if ((j) == 4) TL(33); WAITD(); if ((j) == 5) TL(34); } while (0)
      WK(0); WK(1); WK(2); WK(3);
      WK(4); WK(5);
      WK(6); WK(7);
      if (oj < 0 && qs < 0) WAITV(4);
      TL(24);
      if (ml >= 0) { const u32x4 c0 = *(const u32x4*)lp, c1 = *(const u32x4*)(lp + 1024); SILU_IMAGE(c0, c1, false); }
      lds_barrier();
      if (pz >= 0 && tid == 448) {
        const u32x4 v0_ = *(const u32x4*)(lds + L_B + 192), v1_ = *(const u32x4*)(lds + L_B + 208);
        const int d_ = (int)(v0_[0] + v0_[1] + v0_[2] + v0_[3] + v1_[0] + v1_[1] + v1_[2] + v1_[3]);
        st64(rs, W_FL + pz * 8, u32x2{e0 ^ (oj >= 0 ? pz & 1 : d_ > 0), e1});
      }
      SCHED();
      WK(8); WK(9); WK(10); WK(11);
      TL(25);
      PACE(12); REP4(KLD, 12, WAITD());
      TL(26);
      const int hg0 = (wave * 144 + lane) * 16, hg2 = (wave * 144 + 128 + n) * 16;
#define HREQ() do { dma_sc(ws + W_H, hg0, land); dma_sc(ws + W_H + 18432, hg0, land + 1024); dma_sc(ws + W_H + 1024, hg0, land + 2048); \
                    dma_sc(ws + W_H + 18432 + 1024, hg0, land + 3072); dma_sc(ws + W_H, hg2, land + 4096); dma_sc(ws + W_H + 18432, hg2, land + 5120); } while (0)
#define HGET() do { _Pragma("unroll") for (int i = 0; i < 6; ++i) hq[i] = *(const u32x4*)(lp + i * 1024); } while (0)
#define HQOK() __all((tag_x(hq[0], e0, e1) | tag_x(hq[1], e0, e1) | tag_x(hq[2], e0, e1) | tag_x(hq[3], e0, e1) | tag_x(hq[4], e0, e1) | tag_x(hq[5], e0, e1)) == 0u)
      if (qs >= 0) HREQ();
      PACE(16); REP4(KLD, 16, WAITD());
      if (qs >= 0) WAITV(4);
      TL(27);
      PACE(20); KLD(20); KLD(21);
      if (qs >= 0) {
        u32x4 hq[6]; HGET();
        _Pragma("nounroll") for (int sp = 0; sp < SPIN_CAP && !HQOK(); ++sp) { nap(16); HREQ(); WAITV(0); HGET(); }
        TL(2);
#define HSUM(a, b) cvt2(lo16(a) + lo16(b), hi16(a) + hi16(b))
        *(u32x2*)(lds + L_IMG + hg0 / 2) = u32x2{HSUM(hq[0][0], hq[1][0]), HSUM(hq[0][1], hq[1][1])};
        *(u32x2*)(lds + L_IMG + 512 + hg0 / 2) = u32x2{HSUM(hq[2][0], hq[3][0]), HSUM(hq[2][1], hq[3][1])};
        if (lane < 16) *(u32x2*)(lds + L_IMG + hg2 / 2) = u32x2{HSUM(hq[4][0], hq[5][0]), HSUM(hq[4][1], hq[5][1])};
      } else WAITD();
      KLD(22); KLD(23);
      if (qs >= 0) {
        u32x4 wf[9], af[9];
        lds_barrier();
        slab_frags(lds, L_W, wave, lane, wf);
        img_frags(lds, L_IMG, lane, af);
        const f32x4 s = half_sum(gemv9(wf, af));
        if (n < 8 && grp < 2) *(u32x2*)(lds + L_TILE + n * 128 + wave * 16 + grp * 8) = u32x2{cvt2(s[0], s[1]), cvt2(s[2], s[3])};
      } else WAITD();
      PACE(24); KLD(24); KLD(25);
      if (qs >= 0) {
        lds_barrier();
        if (wave < 2) {
          const u32x2 t2 = *(const u32x2*)(lds + L_TILE + tid * 8);
          st128(rs, W_Q + ((tid >> 4) * 1024 + qs * 16 + n) * 16, u32x4{t2[0], t2[1], e0, e1});
        }
        TL(3);
      }
      SCHED();
    }
    const unsigned char* qg = ws + W_Q + (b_att * 1024 + kvh * 64) * 16;
    KLD(26); WAITD(); KLD(27); WAITD(); PACE(28); KLD(28); WAITD(); KLD(29); WAITD();
    PACE(30); KLD(30); WAITD(); KLD(31); WAITD();
    TL(28);
    if (gs >= 0 && wave >= 2 && wave < 6) {
      const u16* mat = (wave & 1) ? wg : wu;
      const int g0 = ((3 - (int)((uintptr_t)mat >> 7) - xcd * 9) & 7) + (wave >= 4 ? 8 : 0);
      if (g0 < 9) dma_keep(mat, ((idx - 23) * 15 + min(lane, 14)) * 9216 + (xcd * 9 + g0) * 128, lds_base + L_B + 4096 + wave * 256);
    }
    PACE(32); VLD(0); WAITD(); VLD(1); WAITD(); PACE(34); VLD(2); WAITD(); VLD(3); WAITD();
#define QLOOK() do { if (wave == 1) dma_sc(qg, lane * 16, lds_base + L_B + 1024); } while (0)
    QLOOK();
#define FLOOK(l) do { dma_sc(ws + W_FL, (l) * 16, lds_base + L_B + 2048); dma_sc(ws + W_FL, 1024 + min(l, NPZ / 2 - 65) * 16, lds_base + L_B + 3072); } while (0)
    if (wave == 2) FLOOK(lane);
    PACE(36); VLD(4); WAITD(); VLD(5); WAITD(); PACE(38); VLD(6); WAITD(); VLD(7); WAITD();
    QLOOK();
    TL(29);
    PACE(40); VLD(8); WAITD(); VLD(9); WAITD(); PACE(42); QLOOK(); VLD(10); WAITD(); VLD(11); WAITD();
    TIE8(kb, 0); TIE8(kb, 8); TIE8(kb, 16); TIE8(kb, 24);
    PACE(44); QLOOK(); VLD(12); VLD(13);
    u32x4 qf[2];
    {
      const u32x4* ql = (const u32x4*)(lds + L_B + 1024 + ((n >> 3) * 32 + (n & 1) * 8 + grp * 2) * 16);
      u32x4 g[4];
      _Pragma("nounroll") for (int sp = 0; sp < SPIN_CAP; ++sp) {
        g[0] = ql[0]; g[1] = ql[1]; g[2] = ql[16]; g[3] = ql[17];
        if (__all((tag_x(g[0], e0, e1) | tag_x(g[1], e0, e1) | tag_x(g[2], e0, e1) | tag_x(g[3], e0, e1)) == 0u)) break;
        if (wave == 1 && sp >= 6) { dma_sc(qg, lane * 16, lds_base + L_B + 1024); WAITV(0); } else nap(4);
      }
      asm volatile("" ::: "memory");
      g[0] = ql[0]; g[1] = ql[1]; g[2] = ql[16]; g[3] = ql[17];
      qf[0] = u32x4{g[0][0], g[0][1], g[1][0], g[1][1]};
      qf[1] = u32x4{g[2][0], g[2][1], g[3][0], g[3][1]};
    }
    TL(4);
    constexpr float SCALE = 0.088388347648318440f;
    f32x2 sc[16];
    float mx = -INFINITY;
#pragma unroll
    for (int T = 0; T < 16; ++T) {
      f32x4 acc = mfma(kb[T * 2], qf[0], f32x4{0.f, 0.f, 0.f, 0.f});
      acc = mfma(kb[T * 2 + 1], qf[1], acc);
      sc[T] = f32x2{acc[0] + dppf<0xB1>(acc[1]), acc[2] + dppf<0xB1>(acc[3])};
      if (T == 7 || T == 15) { SCHED(); if (T == 7) VLD(14); else VLD(15); SCHED(); }
    }
#pragma unroll
    for (int T = 0; T < 16; ++T)
#pragma unroll
      for (int j = 0; j < 2; ++j) { const float v = rbf(rbf(sc[T][j]) * SCALE); sc[T][j] = v; mx = fmaxf(mx, v); }
    SCHED(); VLD(16); SCHED();
    mx = fmaxf(mx, __shfl_xor(mx, 16));
    mx = fmaxf(mx, __shfl_xor(mx, 32));
    float ls = 0.f;
#pragma unroll
    for (int T = 0; T < 16; ++T)
#pragma unroll
      for (int j = 0; j < 2; ++j) { sc[T][j] = __expf(sc[T][j] - mx); ls += sc[T][j]; }
    SCHED(); VLD(17); SCHED();
    ls += __shfl_xor(ls, 16);
    ls += __shfl_xor(ls, 32);
    float* stat = (float*)(lds + L_B);
    if (lane == 0 || lane == 8) *(f32x2*)(stat + (wave * 2 + (lane >> 3)) * 2) = f32x2{mx, ls};
    SCHED(); VLD(18); VLD(19); SCHED();
    if (wave == 2) {
      int l2 = lane; asm volatile("" : "+v"(l2));
      u32x4 a, b;
      _Pragma("nounroll") for (int sp = 0; sp < SPIN_CAP; ++sp) {
        a = *(const u32x4*)(lds + L_B + 2048 + l2 * 16); b = *(const u32x4*)(lds + L_B + 3072 + l2 * 16);
        if (__all(((((a[0] ^ e0) | (a[2] ^ e0) | (b[0] ^ e0) | (b[2] ^ e0)) & ~1u) | (a[1] ^ e1) | (a[3] ^ e1) | (b[1] ^ e1) | (b[3] ^ e1)) == 0u)) break;
        nap(1); FLOOK(l2); WAITV(0);
      }
      const unsigned long long mk = (1ull << (NPZ / 2 - 64)) - 1;
      const int cnt = __popcll(__ballot(a[0] != e0)) + __popcll(__ballot(a[2] != e0)) + __popcll(__ballot(b[0] != e0) & mk) + __popcll(__ballot(b[2] != e0) & mk);
      if (l2 == 0) *(int*)(lds + L_B + 128) = cnt > NPZ / 2 ? 8192 : 0;
    }
    lds_barrier();
    const int flip = __builtin_amdgcn_readfirstlane(*(const int*)(lds + L_B + 128));
    float M = -INFINITY, L = 0.f;
    {
      f32x2 t[8];
#pragma unroll
      for (int q = 0; q < 8; ++q) { t[q] = *(const f32x2*)(stat + (q * 2 + (n >> 3)) * 2); M = fmaxf(M, t[q].x); }
#pragma unroll
      for (int q = 0; q < 8; ++q) L = fmaf(__expf(t[q].x - M), t[q].y, L);
    }
    if (tid >= 4 && tid < 12) st128(rs, W_ST + (w * 8 + lane - 4) * 16, u32x4{__float_as_uint(M), __float_as_uint(L), e0, e1});
    SCHED();
    TL(30);
    PACE(52); VLD(20); WAITD(); VLD(21); WAITD();
    const int po = W_ST + ((w ^ 1) * 8 + (n >> 3) * 4) * 16;
    int l1 = lane; asm volatile("" : "+v"(l1));
    u32 km[4] = {(l1 & 4) ? 0u : 0xffffu, (l1 & 4) ? 0xffffu : 0u, (l1 & 4) ? 0u : 0xffff0000u, (l1 & 4) ? 0xffff0000u : 0u};
    int px = (l1 & 3) * 0x5555;
    asm volatile("" : "+v"(km[0]), "+v"(km[1]), "+v"(km[2]), "+v"(km[3]), "+v"(px));
    PACE(54);
    VLD(22); WAITD(); VLD(23); WAITD();
    u32x4 ps, ps1, ps2, ps3;
    ald128sc(ps, ws, po);
    PACE(56);
    VLD(24); WAITD(); VLD(25); WAITD();
    TL(31);
    ald128sc(ps1, ws, po);
    PACE(58);
    VLD(26); WAITD(); VLD(27); WAITD();
    ald128sc(ps2, ws, po);
    VLD(28); VLD(29); WAITV(8);
    asm volatile("" : "+v"(ps));
    TIE8(vb, 0); TIE8(vb, 8); TIE8(vb, 16);
    ald128sc(ps3, ws, po);
    VLD(30); VLD(31);
    if (!__all(tag_ok(ps, e0, e1))) {
      WAITV(8);
      asm volatile("" : "+v"(ps1));
      ps = ps1;
      if (!__all(tag_ok(ps, e0, e1))) {
        WAITV(5);
        asm volatile("" : "+v"(ps2));
        ps = ps2;
        if (!__all(tag_ok(ps, e0, e1))) {
          WAITV(2);
          asm volatile("" : "+v"(ps3));
          ps = ps3;
          if (!__all(tag_ok(ps, e0, e1))) {
            int sp = 0;
            _Pragma("nounroll") do { nap(1); ps = ld128(rs, po); } while (!__all(tag_ok(ps, e0, e1)) && ++sp < SPIN_CAP);
          }
        }
      }
    }
    float fac;
    {
      const float M2 = __uint_as_float(ps[0]), L2 = __uint_as_float(ps[1]);
      const float Mg = fmaxf(M, M2);
      const float Lg = __expf(M - Mg) * L + __expf(M2 - Mg) * L2;
      fac = __expf(mx - Mg) / Lg;
    }
    TL(5);
    u32x4 pf[16];
    u32 pp[16];
#pragma unroll
    for (int T = 0; T < 16; ++T) pp[T] = cvt2(sc[T][0] * fac, sc[T][1] * fac);
    SCHED();
#pragma unroll
    for (int T = 0; T < 16; ++T) {
      const u32 p = (u32)__builtin_amdgcn_mov_dpp((int)pp[T], 0xa0, 0xf, 0xf, true);
      pf[T] = u32x4{p & km[0], p & km[1], p & km[2], p & km[3]};
    }
    f32x4 oa = {0.f, 0.f, 0.f, 0.f};
#define PV(T) oa = smf(pf[T], __builtin_shufflevector(vb[2 * (T)], vb[2 * (T) + 1], 0, 1, 2, 3, 4, 5, 6, 7), oa, px)
#pragma unroll
    for (int T = 0; T < 12; ++T) PV(T);
    SCHED();
    int t2_ = threadIdx.x; asm volatile("" : "+v"(t2_));
    const int tid = t2_, lane = tid & 63, n = lane & 15, grp = lane >> 4;
    int oo = W_O + (kvh & 7) * 32768 + (b_att >> 2) * 16384 + ((kvh >> 3) * 8192 ^ flip) + (b_att & 3) * 2048 + s_att * 1024 + tid * 4;
    int mw_ = L_C + wave * 1024 + (((grp >> 1) * 2 + (n >> 3)) * 16 + (grp & 1) * 8 + (n & 7)) * 16, mr = L_C + tid * 4;
    asm volatile("" : "+v"(oo), "+v"(mw_), "+v"(mr), "+v"(pf[12]), "+v"(pf[13]), "+v"(pf[14]), "+v"(pf[15]));
    constexpr int L_H2 = L_W;
    constexpr int L_ACT = L_C + 4608;
    int ia, ra, ix, yo[2], fo[7], tw, tr_, tr4;
    const u32 gv = (lane >> 3) * (FF * 2) + (((lane & 7) ^ ((wave & 1) * 4 + ((lane >> 4) & 2))) << 4);
#define GU(j) do { const size_t o_ = ((size_t)((j) * 64 + wave * 8) * FF + gs * 64) * 2; dma((const char*)wg + o_, gv, lds_base + L_W + ((j) * 8 + wave) * 1024); \
                   dma((const char*)wu + o_, gv ^ 16u, lds_base + L_W + 73728 + ((j) * 8 + wave) * 1024); } while (0)
#define ML(j) dma((const char*)w_mod + ((size_t)((j) * 64 + wave * 8) * NMOD + ml * 64) * 2, (lane >> 3) * (NMOD * 2) + (((lane & 7) ^ ((wave & 1) * 4 + ((lane >> 4) & 2))) << 4), lds_base + L_W + ((j) * 8 + wave) * 1024)
    const bool late = gs >= 0 || mw >= 18;
    if (late) {
      BAR();
      if (gs >= 0) { GU(0); GU(1); } else { ML(0); ML(1); ML(2); ML(3); }
      {
    const int b = wave;
    ia = lds_base + L_H2 + im2_off(b, lane);
    ra = L_H2 + (grp * 16 + (n & 8) + ((n & 7) ^ (grp * 2 + (n >> 3)))) * 16; ix = n < 8 ? 0x4444 : 0xeeee;
    tw = lds_base + L_ACT + ((grp & 1) * 4 + (grp >> 1) * 2 + (n >> 3)) * 144 + (wave >> 1) * 32 + (n & 2) * 8 + (wave & 1) * 8 + (n & 4) + (n & 1) * 2; tr_ = L_ACT + (n & 7) * 144 + grp * 32 + (n & 8) * 2;
    tr4 = L_ACT + ((n & 7) ^ (wave >> 2)) * 144 + grp * 32 + (n & 8) * 2;
    {
      const int g0 = wave * 4 + (grp & 2), g1 = 32 + (grp & 2) + (wave >> 2), yb = W_Y + ((grp & 1) * 8192 ^ flip) + (gs & 31) * 256;
      yo[0] = yb + (gs < 64 ? g0 * 32768 + (gs >> 5) * 16384 : (72 + (g0 >> 2)) * 16384 + (g0 & 3) * 2048) + n * 16;
      yo[1] = yb + (gs < 64 ? g1 * 32768 + (gs >> 5) * 16384 : (72 + (g1 >> 2)) * 16384 + (g1 & 3) * 2048) + (wave & 3) * 64 + n * 4;
    }
    {
      const int st = idx - 23;
      fo[0] = W_GB + (xcd * 144 + st * 16 + (lane >> 2)) * 16;
      fo[1] = W_X1 + (xcd * 152 + st * 16 + (lane >> 2)) * 16; fo[2] = W_X1 + (xcd * 152 + 144) * 16;
      fo[3] = W_Y + ((xcd >> 2) * 8192 ^ flip) + tid * 16;
      fo[4] = L_H2 + (st * 64 + (lane & 56) + (xcd ^ (lane >> 3))) * 16 + (lane & 7) * 2;
      fo[5] = L_C + (lane & 3) * 64 + (st < 8 ? (lane >> 3) + (lane & 4) * 2 : lane >> 2) * 4;
      fo[6] = (xcd * D + st * 64 + lane) * 2;
    }
    asm volatile("" : "+v"(ia), "+v"(ra), "+v"(ix), "+v"(tw), "+v"(tr_), "+v"(tr4), "+v"(yo[0]), "+v"(yo[1]),
                 "+v"(fo[0]), "+v"(fo[1]), "+v"(fo[2]), "+v"(fo[3]), "+v"(fo[4]), "+v"(fo[5]), "+v"(fo[6]));
      }
      WAITV(6);
    } else WAITV(2);
    asm volatile("s_nop 0 ; both branches above cover V up to the last two" : "+v"(vb[24]), "+v"(vb[25]), "+v"(vb[26]), "+v"(vb[27]), "+v"(vb[28]), "+v"(vb[29]));
    asm volatile("" :: "v"(ps1), "v"(ps2), "v"(ps3));
    PV(12); PV(13); PV(14);
    SCHED();
    asm volatile("s_waitcnt vmcnt(4)\n\ts_cmp_ge_i32 %2, 0\n\ts_cbranch_scc1 1 ; throttle\n\ts_waitcnt vmcnt(0)" : "+v"(vb[30]), "+v"(vb[31]) : "s"(max(gs, mw - 18)) : "memory", "scc");
    T2(56, );
    PV(15);
    TL(6);
    *(f32x4*)(lds + mw_) = oa;
    T2(54, );
    lds_barrier();
    T2(55, );
    if (wave < 4) {
      f32x2 m[4];
#pragma unroll
      for (int q = 0; q < 4; ++q) m[q] = f32x2{*(const float*)(lds + mr + q * 2048), *(const float*)(lds + mr + q * 2048 + 1024)};
      SCHED();
      const f32x2 o = (m[0] + m[1]) + (m[2] + m[3]);
      __builtin_amdgcn_raw_buffer_store_b32(__float_as_uint(o.x + o.y), rs, oo, 0, 16);
    }
    TL(7); SCHED();
  if (gs >= 0) {
    const int b = wave;
    BAR(); SCHED();
    u32x8 bw[9];
    u32x8 wd8[5];
    u32x4 gq, xq, rq;
    const int ga0 = L_W + ((lane >> 1) & 1) * 73728 + ((grp >> 1) * 16 + (grp & 1) * 4 + ((n >> 2) & 1) + (n & 8)) * 128 + ((wave ^ ((grp & 1) * 2 + ((n >> 1) & 4)) ^ ((lane >> 1) & 1)) << 4) + (lane & 1) * 8;
#define GUF(S0, S1) do { _Pragma("unroll") for (int s = S0; s < S1; ++s) { const int a_ = ga0 + (s >> 2) * 32768, o_ = (s & 3) * 8192; \
      const u32x2 r0 = trd(lds, a_ + o_), r1 = trd(lds, a_ + o_ + 256), r2 = trd(lds, a_ + o_ + 4096), r3 = trd(lds, a_ + o_ + 4352); \
      bw[s] = u32x8{r0[0], r0[1], r1[0], r1[1], r2[0], r2[1], r3[0], r3[1]}; } } while (0)
    {
      u32x4 rd[8];
      const char* db = (const char*)(wd + (size_t)gs * 64 * D + wave * 64);
      const u32 dv = (n >> 3) * 36864 + grp * 9216 + (n & 7) * 16;
#pragma unroll
      for (int i = 0; i < 8; ++i) ald128nt(rd[i], db + i * 1152, dv);
      {
        const int r = lane >> 3;
        dma((const char*)(wd + (size_t)(gs * 64 + wave * 8) * D + 512), r * 1152 + (((lane & 7) ^ (r & 2) ^ ((wave & 1) * 4)) << 4), lds_base + L_B + wave * 1024);
      }
      TL(19);
      asm volatile("s_waitcnt vmcnt(9)\n\ts_barrier" ::: "memory");
      TL(22);
      GUF(0, 2);
      SCHED();
#define GU2(i) do { const int u_ = 16 + (wave & 3) + 4 * (i); const size_t o_ = ((size_t)u_ * 8 * FF + gs * 64) * 2; dma((const char*)wg + o_, gv, lds_base + L_W + u_ * 1024); \
                    dma((const char*)wu + o_, gv ^ 16u, lds_base + L_W + 73728 + u_ * 1024); } while (0)
#define WAIT2(c, a, b) "s_waitcnt vmcnt(" STR(a) ")\n\ts_cmp_" c "_u32 %[wv], 4\n\ts_cbranch_scc1 1 ; throttle\n\ts_waitcnt vmcnt(" STR(b) ")"
      if (wave >= 4) { GU2(0); GU2(1); TL(20); }
      asm volatile(WAIT2("ge", 5, 1) : "+v"(rd[0]), "+v"(rd[1]), "+v"(rd[2]), "+v"(rd[3]), "+v"(rd[4]), "+v"(rd[5]), "+v"(rd[6]), "+v"(rd[7]) : [wv] "s"(wave) : "memory", "scc");
#pragma unroll
      for (int j = 0; j < 4; ++j)
#pragma unroll
        for (int t = 0; t < 4; ++t) {
          const u32 a = (t & 1) ? phi(rd[2 * j][t >> 1], rd[2 * j + 1][t >> 1]) : plo(rd[2 * j][t >> 1], rd[2 * j + 1][t >> 1]);
          const u32 b_ = (t & 1) ? phi(rd[2 * j][2 + (t >> 1)], rd[2 * j + 1][2 + (t >> 1)]) : plo(rd[2 * j][2 + (t >> 1)], rd[2 * j + 1][2 + (t >> 1)]);
          wd8[t][j] = (u32)__builtin_amdgcn_update_dpp((int)a, (int)b_, 0x128, 0xf, 0xc, false);
          wd8[t][4 + j] = (u32)__builtin_amdgcn_update_dpp((int)b_, (int)a, 0x128, 0xf, 0x3, false);
        }
#pragma unroll
      for (int t = 0; t < 4; ++t) asm volatile("" : "+v"(wd8[t]));
      TL(21);
    }
    f32x4 dd[5] = {};
    int ys = gs < 64 ? 32768 : 2048;
    asm volatile("" : "+{v[176:179]}"(dd[0]), "+{v[180:183]}"(dd[1]), "+{v[184:187]}"(dd[2]), "+{v[188:191]}"(dd[3]), "+{v[192:195]}"(dd[4]), "+s"(ys));
    f32x4 d = {0.f, 0.f, 0.f, 0.f};
    int h0 = W_H2 + (b >> 2) * 16384 + ((xcd >> 2) * 8192 ^ flip) + (b & 3) * 1152 + lane * 16;
    int a0 = L_B + (8 * grp + (n >> 2)) * 128 + ((((wave & 3) * 2 + ((lane >> 1) & 1)) ^ ((n >> 2) & 2) ^ ((grp & 1) * 4)) << 4) + (lane & 1) * 8;
    asm volatile("" : "+{v[196:199]}"(d), "+v"(h0), "+v"(a0));
#define WD4() do { const u32x2 r0 = trd(lds, a0), r1 = trd(lds, a0 + 512), r2 = trd(lds, a0 + 4096), r3 = trd(lds, a0 + 4608); \
      wd8[4] = u32x8{r0[0], r0[1], r1[0], r1[1], r2[0], r2[1], r3[0], r3[1]}; } while (0)
    {
      u32x4 hp[2]; u32 cnt;
      if (wave < 4) { GU2(2); GU2(3); GU2(4); GU2(5); GU2(6); GU2(7); GU2(8); }
      asm volatile(WAIT2("lt", 10, 0) "\n\ts_waitcnt lgkmcnt(0)\n\ts_barrier" :: [wv] "s"(wave) : "memory", "scc");
      TL(40);
      GUF(2, 4); WD4();
      SCHED();
      if (wave < 4) { GU2(9); GU2(10); GU2(11); GU2(12); }
      asm volatile("s_waitcnt vmcnt(10)\n\ts_barrier" ::: "memory");
      TL(23);
      GUF(4, 6);
      SCHED();
      if (wave < 4) { GU2(13); TL(20); }
      asm volatile("s_waitcnt vmcnt(8)\n\ts_barrier\n"
                   "s_mov_b64 exec, 0xff\nbuffer_load_dwordx4 %1, %2, %3, 0 offen offset:1024 sc1\ns_mov_b64 exec, -1\nbuffer_load_dwordx4 %0, %2, %3, 0 offen sc1"
                   : "=&{v[204:207]}"(hp[0]), "=&{v[208:211]}"(hp[1]) : "v"(h0), "s"(rs) : "memory", "scc");
      TL(50);
      GUF(6, 7);
      asm volatile("s_waitcnt vmcnt(6)\n\ts_barrier" ::: "memory");
      GUF(7, 8);
      asm volatile("s_waitcnt vmcnt(2)\n\ts_barrier" ::: "memory");
      GUF(8, 9);
#pragma unroll
      for (int s = 0; s < 9; ++s) asm volatile("" : "+v"(bw[s]));
      asm volatile("" : "+v"(wd8[4]));
      TL(12);
      asm volatile(
        "s_mov_b32 %3, 0x8000\ns_branch 1f ; throttle\n0:\ns_sleep 2\ns_waitcnt lgkmcnt(0)\ns_and_b64 exec, vcc, 0xff\nbuffer_load_dwordx4 %2, %4, %6, 0 offen offset:1024 sc1 ; throttle\n"
        "s_mov_b64 exec, vcc\nbuffer_load_dwordx4 %1, %4, %6, 0 offen sc1 ; throttle\n1:\ns_mov_b64 exec, -1\nv_mov_b32 v212, 0\ns_mov_b64 exec, 0xff\ns_sub_u32 %3, %3, 1\ns_waitcnt vmcnt(1)\n"
        "ds_write_b128 %5, %2 offset:8192\nv_max_u32 v212, v208, v209\nv_max3_u32 v212, v212, v210, v211\ns_mov_b64 exec, -1\ns_waitcnt vmcnt(0)\nds_write_b128 %5, %1\n"
        "v_max3_u32 v212, v212, v204, v205\nv_max3_u32 v212, v212, v206, v207\nv_cmp_eq_u32 vcc, -1, v212\ns_cbranch_scc1 2f ; throttle\ns_cbranch_vccnz 0b ; throttle\n2:\n" ATL(13)
        "s_waitcnt lgkmcnt(0)\ns_barrier"
        : "+v"(tlv_), "+{v[204:207]}"(hp[0]), "+{v[208:211]}"(hp[1]), "=&s"(cnt) : "v"(h0), "v"(ia), "s"(rs) : "memory", "vcc", "scc", "v212", "s72", "s73");
      T2(47, );
      u32x4 af[9];
#pragma unroll
      for (int s = 0; s < 9; ++s) af[s] = *(const u32x4*)(lds + ra + s * 1024);
      SCHED();
      if (wave == 7) { ald128sc(gq, ws, fo[0]); ald128sc(xq, ws, fo[1]); ald128sc(rq, ws, fo[2]); }
#pragma unroll
      for (int s = 0; s < 9; ++s) d = smf(af[s], bw[s], d, ix);
      T2(48, "+v"(d));
      asm volatile("s_nop 7\nv_permlane32_swap_b32 v197, v199\nv_permlane32_swap_b32 v196, v198\nv_add_f32 v197, v197, v199\nv_add_f32 v196, v196, v198\n"
        "v_cvt_pk_bf16_f32 v197, 0, v197\nv_cvt_pk_bf16_f32 v198, 0, v196\nv_cvt_pk_bf16_f32 v196, 0, v196\nv_mov_b32_dpp v196, v197 row_ror:8 row_mask:0xf bank_mask:0xc\n"
        "v_mul_f32 v199, 0xbfb8aa3b, v196\nv_exp_f32 v199, v199\nv_mov_b32_dpp v197, v198 row_ror:8 row_mask:0xf bank_mask:0x3\nv_add_f32 v199, 1.0, v199\nv_rcp_f32 v198, v199\ns_nop 0\n"
        "v_mul_f32 v200, v196, v198\nv_fma_f32 v201, -v200, v199, v196\nv_fmac_f32 v200, v201, v198\nv_div_fixup_f32 v196, v200, v199, v196\n"
        "v_cvt_pk_bf16_f32 v196, 0, v196\nv_mul_f32 v196, v196, v197\nv_cvt_pk_bf16_f32 v196, v196, v196\nds_write_b16 %1, v196\ns_waitcnt lgkmcnt(0)\ns_barrier"
        : "+{v[196:199]}"(d) : "v"(tw) : "memory", "v200", "v201");
    }
    TL(14);
    {
      const u32x4 af = *(const u32x4*)(lds + tr_), a4 = *(const u32x4*)(lds + tr4);
      asm volatile(
        "v_smfmac_f32_16x16x64_bf16 v[176:179], %5, %7, %12\nv_smfmac_f32_16x16x64_bf16 v[180:183], %5, %8, %12\nv_smfmac_f32_16x16x64_bf16 v[184:187], %5, %9, %12\n"
        "v_smfmac_f32_16x16x64_bf16 v[188:191], %5, %10, %12\nv_smfmac_f32_16x16x64_bf16 v[192:195], %6, %11, %12\ns_nop 3\nv_permlane32_swap_b32 v176, v178\n"
        "v_permlane32_swap_b32 v180, v182\nv_permlane32_swap_b32 v184, v186\nv_permlane32_swap_b32 v188, v190\nv_add_f32 v196, v176, v178\nv_add_f32 v197, v180, v182\n"
        "v_add_f32 v198, v184, v186\nv_add_f32 v199, v188, v190\nbuffer_store_dwordx4 v[196:199], %13, %15, 0 offen sc1\nv_permlane32_swap_b32 v177, v179\n"
        "v_permlane32_swap_b32 v181, v183\nv_permlane32_swap_b32 v185, v187\nv_permlane32_swap_b32 v189, v191\nv_add_f32 v200, v177, v179\nv_add_f32 v201, v181, v183\n"
        "v_add_f32 v202, v185, v187\nv_add_f32 v203, v189, v191\nbuffer_store_dwordx4 v[200:203], %13, %15, %16 offen sc1\nv_permlane32_swap_b32 v192, v194\nv_add_f32 v193, v192, v194\n"
        "buffer_store_dword v193, %14, %15, 0 offen sc1\n"
        : "+{v[176:179]}"(dd[0]), "+{v[180:183]}"(dd[1]), "+{v[184:187]}"(dd[2]), "+{v[188:191]}"(dd[3]), "+{v[192:195]}"(dd[4])
        : "v"(af), "v"(a4), "v"(wd8[0]), "v"(wd8[1]), "v"(wd8[2]), "v"(wd8[3]), "v"(wd8[4]), "v"(ix), "v"(yo[0]), "v"(yo[1]), "s"(rs), "s"(ys)
        : "memory", "v196", "v197", "v198", "v199", "v200", "v201", "v202", "v203");
    }
    TL(15);
    {
      float gbv = 0.f, x1v = 0.f, h2 = 0.f, rsel = 0.f;
      if (wave == 7) {
        asm volatile("s_waitcnt vmcnt(3)" : "+v"(gq), "+v"(xq), "+v"(rq) :: "memory");
        if (!__all((tag_x(gq, e0, e1) | tag_x(xq, e0, e1) | tag_x(rq, e0, e1)) == 0u)) {
          int sp = 0;
          _Pragma("nounroll") do { nap(1); gq = ld128(rs, fo[0]); xq = ld128(rs, fo[1]); rq = ld128(rs, fo[2]); } while (!__all((tag_x(gq, e0, e1) | tag_x(xq, e0, e1) | tag_x(rq, e0, e1)) == 0u) && ++sp < SPIN_CAP);
        }
        const u32 gw = (lane & 2) ? gq[1] : gq[0], xw = (lane & 2) ? xq[1] : xq[0];
        gbv = (lane & 1) ? hi16(gw) : lo16(gw); x1v = (lane & 1) ? hi16(xw) : lo16(xw);
        h2 = b2f(*(const u16*)(lds + fo[4])); rsel = __uint_as_float(rq[0]);
        asm volatile("" : "+v"(gbv), "+v"(x1v), "+v"(h2), "+v"(rsel));
      } else nap(5);
      rsrc_t ro = __builtin_amdgcn_make_buffer_rsrc(out, 0, 8 * D * 2, 0x00020000);
      asm volatile("" : "+s"(ro));
      T2(51, );
      const int gy = (idx - 23) * 4 + (xcd & 3);
      u32 cnt;
      asm volatile(
        "v_mov_b64 v[48:49], 0\nv_mov_b64 v[50:51], 0\ns_mov_b64 s[64:65], -1\ns_mov_b64 s[66:67], -1\ns_mov_b32 s68, %8\ns_mov_b32 s69, %8\ns_mov_b32 %1, 0x8000\ns_branch 1f ; throttle\n0:\n"
        "v_max_u32 v52, v40, v41\nv_max_u32 v53, v44, v45\nv_max_u32 v54, v48, v49\nv_max3_u32 v52, v52, v42, v43\n"
        "v_max3_u32 v53, v53, v46, v47\nv_max3_u32 v54, v54, v50, v51\nv_cmp_eq_u32 s[64:65], -1, v52\nv_cmp_eq_u32 s[66:67], -1, v53\nv_cmp_eq_u32 s[68:69], -1, v54\n"
        "s_or_b64 vcc, s[64:65], s[66:67]\ns_or_b64 vcc, vcc, s[68:69]\ns_cbranch_scc0 2f ; throttle\ns_sub_u32 %1, %1, 1\ns_cbranch_scc1 2f ; throttle\n1:\n"
        "s_mov_b64 exec, s[68:69]\nbuffer_load_dwordx4 v[48:51], %2, %4, %7 offen sc1\ns_mov_b64 exec, s[64:65]\nbuffer_load_dwordx4 v[40:43], %2, %4, %5 offen sc1\n"
        "s_mov_b64 exec, s[66:67]\nbuffer_load_dwordx4 v[44:47], %2, %4, %6 offen sc1\ns_mov_b64 exec, -1\ns_waitcnt vmcnt(1)\n"
        "v_pk_add_f32 v[56:57], v[40:41], v[48:49]\nv_pk_add_f32 v[58:59], v[42:43], v[50:51]\ns_waitcnt vmcnt(0)\nv_pk_add_f32 v[56:57], v[56:57], v[44:45]\nv_pk_add_f32 v[58:59], v[58:59], v[46:47]\n"
        "s_nop 1\nv_permlane32_swap_b32 v56, v58\nv_permlane32_swap_b32 v57, v59\nv_pk_add_f32 v[56:57], v[56:57], v[58:59]\ns_nop 1\nv_permlane16_swap_b32 v56, v57\nv_add_f32 v55, v56, v57\n"
        "ds_write_b32 %3, v55\nv_cmp_u_f32 vcc, v55, v55\ns_cbranch_vccnz 0b ; throttle\n2:\n" ATL(16) "s_waitcnt lgkmcnt(0)\ns_barrier"
        : "+v"(tlv_), "=&s"(cnt)
        : "v"(fo[3]), "v"(lds_base + mr), "s"(rs), "s"(gy * 32768), "s"(gy * 32768 + 16384), "s"((72 + (gy >> 2)) * 16384 + (gy & 3) * 2048), "s"((wave - 2) >> 31)
        : "memory", "vcc", "scc", "s64", "s65", "s66", "s67", "s68", "s69", "s72", "s73", "v40", "v41", "v42", "v43", "v44", "v45", "v46", "v47", "v48", "v49", "v50", "v51",
          "v52", "v53", "v54", "v55", "v56", "v57", "v58", "v59");
      T2(53, );
      if (wave == 7) {
        f32x2 t[4];
#pragma unroll
        for (int ww = 0; ww < 4; ++ww) t[ww] = f32x2{*(const float*)(lds + fo[5] + ww * 512), *(const float*)(lds + fo[5] + ww * 512 + 256)};
        SCHED();
        const f32x2 t2 = (t[0] + t[1]) + (t[2] + t[3]);
        const float y2 = rbf(rbf(t2.x + t2.y) * rsel);
        const float res = x1v + rbf(gbv * rbf(h2 + y2));
        __builtin_amdgcn_raw_buffer_store_b16((u16)(cvt2(res, 0.f) & 0xffffu), ro, fo[6], 0, 0);
      }
      TL(17);
      TLOUT();
      asm volatile(".Lcode_end:");
      __builtin_amdgcn_endpgm();
    }
  }
  {
  int t3_ = threadIdx.x; asm volatile("" : "+v"(t3_));
  const int tid = t3_, lane = tid & 63, n = lane & 15, grp = lane >> 4;
  if (qs >= 0) {
    const u16* mat = (wave & 1) ? wg : wu;
    BAR();
    const int g0 = ((3 - (int)((uintptr_t)mat >> 7) - xcd * 9) & 7) + (wave >= 4 ? 8 : 0);
    if (g0 < 9) dma_keep(mat, (128 + (idx - 6) * 40 + ((wave >> 1) & 1) * 20 + min(lane, 19)) * 9216 + (xcd * 9 + g0) * 128, lds_base + L_W + wave * 256);
    const int l9 = min(lane, 44), rr = l9 / 9;
#pragma unroll
    for (int i = 0; i < 2; ++i) dma_keep(mat, (128 + ((i * 8 + idx - 6) * 4 + (wave >> 1)) * 5 + rr) * 9216 + (xcd * 9 + l9 - rr * 9) * 128, lds_base + L_W + wave * 256);
  }
#define MH() do { const int l9 = min(lane, 35), rr = l9 / 9; \
    dma_keep((wave & 1) ? wg : wu, (448 + (idx & 3) * 16 + (wave >> 1) * 4 + rr) * 9216 + (xcd * 9 + l9 - rr * 9) * 128, lds_base + 98304 + wave * 256); } while (0)
  if (mw == 16 || mw == 17) { BAR(); MH(); }
  if (ml >= 0) {
    BAR();
    if (mw < 18) { ML(0); ML(1); ML(2); ML(3); }
    ML(4); ML(5); ML(6); ML(7); ML(8);
    TL(38);
    u32x4 af[9];
    f32x4 d = {0.f, 0.f, 0.f, 0.f};
    const int q = n >> 2, ch = (ml - 18) / 9, sl = ml - 18 - ch * 9;
    int a0 = L_W + ((grp >> 1) * 16 + (grp & 1) * 4 + (q & 1) + (q >> 1) * 8) * 128 + (((wave & 3) ^ ((grp & 1) + (q & 2))) << 5) + (lane & 3) * 8, ix = n < 8 ? 0x4444 : 0xeeee;
    int mo = (ch < 3 ? W_M + ((grp >> 1) * 2 + (n & 1)) * 16384 + ((grp & 1) * 8192 ^ flip) + ch * 2304 : W_GB + ((grp & 1) * 4 + (grp >> 1) * 2 + (n & 1)) * 2304) + (sl * 16 + wave * 4 + q) * 16;
#pragma unroll
    for (int s = 0; s < 9; ++s) af[s] = *(const u32x4*)(lds + L_IM2 + (grp * 16 + (n & 8) + ((n & 7) ^ (grp * 2 + (n >> 3)))) * 16 + s * 1024);
#define MLS(S0, S1) do { if (wave < 4) { u32x8 f_[S1 - S0]; _Pragma("unroll") for (int s = S0; s < S1; ++s) { const int a_ = a0 + (s >> 2) * 32768, o_ = (s & 3) * 8192; \
      const u32x2 r0 = trd(lds, a_ + o_), r1 = trd(lds, a_ + o_ + 256), r2 = trd(lds, a_ + o_ + 4096), r3 = trd(lds, a_ + o_ + 4352); \
      f_[s - S0] = u32x8{r0[0], r0[1], r1[0], r1[1], r2[0], r2[1], r3[0], r3[1]}; } SCHED(); \
      _Pragma("unroll") for (int s = S0; s < S1; ++s) d = smf(af[s], f_[s - S0], d, ix); } } while (0)
    asm volatile("s_waitcnt vmcnt(5)\n\ts_barrier" : "+v"(a0), "+v"(ix), "+v"(mo) :: "memory");
    MH();
    MLS(0, 4);
    asm volatile("s_waitcnt vmcnt(3)\n\ts_barrier" : "+v"(d) :: "memory");
    MLS(4, 7);
    asm volatile("s_waitcnt vmcnt(1)\n\ts_barrier" : "+v"(d), "+v"(af[7]), "+v"(af[8]) :: "memory");
    TL(39);
    MLS(7, 9);
    if (wave < 4) {
      float r[2];
#pragma unroll
      for (int j = 0; j < 2; ++j) {
        const auto t = __builtin_amdgcn_permlane32_swap(__float_as_uint(d[j]), __float_as_uint(d[j + 2]), false, false);
        r[j] = __uint_as_float(t[0]) + __uint_as_float(t[1]);
      }
      const float o = dppf<0xB1>((n & 1) ? r[0] : r[1]);
      const u32 pk = (n & 1) ? cvt2(o, r[1]) : cvt2(r[0], o);
      const u32 p2 = (u32)__builtin_amdgcn_mov_dpp((int)pk, 0x4E, 0xf, 0xf, true);
      if (!(n & 2)) st128(rs, mo, u32x4{pk, p2, e0, e1});
    }
    if (ml < 45) TL(18); else TL(36);
  }

  if (lnb >= 0 && wave < 3) {
    const int b = lnb, g = lane < 48 ? wave * 48 + lane : 0, hb = (b >> 2) * 8192 ^ flip;
    const bool on = lane < 48;
    float* ex = (float*)(lds + L_TILE);
    const u32x2 xa = __builtin_amdgcn_raw_buffer_load_b64(__builtin_amdgcn_make_buffer_rsrc((void*)x, 0, 8 * D * 2, 0x00020000), (b * D + g * 4) * 2, 0, 0);
    const u32x2 wra = __builtin_amdgcn_raw_buffer_load_b64(__builtin_amdgcn_make_buffer_rsrc((void*)w_r, 0, D * 2, 0x00020000), g * 8, 0, 0);
    f32x2 wr[2] = {up2(wra[0]), up2(wra[1])};
    asm volatile("" : "+v"(wr[0]), "+v"(wr[1]));
    const int ho = W_H2 + (b >> 2) * 16384 + hb + (b & 3) * 1152 + g * 8;
    u32x2 hw; u32 cnt;
#define XPL(c) "buffer_load_dwordx4 v[100:103], %5, %12, 0 offen sc1" c "\nbuffer_load_dwordx4 v[104:107], %5, %12, 0 offen offset:2304 sc1" c "\n" \
      "buffer_load_dwordx4 v[108:111], %5, %12, %14 offen sc1" c "\nbuffer_load_dwordx4 v[112:115], %5, %12, %15 offen sc1" c "\n" \
      "buffer_load_dwordx4 v[116:119], %5, %12, %15 offen offset:2304 sc1" c "\nbuffer_load_dwordx4 v[120:123], %5, %12, %16 offen sc1" c "\n" \
      "buffer_load_dwordx4 v[124:127], %5, %12, %17 offen sc1" c "\nbuffer_load_dwordx4 v[128:131], %5, %12, %17 offen offset:2304 sc1" c "\n"
#define RB(d, s) "v_cvt_pk_bf16_f32 v" #d ", 0, v" #s "\n"
#define RB4 RB(132, 132) RB(133, 133) RB(134, 134) RB(135, 135)
#define UP4(a, b, c, d) "v_lshlrev_b32 v" #c ", 16, v" #b "\nv_and_b32 v" #d ", 0xffff0000, v" #b "\nv_and_b32 v" #b ", 0xffff0000, v" #a "\nv_lshlrev_b32 v" #a ", 16, v" #a "\n"
#define ACC(n, a, b) "s_waitcnt vmcnt(" #n ")\nv_pk_add_f32 v[132:133], v[132:133], " a "\nv_pk_add_f32 v[134:135], v[134:135], " b "\n"
#define RED(c) "s_nop 1\nv_add_f32_dpp v150, v150, v150 " c "\n"
#define SUB " op_sel_hi:[1,0] neg_lo:[0,1] neg_hi:[0,1]\n"
    asm volatile(
      "s_mov_b32 s68, 0x3ae38e39\nv_mov_b64 v[150:151], 0\nv_mov_b64 v[154:155], %13\ns_mov_b32 %2, 0x8000\n0:\n"
      "buffer_load_dwordx4 v[156:159], %6, %12, 0 offen sc1\nbuffer_load_dwordx4 v[160:163], %6, %12, 0 offen offset:2304 sc1\nbuffer_load_dwordx4 v[164:167], %6, %12, %14 offen sc1\n"
      XPL("") "s_waitcnt vmcnt(8)\nv_cmp_ne_u64 s[64:65], %13, v[158:159]\nv_cmp_ne_u64 s[66:67], %13, v[162:163]\nv_cmp_ne_u64 vcc, %13, v[166:167]\n"
      "s_or_b64 s[64:65], s[64:65], s[66:67]\ns_or_b64 vcc, vcc, s[64:65]\ns_cbranch_scc0 4f ; throttle\ns_sleep 2\ns_sub_u32 %2, %2, 1\ns_cbranch_scc0 0b ; throttle\n4:\n" ATL(37)
      UP4(164, 165, 166, 167) UP4(156, 157, 158, 159) "v_pk_add_f32 v[164:165], v[164:165], 1.0 op_sel_hi:[1,0]\nv_pk_add_f32 v[166:167], v[166:167], 1.0 op_sel_hi:[1,0]\n"
      UP4(160, 161, 162, 163) RB(164, 164) RB(165, 165) RB(166, 166) RB(167, 167) "s_mov_b32 %2, 0x8000\ns_branch 1f ; throttle\n3:\n"
      "v_max3_u32 v142, v100, v101, v102 ; throttle\nv_max3_u32 v143, v103, v104, v105 ; throttle\nv_max3_u32 v144, v106, v107, v108 ; throttle\nv_max3_u32 v145, v109, v110, v111 ; throttle\n"
      "v_max3_u32 v142, v142, v112, v113 ; throttle\nv_max3_u32 v143, v143, v114, v115 ; throttle\nv_max3_u32 v144, v144, v116, v117 ; throttle\nv_max3_u32 v145, v145, v118, v119 ; throttle\n"
      "v_max3_u32 v142, v142, v120, v121 ; throttle\nv_max3_u32 v143, v143, v122, v123 ; throttle\nv_max3_u32 v144, v144, v124, v125 ; throttle\nv_max3_u32 v145, v145, v126, v127 ; throttle\n"
      "v_max3_u32 v142, v142, v128, v129 ; throttle\nv_max3_u32 v143, v143, v130, v131 ; throttle\nv_max3_u32 v142, v142, v144, v145\nv_max_u32 v142, v142, v143\n"
      "v_cmp_eq_u32 vcc, -1, v142\ns_cbranch_vccz 2f ; throttle\ns_sub_u32 %2, %2, 1\ns_cbranch_scc1 2f ; throttle\n" XPL(" ; throttle")
      "1:\ns_waitcnt vmcnt(6)\nv_pk_add_f32 v[132:133], v[100:101], v[104:105]\nv_pk_add_f32 v[134:135], v[102:103], v[106:107]\n"
      ACC(5, "v[108:109]", "v[110:111]") ACC(4, "v[112:113]", "v[114:115]") ACC(3, "v[116:117]", "v[118:119]") ACC(2, "v[120:121]", "v[122:123]")
      ACC(1, "v[124:125]", "v[126:127]") ACC(0, "v[128:129]", "v[130:131]")
      RB(132, 132) RB(133, 133) "v_pk_add_f32 v[142:143], v[132:133], v[134:135]\n" RB(134, 134) "v_cmp_u_f32 vcc, v142, v143\n" RB(135, 135) "s_cbranch_vccnz 3b ; throttle\n2:\n" ATL(10)
      "v_pk_mul_f32 v[132:133], v[132:133], v[156:157]\nv_pk_mul_f32 v[134:135], v[134:135], v[158:159]\n" RB4
      "v_pk_add_f32 v[132:133], v[132:133], %3\nv_pk_add_f32 v[134:135], v[134:135], %4\n" RB(136, 132) RB(137, 133) "v_readlane_b32 s70, v136, 63\n" RB(138, 134) RB(139, 135)
      "v_pk_add_f32 v[142:143], v[136:137], s[70:71]" SUB "v_pk_add_f32 v[144:145], v[138:139], s[70:71]" SUB
      "v_pk_mul_f32 v[146:147], v[142:143], v[142:143]\nv_pk_add_f32 v[148:149], v[142:143], v[144:145]\nv_pk_fma_f32 v[146:147], v[144:145], v[144:145], v[146:147]\n"
      "v_cvt_pk_bf16_f32 v152, v136, v137\nv_add_f32_dpp v150, v148, v149 quad_perm:[0,1,2,3] row_mask:0x7 bank_mask:0xf\n"
      "v_add_f32_dpp v151, v146, v147 quad_perm:[0,1,2,3] row_mask:0x7 bank_mask:0xf\nv_cvt_pk_bf16_f32 v153, v138, v139\ns_nop 0\n"
      "v_permlane32_swap_b32 v150, v151\nv_add_f32 v150, v150, v151\n"
      RED("quad_perm:[1,0,3,2] row_mask:0xf bank_mask:0xf") RED("quad_perm:[2,3,0,1] row_mask:0xf bank_mask:0xf") RED("row_half_mirror row_mask:0xf bank_mask:0xf")
      RED("row_mirror row_mask:0xf bank_mask:0xf") RED("row_bcast:15 row_mask:0xa bank_mask:0xf")
      "ds_write_b32 %10, v150\ns_mov_b32 exec_hi, 0xffff\ns_waitcnt lgkmcnt(0)\ns_barrier\nds_read_b128 v[142:145], %11\nds_read_b64 v[146:147], %11 offset:16\n"
      "s_waitcnt lgkmcnt(1)\nv_pk_add_f32 v[142:143], v[142:143], v[144:145]\ns_waitcnt lgkmcnt(0)\nv_pk_add_f32 v[142:143], v[142:143], v[146:147]\n"
      "s_nop 0\nv_pk_mul_f32 v[142:143], v[142:143], s[68:69] op_sel_hi:[1,0]\ns_nop 0\nv_fma_f32 v144, -v142, v142, v143\nv_add_f32 v146, s70, v142\n" RB(144, 144) RB(146, 146)
      "v_add_f32 v144, 0x3727c5ac, v144\nv_pk_add_f32 v[132:133], v[136:137], v[146:147]" SUB RB(144, 144) "v_pk_add_f32 v[134:135], v[138:139], v[146:147]" SUB
      "v_sqrt_f32 v144, v144\n" RB(132, 132) RB(133, 133) RB(144, 144) RB(134, 134) "v_rcp_f32 v144, v144\n" RB(135, 135)
      "v_pk_mul_f32 v[132:133], v[132:133], v[144:145] op_sel_hi:[1,0]\nv_pk_mul_f32 v[134:135], v[134:135], v[144:145] op_sel_hi:[1,0]\n" RB4
      "v_pk_mul_f32 v[132:133], v[132:133], v[164:165]\nv_pk_mul_f32 v[134:135], v[134:135], v[166:167]\n" RB4
      "v_pk_add_f32 v[132:133], v[132:133], v[160:161]\nv_pk_add_f32 v[134:135], v[134:135], v[162:163]\n"
      "v_cvt_pk_bf16_f32 v140, v132, v133\nv_cvt_pk_bf16_f32 v141, v134, v135\n"
      "buffer_store_dwordx2 v[140:141], %7, %12, 0 offen sc1\nbuffer_store_dwordx2 v[140:141], %8, %12, 0 offen sc1\n"
      "buffer_store_dwordx4 v[152:155], %9, %12, 0 offen sc1\ns_mov_b32 exec_hi, -1\ns_nop 0\n"
      : "+v"(tlv_), "=&{v[140:141]}"(hw), "=&s"(cnt)
      : "v"(up2(xa[0])), "v"(up2(xa[1])), "v"(W_XP + (b & 3) * 49152 + hb + g * 16), "v"(W_M + (b & 3) * 16384 + hb + g * 16), "v"(ho ^ 8192), "v"(ho), "v"(W_X1 + (b * 152 + g) * 16),
        "v"(lds_base + L_TILE + ((lane & 31) == 16 ? wave * 8 + (lane >> 5) * 4 : 64 + tid * 4)), "v"(lds_base + L_TILE), "s"(rs), "s"(epoch), "s"(4608), "s"(16384), "s"(20992), "s"(32768)
      : "memory", "vcc", "scc", "s64", "s65", "s66", "s67", "s68", "s69", "s70", "s71", "s72", "s73",
        "v100", "v101", "v102", "v103", "v104", "v105", "v106", "v107", "v108", "v109", "v110", "v111", "v112", "v113", "v114", "v115", "v116", "v117", "v118", "v119",
        "v120", "v121", "v122", "v123", "v124", "v125", "v126", "v127", "v128", "v129", "v130", "v131", "v132", "v133", "v134", "v135", "v136", "v137", "v138", "v139",
        "v142", "v143", "v144", "v145", "v146", "v147", "v148", "v149", "v150", "v151", "v152", "v153", "v154", "v155", "v156", "v157", "v158", "v159",
        "v160", "v161", "v162", "v163", "v164", "v165", "v166", "v167");
    TL(11);
    const f32x2 r2 = up2(hw[0]) * wr[0] + up2(hw[1]) * wr[1];
    const float rw = wsum(on ? r2.x + r2.y : 0.f);
    if (lane == 0) ex[8 + wave] = rw;
    lds_barrier();
    if (tid < 8) {
      const float r = rbf((ex[8] + ex[9]) + ex[10]);
      st128(rs, W_X1 + (b * 152 + 144 + tid) * 16, u32x4{__float_as_uint(rbf(1.f / (1.f + __expf(-r)))), 0u, e0, e1});
    }
  }

  if (oj >= 0) {
    const int ob = W_O + ((op & 3) * 2 + (wave >> 2)) * 32768 + (lane >> 5) * 16384 + ((op >> 2) * 8192 ^ flip) + ((lane >> 3) & 3) * 2048 + (wave & 3) * 256 + (lane & 7) * 16;
    u32x4 a[4];
#pragma unroll
    for (int p = 0; p < 4; ++p) a[p] = ld128(rs, ob + (p >> 1) * 1024 + (p & 1) * 128);
    SCHED();
    u32x8 wf[8];
    if (wave < 4) {
      const int q = n >> 2, p = lane & 3;
      const int a0 = L_W + ((grp >> 1) * 16 + (grp & 1) * 4 + (q & 1) + (q >> 1) * 8) * 128 + ((wave ^ ((grp & 1) + (q & 2))) << 5) + p * 8;
#pragma unroll
      for (int s = 0; s < 8; ++s) {
        const u32x2 r0 = trd(lds, a0 + s * 8192), r1 = trd(lds, a0 + s * 8192 + 256), r2 = trd(lds, a0 + s * 8192 + 4096), r3 = trd(lds, a0 + s * 8192 + 4352);
        wf[s] = u32x8{r0[0], r0[1], r1[0], r1[1], r2[0], r2[1], r3[0], r3[1]};
      }
#pragma unroll
      for (int s = 0; s < 8; ++s) asm volatile("" : "+v"(wf[s]));
    }
    u32 cnt;
#define OSM(r, w) "v_smfmac_f32_16x16x64_bf16 v[132:135], " r ", " w ", %18\n"
    asm volatile(
      "v_mov_b64 v[132:133], 0\nv_mov_b64 v[134:135], 0\ns_mov_b32 %5, 0x8000\ns_branch 1f ; throttle\n0:\ns_sleep 2\n"
      "s_mov_b64 exec, s[64:65]\nbuffer_load_dwordx4 %1, %14, %19, 0 offen sc1\nbuffer_load_dwordx4 %2, %14, %19, 0 offen offset:128 sc1\n"
      "s_mov_b64 exec, s[66:67]\nbuffer_load_dwordx4 %3, %14, %19, 0 offen offset:1024 sc1\nbuffer_load_dwordx4 %4, %14, %19, 0 offen offset:1152 sc1\ns_mov_b64 exec, -1\n"
      "s_waitcnt vmcnt(0) lgkmcnt(0)\n1:\nv_pk_add_f32 v[116:117], v[100:101], v[108:109]\nv_pk_add_f32 v[118:119], v[102:103], v[110:111]\n"
      "v_pk_add_f32 v[120:121], v[104:105], v[112:113]\nv_pk_add_f32 v[122:123], v[106:107], v[114:115]\nv_cvt_pk_bf16_f32 v124, v116, v117\nv_cvt_pk_bf16_f32 v125, v118, v119\n"
      "v_cvt_pk_bf16_f32 v126, v120, v121\nv_cvt_pk_bf16_f32 v127, v122, v123\nds_write_b128 %15, v[124:127]\n"
      "v_max3_u32 v116, v100, v101, v102\nv_max3_u32 v117, v108, v109, v110\nv_max3_u32 v116, v116, v103, v104\nv_max3_u32 v117, v117, v111, v112\n"
      "v_max3_u32 v116, v116, v105, v106\nv_max3_u32 v117, v117, v113, v114\nv_max_u32 v116, v116, v107\nv_max_u32 v117, v117, v115\n"
      "v_cmp_eq_u32 s[64:65], -1, v116\nv_cmp_eq_u32 s[66:67], -1, v117\ns_sub_u32 %5, %5, 1\ns_cbranch_scc1 2f ; throttle\ns_or_b64 vcc, s[64:65], s[66:67]\ns_cbranch_scc1 0b ; throttle\n2:\n" ATL(8)
      "s_cmp_gt_u32 %21, 3\ns_waitcnt lgkmcnt(0)\ns_barrier\ns_cbranch_scc1 3f ; throttle\n"
      "ds_read_b128 v[100:103], %16\nds_read_b128 v[104:107], %16 offset:1024\nds_read_b128 v[108:111], %16 offset:2048\nds_read_b128 v[112:115], %16 offset:3072\n"
      "ds_read_b128 v[116:119], %16 offset:4096\nds_read_b128 v[120:123], %16 offset:5120\nds_read_b128 v[124:127], %16 offset:6144\nds_read_b128 v[128:131], %16 offset:7168\n"
      "s_waitcnt lgkmcnt(7)\n" OSM("v[100:103]", "%6") "s_waitcnt lgkmcnt(6)\n" OSM("v[104:107]", "%7") "s_waitcnt lgkmcnt(5)\n" OSM("v[108:111]", "%8") "s_waitcnt lgkmcnt(4)\n" OSM("v[112:115]", "%9")
      "s_waitcnt lgkmcnt(3)\n" OSM("v[116:119]", "%10") "s_waitcnt lgkmcnt(2)\n" OSM("v[120:123]", "%11") "s_waitcnt lgkmcnt(1)\n" OSM("v[124:127]", "%12") "s_waitcnt lgkmcnt(0)\n" OSM("v[128:131]", "%13")
      "s_nop 7\nv_permlane32_swap_b32 v132, v134\nv_permlane32_swap_b32 v133, v135\nv_pk_add_f32 v[132:133], v[132:133], v[134:135]\n"
      "buffer_store_dword v132, %17, %19, 0 offen sc1\nbuffer_store_dword v133, %17, %19, %20 offen sc1\n3:\n"
      : "+v"(tlv_), "+{v[100:103]}"(a[0]), "+{v[104:107]}"(a[1]), "+{v[108:111]}"(a[2]), "+{v[112:115]}"(a[3]), "=&s"(cnt)
      : "v"(wf[0]), "v"(wf[1]), "v"(wf[2]), "v"(wf[3]), "v"(wf[4]), "v"(wf[5]), "v"(wf[6]), "v"(wf[7]), "v"(ob),
        "v"(lds_base + L_OI + wave * 1024 + (((lane >> 1) & 3) * 16 + (lane & 1) * 8 + ((lane >> 3) ^ (lane & 7))) * 16),
        "v"(lds_base + L_OI + (grp * 16 + (n & 8) + ((n & 7) ^ (grp * 2 + (n >> 3)))) * 16),
        "v"(W_XP + ((grp >> 1) * 6 + op / 3) * 16384 + ((grp & 1) * 8192 ^ flip) + op % 3 * 2304 + oj * 256 + wave * 64 + n * 4), "v"(n < 8 ? 0x4444 : 0xeeee), "s"(rs), "s"(49152), "s"(wave)
      : "memory", "vcc", "scc", "s64", "s65", "s66", "s67", "s72", "s73", "v116", "v117", "v118", "v119", "v120", "v121", "v122", "v123", "v124", "v125", "v126", "v127",
        "v128", "v129", "v130", "v131", "v132", "v133", "v134", "v135");
    TL(9);
    if (wave < 4) dma_keep((wave & 1) ? wg : wu, (512 + (wave >> 1) * 32 + (lane & 31)) * 9216 + (xcd * 9 + oj) * 128, lds_base + 98304 + wave * 256);
  }
  TLOUT();
  }
  }
}

extern "C" void kernel_launch(void* const* d_in, const int* in_sizes, int n_in, void* d_out, int out_size, void* d_ws, size_t ws_size,
                              hipStream_t stream) {
  fused_kernel<<<NWG, NT, 0, stream>>>((const u16*)d_in[0], (const u16*)d_in[1], (const u16*)d_in[10], (const u16*)d_in[2], (const u16*)d_in[3], (const u16*)d_in[4],
                                       (const u16*)d_in[6], (const u16*)d_in[7], (const u16*)d_in[8], (const u16*)d_in[9],
                                       (const u16*)d_in[11], (unsigned char*)d_ws, (u16*)d_out);
}
```

```cpp
#include <hip/hip_runtime.h>
#include <hip/hip_bf16.h>
#include <cstdint>

typedef unsigned u32;
typedef unsigned short u16;
typedef u32 u32x4 __attribute__((ext_vector_type(4)));
typedef u32 u32x2 __attribute__((ext_vector_type(2)));
typedef float f32x4 __attribute__((ext_vector_type(4)));
typedef float f32x2 __attribute__((ext_vector_type(2)));
typedef short s16x4 __attribute__((ext_vector_type(4)));
typedef __bf16 bf16x8 __attribute__((ext_vector_type(8)));
typedef __bf16 bf16x2 __attribute__((ext_vector_type(2)));
typedef __bf16 bf16x16 __attribute__((ext_vector_type(16)));
typedef u32 u32x8 __attribute__((ext_vector_type(8)));
typedef __attribute__((address_space(3))) unsigned char lds_t;

constexpr int D = 576, SK = 8192, WIN = 2048, FF = 4608, NQ = 4096, NMOD = 6 * D;
constexpr int NWG = 256, NT = 512;
constexpr int SPIN_CAP = 1 << 15;

constexpr int W_O = 0;
constexpr int W_XP = W_O + 262144;
constexpr int W_Y = W_XP + 12 * 16384;
constexpr int W_H2 = W_Y + 81 * 16384;
constexpr int W_MS = W_H2 + 2 * 16384;
constexpr int W_M = W_MS;
constexpr int W_PR = W_M + 4 * 16384;
constexpr int W_GB = W_PR + 2 * 16384;
constexpr int W_FL = W_GB + 18432;
constexpr int W_H = W_FL + 2048;
constexpr int W_Q = W_H + 36864;
constexpr int W_ST = W_Q + 131072;
constexpr int W_X1 = W_ST + 32768;
constexpr int W_END = W_X1 + 8 * 152 * 16;
constexpr int NPZ = 174, PZ = (W_MS / 16 + NPZ - 1) / NPZ;
static_assert(PZ <= 2 * NT, "poison split");

constexpr int L_W = 0;
constexpr int L_IMG = 73728;
constexpr int L_TILE = L_IMG + 9216;
constexpr int L_IM2 = 86016;
constexpr int L_OI = 65536;
constexpr int L_B = 147456;
constexpr int L_C = 155648;
constexpr int LDS_BYTES = 163840;

#define SCHED() __builtin_amdgcn_sched_barrier(0)
#define BAR() asm volatile("s_barrier" ::: "memory")
#define TL(i) do {} while (0)
#define ATL(i)
#define TLOUT() do {} while (0)
#define T2(i, ...) do {} while (0)
#define WAITV_(n) do { asm volatile("s_waitcnt vmcnt(" #n ")" ::: "memory"); SCHED(); } while (0)
#define WAITV(n) WAITV_(n)
#define W0 3
#define W1 5
#define W2 7
#define TH1 15
#define TH2 40
#define STR_(x) #x
#define STR(x) STR_(x)
#define V10(d) "v" #d "0", "v" #d "1", "v" #d "2", "v" #d "3", "v" #d "4", "v" #d "5", "v" #d "6", "v" #d "7", "v" #d "8", "v" #d "9"
#define WAITD() do { asm volatile("s_waitcnt vmcnt(" STR(W2) ")\n\ts_cmpk_gt_i32 %0, " STR(TH2) " ; throttle\n\ts_cbranch_scc1 4 ; throttle\n\ts_waitcnt vmcnt(" STR(W1) ") ; throttle\n\t" \
    "s_cmpk_gt_i32 %0, " STR(TH1) " ; throttle\n\ts_cbranch_scc1 1 ; throttle\n\ts_waitcnt vmcnt(" STR(W0) ") ; throttle" :: "s"(__builtin_amdgcn_readfirstlane(lag)) : "memory", "scc"); SCHED(); } while (0)
#define PR 114
#define R1 34
#define G1 10
#define WL 3
#define OH 140
#define OF 85
#define OL 60

__device__ __forceinline__ float lo16(u32 u) { return __uint_as_float(u << 16); }
__device__ __forceinline__ float hi16(u32 u) { return __uint_as_float(u & 0xffff0000u); }
__device__ __forceinline__ float b2f(u16 u) { return __uint_as_float((u32)u << 16); }
__device__ __forceinline__ u32 cvt2(float a, float b) { return __builtin_bit_cast(u32, __builtin_convertvector(f32x2{a, b}, bf16x2)); }
__device__ __forceinline__ float rbf(float v) { return __uint_as_float(cvt2(0.f, v)); }
__device__ __forceinline__ f32x2 up2(u32 u) { return f32x2{lo16(u), hi16(u)}; }
__device__ __forceinline__ void lds_barrier() { asm volatile("s_waitcnt lgkmcnt(0)\n\ts_barrier" ::: "memory"); }
__device__ __forceinline__ f32x4 mfma(u32x4 a, u32x4 b, f32x4 c) {
  return __builtin_amdgcn_mfma_f32_16x16x32_bf16(__builtin_bit_cast(bf16x8, a), __builtin_bit_cast(bf16x8, b), c, 0, 0, 0);
}
__device__ __forceinline__ f32x4 smf(u32x4 a, u32x8 b, f32x4 c, int idx) {
  return __builtin_amdgcn_smfmac_f32_16x16x64_bf16(__builtin_bit_cast(bf16x8, a), __builtin_bit_cast(bf16x16, b), c, idx, 0, 0);
}
__device__ __forceinline__ u32 plo(u32 a, u32 b) { return __builtin_amdgcn_perm(b, a, 0x05040100u); }
__device__ __forceinline__ u32 phi(u32 a, u32 b) { return __builtin_amdgcn_perm(b, a, 0x07060302u); }

extern "C" __device__ __attribute__((const)) unsigned long long llvm_dispatch_id() __asm("llvm.amdgcn.dispatch.id");
typedef __amdgpu_buffer_rsrc_t rsrc_t;
__device__ __forceinline__ u32x4 ld128(rsrc_t rs, int off) { return __builtin_amdgcn_raw_buffer_load_b128(rs, off, 0, 16); }
__device__ __forceinline__ void st128(rsrc_t rs, int off, u32x4 v) { __builtin_amdgcn_raw_buffer_store_b128(v, rs, off, 0, 16); }
__device__ __forceinline__ void st64(rsrc_t rs, int off, u32x2 v) { __builtin_amdgcn_raw_buffer_store_b64(v, rs, off, 0, 16); }
__device__ __forceinline__ void dma(const void* gbase, u32 voff, unsigned lds_dst) {
  unsigned keep;
  asm volatile("s_mov_b32 %0, m0\n\ts_mov_b32 m0, %3\n\ts_nop 2\n\tglobal_load_lds_dwordx4 %1, %2 nt\n\ts_mov_b32 m0, %0"
               : "=&s"(keep) : "v"(voff), "s"(gbase), "s"(lds_dst) : "memory");
}
__device__ __forceinline__ void dma_keep(const void* gbase, u32 voff, unsigned lds_dst) {
  unsigned keep;
  asm volatile("s_mov_b32 %0, m0\n\ts_mov_b32 m0, %3\n\ts_nop 2\n\tglobal_load_lds_dword %1, %2\n\ts_mov_b32 m0, %0"
               : "=&s"(keep) : "v"(voff), "s"(gbase), "s"(lds_dst) : "memory");
}
__device__ __forceinline__ void dma_sc(const void* gbase, u32 voff, unsigned lds_dst) {
  unsigned keep;
  asm volatile("s_mov_b32 %0, m0\n\ts_mov_b32 m0, %3\n\ts_nop 2\n\tglobal_load_lds_dwordx4 %1, %2 sc1\n\ts_mov_b32 m0, %0"
               : "=&s"(keep) : "v"(voff), "s"(gbase), "s"(lds_dst) : "memory");
}
__device__ __forceinline__ void ald64(u32x2& d, const void* gbase, u32 voff) { unsigned long long t; asm volatile("s_mov_b64 %1, %3\n\tglobal_load_dwordx2 %0, %2, %1" : "=v"(d), "=&s"(t) : "v"(voff), "s"(gbase) : "memory"); }
__device__ __forceinline__ void ald128nt(u32x4& d, const void* gbase, u32 voff) { unsigned long long t; asm volatile("s_mov_b64 %1, %3\n\tglobal_load_dwordx4 %0, %2, %1 nt" : "=v"(d), "=&s"(t) : "v"(voff), "s"(gbase) : "memory"); }
__device__ __forceinline__ void ald128sc(u32x4& d, const void* gbase, u32 voff) { unsigned long long t; asm volatile("s_mov_b64 %1, %3\n\tglobal_load_dwordx4 %0, %2, %1 sc1" : "=v"(d), "=&s"(t) : "v"(voff), "s"(gbase) : "memory"); }
__device__ __forceinline__ u32x2 trd(const unsigned char* lds, int off) {
  return __builtin_bit_cast(u32x2, __builtin_amdgcn_ds_read_tr16_b64_v4i16((__attribute__((address_space(3))) s16x4*)((lds_t*)lds + off)));
}
#define nap(n) do { __builtin_amdgcn_s_sleep(n); asm volatile("" ::: "memory"); } while (0)
__device__ __forceinline__ float frcp(float v) { return __builtin_amdgcn_rcpf(v); }
__device__ __forceinline__ float fdiv(float a, float d, float r) { const float q = a * r; return fmaf(fmaf(-q, d, a), r, q); }
__device__ __forceinline__ float silu(float v) { const float d = 1.f + __expf(-v); const float r = frcp(d); return d < 3.0e38f ? fdiv(v, d, r) : v * r; }
template <int CTRL> __device__ __forceinline__ float dppf(float x) {
  return __builtin_bit_cast(float, __builtin_amdgcn_mov_dpp(__builtin_bit_cast(int, x), CTRL, 0xf, 0xf, true));
}
__device__ __forceinline__ float rl(float v, int l) { return __builtin_bit_cast(float, __builtin_amdgcn_readlane(__builtin_bit_cast(int, v), l)); }
__device__ __forceinline__ float wsum(float v) {
  v += dppf<0xB1>(v); v += dppf<0x4E>(v); v += dppf<0x141>(v); v += dppf<0x140>(v);
  return (rl(v, 0) + rl(v, 16)) + (rl(v, 32) + rl(v, 48));
}
__device__ __forceinline__ float up32(float v) { const u32 u = __float_as_uint(v); return __uint_as_float(__builtin_amdgcn_permlane32_swap(u, u, false, false)[1]); }
__device__ __forceinline__ f32x4 half_sum(f32x4 d) {
  f32x4 r;
#pragma unroll
  for (int j = 0; j < 4; ++j) r[j] = d[j] + dppf<0x108>(up32(d[j]));
  return r;
}
__device__ __forceinline__ void unpack8(u32x4 u, float* f) {
#pragma unroll
  for (int j = 0; j < 4; ++j) { f[2 * j] = lo16(u[j]); f[2 * j + 1] = hi16(u[j]); }
}
__device__ __forceinline__ void ln_row(const float (&v)[16], bool has2, float& mean, float& den) {
  float s = 0.f;
#pragma unroll
  for (int i = 0; i < 8; ++i) s += v[i];
  if (has2) {
#pragma unroll
    for (int i = 8; i < 16; ++i) s += v[i];
  }
  s = wsum(s);
  const float mu = s * (1.f / D);
  mean = rbf(mu);
  float q = 0.f;
#pragma unroll
  for (int i = 0; i < 8; ++i) { const float t = v[i] - mu; q = fmaf(t, t, q); }
  if (has2) {
#pragma unroll
    for (int i = 8; i < 16; ++i) { const float t = v[i] - mu; q = fmaf(t, t, q); }
  }
  q = wsum(q);
  const float var = rbf(q * (1.f / D));
  den = rbf(sqrtf(rbf(var + 1e-5f)));
}
template <int J0 = 0, int J1 = 9>
__device__ __forceinline__ void slab_dma(const u16* base, int stride_b, unsigned lds_dst, int wave, int lane) {
  const u32 v0 = (lane >> 3) * stride_b + (((lane & 7) ^ ((wave & 1) * 4 + (lane >> 4))) << 4), v1 = v0 ^ 32u;
  const u32 v4 = wave >= 4 ? v1 : v0;
  const char* gb = (const char*)base + (size_t)wave * 8 * stride_b;
#pragma unroll
  for (int j = J0; j < J1; ++j) dma(gb + (size_t)j * 64 * stride_b, j < 4 ? v0 : j == 4 ? v4 : v1, lds_dst + (j * 8 + wave) * 1024);
}
__device__ __forceinline__ void slab_frags(const unsigned char* lds, int base, int wave, int lane, u32x4 (&wf)[9]) {
  const int g = lane >> 4, q = (lane & 15) >> 2, p = lane & 3, hf = p >> 1;
  const int a0 = base + (hf * 288 + 8 * g + q) * 128 + ((wave ^ ((g & 1) * 4 + (q >> 1)) ^ (hf * 2)) << 4) + (p & 1) * 8, a1 = (a0 ^ 32) + 512;
#pragma unroll
  for (int s = 0; s < 9; ++s) {
    const u32x2 lo = trd(lds, a0 + s * 4096), hi = trd(lds, a1 + s * 4096);
    wf[s] = u32x4{lo[0], lo[1], hi[0], hi[1]};
  }
}
__device__ __forceinline__ void img_frags(const unsigned char* lds, int img, int lane, u32x4 (&af)[9]) {
#pragma unroll
  for (int s = 0; s < 9; ++s) af[s] = *(const u32x4*)(lds + img + (s * 64 + lane) * 16);
}
__device__ __forceinline__ f32x4 gemv9(const u32x4 (&wf)[9], const u32x4 (&af)[9]) {
  f32x4 acc[3];
  SCHED();
#pragma unroll
  for (int s = 0; s < 9; ++s) acc[s % 3] = mfma(wf[s], af[s], s < 3 ? f32x4{0.f, 0.f, 0.f, 0.f} : acc[s % 3]);
  return (acc[0] + acc[1]) + acc[2];
}
__device__ __forceinline__ int img_off(int b, int k8) {
  const int hf = k8 >= 36 ? 1 : 0, kk = k8 - hf * 36;
  return ((kk >> 2) * 64 + (kk & 3) * 16 + hf * 8 + b) * 16;
}
__device__ __forceinline__ int im2_off(int b, int k8) { return (k8 * 8 + (b ^ (k8 & 7))) * 16; }
__device__ __forceinline__ u32 tag_x(u32x4 v, u32 e0, u32 e1) { return (v[2] ^ e0) | (v[3] ^ e1); }
__device__ __forceinline__ bool tag_ok(u32x4 v, u32 e0, u32 e1) { return tag_x(v, e0, e1) == 0u; }

__global__ void __launch_bounds__(NT, 2) fused_kernel(const u16* __restrict__ x, const u16* __restrict__ kc, const u16* __restrict__ c, const u16* __restrict__ vc,
                                                      const u16* __restrict__ wq, const u16* __restrict__ wo, const u16* __restrict__ w_r,
                                                      const u16* __restrict__ wg, const u16* __restrict__ wu, const u16* __restrict__ wd,
                                                      const u16* __restrict__ w_mod, unsigned char* ws, u16* out) {
  __shared__ __attribute__((aligned(16))) unsigned char lds[LDS_BYTES];
  SCHED();
  const int w = blockIdx.x, xcd = w & 7, idx = w >> 3;
  const int tid = threadIdx.x, lane = tid & 63, wave = __builtin_amdgcn_readfirstlane(tid >> 6);
  const int n = lane & 15, grp = lane >> 4;
  const unsigned lds_base = (unsigned)(uintptr_t)(lds_t*)lds;
  {
    unsigned long long pc0; unsigned keep; int co = (w >> 3) * 8192 - 27 * 8192 + lane * 128;
    asm volatile("s_getpc_b64 %0\n1:\tv_min_i32 %2, .Lcode_end - 1b, %2\n\ts_mov_b32 %1, m0\n\ts_mov_b32 m0, %3\n\ts_cmp_lt_i32 %4, 216\n\ts_cbranch_scc1 2 ; throttle\n\tglobal_load_lds_dword %2, %0\n\ts_mov_b32 m0, %1"
                 : "=&s"(pc0), "=&s"(keep), "+v"(co) : "s"(lds_base + 107520), "s"(wave ? 0 : w) : "memory", "scc");
  }
  const int s_att = w & 1, kvh = (w & 4) * 2 + ((w >> 2) & 6) + ((w >> 1) & 1), b_att = w >> 5;
  size_t pairbase = ((size_t)(b_att * 16 + kvh) * SK + (SK - WIN)) * 256;
  int wpart = (wave & 3) * 4096 + s_att * 16384 + (wave >> 2) * 262144;
  int ko = wpart + (n >> 1) * 256 + (n & 1) * 64 + grp * 16;
  int eo[4] = {tid * 16, 8192 + lane * 16, wave * 1152 + lane * 16, wave * 1152 + (lane & 7) * 16};
  asm volatile("" : "+s"(pairbase), "+s"(wpart), "+v"(ko), "+v"(eo[0]), "+v"(eo[1]), "+v"(eo[2]), "+v"(eo[3]));
  SCHED();
  int tlv_ = 0;
  const char* kp = (const char*)kc + pairbase;
#define KLD(u) do { unsigned long long t_; asm volatile("s_mov_b64 %1, %3\n\tglobal_load_dwordx4 %0, %2, %1 offset:%4 nt" : "=v"(kb[u]), "=&s"(t_) : "v"(ko), "s"(kp + ((u) >> 2) * 32768), "i"((((u) >> 1) & 1) * 2048 + ((u) & 1) * 128) : "memory"); } while (0)
  u32x4 kb[32], vb[32];
  asm volatile("s_cmp_gt_i32 %12, 17\n\ts_cbranch_scc1 9 ; throttle\n\tglobal_load_dwordx4 %0, %4, %9 ; throttle\n\tglobal_load_dwordx4 %1, %5, %9 ; throttle\n\t"
               "global_load_dwordx4 %2, %6, %10 ; throttle\n\tglobal_load_dwordx4 %3, %7, %10 offset:1024 ; throttle\n\ts_branch 8 ; throttle\n\t"
               "global_load_dwordx4 %0, %8, %11 nt\n\tglobal_load_dwordx4 %1, %8, %11 offset:128 nt\n\tglobal_load_dwordx4 %2, %8, %11 offset:2048 nt\n\tglobal_load_dwordx4 %3, %8, %11 offset:2176 nt"
               : "=&v"(kb[0]), "=&v"(kb[1]), "=&v"(kb[2]), "=&v"(kb[3]) : "v"(eo[0]), "v"(eo[1]), "v"(eo[2]), "v"(eo[3]), "v"(ko), "s"(c), "s"(x), "s"(kp), "s"(w) : "memory", "scc");
  TL(32);
  SCHED();
  asm volatile("" : "+s"(vc), "+s"(w_r), "+s"(wu), "+s"(wd), "+s"(out), "+s"(x), "+s"(w_mod), "+s"(wg), "+s"(wq), "+s"(wo), "+s"(c), "+s"(ws));
  const rsrc_t rs = __builtin_amdgcn_make_buffer_rsrc(ws, 0, W_END, 0x00020000);
  const unsigned long long epoch = (llvm_dispatch_id() + 1ull) * 0x9E3779B97F4A7C15ull;
  const u32 e0 = (u32)epoch, e1 = (u32)(epoch >> 32);

  const int mw = idx < 6 ? idx * 8 + xcd : -1;
  const int md = mw < 18 ? mw : -1;
  const int ml = mw >= 18 ? mw : mw >= 0 && mw < 6 ? 48 + mw : -1;
  const int lnb = mw >= 6 && mw < 14 ? xcd : -1;
  const int qs = idx >= 6 && idx < 14 ? xcd * 8 + idx - 6 : -1;
  const int oj = idx >= 14 && idx < 23 ? idx - 14 : -1, op = xcd;
  const int gs = idx >= 23 ? xcd * 9 + idx - 23 : -1;

  const char* wl = (const char*)(gs >= 0 ? wd + (size_t)gs * 64 * D : oj >= 0 ? wo + (size_t)(op * 512 + wave * 64) * D + oj * 64 : w_mod + (size_t)wave * 8 * NMOD + ml * 64);
  asm volatile("" : "+s"(wl));
#define WO(j) dma(wl + (j) * 8 * 1152, (lane >> 3) * 1152 + (((lane & 7) ^ ((lane >> 4) & 2) ^ (((j) & 1) * 4)) << 4), lds_base + L_W + (wave * 8 + (j)) * 1024)

  {
    const char* vp = (const char*)vc + pairbase;
    const int vo = wpart + lane * 16;
#define VLD(u) do { unsigned long long t_; asm volatile("s_mov_b64 %1, %3\n\tglobal_load_dwordx4 %0, %2, %1 offset:%4 nt" : "=v"(vb[u]), "=&s"(t_) : "v"(vo), "s"(vp + ((u) >> 2) * 32768), "i"(((u) & 3) * 1024) : "memory"); } while (0)
#define TIE8(a, o) asm volatile("" : "+v"(a[o]), "+v"(a[(o) + 1]), "+v"(a[(o) + 2]), "+v"(a[(o) + 3]), "+v"(a[(o) + 4]), "+v"(a[(o) + 5]), "+v"(a[(o) + 6]), "+v"(a[(o) + 7]))
    const u32 tp0 = (u32)__builtin_amdgcn_s_memrealtime() + (gs >= 0 ? OF : mw >= 18 ? OL : OH);
    int lag = gs >= 0 || mw >= 18 ? 0 : 1000;
#define PACE(i) do { const int t_ = ((i) * PR >> 2) + ((i) >= R1 ? G1 : 0); lag = (int)((u32)__builtin_amdgcn_s_memrealtime() - tp0) - t_; \
      if (lag < 0) { _Pragma("nounroll") do __builtin_amdgcn_s_sleep(2); while ((int)((u32)__builtin_amdgcn_s_memrealtime() - tp0) < t_); } SCHED(); } while (0)
#define REP4(M, u, W) do { M(u); W; M((u) + 1); W; M((u) + 2); W; M((u) + 3); W; } while (0)
    static_assert(W2 <= 7 && WL <= 4, "the hand-placed waits assume it");
    const int pz = mw >= 18 ? mw - 18 : idx >= 14 ? w - 82 : -1;
#define POISON() do { if (pz >= 0) { _Pragma("unroll") for (int i = 0; i < 2; ++i) { const int g_ = pz * PZ + i * NT + tid; \
      if (i * NT + tid < PZ && g_ < W_MS / 16) st128(rs, g_ * 16, u32x4{0xffffffffu, 0xffffffffu, 0xffffffffu, 0xffffffffu}); } } SCHED(); } while (0)
#define SILU_IMAGE(c0, c1, both) do { float f[8]; unpack8(c0, f); u32x4 o; \
        _Pragma("unroll") for (int j = 0; j < 4; ++j) o[j] = cvt2(silu(f[2 * j]), silu(f[2 * j + 1])); \
        const int b_ = tid / 72, ch_ = tid - b_ * 72; \
        if (both) *(u32x4*)(lds + L_IMG + img_off(b_, ch_)) = o; \
        *(u32x4*)(lds + L_IM2 + im2_off(b_, ch_)) = o; \
        if (wave == 0) { unpack8(c1, f); \
          _Pragma("unroll") for (int j = 0; j < 4; ++j) o[j] = cvt2(silu(f[2 * j]), silu(f[2 * j + 1])); \
          if (both) *(u32x4*)(lds + L_IMG + img_off(7, 8 + lane)) = o; \
          *(u32x4*)(lds + L_IM2 + im2_off(7, 8 + lane)) = o; } } while (0)
    if (md >= 0) {
      u32x2 xc;
      int ho = W_H + img_off(n & 7, (md >= 9 ? md - 9 : md) * 8 + wave) * 2 + (grp & 1) * 16;
      ald64(xc, x, (n & 7) * 1152 + ((md >= 9 ? md - 9 : 0) * 64 + wave * 8 + (grp & 1) * 4) * 2);
      TL(35);
      BAR();
      float* st = (float*)(lds + L_TILE + 1024);
      if (wave < 4) {
        slab_dma(w_mod + md * 64, NMOD * 2, lds_base + L_W, wave, lane);
        asm volatile("s_waitcnt vmcnt(9)" ::: "memory");
      } else asm volatile("s_waitcnt vmcnt(0)" ::: "memory");
      asm volatile("s_nop 0 ; both branches above cover V" : "+v"(kb[0]), "+v"(kb[1]), "+v"(kb[2]), "+v"(kb[3]), "+v"(xc) :: "memory");
      {
        SILU_IMAGE(kb[0], kb[1], true);
        float v[16]; unpack8(kb[2], v); unpack8(kb[3], v + 8);
        float mean, den; ln_row(v, lane < 8, mean, den);
        if (lane == 0) { st[wave] = mean; st[8 + wave] = den; }
      }
      if (wave >= 4) slab_dma(w_mod + md * 64, NMOD * 2, lds_base + L_W, wave, lane);
      lds_barrier();
      REP4(KLD, 0, );
      u32x4 wf[9], af[9];
      img_frags(lds, L_IMG, lane, af);
      const float mean = st[n & 7], rd = frcp(st[8 + (n & 7)]);
      float ln[4] = {rbf(rbf(lo16(xc[0]) - mean) * rd), rbf(rbf(hi16(xc[0]) - mean) * rd), rbf(rbf(lo16(xc[1]) - mean) * rd), rbf(rbf(hi16(xc[1]) - mean) * rd)};
#pragma unroll
      for (int s = 0; s < 9; ++s) asm volatile("" : "+v"(af[s]));
      asm volatile("" : "+v"(ln[0]), "+v"(ln[1]), "+v"(ln[2]), "+v"(ln[3]), "+v"(ho));
      WAITV(4); BAR(); SCHED();
      slab_frags(lds, L_W, wave, lane, wf);
      const f32x4 s = half_sum(gemv9(wf, af));
      f32x4 r = s;
      if (md >= 9) {
#pragma unroll
        for (int j = 0; j < 4; ++j) r[j] = ln[j] * rbf(1.f + rbf(s[j]));
      }
      if (n < 8 && grp < 2) st128(rs, ho + (md >= 9 ? 18432 : 0), u32x4{cvt2(r[0], r[1]), cvt2(r[2], r[3]), e0, e1});
      SCHED();
      TL(1);
      BAR();
      KLD(4); KLD(5); WAITV(W2);
      KLD(6); WAITV(W2); KLD(7); WAITV(W2); KLD(8); WAITV(W2); KLD(9); WAITV(W2); KLD(10); WAITV(W2);
      KLD(11); WAITV(W2); REP4(KLD, 12, WAITV(W2)); REP4(KLD, 16, WAITV(W2));
      REP4(KLD, 20, WAITV(W2)); KLD(24); WAITV(W2); KLD(25);
    } else {
      constexpr int L_LAND = 98304;
      const unsigned land = lds_base + L_LAND + wave * 6144;
      const unsigned char* lp = lds + L_LAND + wave * 6144 + lane * 16;
      const int sl_ = (3 - (int)((uintptr_t)wq >> 7)) & 7;
      const int s2_ = (63 - (int)((uintptr_t)wq >> 7)) & 63;
      const bool slowq = qs >= 0 && ((qs & 7) == sl_ || qs == s2_);
      if (gs >= 0 && wave == 3 && xcd == (s2_ >> 3)) dma_keep((const char*)wq + (size_t)(idx - 23) * 64 * 8192 + s2_ * 128, lane * 8192, land);
      if (gs >= 0 && wave == 0) dma_keep((const char*)wq + (size_t)(idx - 23) * 64 * 8192 + xcd * 1024 + sl_ * 128, lane * 8192, land);
      if (gs >= 0 && (wave == 1 || (wave == 2 && xcd < 2))) dma_keep((const char*)w_mod + (size_t)((idx - 23) * 64 + 32) * (NMOD * 2) + xcd * 128 + (wave - 1) * 2048, (lane & 31) * (NMOD * 2) + (wave == 1 ? (lane >> 5) * 1024 : 0), land);
      POISON();
      if (ml >= 0) { dma_sc(c, tid * 16, land); dma_sc(c, (512 + lane) * 16, land + 1024); }
      if (oj < 0 && qs < 0) {
        const int i_ = (gs >= 0 ? idx - 19 : idx - 2) * 8 + wave, o_ = (i_ >> 6) * 16384 + (i_ & 63) * 128 + (((xcd >> 2) ^ wave) & 1) * 8192;
        unsigned long long t0, t1, t2; u32 d_;
        asm volatile("s_memtime %0\n\ts_waitcnt lgkmcnt(0)\n\ts_load_dword %3, %4, %5\n\ts_waitcnt lgkmcnt(0)\n\ts_memtime %1\n\ts_waitcnt lgkmcnt(0)\n\t"
                     "s_load_dword %3, %4, %6\n\ts_waitcnt lgkmcnt(0)\n\ts_memtime %2\n\ts_waitcnt lgkmcnt(0)"
                     : "=&s"(t0), "=&s"(t1), "=&s"(t2), "=&s"(d_) : "s"(ws + W_PR), "s"(o_), "s"(o_ ^ 8192) : "memory");
        const int a_ = (int)((u32)t1 - (u32)t0), b_ = (int)((u32)t2 - (u32)t1);
        if (lane == 0) *(int*)(lds + L_B + 192 + wave * 4) = (wave & 1) ? b_ - a_ : a_ - b_;
        TL(58);
      }
#define WK(j) do { if (oj >= 0) { if ((j) < 8 - WL) WO(j); } \
                   else if (slowq) { if ((j) == 8) slab_dma(wq + qs * 64, NQ * 2, lds_base + L_W, wave, lane); } \
                   else if (qs >= 0 && (j) < 9) slab_dma<(j) < 9 ? (j) : 0, (j) < 9 ? (j) + 1 : 0>(wq + qs * 64, NQ * 2, lds_base + L_W, wave, lane); \
                   else if (qs < 0 && (j) >= 4) PACE(j); \
                   if ((j) >= 4) KLD(j); if ((j) == 4) TL(33); WAITD(); if ((j) == 5) TL(34); } while (0)
      WK(0); WK(1); WK(2); WK(3);
      WK(4); WK(5);
      WK(6); WK(7);
      if (oj < 0 && qs < 0) WAITV(4);
      TL(24);
      if (ml >= 0) { const u32x4 c0 = *(const u32x4*)lp, c1 = *(const u32x4*)(lp + 1024); SILU_IMAGE(c0, c1, false); }
      lds_barrier();
      if (pz >= 0 && tid == 448) {
        const u32x4 v0_ = *(const u32x4*)(lds + L_B + 192), v1_ = *(const u32x4*)(lds + L_B + 208);
        const int d_ = (int)(v0_[0] + v0_[1] + v0_[2] + v0_[3] + v1_[0] + v1_[1] + v1_[2] + v1_[3]);
        st64(rs, W_FL + pz * 8, u32x2{e0 ^ (oj >= 0 ? pz & 1 : d_ > 0), e1});
      }
      SCHED();
      WK(8); WK(9); WK(10); WK(11);
      TL(25);
      PACE(12); REP4(KLD, 12, WAITD());
      TL(26);
      const int hg0 = (wave * 144 + lane) * 16, hg2 = (wave * 144 + 128 + n) * 16;
#define HREQ() do { dma_sc(ws + W_H, hg0, land); dma_sc(ws + W_H + 18432, hg0, land + 1024); dma_sc(ws + W_H + 1024, hg0, land + 2048); \
                    dma_sc(ws + W_H + 18432 + 1024, hg0, land + 3072); dma_sc(ws + W_H, hg2, land + 4096); dma_sc(ws + W_H + 18432, hg2, land + 5120); } while (0)
#define HGET() do { _Pragma("unroll") for (int i = 0; i < 6; ++i) hq[i] = *(const u32x4*)(lp + i * 1024); } while (0)
#define HQOK() __all((tag_x(hq[0], e0, e1) | tag_x(hq[1], e0, e1) | tag_x(hq[2], e0, e1) | tag_x(hq[3], e0, e1) | tag_x(hq[4], e0, e1) | tag_x(hq[5], e0, e1)) == 0u)
      if (qs >= 0) HREQ();
      PACE(16); REP4(KLD, 16, WAITD());
      if (qs >= 0) WAITV(4);
      TL(27);
      PACE(20); KLD(20); KLD(21);
      if (qs >= 0) {
        u32x4 hq[6]; HGET();
        _Pragma("nounroll") for (int sp = 0; sp < SPIN_CAP && !HQOK(); ++sp) { nap(16); HREQ(); WAITV(0); HGET(); }
        TL(2);
#define HSUM(a, b) cvt2(lo16(a) + lo16(b), hi16(a) + hi16(b))
        *(u32x2*)(lds + L_IMG + hg0 / 2) = u32x2{HSUM(hq[0][0], hq[1][0]), HSUM(hq[0][1], hq[1][1])};
        *(u32x2*)(lds + L_IMG + 512 + hg0 / 2) = u32x2{HSUM(hq[2][0], hq[3][0]), HSUM(hq[2][1], hq[3][1])};
        if (lane < 16) *(u32x2*)(lds + L_IMG + hg2 / 2) = u32x2{HSUM(hq[4][0], hq[5][0]), HSUM(hq[4][1], hq[5][1])};
      } else WAITD();
      KLD(22); KLD(23);
      if (qs >= 0) {
        u32x4 wf[9], af[9];
        lds_barrier();
        slab_frags(lds, L_W, wave, lane, wf);
        img_frags(lds, L_IMG, lane, af);
        const f32x4 s = half_sum(gemv9(wf, af));
        if (n < 8 && grp < 2) *(u32x2*)(lds + L_TILE + n * 128 + wave * 16 + grp * 8) = u32x2{cvt2(s[0], s[1]), cvt2(s[2], s[3])};
      } else WAITD();
      PACE(24); KLD(24); KLD(25);
      if (qs >= 0) {
        lds_barrier();
        if (wave < 2) {
          const u32x2 t2 = *(const u32x2*)(lds + L_TILE + tid * 8);
          st128(rs, W_Q + ((tid >> 4) * 1024 + qs * 16 + n) * 16, u32x4{t2[0], t2[1], e0, e1});
        }
        TL(3);
      }
      SCHED();
    }
    const unsigned char* qg = ws + W_Q + (b_att * 1024 + kvh * 64) * 16;
    KLD(26); WAITD(); KLD(27); WAITD(); PACE(28); KLD(28); WAITD(); KLD(29); WAITD();
    PACE(30); KLD(30); WAITD(); KLD(31); WAITD();
    TL(28);
    if (gs >= 0 && wave >= 2 && wave < 6) {
      const u16* mat = (wave & 1) ? wg : wu;
      const int g0 = ((3 - (int)((uintptr_t)mat >> 7) - xcd * 9) & 7) + (wave >= 4 ? 8 : 0);
      if (g0 < 9) dma_keep(mat, ((idx - 23) * 15 + min(lane, 14)) * 9216 + (xcd * 9 + g0) * 128, lds_base + L_B + 4096 + wave * 256);
    }
    PACE(32); VLD(0); WAITD(); VLD(1); WAITD(); PACE(34); VLD(2); WAITD(); VLD(3); WAITD();
#define QLOOK() do { if (wave == 1) dma_sc(qg, lane * 16, lds_base + L_B + 1024); } while (0)
    QLOOK();
#define FLOOK(l) do { dma_sc(ws + W_FL, (l) * 16, lds_base + L_B + 2048); dma_sc(ws + W_FL, 1024 + min(l, NPZ / 2 - 65) * 16, lds_base + L_B + 3072); } while (0)
    if (wave == 2) FLOOK(lane);
    PACE(36); VLD(4); WAITD(); VLD(5); WAITD(); PACE(38); VLD(6); WAITD(); VLD(7); WAITD();
    QLOOK();
    TL(29);
    PACE(40); VLD(8); WAITD(); VLD(9); WAITD(); PACE(42); QLOOK(); VLD(10); WAITD(); VLD(11); WAITD();
    TIE8(kb, 0); TIE8(kb, 8); TIE8(kb, 16); TIE8(kb, 24);
    PACE(44); QLOOK(); VLD(12); VLD(13);
    u32x4 qf[2];
    {
      const u32x4* ql = (const u32x4*)(lds + L_B + 1024 + ((n >> 3) * 32 + (n & 1) * 8 + grp * 2) * 16);
      u32x4 g[4];
      _Pragma("nounroll") for (int sp = 0; sp < SPIN_CAP; ++sp) {
        g[0] = ql[0]; g[1] = ql[1]; g[2] = ql[16]; g[3] = ql[17];
        if (__all((tag_x(g[0], e0, e1) | tag_x(g[1], e0, e1) | tag_x(g[2], e0, e1) | tag_x(g[3], e0, e1)) == 0u)) break;
        if (wave == 1 && sp >= 6) { dma_sc(qg, lane * 16, lds_base + L_B + 1024); WAITV(0); } else nap(4);
      }
      asm volatile("" ::: "memory");
      g[0] = ql[0]; g[1] = ql[1]; g[2] = ql[16]; g[3] = ql[17];
      qf[0] = u32x4{g[0][0], g[0][1], g[1][0], g[1][1]};
      qf[1] = u32x4{g[2][0], g[2][1], g[3][0], g[3][1]};
    }
    TL(4);
    constexpr float SCALE = 0.088388347648318440f;
    f32x2 sc[16];
    float mx = -INFINITY;
#pragma unroll
    for (int T = 0; T < 16; ++T) {
      f32x4 acc = mfma(kb[T * 2], qf[0], f32x4{0.f, 0.f, 0.f, 0.f});
      acc = mfma(kb[T * 2 + 1], qf[1], acc);
      sc[T] = f32x2{acc[0] + dppf<0xB1>(acc[1]), acc[2] + dppf<0xB1>(acc[3])};
      if (T == 7 || T == 15) { SCHED(); if (T == 7) VLD(14); else VLD(15); SCHED(); }
    }
#pragma unroll
    for (int T = 0; T < 16; ++T)
#pragma unroll
      for (int j = 0; j < 2; ++j) { const float v = rbf(rbf(sc[T][j]) * SCALE); sc[T][j] = v; mx = fmaxf(mx, v); }
    SCHED(); VLD(16); SCHED();
    mx = fmaxf(mx, __shfl_xor(mx, 16));
    mx = fmaxf(mx, __shfl_xor(mx, 32));
    float ls = 0.f;
#pragma unroll
    for (int T = 0; T < 16; ++T)
#pragma unroll
      for (int j = 0; j < 2; ++j) { sc[T][j] = __expf(sc[T][j] - mx); ls += sc[T][j]; }
    SCHED(); VLD(17); SCHED();
    ls += __shfl_xor(ls, 16);
    ls += __shfl_xor(ls, 32);
    float* stat = (float*)(lds + L_B);
    if (lane == 0 || lane == 8) *(f32x2*)(stat + (wave * 2 + (lane >> 3)) * 2) = f32x2{mx, ls};
    SCHED(); VLD(18); VLD(19); SCHED();
    if (wave == 2) {
      int l2 = lane; asm volatile("" : "+v"(l2));
      u32x4 a, b;
      _Pragma("nounroll") for (int sp = 0; sp < SPIN_CAP; ++sp) {
        a = *(const u32x4*)(lds + L_B + 2048 + l2 * 16); b = *(const u32x4*)(lds + L_B + 3072 + l2 * 16);
        if (__all(((((a[0] ^ e0) | (a[2] ^ e0) | (b[0] ^ e0) | (b[2] ^ e0)) & ~1u) | (a[1] ^ e1) | (a[3] ^ e1) | (b[1] ^ e1) | (b[3] ^ e1)) == 0u)) break;
        nap(1); FLOOK(l2); WAITV(0);
      }
      const unsigned long long mk = (1ull << (NPZ / 2 - 64)) - 1;
      const int cnt = __popcll(__ballot(a[0] != e0)) + __popcll(__ballot(a[2] != e0)) + __popcll(__ballot(b[0] != e0) & mk) + __popcll(__ballot(b[2] != e0) & mk);
      if (l2 == 0) *(int*)(lds + L_B + 128) = cnt > NPZ / 2 ? 8192 : 0;
    }
    lds_barrier();
    const int flip = __builtin_amdgcn_readfirstlane(*(const int*)(lds + L_B + 128));
    float M = -INFINITY, L = 0.f;
    {
      f32x2 t[8];
#pragma unroll
      for (int q = 0; q < 8; ++q) { t[q] = *(const f32x2*)(stat + (q * 2 + (n >> 3)) * 2); M = fmaxf(M, t[q].x); }
#pragma unroll
      for (int q = 0; q < 8; ++q) L = fmaf(__expf(t[q].x - M), t[q].y, L);
    }
    if (tid >= 4 && tid < 12) st128(rs, W_ST + (w * 8 + lane - 4) * 16, u32x4{__float_as_uint(M), __float_as_uint(L), e0, e1});
    SCHED();
    TL(30);
    PACE(52); VLD(20); WAITD(); VLD(21); WAITD();
    const int po = W_ST + ((w ^ 1) * 8 + (n >> 3) * 4) * 16;
    int l1 = lane; asm volatile("" : "+v"(l1));
    u32 km[4] = {(l1 & 4) ? 0u : 0xffffu, (l1 & 4) ? 0xffffu : 0u, (l1 & 4) ? 0u : 0xffff0000u, (l1 & 4) ? 0xffff0000u : 0u};
    int px = (l1 & 3) * 0x5555;
    asm volatile("" : "+v"(km[0]), "+v"(km[1]), "+v"(km[2]), "+v"(km[3]), "+v"(px));
    PACE(54);
    VLD(22); WAITD(); VLD(23); WAITD();
    u32x4 ps, ps1, ps2, ps3;
    ald128sc(ps, ws, po);
    PACE(56);
    VLD(24); WAITD(); VLD(25); WAITD();
    TL(31);
    ald128sc(ps1, ws, po);
    PACE(58);
    VLD(26); WAITD(); VLD(27); WAITD();
    ald128sc(ps2, ws, po);
    VLD(28); VLD(29); WAITV(8);
    asm volatile("" : "+v"(ps));
    TIE8(vb, 0); TIE8(vb, 8); TIE8(vb, 16);
    ald128sc(ps3, ws, po);
    VLD(30); VLD(31);
    if (!__all(tag_ok(ps, e0, e1))) {
      WAITV(8);
      asm volatile("" : "+v"(ps1));
      ps = ps1;
      if (!__all(tag_ok(ps, e0, e1))) {
        WAITV(5);
        asm volatile("" : "+v"(ps2));
        ps = ps2;
        if (!__all(tag_ok(ps, e0, e1))) {
          WAITV(2);
          asm volatile("" : "+v"(ps3));
          ps = ps3;
          if (!__all(tag_ok(ps, e0, e1))) {
            int sp = 0;
            _Pragma("nounroll") do { nap(1); ps = ld128(rs, po); } while (!__all(tag_ok(ps, e0, e1)) && ++sp < SPIN_CAP);
          }
        }
      }
    }
    float fac;
    {
      const float Mp = __uint_as_float(ps[0]), L2 = __uint_as_float(ps[1]);
      const float Mg = fmaxf(M, Mp);
      const float Lg = __expf(M - Mg) * L + __expf(Mp - Mg) * L2;
      fac = __expf(mx - Mg) / Lg;
    }
    TL(5);
    u32x4 pf[16];
    u32 pp[16];
#pragma unroll
    for (int T = 0; T < 16; ++T) pp[T] = cvt2(sc[T][0] * fac, sc[T][1] * fac);
    SCHED();
#pragma unroll
    for (int T = 0; T < 16; ++T) {
      const u32 p = (u32)__builtin_amdgcn_mov_dpp((int)pp[T], 0xa0, 0xf, 0xf, true);
      pf[T] = u32x4{p & km[0], p & km[1], p & km[2], p & km[3]};
    }
    f32x4 oa = {0.f, 0.f, 0.f, 0.f};
#define PV(T) oa = smf(pf[T], __builtin_shufflevector(vb[2 * (T)], vb[2 * (T) + 1], 0, 1, 2, 3, 4, 5, 6, 7), oa, px)
#pragma unroll
    for (int T = 0; T < 12; ++T) PV(T);
    SCHED();
    int t2_ = threadIdx.x; asm volatile("" : "+v"(t2_));
    const int tid = t2_, lane = tid & 63, n = lane & 15, grp = lane >> 4;
    int oo = W_O + (kvh & 7) * 32768 + (b_att >> 2) * 16384 + ((kvh >> 3) * 8192 ^ flip) + (b_att & 3) * 2048 + s_att * 1024 + tid * 4;
    int mw_ = L_C + wave * 1024 + (((grp >> 1) * 2 + (n >> 3)) * 16 + (grp & 1) * 8 + (n & 7)) * 16, mr = L_C + tid * 4;
    asm volatile("" : "+v"(oo), "+v"(mw_), "+v"(mr), "+v"(pf[12]), "+v"(pf[13]), "+v"(pf[14]), "+v"(pf[15]));
    constexpr int L_H2 = L_W;
    constexpr int L_ACT = L_C + 4608;
    int ia, ra, ix, yo[2], fo[7], tw, tr_, tr4;
    const u32 gv = (lane >> 3) * (FF * 2) + (((lane & 7) ^ ((wave & 1) * 4 + ((lane >> 4) & 2))) << 4);
#define GU(j) do { const size_t o_ = ((size_t)((j) * 64 + wave * 8) * FF + gs * 64) * 2; dma((const char*)wg + o_, gv, lds_base + L_W + ((j) * 8 + wave) * 1024); \
                   dma((const char*)wu + o_, gv ^ 16u, lds_base + L_W + 73728 + ((j) * 8 + wave) * 1024); } while (0)
#define ML(j) dma(wl + (j) * 64 * NMOD * 2, (lane >> 3) * (NMOD * 2) + (((lane & 7) ^ ((wave & 1) * 4 + ((lane >> 4) & 2))) << 4), lds_base + L_W + ((j) * 8 + wave) * 1024)
    const bool late = gs >= 0 || mw >= 18;
    if (late) {
      BAR();
      if (gs >= 0) { GU(0); GU(1); } else { ML(0); ML(1); ML(2); ML(3); }
      {
    const int b = wave;
    ia = lds_base + L_H2 + im2_off(b, lane);
    ra = L_H2 + (grp * 16 + (n & 8) + ((n & 7) ^ (grp * 2 + (n >> 3)))) * 16; ix = n < 8 ? 0x4444 : 0xeeee;
    tw = lds_base + L_ACT + ((grp & 1) * 4 + (grp >> 1) * 2 + (n >> 3)) * 144 + (wave >> 1) * 32 + (n & 2) * 8 + (wave & 1) * 8 + (n & 4) + (n & 1) * 2; tr_ = L_ACT + (n & 7) * 144 + grp * 32 + (n & 8) * 2;
    tr4 = L_ACT + ((n & 7) ^ (wave >> 2)) * 144 + grp * 32 + (n & 8) * 2;
    {
      const int g0 = wave * 4 + (grp & 2), g1 = 32 + (grp & 2) + (wave >> 2), yb = W_Y + ((grp & 1) * 8192 ^ flip) + (gs & 31) * 256;
      yo[0] = yb + (gs < 64 ? g0 * 32768 + (gs >> 5) * 16384 : (72 + (g0 >> 2)) * 16384 + (g0 & 3) * 2048) + n * 16;
      yo[1] = yb + (gs < 64 ? g1 * 32768 + (gs >> 5) * 16384 : (72 + (g1 >> 2)) * 16384 + (g1 & 3) * 2048) + (wave & 3) * 64 + n * 4;
    }
    {
      const int st = idx - 23;
      fo[0] = W_GB + (xcd * 144 + st * 16 + (lane >> 2)) * 16;
      fo[1] = W_X1 + (xcd * 152 + st * 16 + (lane >> 2)) * 16; fo[2] = W_X1 + (xcd * 152 + 144) * 16;
      fo[3] = W_Y + ((xcd >> 2) * 8192 ^ flip) + tid * 16;
      fo[4] = L_H2 + (st * 64 + (lane & 56) + (xcd ^ (lane >> 3))) * 16 + (lane & 7) * 2;
      fo[5] = L_C + (lane & 3) * 64 + (st < 8 ? (lane >> 3) + (lane & 4) * 2 : lane >> 2) * 4;
      fo[6] = (xcd * D + st * 64 + lane) * 2;
    }
    asm volatile("" : "+v"(ia), "+v"(ra), "+v"(ix), "+v"(tw), "+v"(tr_), "+v"(tr4), "+v"(yo[0]), "+v"(yo[1]),
                 "+v"(fo[0]), "+v"(fo[1]), "+v"(fo[2]), "+v"(fo[3]), "+v"(fo[4]), "+v"(fo[5]), "+v"(fo[6]));
      }
      WAITV(6);
    } else if (oj >= 0) { BAR(); _Pragma("unroll") for (int j = 8 - WL; j < 8; ++j) WO(j); WAITV(2 + WL); } else WAITV(2);
    asm volatile("s_nop 0 ; both branches above cover V up to the last two" : "+v"(vb[24]), "+v"(vb[25]), "+v"(vb[26]), "+v"(vb[27]), "+v"(vb[28]), "+v"(vb[29]));
    asm volatile("" :: "v"(ps1), "v"(ps2), "v"(ps3));
    PV(12); PV(13); PV(14);
    SCHED();
    asm volatile("s_waitcnt vmcnt(4)\n\ts_cmp_ge_i32 %2, 0\n\ts_cbranch_scc1 4 ; throttle\n\ts_waitcnt vmcnt(" STR(WL) ")\n\ts_cmp_ge_i32 %3, 14\n\ts_cbranch_scc1 1 ; throttle\n\ts_waitcnt vmcnt(0)" : "+v"(vb[30]), "+v"(vb[31]) : "s"(max(gs, mw - 18)), "s"(idx) : "memory", "scc");
    T2(56, );
    PV(15);
    TL(6);
    *(f32x4*)(lds + mw_) = oa;
    T2(54, );
    lds_barrier();
    T2(55, );
    if (wave < 4) {
      f32x2 m[4];
#pragma unroll
      for (int q = 0; q < 4; ++q) m[q] = f32x2{*(const float*)(lds + mr + q * 2048), *(const float*)(lds + mr + q * 2048 + 1024)};
      SCHED();
      const f32x2 o = (m[0] + m[1]) + (m[2] + m[3]);
      __builtin_amdgcn_raw_buffer_store_b32(__float_as_uint(o.x + o.y), rs, oo, 0, 16);
    }
    TL(7); SCHED();
  if (gs >= 0) {
    const int b = wave;
    BAR(); SCHED();
    u32x8 bw[9];
    u32x8 wd8[5];
    u32x4 gq, xq, rq;
    const int ga0 = L_W + ((lane >> 1) & 1) * 73728 + ((grp >> 1) * 16 + (grp & 1) * 4 + ((n >> 2) & 1) + (n & 8)) * 128 + ((wave ^ ((grp & 1) * 2 + ((n >> 1) & 4)) ^ ((lane >> 1) & 1)) << 4) + (lane & 1) * 8;
#define GUF(S0, S1) do { _Pragma("unroll") for (int s = S0; s < S1; ++s) { const int a_ = ga0 + (s >> 2) * 32768, o_ = (s & 3) * 8192; \
      const u32x2 r0 = trd(lds, a_ + o_), r1 = trd(lds, a_ + o_ + 256), r2 = trd(lds, a_ + o_ + 4096), r3 = trd(lds, a_ + o_ + 4352); \
      bw[s] = u32x8{r0[0], r0[1], r1[0], r1[1], r2[0], r2[1], r3[0], r3[1]}; } } while (0)
    {
      u32x4 rd[8];
      const char* db = wl + wave * 128;
      const u32 dv = (n >> 3) * 36864 + grp * 9216 + (n & 7) * 16;
#pragma unroll
      for (int i = 0; i < 8; ++i) ald128nt(rd[i], db + i * 1152, dv);
      {
        const int r = lane >> 3;
        dma(wl + wave * 9216 + 1024, r * 1152 + (((lane & 7) ^ (r & 2) ^ ((wave & 1) * 4)) << 4), lds_base + L_B + wave * 1024);
      }
      TL(19);
      asm volatile("s_waitcnt vmcnt(9)\n\ts_barrier" ::: "memory");
      TL(22);
      GUF(0, 2);
      SCHED();
#define GU2(i) do { const int u_ = 16 + (wave & 3) + 4 * (i); const size_t o_ = ((size_t)u_ * 8 * FF + gs * 64) * 2; dma((const char*)wg + o_, gv, lds_base + L_W + u_ * 1024); \
                    dma((const char*)wu + o_, gv ^ 16u, lds_base + L_W + 73728 + u_ * 1024); } while (0)
#define WAIT2(c, a, b) "s_waitcnt vmcnt(" STR(a) ")\n\ts_cmp_" c "_u32 %[wv], 4\n\ts_cbranch_scc1 1 ; throttle\n\ts_waitcnt vmcnt(" STR(b) ")"
      if (wave >= 4) { GU2(0); GU2(1); TL(20); }
      asm volatile(WAIT2("ge", 5, 1) : "+v"(rd[0]), "+v"(rd[1]), "+v"(rd[2]), "+v"(rd[3]), "+v"(rd[4]), "+v"(rd[5]), "+v"(rd[6]), "+v"(rd[7]) : [wv] "s"(wave) : "memory", "scc");
#pragma unroll
      for (int j = 0; j < 4; ++j)
#pragma unroll
        for (int t = 0; t < 4; ++t) {
          const u32 a = (t & 1) ? phi(rd[2 * j][t >> 1], rd[2 * j + 1][t >> 1]) : plo(rd[2 * j][t >> 1], rd[2 * j + 1][t >> 1]);
          const u32 b_ = (t & 1) ? phi(rd[2 * j][2 + (t >> 1)], rd[2 * j + 1][2 + (t >> 1)]) : plo(rd[2 * j][2 + (t >> 1)], rd[2 * j + 1][2 + (t >> 1)]);
          wd8[t][j] = (u32)__builtin_amdgcn_update_dpp((int)a, (int)b_, 0x128, 0xf, 0xc, false);
          wd8[t][4 + j] = (u32)__builtin_amdgcn_update_dpp((int)b_, (int)a, 0x128, 0xf, 0x3, false);
        }
#pragma unroll
      for (int t = 0; t < 4; ++t) asm volatile("" : "+v"(wd8[t]));
      TL(21);
    }
    f32x4 dd[5] = {};
    int ys = gs < 64 ? 32768 : 2048;
    asm volatile("" : "+{v[176:179]}"(dd[0]), "+{v[180:183]}"(dd[1]), "+{v[184:187]}"(dd[2]), "+{v[188:191]}"(dd[3]), "+{v[192:195]}"(dd[4]), "+s"(ys));
    f32x4 d = {0.f, 0.f, 0.f, 0.f};
    int h0 = W_H2 + (b >> 2) * 16384 + ((xcd >> 2) * 8192 ^ flip) + (b & 3) * 1152 + lane * 16;
    int a0 = L_B + (8 * grp + (n >> 2)) * 128 + ((((wave & 3) * 2 + ((lane >> 1) & 1)) ^ ((n >> 2) & 2) ^ ((grp & 1) * 4)) << 4) + (lane & 1) * 8;
    asm volatile("" : "+{v[196:199]}"(d), "+v"(h0), "+v"(a0));
#define WD4() do { const u32x2 r0 = trd(lds, a0), r1 = trd(lds, a0 + 512), r2 = trd(lds, a0 + 4096), r3 = trd(lds, a0 + 4608); \
      wd8[4] = u32x8{r0[0], r0[1], r1[0], r1[1], r2[0], r2[1], r3[0], r3[1]}; } while (0)
    {
      u32x4 hp[2]; u32 cnt;
      if (wave < 4) { GU2(2); GU2(3); GU2(4); GU2(5); GU2(6); GU2(7); GU2(8); }
      asm volatile(WAIT2("lt", 10, 0) "\n\ts_waitcnt lgkmcnt(0)\n\ts_barrier" :: [wv] "s"(wave) : "memory", "scc");
      TL(40);
      GUF(2, 4); WD4();
      SCHED();
      if (wave < 4) { GU2(9); GU2(10); GU2(11); GU2(12); }
      asm volatile("s_waitcnt vmcnt(10)\n\ts_barrier" ::: "memory");
      TL(23);
      GUF(4, 6);
      SCHED();
      if (wave < 4) { GU2(13); TL(20); }
      asm volatile("s_waitcnt vmcnt(8)\n\ts_barrier\n"
                   "s_mov_b64 exec, 0xff\nbuffer_load_dwordx4 %1, %2, %3, 0 offen offset:1024 sc1\ns_mov_b64 exec, -1\nbuffer_load_dwordx4 %0, %2, %3, 0 offen sc1"
                   : "=&{v[204:207]}"(hp[0]), "=&{v[208:211]}"(hp[1]) : "v"(h0), "s"(rs) : "memory", "scc");
      TL(50);
      GUF(6, 7);
      asm volatile("s_waitcnt vmcnt(6)\n\ts_barrier" ::: "memory");
      GUF(7, 8);
      asm volatile("s_waitcnt vmcnt(2)\n\ts_barrier" ::: "memory");
      GUF(8, 9);
#pragma unroll
      for (int s = 0; s < 9; ++s) asm volatile("" : "+v"(bw[s]));
      asm volatile("" : "+v"(wd8[4]));
      TL(12);
      asm volatile(
        "s_mov_b32 %3, 0x8000\ns_branch 1f ; throttle\n0:\ns_sleep 2\ns_waitcnt lgkmcnt(0)\ns_and_b64 exec, vcc, 0xff\nbuffer_load_dwordx4 %2, %4, %6, 0 offen offset:1024 sc1 ; throttle\n"
        "s_mov_b64 exec, vcc\nbuffer_load_dwordx4 %1, %4, %6, 0 offen sc1 ; throttle\n1:\ns_mov_b64 exec, -1\nv_mov_b32 v212, 0\ns_mov_b64 exec, 0xff\ns_sub_u32 %3, %3, 1\ns_waitcnt vmcnt(1)\n"
        "ds_write_b128 %5, %2 offset:8192\nv_max_u32 v212, v208, v209\nv_max3_u32 v212, v212, v210, v211\ns_mov_b64 exec, -1\ns_waitcnt vmcnt(0)\nds_write_b128 %5, %1\n"
        "v_max3_u32 v212, v212, v204, v205\nv_max3_u32 v212, v212, v206, v207\nv_cmp_eq_u32 vcc, -1, v212\ns_cbranch_scc1 2f ; throttle\ns_cbranch_vccnz 0b ; throttle\n2:\n" ATL(13)
        "s_waitcnt lgkmcnt(0)\ns_barrier"
        : "+v"(tlv_), "+{v[204:207]}"(hp[0]), "+{v[208:211]}"(hp[1]), "=&s"(cnt) : "v"(h0), "v"(ia), "s"(rs) : "memory", "vcc", "scc", "v212", "s72", "s73");
      T2(47, );
      u32x4 af[9];
#pragma unroll
      for (int s = 0; s < 9; ++s) af[s] = *(const u32x4*)(lds + ra + s * 1024);
      SCHED();
#pragma unroll
      for (int s = 0; s < 9; ++s) d = smf(af[s], bw[s], d, ix);
      T2(48, "+v"(d));
      asm volatile("s_nop 7\nv_permlane32_swap_b32 v197, v199\nv_permlane32_swap_b32 v196, v198\nv_add_f32 v197, v197, v199\nv_add_f32 v196, v196, v198\n"
        "v_cvt_pk_bf16_f32 v197, 0, v197\nv_cvt_pk_bf16_f32 v198, 0, v196\nv_cvt_pk_bf16_f32 v196, 0, v196\nv_mov_b32_dpp v196, v197 row_ror:8 row_mask:0xf bank_mask:0xc\n"
        "v_mul_f32 v199, 0xbfb8aa3b, v196\nv_exp_f32 v199, v199\nv_mov_b32_dpp v197, v198 row_ror:8 row_mask:0xf bank_mask:0x3\nv_add_f32 v199, 1.0, v199\nv_rcp_f32 v198, v199\ns_nop 0\n"
        "v_mul_f32 v200, v196, v198\nv_fma_f32 v201, -v200, v199, v196\nv_fmac_f32 v200, v201, v198\nv_div_fixup_f32 v196, v200, v199, v196\n"
        "v_cvt_pk_bf16_f32 v196, 0, v196\nv_mul_f32 v196, v196, v197\nv_cvt_pk_bf16_f32 v196, v196, v196\nds_write_b16 %1, v196\ns_waitcnt lgkmcnt(0)\ns_barrier"
        : "+{v[196:199]}"(d) : "v"(tw) : "memory", "v200", "v201");
    }
    TL(14);
    {
      const u32x4 af = *(const u32x4*)(lds + tr_), a4 = *(const u32x4*)(lds + tr4);
      SCHED();
      if (wave == 7) { ald128sc(gq, ws, fo[0]); ald128sc(xq, ws, fo[1]); ald128sc(rq, ws, fo[2]); }
      asm volatile(
        "v_smfmac_f32_16x16x64_bf16 v[176:179], %5, %7, %12\nv_smfmac_f32_16x16x64_bf16 v[180:183], %5, %8, %12\nv_smfmac_f32_16x16x64_bf16 v[184:187], %5, %9, %12\n"
        "v_smfmac_f32_16x16x64_bf16 v[188:191], %5, %10, %12\nv_smfmac_f32_16x16x64_bf16 v[192:195], %6, %11, %12\ns_nop 3\nv_permlane32_swap_b32 v176, v178\n"
        "v_permlane32_swap_b32 v180, v182\nv_permlane32_swap_b32 v184, v186\nv_permlane32_swap_b32 v188, v190\nv_add_f32 v196, v176, v178\nv_add_f32 v197, v180, v182\n"
        "v_add_f32 v198, v184, v186\nv_add_f32 v199, v188, v190\nbuffer_store_dwordx4 v[196:199], %13, %15, 0 offen sc1\nv_permlane32_swap_b32 v177, v179\n"
        "v_permlane32_swap_b32 v181, v183\nv_permlane32_swap_b32 v185, v187\nv_permlane32_swap_b32 v189, v191\nv_add_f32 v200, v177, v179\nv_add_f32 v201, v181, v183\n"
        "v_add_f32 v202, v185, v187\nv_add_f32 v203, v189, v191\nbuffer_store_dwordx4 v[200:203], %13, %15, %16 offen sc1\nv_permlane32_swap_b32 v192, v194\nv_add_f32 v193, v192, v194\n"
        "buffer_store_dword v193, %14, %15, 0 offen sc1\n"
        : "+{v[176:179]}"(dd[0]), "+{v[180:183]}"(dd[1]), "+{v[184:187]}"(dd[2]), "+{v[188:191]}"(dd[3]), "+{v[192:195]}"(dd[4])
        : "v"(af), "v"(a4), "v"(wd8[0]), "v"(wd8[1]), "v"(wd8[2]), "v"(wd8[3]), "v"(wd8[4]), "v"(ix), "v"(yo[0]), "v"(yo[1]), "s"(rs), "s"(ys)
        : "memory", "v196", "v197", "v198", "v199", "v200", "v201", "v202", "v203");
    }
    TL(15);
    {
      float gbv = 0.f, x1v = 0.f, h2 = 0.f, rsel = 0.f;
      const int gy = (idx - 23) * 4 + (xcd & 3);
      int y0 = gy * 32768, y1 = gy * 32768 + 16384, y2_ = (72 + (gy >> 2)) * 16384 + (gy & 3) * 2048;
      u32x4 ya, yb, yc;
#define YLD(c) "s_mov_b64 exec, s[68:69]\nbuffer_load_dwordx4 v[48:51], %[o], %[rs], %[y2] offen sc1" c "\ns_mov_b64 exec, s[64:65]\nbuffer_load_dwordx4 v[40:43], %[o], %[rs], %[y0] offen sc1" c "\n" \
        "s_mov_b64 exec, s[66:67]\nbuffer_load_dwordx4 v[44:47], %[o], %[rs], %[y1] offen sc1" c "\ns_mov_b64 exec, -1\n"
      asm volatile("v_mov_b64 v[48:49], 0\nv_mov_b64 v[50:51], 0\ns_mov_b64 s[64:65], -1\ns_mov_b64 s[66:67], -1\ns_mov_b32 s68, %[m]\ns_mov_b32 s69, %[m]\ns_sleep 9\n" YLD("")
        : "=&{v[40:43]}"(ya), "=&{v[44:47]}"(yb), "=&{v[48:51]}"(yc), [y0] "+s"(y0), [y1] "+s"(y1), [y2] "+s"(y2_) : [o] "v"(fo[3]), [rs] "s"(rs), [m] "s"((wave - 2) >> 31)
        : "memory", "s64", "s65", "s66", "s67", "s68", "s69");
      if (wave == 7) {
        asm volatile("s_waitcnt vmcnt(5)" : "+v"(gq), "+v"(xq), "+v"(rq) :: "memory");
        if (!__all((tag_x(gq, e0, e1) | tag_x(xq, e0, e1) | tag_x(rq, e0, e1)) == 0u)) {
          int sp = 0;
          _Pragma("nounroll") do { nap(1); gq = ld128(rs, fo[0]); xq = ld128(rs, fo[1]); rq = ld128(rs, fo[2]); } while (!__all((tag_x(gq, e0, e1) | tag_x(xq, e0, e1) | tag_x(rq, e0, e1)) == 0u) && ++sp < SPIN_CAP);
        }
        const u32 gw = (lane & 2) ? gq[1] : gq[0], xw = (lane & 2) ? xq[1] : xq[0];
        gbv = (lane & 1) ? hi16(gw) : lo16(gw); x1v = (lane & 1) ? hi16(xw) : lo16(xw);
        h2 = b2f(*(const u16*)(lds + fo[4])); rsel = __uint_as_float(rq[0]);
        asm volatile("" : "+v"(gbv), "+v"(x1v), "+v"(h2), "+v"(rsel));
      }
      rsrc_t ro = __builtin_amdgcn_make_buffer_rsrc(out, 0, 8 * D * 2, 0x00020000);
      asm volatile("" : "+s"(ro));
      T2(51, );
      u32 cnt;
      asm volatile(
        "s_mov_b32 %1, 0x8000\ns_branch 1f ; throttle\n0:\n"
        "v_max_u32 v52, v40, v41 ; throttle\nv_max_u32 v53, v44, v45 ; throttle\nv_max_u32 v54, v48, v49 ; throttle\nv_max3_u32 v52, v52, v42, v43 ; throttle\n"
        "v_max3_u32 v53, v53, v46, v47 ; throttle\nv_max3_u32 v54, v54, v50, v51 ; throttle\nv_cmp_eq_u32 s[64:65], -1, v52\nv_cmp_eq_u32 s[66:67], -1, v53\nv_cmp_eq_u32 s[68:69], -1, v54\n"
        "s_or_b64 vcc, s[64:65], s[66:67]\ns_or_b64 vcc, vcc, s[68:69]\ns_cbranch_scc0 2f ; throttle\ns_sub_u32 %1, %1, 1\ns_cbranch_scc1 2f ; throttle\n" YLD(" ; throttle") "s_waitcnt vmcnt(0) ; throttle\n1:\ns_waitcnt vmcnt(1)\n"
        "v_pk_add_f32 v[56:57], v[40:41], v[48:49]\nv_pk_add_f32 v[58:59], v[42:43], v[50:51]\ns_waitcnt vmcnt(0)\nv_pk_add_f32 v[56:57], v[56:57], v[44:45]\nv_pk_add_f32 v[58:59], v[58:59], v[46:47]\n"
        "s_nop 1\nv_permlane32_swap_b32 v56, v58\nv_permlane32_swap_b32 v57, v59\nv_pk_add_f32 v[56:57], v[56:57], v[58:59]\ns_nop 1\nv_permlane16_swap_b32 v56, v57\nv_add_f32 v55, v56, v57\n"
        "ds_write_b32 %[w], v55\nv_cmp_u_f32 vcc, v55, v55\ns_cbranch_vccnz 0b ; throttle\n2:\n" ATL(16) "s_waitcnt lgkmcnt(0)\ns_barrier"
        : "+v"(tlv_), "=&s"(cnt), "+{v[40:43]}"(ya), "+{v[44:47]}"(yb), "+{v[48:51]}"(yc)
        : [o] "v"(fo[3]), [w] "v"(lds_base + mr), [rs] "s"(rs), [y0] "s"(y0), [y1] "s"(y1), [y2] "s"(y2_)
        : "memory", "vcc", "scc", "s64", "s65", "s66", "s67", "s68", "s69", "s72", "s73", "v52", "v53", "v54", "v55", "v56", "v57", "v58", "v59");
      T2(53, );
      if (wave == 7) {
        f32x2 t[4];
#pragma unroll
        for (int ww = 0; ww < 4; ++ww) t[ww] = f32x2{*(const float*)(lds + fo[5] + ww * 512), *(const float*)(lds + fo[5] + ww * 512 + 256)};
        SCHED();
        const f32x2 t2 = (t[0] + t[1]) + (t[2] + t[3]);
        const float y2 = rbf(rbf(t2.x + t2.y) * rsel);
        const float res = x1v + rbf(gbv * rbf(h2 + y2));
        __builtin_amdgcn_raw_buffer_store_b16((u16)(cvt2(res, 0.f) & 0xffffu), ro, fo[6], 0, 0);
      }
      TL(17);
      TLOUT();
      asm volatile(".Lcode_end:");
      __builtin_amdgcn_endpgm();
    }
  }
  {
  int t3_ = threadIdx.x; asm volatile("" : "+v"(t3_));
  const int tid = t3_, lane = tid & 63, n = lane & 15, grp = lane >> 4;
  if (qs >= 0) {
    const u16* mat = (wave & 1) ? wg : wu;
    BAR();
    const int g0 = ((3 - (int)((uintptr_t)mat >> 7) - xcd * 9) & 7) + (wave >= 4 ? 8 : 0);
    if (g0 < 9) dma_keep(mat, (128 + (idx - 6) * 40 + ((wave >> 1) & 1) * 20 + min(lane, 19)) * 9216 + (xcd * 9 + g0) * 128, lds_base + L_W + wave * 256);
    const int rr = lane / 9;
#define QH(r0, c) do { if (lane < 9 * (c)) dma_keep(mat, ((r0) + ((idx - 6) * 4 + (wave >> 1)) * (c) + rr) * 9216 + (xcd * 9 + lane - rr * 9) * 128, lds_base + L_W + wave * 256); } while (0)
    QH(128, 2); QH(192, 3); QH(352, 2);
  }
#define MH(r0, c) do { const int l9 = min(lane, 9 * (c) - 1), rr = l9 / 9; \
    dma_keep((wave & 1) ? wg : wu, ((r0) + ((idx & 3) * 4 + (wave >> 1)) * (c) + rr) * 9216 + (xcd * 9 + l9 - rr * 9) * 128, lds_base + 98304 + wave * 256); } while (0)
  if (mw == 16 || mw == 17) { BAR(); MH(288, 4); MH(544, 2); }
  if (ml >= 0) {
    BAR();
    if (mw < 18) { ML(0); ML(1); ML(2); ML(3); }
    ML(4); ML(5); ML(6); ML(7); ML(8);
    TL(38);
    u32x4 af[9];
    f32x4 d = {0.f, 0.f, 0.f, 0.f};
    const int q = n >> 2, ch = (ml - 18) / 9, sl = ml - 18 - ch * 9;
    int a0 = L_W + ((grp >> 1) * 16 + (grp & 1) * 4 + (q & 1) + (q >> 1) * 8) * 128 + (((wave & 3) ^ ((grp & 1) + (q & 2))) << 5) + (lane & 3) * 8, ix = n < 8 ? 0x4444 : 0xeeee;
    int mo = (ch < 3 ? W_M + ((grp >> 1) * 2 + (n & 1)) * 16384 + ((grp & 1) * 8192 ^ flip) + ch * 2304 : W_GB + ((grp & 1) * 4 + (grp >> 1) * 2 + (n & 1)) * 2304) + (sl * 16 + wave * 4 + q) * 16;
#pragma unroll
    for (int s = 0; s < 9; ++s) af[s] = *(const u32x4*)(lds + L_IM2 + (grp * 16 + (n & 8) + ((n & 7) ^ (grp * 2 + (n >> 3)))) * 16 + s * 1024);
#define MLS(S0, S1) do { if (wave < 4) { u32x8 f_[S1 - S0]; _Pragma("unroll") for (int s = S0; s < S1; ++s) { const int a_ = a0 + (s >> 2) * 32768, o_ = (s & 3) * 8192; \
      const u32x2 r0 = trd(lds, a_ + o_), r1 = trd(lds, a_ + o_ + 256), r2 = trd(lds, a_ + o_ + 4096), r3 = trd(lds, a_ + o_ + 4352); \
      f_[s - S0] = u32x8{r0[0], r0[1], r1[0], r1[1], r2[0], r2[1], r3[0], r3[1]}; } SCHED(); \
      _Pragma("unroll") for (int s = S0; s < S1; ++s) d = smf(af[s], f_[s - S0], d, ix); } } while (0)
    asm volatile("s_waitcnt vmcnt(5)\n\ts_barrier" : "+v"(a0), "+v"(ix), "+v"(mo) :: "memory");
    MH(288, 4);
    MLS(0, 4);
    asm volatile("s_waitcnt vmcnt(3)\n\ts_barrier" : "+v"(d) :: "memory");
    MLS(4, 7);
    asm volatile("s_waitcnt vmcnt(1)\n\ts_barrier" : "+v"(d), "+v"(af[7]), "+v"(af[8]) :: "memory");
    TL(39);
    MLS(7, 9);
    if (wave < 4) {
      float r[2];
#pragma unroll
      for (int j = 0; j < 2; ++j) {
        const auto t = __builtin_amdgcn_permlane32_swap(__float_as_uint(d[j]), __float_as_uint(d[j + 2]), false, false);
        r[j] = __uint_as_float(t[0]) + __uint_as_float(t[1]);
      }
      const float o = dppf<0xB1>((n & 1) ? r[0] : r[1]);
      const u32 pk = (n & 1) ? cvt2(o, r[1]) : cvt2(r[0], o);
      const u32 p2 = (u32)__builtin_amdgcn_mov_dpp((int)pk, 0x4E, 0xf, 0xf, true);
      if (!(n & 2)) st128(rs, mo, u32x4{pk, p2, e0, e1});
    }
    if (ml < 45) TL(18); else TL(36);
    if (mw >= 18) { BAR(); MH(544, 2); }
  }

  if (lnb >= 0 && wave < 3) {
    const int b = lnb, g = lane < 48 ? wave * 48 + lane : 0, hb = (b >> 2) * 8192 ^ flip;
    const bool on = lane < 48;
    float* ex = (float*)(lds + L_TILE);
    const u32x2 xa = __builtin_amdgcn_raw_buffer_load_b64(__builtin_amdgcn_make_buffer_rsrc((void*)x, 0, 8 * D * 2, 0x00020000), (b * D + g * 4) * 2, 0, 0);
    const u32x2 wra = __builtin_amdgcn_raw_buffer_load_b64(__builtin_amdgcn_make_buffer_rsrc((void*)w_r, 0, D * 2, 0x00020000), g * 8, 0, 0);
    f32x2 wr[2] = {up2(wra[0]), up2(wra[1])};
    asm volatile("" : "+v"(wr[0]), "+v"(wr[1]));
    const int ho = W_H2 + (b >> 2) * 16384 + hb + (b & 3) * 1152 + g * 8;
    u32x2 hw; u32 cnt;
#define XPL(c) "buffer_load_dwordx4 v[100:103], %5, %12, 0 offen sc1" c "\nbuffer_load_dwordx4 v[104:107], %5, %12, 0 offen offset:2304 sc1" c "\n" \
      "buffer_load_dwordx4 v[108:111], %5, %12, %14 offen sc1" c "\nbuffer_load_dwordx4 v[112:115], %5, %12, %15 offen sc1" c "\n" \
      "buffer_load_dwordx4 v[116:119], %5, %12, %15 offen offset:2304 sc1" c "\nbuffer_load_dwordx4 v[120:123], %5, %12, %16 offen sc1" c "\n" \
      "buffer_load_dwordx4 v[124:127], %5, %12, %17 offen sc1" c "\nbuffer_load_dwordx4 v[128:131], %5, %12, %17 offen offset:2304 sc1" c "\n"
#define RB(d, s) "v_cvt_pk_bf16_f32 v" #d ", 0, v" #s "\n"
#define RB4 RB(132, 132) RB(133, 133) RB(134, 134) RB(135, 135)
#define UP4(a, b, c, d) "v_lshlrev_b32 v" #c ", 16, v" #b "\nv_and_b32 v" #d ", 0xffff0000, v" #b "\nv_and_b32 v" #b ", 0xffff0000, v" #a "\nv_lshlrev_b32 v" #a ", 16, v" #a "\n"
#define ACC(n, a, b) "s_waitcnt vmcnt(" #n ")\nv_pk_add_f32 v[132:133], v[132:133], " a "\nv_pk_add_f32 v[134:135], v[134:135], " b "\n"
#define RED(c) "s_nop 1\nv_add_f32_dpp v150, v150, v150 " c "\n"
#define SUB " op_sel_hi:[1,0] neg_lo:[0,1] neg_hi:[0,1]\n"
    asm volatile(
      "s_mov_b32 s68, 0x3ae38e39\nv_mov_b64 v[150:151], 0\nv_mov_b64 v[154:155], %13\ns_mov_b32 %2, 0x8000\n0:\n"
      "buffer_load_dwordx4 v[156:159], %6, %12, 0 offen sc1\nbuffer_load_dwordx4 v[160:163], %6, %12, 0 offen offset:2304 sc1\nbuffer_load_dwordx4 v[164:167], %6, %12, %14 offen sc1\n"
      XPL("") "s_waitcnt vmcnt(8)\nv_cmp_ne_u64 s[64:65], %13, v[158:159]\nv_cmp_ne_u64 s[66:67], %13, v[162:163]\nv_cmp_ne_u64 vcc, %13, v[166:167]\n"
      "s_or_b64 s[64:65], s[64:65], s[66:67]\ns_or_b64 vcc, vcc, s[64:65]\ns_cbranch_scc0 4f ; throttle\ns_sleep 2\ns_sub_u32 %2, %2, 1\ns_cbranch_scc0 0b ; throttle\n4:\n" ATL(37)
      UP4(164, 165, 166, 167) UP4(156, 157, 158, 159) "v_pk_add_f32 v[164:165], v[164:165], 1.0 op_sel_hi:[1,0]\nv_pk_add_f32 v[166:167], v[166:167], 1.0 op_sel_hi:[1,0]\n"
      UP4(160, 161, 162, 163) RB(164, 164) RB(165, 165) RB(166, 166) RB(167, 167) "s_mov_b32 %2, 0x8000\ns_branch 1f ; throttle\n3:\n"
      "v_max3_u32 v142, v100, v101, v102 ; throttle\nv_max3_u32 v143, v103, v104, v105 ; throttle\nv_max3_u32 v144, v106, v107, v108 ; throttle\nv_max3_u32 v145, v109, v110, v111 ; throttle\n"
      "v_max3_u32 v142, v142, v112, v113 ; throttle\nv_max3_u32 v143, v143, v114, v115 ; throttle\nv_max3_u32 v144, v144, v116, v117 ; throttle\nv_max3_u32 v145, v145, v118, v119 ; throttle\n"
      "v_max3_u32 v142, v142, v120, v121 ; throttle\nv_max3_u32 v143, v143, v122, v123 ; throttle\nv_max3_u32 v144, v144, v124, v125 ; throttle\nv_max3_u32 v145, v145, v126, v127 ; throttle\n"
      "v_max3_u32 v142, v142, v128, v129 ; throttle\nv_max3_u32 v143, v143, v130, v131 ; throttle\nv_max3_u32 v142, v142, v144, v145\nv_max_u32 v142, v142, v143\n"
      "v_cmp_eq_u32 vcc, -1, v142\ns_cbranch_vccz 2f ; throttle\ns_sub_u32 %2, %2, 1\ns_cbranch_scc1 2f ; throttle\n" XPL(" ; throttle")
      "1:\ns_waitcnt vmcnt(6)\nv_pk_add_f32 v[132:133], v[100:101], v[104:105]\nv_pk_add_f32 v[134:135], v[102:103], v[106:107]\n"
      ACC(5, "v[108:109]", "v[110:111]") ACC(4, "v[112:113]", "v[114:115]") ACC(3, "v[116:117]", "v[118:119]") ACC(2, "v[120:121]", "v[122:123]")
      ACC(1, "v[124:125]", "v[126:127]") ACC(0, "v[128:129]", "v[130:131]")
      RB(132, 132) RB(133, 133) "v_pk_add_f32 v[142:143], v[132:133], v[134:135]\n" RB(134, 134) "v_cmp_u_f32 vcc, v142, v143\n" RB(135, 135) "s_cbranch_vccnz 3b ; throttle\n2:\n" ATL(10)
      "v_pk_mul_f32 v[132:133], v[132:133], v[156:157]\nv_pk_mul_f32 v[134:135], v[134:135], v[158:159]\n" RB4
      "v_pk_add_f32 v[132:133], v[132:133], %3\nv_pk_add_f32 v[134:135], v[134:135], %4\n" RB(136, 132) RB(137, 133) "v_readlane_b32 s70, v136, 63\n" RB(138, 134) RB(139, 135)
      "v_pk_add_f32 v[142:143], v[136:137], s[70:71]" SUB "v_pk_add_f32 v[144:145], v[138:139], s[70:71]" SUB
      "v_pk_mul_f32 v[146:147], v[142:143], v[142:143]\nv_pk_add_f32 v[148:149], v[142:143], v[144:145]\nv_pk_fma_f32 v[146:147], v[144:145], v[144:145], v[146:147]\n"
      "v_cvt_pk_bf16_f32 v152, v136, v137\nv_add_f32_dpp v150, v148, v149 quad_perm:[0,1,2,3] row_mask:0x7 bank_mask:0xf\n"
      "v_add_f32_dpp v151, v146, v147 quad_perm:[0,1,2,3] row_mask:0x7 bank_mask:0xf\nv_cvt_pk_bf16_f32 v153, v138, v139\ns_nop 0\n"
      "v_permlane32_swap_b32 v150, v151\nv_add_f32 v150, v150, v151\n"
      RED("quad_perm:[1,0,3,2] row_mask:0xf bank_mask:0xf") RED("quad_perm:[2,3,0,1] row_mask:0xf bank_mask:0xf") RED("row_half_mirror row_mask:0xf bank_mask:0xf")
      RED("row_mirror row_mask:0xf bank_mask:0xf") RED("row_bcast:15 row_mask:0xa bank_mask:0xf")
      "ds_write_b32 %10, v150\ns_mov_b32 exec_hi, 0xffff\ns_waitcnt lgkmcnt(0)\ns_barrier\nds_read_b128 v[142:145], %11\nds_read_b64 v[146:147], %11 offset:16\n"
      "s_waitcnt lgkmcnt(1)\nv_pk_add_f32 v[142:143], v[142:143], v[144:145]\ns_waitcnt lgkmcnt(0)\nv_pk_add_f32 v[142:143], v[142:143], v[146:147]\n"
      "s_nop 0\nv_pk_mul_f32 v[142:143], v[142:143], s[68:69] op_sel_hi:[1,0]\ns_nop 0\nv_fma_f32 v144, -v142, v142, v143\nv_add_f32 v146, s70, v142\n" RB(144, 144) RB(146, 146)
      "v_add_f32 v144, 0x3727c5ac, v144\nv_pk_add_f32 v[132:133], v[136:137], v[146:147]" SUB RB(144, 144) "v_pk_add_f32 v[134:135], v[138:139], v[146:147]" SUB
      "v_sqrt_f32 v144, v144\n" RB(132, 132) RB(133, 133) RB(144, 144) RB(134, 134) "v_rcp_f32 v144, v144\n" RB(135, 135)
      "v_pk_mul_f32 v[132:133], v[132:133], v[144:145] op_sel_hi:[1,0]\nv_pk_mul_f32 v[134:135], v[134:135], v[144:145] op_sel_hi:[1,0]\n" RB4
      "v_pk_mul_f32 v[132:133], v[132:133], v[164:165]\nv_pk_mul_f32 v[134:135], v[134:135], v[166:167]\n" RB4
      "v_pk_add_f32 v[132:133], v[132:133], v[160:161]\nv_pk_add_f32 v[134:135], v[134:135], v[162:163]\n"
      "v_cvt_pk_bf16_f32 v140, v132, v133\nv_cvt_pk_bf16_f32 v141, v134, v135\n"
      "buffer_store_dwordx2 v[140:141], %7, %12, 0 offen sc1\nbuffer_store_dwordx2 v[140:141], %8, %12, 0 offen sc1\n"
      "buffer_store_dwordx4 v[152:155], %9, %12, 0 offen sc1\ns_mov_b32 exec_hi, -1\ns_nop 0\n"
      : "+v"(tlv_), "=&{v[140:141]}"(hw), "=&s"(cnt)
      : "v"(up2(xa[0])), "v"(up2(xa[1])), "v"(W_XP + (b & 3) * 49152 + hb + g * 16), "v"(W_M + (b & 3) * 16384 + hb + g * 16), "v"(ho ^ 8192), "v"(ho), "v"(W_X1 + (b * 152 + g) * 16),
        "v"(lds_base + L_TILE + ((lane & 31) == 16 ? wave * 8 + (lane >> 5) * 4 : 64 + tid * 4)), "v"(lds_base + L_TILE), "s"(rs), "s"(epoch), "s"(4608), "s"(16384), "s"(20992), "s"(32768)
      : "memory", "vcc", "scc", "s64", "s65", "s66", "s67", "s68", "s69", "s70", "s71", "s72", "s73",
        "v100", "v101", "v102", "v103", "v104", "v105", "v106", "v107", "v108", "v109", "v110", "v111", "v112", "v113", "v114", "v115", "v116", "v117", "v118", "v119",
        "v120", "v121", "v122", "v123", "v124", "v125", "v126", "v127", "v128", "v129", "v130", "v131", "v132", "v133", "v134", "v135", "v136", "v137", "v138", "v139",
        "v142", "v143", "v144", "v145", "v146", "v147", "v148", "v149", "v150", "v151", "v152", "v153", "v154", "v155", "v156", "v157", "v158", "v159",
        "v160", "v161", "v162", "v163", "v164", "v165", "v166", "v167");
    TL(11);
    const f32x2 r2 = up2(hw[0]) * wr[0] + up2(hw[1]) * wr[1];
    const float rw = wsum(on ? r2.x + r2.y : 0.f);
    if (lane == 0) ex[8 + wave] = rw;
    lds_barrier();
    if (tid < 8) {
      const float r = rbf((ex[8] + ex[9]) + ex[10]);
      st128(rs, W_X1 + (b * 152 + 144 + tid) * 16, u32x4{__float_as_uint(rbf(1.f / (1.f + __expf(-r)))), 0u, e0, e1});
    }
  }

  if (oj >= 0) {
    const int ob = W_O + ((op & 3) * 2 + (wave >> 2)) * 32768 + (lane >> 5) * 16384 + ((op >> 2) * 8192 ^ flip) + ((lane >> 3) & 3) * 2048 + (wave & 3) * 256 + (lane & 7) * 16;
    const int q = n >> 2;
    u32 cnt;
#define OTR(k, r, o) "ds_read_b64_tr_b16 v[136 + 8 * " #k " + " #r ":137 + 8 * " #k " + " #r "], %2 offset:8192 * " #k " + " #o "\nds_read_b64_tr_b16 v[138 + 8 * " #k " + " #r ":139 + 8 * " #k " + " #r "], %2 offset:8192 * " #k " + 256 + " #o "\n"
#define OTR8(l, r, o) "s_cmp_gt_u32 %10, 3\ns_cbranch_scc1 " #l "f ; throttle\n" OTR(0, r, o) OTR(1, r, o) OTR(2, r, o) OTR(3, r, o) OTR(4, r, o) OTR(5, r, o) OTR(6, r, o) OTR(7, r, o)
#define OLD(c) "s_mov_b64 exec, s[64:65]\nbuffer_load_dwordx4 v[100:103], %3, %8, 0 offen sc1" c "\nbuffer_load_dwordx4 v[104:107], %3, %8, 0 offen offset:128 sc1" c "\n" \
      "s_mov_b64 exec, s[66:67]\nbuffer_load_dwordx4 v[108:111], %3, %8, 0 offen offset:1024 sc1" c "\nbuffer_load_dwordx4 v[112:115], %3, %8, 0 offen offset:1152 sc1" c "\ns_mov_b64 exec, -1\n"
#define OMK "v_max3_u32 v116, v100, v101, v102\nv_max3_u32 v117, v108, v109, v110\nv_max3_u32 v116, v116, v103, v104\nv_max3_u32 v117, v117, v111, v112\n" \
      "v_max3_u32 v116, v116, v105, v106\nv_max3_u32 v117, v117, v113, v114\nv_max_u32 v116, v116, v107\nv_max_u32 v117, v117, v115\n" \
      "v_cmp_eq_u32 s[64:65], -1, v116\nv_cmp_eq_u32 s[66:67], -1, v117\n"
#define OSM(n, r, k) "s_waitcnt lgkmcnt(" #n ")\nv_smfmac_f32_16x16x64_bf16 v[132:135], " r ", v[136 + 8 * " #k ":143 + 8 * " #k "], %7\n"
    asm volatile(
      "s_mov_b64 s[64:65], -1\ns_mov_b64 s[66:67], -1\n" OLD("") OTR8(4, 0, 0) "4:\nv_mov_b64 v[132:133], 0\nv_mov_b64 v[134:135], 0\ns_mov_b32 %1, 0x8000\ns_waitcnt vmcnt(0)\n" OMK OLD("")
      "s_barrier\n" ATL(41) OTR8(1, 4, 4096) "s_branch 1f ; throttle\n0:\ns_sleep 2\n" OLD(" ; throttle")
      "1:\ns_waitcnt vmcnt(0) lgkmcnt(0)\nv_pk_add_f32 v[116:117], v[100:101], v[108:109]\nv_pk_add_f32 v[118:119], v[102:103], v[110:111]\n"
      "v_pk_add_f32 v[120:121], v[104:105], v[112:113]\nv_pk_add_f32 v[122:123], v[106:107], v[114:115]\nv_cvt_pk_bf16_f32 v124, v116, v117\nv_cvt_pk_bf16_f32 v125, v118, v119\n"
      "v_cvt_pk_bf16_f32 v126, v120, v121\nv_cvt_pk_bf16_f32 v127, v122, v123\nds_write_b128 %4, v[124:127]\n" OMK
      "s_sub_u32 %1, %1, 1\ns_cbranch_scc1 2f ; throttle\ns_or_b64 vcc, s[64:65], s[66:67]\ns_cbranch_scc1 0b ; throttle\n2:\n" ATL(8)
      "s_cmp_gt_u32 %10, 3\ns_waitcnt lgkmcnt(0)\ns_barrier\ns_cbranch_scc1 3f ; throttle\n"
      "ds_read_b128 v[100:103], %5\nds_read_b128 v[104:107], %5 offset:1024\nds_read_b128 v[108:111], %5 offset:2048\nds_read_b128 v[112:115], %5 offset:3072\n"
      "ds_read_b128 v[116:119], %5 offset:4096\nds_read_b128 v[120:123], %5 offset:5120\nds_read_b128 v[124:127], %5 offset:6144\nds_read_b128 v[128:131], %5 offset:7168\n"
      OSM(7, "v[100:103]", 0) OSM(6, "v[104:107]", 1) OSM(5, "v[108:111]", 2) OSM(4, "v[112:115]", 3) OSM(3, "v[116:119]", 4) OSM(2, "v[120:123]", 5) OSM(1, "v[124:127]", 6) OSM(0, "v[128:131]", 7)
      "s_nop 7\nv_permlane32_swap_b32 v132, v134\nv_permlane32_swap_b32 v133, v135\nv_pk_add_f32 v[132:133], v[132:133], v[134:135]\n"
      "buffer_store_dword v132, %6, %8, 0 offen sc1\nbuffer_store_dword v133, %6, %8, %9 offen sc1\n3:\n"
      : "+v"(tlv_), "=&s"(cnt)
      : "v"(lds_base + L_W + ((grp >> 1) * 16 + (grp & 1) * 4 + (q & 1) + (q >> 1) * 8) * 128 + ((wave ^ ((grp & 1) + (q & 2))) << 5) + (lane & 3) * 8), "v"(ob),
        "v"(lds_base + L_OI + wave * 1024 + (((lane >> 1) & 3) * 16 + (lane & 1) * 8 + ((lane >> 3) ^ (lane & 7))) * 16),
        "v"(lds_base + L_OI + (grp * 16 + (n & 8) + ((n & 7) ^ (grp * 2 + (n >> 3)))) * 16),
        "v"(W_XP + ((grp >> 1) * 6 + op / 3) * 16384 + ((grp & 1) * 8192 ^ flip) + op % 3 * 2304 + oj * 256 + wave * 64 + n * 4), "v"(n < 8 ? 0x4444 : 0xeeee), "s"(rs), "s"(49152), "s"(wave)
      : "memory", "vcc", "scc", "s64", "s65", "s66", "s67", "s72", "s73", V10(10), V10(11), V10(12), V10(13), V10(14), V10(15), V10(16), V10(17), V10(18), V10(19));
    TL(9);
    if (wave < 4 && lane < 32) {
#pragma unroll
      for (int i = 0; i < 2; ++i) dma_keep((wave & 1) ? wg : wu, (416 + (i * 2 + (wave >> 1)) * 32 + lane) * 9216 + (xcd * 9 + oj) * 128, lds_base + 98304 + wave * 256);
    }
  }
  TLOUT();
  }
  }
}

extern "C" void kernel_launch(void* const* d_in, const int* in_sizes, int n_in, void* d_out, int out_size, void* d_ws, size_t ws_size,
                              hipStream_t stream) {
  fused_kernel<<<NWG, NT, 0, stream>>>((const u16*)d_in[0], (const u16*)d_in[1], (const u16*)d_in[10], (const u16*)d_in[2], (const u16*)d_in[3], (const u16*)d_in[4],
                                       (const u16*)d_in[6], (const u16*)d_in[7], (const u16*)d_in[8], (const u16*)d_in[9],
                                       (const u16*)d_in[11], (unsigned char*)d_ws, (u16*)d_out);
}
```
